```python
import torch

def _softmax(x, axis=-1):
    return torch.softmax(x, dim=axis)

def _gelu(x):
    return torch.nn.functional.gelu(x, approximate='tanh')

def _silu(x):
    return torch.nn.functional.silu(x)

def _sigmoid(x):
    return torch.sigmoid(x)

def _layernorm(x, axis=-1, eps=1e-05):
    m = x.mean(dim=axis, keepdim=True)
    v = x.var(dim=axis, keepdim=True, unbiased=False)
    return (x - m) / torch.sqrt(v + eps)

def _scatter_add(tgt, idx, src, axis=0):
    return tgt.clone().index_add_(axis, idx, src)

def _segment_sum(src, seg, n):
    out = torch.zeros((n,) + src.shape[1:], dtype=src.dtype)
    return out.index_add_(0, seg, src)

def _pad(x, pw):
    flat = tuple((v for pair in reversed(pw) for v in pair))
    return torch.nn.functional.pad(x, flat)
kernel_name = 'adaLN_cond__mha_block_cached__glu_gated_reglu__prompt'

def setup_inputs():

    _w = lambda a: (lambda t: t.to(torch.bfloat16) if t.is_floating_point() else t)(torch.as_tensor(a))

    def _A_setup():
        B, Sq, D = (1, 2048, 576)
        H, Sk, Dh = (32, 2048, 128)
        return {'x': _w((lambda *s: torch.randn(*s, dtype=torch.bfloat16) * 0.05)(B, Sq, D)), 'k_cache': _w((lambda *s: torch.randn(*s, dtype=torch.bfloat16) * 1.6)(B, H, Sk, Dh)), 'v_cache': _w((lambda *s: torch.randn(*s, dtype=torch.bfloat16) * 0.8)(B, H, Sk, Dh)), 'wq': _w((lambda *s: torch.randn(*s, dtype=torch.bfloat16) * 1.6)(D, H * Dh) * D ** (-0.5)), 'wo': _w((lambda *s: torch.randn(*s, dtype=torch.bfloat16) * 0.8)(H * Dh, D) * (H * Dh) ** (-0.5))}
    _A_in = _A_setup()

    def _B_setup():
        B, S, D, F = (1, 2048, 576, 4608)
        return {'x': _w((lambda *s: torch.randn(*s, dtype=torch.bfloat16) * 0.05)(B, S, D)), 'W_gate': _w((lambda *s: torch.randn(*s, dtype=torch.bfloat16) * 0.4)(D, F) * (2.0 / D) ** 0.5), 'W_up': _w((lambda *s: torch.randn(*s, dtype=torch.bfloat16) * 0.4)(D, F) * (2.0 / D) ** 0.5), 'W_down': _w((lambda *s: torch.randn(*s, dtype=torch.bfloat16) * 0.4)(F, D) * (1.0 / F) ** 0.5)}
    _B_in = _B_setup()
    return {'A_x': _A_in['x'], 'A_k_cache': _A_in['k_cache'], 'A_v_cache': _A_in['v_cache'], 'A_wq': _A_in['wq'], 'A_wo': _A_in['wo'], 'B_x': _B_in['x'], 'B_W_gate': _B_in['W_gate'], 'B_W_up': _B_in['W_up'], 'B_W_down': _B_in['W_down'], 'c': _w((lambda *s: torch.randn(*s, dtype=torch.bfloat16) * 0.1)(1, 576) * 8.0), 'w_mod': _w((lambda *s: torch.randn(*s, dtype=torch.bfloat16) * 0.1)(576, 6 * 576) * 576 ** (-0.5) * 8.0)}

def reference(A_x, A_k_cache, A_v_cache, A_wq, A_wo, B_x, B_W_gate, B_W_up, B_W_down, c, w_mod):

    def _A_ref(x, k_cache, v_cache, wq, wo):
        B, Sq, D = x.shape
        H, Dh = (32, 128)
        q = (lambda x, *d: x.permute(*d))((x @ wq).reshape(B, Sq, H, Dh), 0, 2, 1, 3)
        scores = torch.einsum('bhqd,bhkd->bhqk', q, k_cache) * Dh ** (-0.5)
        p = _softmax(scores, axis=-1)
        o = torch.einsum('bhqk,bhkd->bhqd', p, v_cache)
        return (lambda x, *d: x.permute(*d))(o, 0, 2, 1, 3).reshape(B, Sq, H * Dh) @ wo

    def _B_ref(x, W_gate, W_up, W_down):
        gate = torch.einsum('bsd,df->bsf', x, W_gate)
        up = torch.einsum('bsd,df->bsf', x, W_up)
        gate = gate * (lambda x, dt: x.to(dt))(gate > 0, gate.dtype)
        h = gate * up
        return torch.einsum('bsf,fd->bsd', h, W_down)
    x = A_x
    mod = (_silu(c) @ w_mod).reshape(c.shape[0], 6, 1, -1)
    sa, ca, ga, sb, cb, gb = (mod[:, 0].clone(), mod[:, 1].clone(), mod[:, 2].clone(), mod[:, 3].clone(), mod[:, 4].clone(), mod[:, 5].clone())
    h = _layernorm(x, axis=-1) * (1 + ca) + sa
    x = x + ga * _A_ref(h, A_k_cache, A_v_cache, A_wq, A_wo)
    h = _layernorm(x, axis=-1) * (1 + cb) + sb
    x = x + gb * _B_ref(h, B_W_gate, B_W_up, B_W_down)
    return x
```

```cpp
#include <hip/hip_runtime.h>
#include <hip/hip_bf16.h>
#include <stdint.h>

typedef unsigned short u16;
typedef __attribute__((ext_vector_type(8))) short bf16x8;
typedef __attribute__((ext_vector_type(16))) float f32x16;
typedef __attribute__((ext_vector_type(4))) unsigned u32x4;
typedef __attribute__((ext_vector_type(4))) float f32x4;
#define DI __device__ __forceinline__

constexpr int S = 2048, D = 576, H = 32, DH = 128, HD = 4096, F = 4608;

DI float bf2f(u16 x) { return __uint_as_float(((unsigned)x) << 16); }
DI u16 f2bf(float x) { __hip_bfloat16 b = __float2bfloat16(x); return *reinterpret_cast<u16*>(&b); }
DI float rbf(float x) { return bf2f(f2bf(x)); }

DI long t32(int m, int k, int K) { return ((((long)(m >> 5) * (K >> 4) + (k >> 4)) * 64 + (m & 31) + 32 * ((k >> 3) & 1)) << 3) + (k & 7); }

__global__ __launch_bounds__(576) void k_mod(const u16* __restrict__ c, const u16* __restrict__ w, float* __restrict__ mod) {
  __shared__ float red[144][33];
  const int t = threadIdx.x, q = t & 3, r0 = t >> 2;
  const int col0 = blockIdx.x * 32 + q * 8;
  float acc[8] = {0, 0, 0, 0, 0, 0, 0, 0};
  u32x4 wv[4]; float sc[4];
#pragma unroll
  for (int i = 0; i < 4; ++i) { const int r = r0 + 144 * i; wv[i] = *(const u32x4*)(w + (long)r * 3456 + col0); sc[i] = bf2f(c[r]); }
#pragma unroll
  for (int i = 0; i < 4; ++i) {
    const float x = sc[i]; const float s = rbf(x / (1.f + __expf(-x)));
#pragma unroll
    for (int j = 0; j < 4; ++j) { acc[2 * j] += s * __uint_as_float(wv[i][j] << 16); acc[2 * j + 1] += s * __uint_as_float(wv[i][j] & 0xffff0000u); }
  }
#pragma unroll
  for (int j = 0; j < 8; ++j) red[r0][q * 8 + j] = acc[j];
  __syncthreads();
  if (t < 32 * 8) { const int cc = t & 31, g = t >> 5; float s = 0; for (int i = 0; i < 18; ++i) s += red[g * 18 + i][cc]; red[g * 18][cc] = s; }
  __syncthreads();
  if (t < 32) { float s = 0; for (int g = 0; g < 8; ++g) s += red[g * 18][t]; mod[blockIdx.x * 32 + t] = rbf(s); }
}

template <int NPART, bool FIRST>
__global__ __launch_bounds__(384) void k_ln(const u16* __restrict__ x, const float* __restrict__ part, const float* __restrict__ mod,
                                            u16* __restrict__ x1, u16* __restrict__ ht) {
  __shared__ float red[2][6][16];
  const int t = threadIdx.x, w = t >> 6, l = t & 63, r = l & 15, g = l >> 4;
  const int m = blockIdx.x * 16 + r;
  float v[3][8];
  const float* sh = mod + (FIRST ? 0 : 3 * D); const float* scp = mod + (FIRST ? D : 4 * D); const float* gt = mod + 2 * D;
#pragma unroll
  for (int i = 0; i < 3; ++i) {
    const int k0 = (12 * w + 4 * i + g) * 8;
    u32x4 xv = *(const u32x4*)(x + (long)m * D + k0);
#pragma unroll
    for (int j = 0; j < 4; ++j) { v[i][2 * j] = __uint_as_float(xv[j] << 16); v[i][2 * j + 1] = __uint_as_float(xv[j] & 0xffff0000u); }
    if (!FIRST) {
      float a[8] = {0, 0, 0, 0, 0, 0, 0, 0};
#pragma unroll
      for (int p = 0; p < NPART; ++p) {
        const float* pp = part + ((long)p * S + m) * D + k0;
        f32x4 a0 = *(const f32x4*)pp, a1 = *(const f32x4*)(pp + 4);
#pragma unroll
        for (int j = 0; j < 4; ++j) { a[j] += a0[j]; a[4 + j] += a1[j]; }
      }
      u32x4 o;
#pragma unroll
      for (int j = 0; j < 8; ++j) v[i][j] = rbf(v[i][j] + rbf(gt[k0 + j] * rbf(a[j])));
#pragma unroll
      for (int j = 0; j < 4; ++j) o[j] = (__float_as_uint(v[i][2 * j]) >> 16) | (__float_as_uint(v[i][2 * j + 1]) & 0xffff0000u);
      *(u32x4*)(x1 + (long)m * D + k0) = o;
    }
  }
  float s = 0;
#pragma unroll
  for (int i = 0; i < 3; ++i)
#pragma unroll
    for (int j = 0; j < 8; ++j) s += v[i][j];
  s += __shfl_xor(s, 16); s += __shfl_xor(s, 32);
  if (g == 0) red[0][w][r] = s;
  __syncthreads();
  float mean = 0;
#pragma unroll
  for (int i = 0; i < 6; ++i) mean += red[0][i][r];
  mean *= (1.f / D);
  float q = 0;
#pragma unroll
  for (int i = 0; i < 3; ++i)
#pragma unroll
    for (int j = 0; j < 8; ++j) { const float d = v[i][j] - mean; q += d * d; }
  q += __shfl_xor(q, 16); q += __shfl_xor(q, 32);
  if (g == 0) red[1][w][r] = q;
  __syncthreads();
  float var = 0;
#pragma unroll
  for (int i = 0; i < 6; ++i) var += red[1][i][r];
  var = rbf(var * (1.f / D));
  const float mb = rbf(mean);
  const float sd = rbf(sqrtf(rbf(var + 1e-5f)));
#pragma unroll
  for (int i = 0; i < 3; ++i) {
    const int k0 = (12 * w + 4 * i + g) * 8;
    float hv[8];
#pragma unroll
    for (int j = 0; j < 8; ++j) {
      const float n = rbf(rbf(v[i][j] - mb) / sd);
      hv[j] = rbf(rbf(n * rbf(1.f + scp[k0 + j])) + sh[k0 + j]);
    }
    u32x4 o;
#pragma unroll
    for (int j = 0; j < 4; ++j) o[j] = (__float_as_uint(hv[2 * j]) >> 16) | (__float_as_uint(hv[2 * j + 1]) & 0xffff0000u);
    *(u32x4*)(ht + t32(m, k0, D)) = o;
  }
}

template <int NPART>
__global__ __launch_bounds__(256) void k_final(const u16* __restrict__ x1, const float* __restrict__ part, const float* __restrict__ mod, u16* __restrict__ out) {
  const long i = ((long)blockIdx.x * 256 + threadIdx.x) * 8;
  const int k0 = (int)(i % D);
  u32x4 xv = *(const u32x4*)(x1 + i);
  float a[8] = {0, 0, 0, 0, 0, 0, 0, 0};
#pragma unroll
  for (int p = 0; p < NPART; ++p) {
    const float* pp = part + (long)p * S * D + i;
    f32x4 a0 = *(const f32x4*)pp, a1 = *(const f32x4*)(pp + 4);
#pragma unroll
    for (int j = 0; j < 4; ++j) { a[j] += a0[j]; a[4 + j] += a1[j]; }
  }
  const float* gt = mod + 5 * D + k0;
  float v[8];
#pragma unroll
  for (int j = 0; j < 4; ++j) { v[2 * j] = __uint_as_float(xv[j] << 16); v[2 * j + 1] = __uint_as_float(xv[j] & 0xffff0000u); }
#pragma unroll
  for (int j = 0; j < 8; ++j) v[j] = rbf(v[j] + rbf(gt[j] * rbf(a[j])));
  u32x4 o;
#pragma unroll
  for (int j = 0; j < 4; ++j) o[j] = (__float_as_uint(v[2 * j]) >> 16) | (__float_as_uint(v[2 * j + 1]) & 0xffff0000u);
  *(u32x4*)(out + i) = o;
}

template <int MODE>
__global__ __launch_bounds__(64) void k_gemm(const u16* __restrict__ At, const u16* __restrict__ W, const u16* __restrict__ W2, void* __restrict__ outp, int K, int N) {
  const int l = threadIdx.x, r = l & 31, h = l >> 5;
  const int nb = blockIdx.x, mb = blockIdx.y;
  f32x16 acc = {}, acc2 = {};
  for (int ks = 0; ks < K / 16; ++ks) {
    bf16x8 b = *(const bf16x8*)(At + (((long)mb * (K >> 4) + ks) * 64 + l) * 8);
    bf16x8 a, a2;
#pragma unroll
    for (int j = 0; j < 8; ++j) a[j] = (short)W[(long)(ks * 16 + 8 * h + j) * N + nb * 32 + r];
    acc = __builtin_amdgcn_mfma_f32_32x32x16_bf16(a, b, acc, 0, 0, 0);
    if (MODE == 2) {
#pragma unroll
      for (int j = 0; j < 8; ++j) a2[j] = (short)W2[(long)(ks * 16 + 8 * h + j) * N + nb * 32 + r];
      acc2 = __builtin_amdgcn_mfma_f32_32x32x16_bf16(a2, b, acc2, 0, 0, 0);
    }
  }
  const int m = mb * 32 + r;
#pragma unroll
  for (int i = 0; i < 16; ++i) {
    const int n = nb * 32 + (i & 3) + 8 * (i >> 2) + 4 * h;
    if (MODE == 0) ((u16*)outp)[t32(m, n, N)] = f2bf(acc[i] * 0.12751743082f);
    if (MODE == 1) ((float*)outp)[(long)m * N + n] = acc[i];
    if (MODE == 2) { const float gg = rbf(acc[i]), uu = rbf(acc2[i]); ((u16*)outp)[t32(m, n, N)] = f2bf(gg > 0.f ? gg * uu : 0.f); }
  }
}

__global__ __launch_bounds__(64) void k_attn(const u16* __restrict__ Qt, const u16* __restrict__ Kc, const u16* __restrict__ Vc, u16* __restrict__ Ot) {
  const int l = threadIdx.x, r = l & 31, h = l >> 5;
  const int mb = blockIdx.x, hd = blockIdx.y;
  bf16x8 q[8];
#pragma unroll
  for (int d0 = 0; d0 < 8; ++d0) q[d0] = *(const bf16x8*)(Qt + (((long)mb * (HD >> 4) + hd * 8 + d0) * 64 + l) * 8);
  const u16* Kh = Kc + (long)hd * S * DH; const u16* Vh = Vc + (long)hd * S * DH;
  f32x16 o[4] = {};
  float mx = -1e30f, lsum = 0.f;
  for (int kb = 0; kb < S / 32; ++kb) {
    f32x16 s = {};
#pragma unroll
    for (int d0 = 0; d0 < 8; ++d0) {
      bf16x8 kf = *(const bf16x8*)(Kh + (long)(kb * 32 + r) * DH + d0 * 16 + 8 * h);
      s = __builtin_amdgcn_mfma_f32_32x32x16_bf16(kf, q[d0], s, 0, 0, 0);
    }
    float tm = s[0];
#pragma unroll
    for (int i = 1; i < 16; ++i) tm = fmaxf(tm, s[i]);
    tm = fmaxf(tm, __shfl_xor(tm, 32));
    const float mn = fmaxf(mx, tm), f = exp2f(mx - mn);
    mx = mn;
    float ps = 0; bf16x8 p[2];
#pragma unroll
    for (int i = 0; i < 16; ++i) { const float e = exp2f(s[i] - mn); ps += e; p[i >> 3][i & 7] = (short)f2bf(e); }
    lsum = lsum * f + ps;
#pragma unroll
    for (int db = 0; db < 4; ++db) {
#pragma unroll
      for (int i = 0; i < 16; ++i) o[db][i] *= f;
#pragma unroll
      for (int ss = 0; ss < 2; ++ss) {
        bf16x8 vf;
#pragma unroll
        for (int j = 0; j < 8; ++j) vf[j] = (short)Vh[(long)(kb * 32 + 16 * ss + 8 * (j >> 2) + 4 * h + (j & 3)) * DH + db * 32 + r];
        o[db] = __builtin_amdgcn_mfma_f32_32x32x16_bf16(vf, p[ss], o[db], 0, 0, 0);
      }
    }
  }
  lsum += __shfl_xor(lsum, 32);
  const float inv = 1.f / lsum;
  const int m = mb * 32 + r;
#pragma unroll
  for (int db = 0; db < 4; ++db)
#pragma unroll
    for (int i = 0; i < 16; ++i) { const int d = db * 32 + (i & 3) + 8 * (i >> 2) + 4 * h; Ot[t32(m, hd * DH + d, HD)] = f2bf(o[db][i] * inv); }
}

extern "C" void kernel_launch(void* const* d_in, const int* in_sizes, int n_in, void* d_out, int out_size, void* d_ws, size_t ws_size, hipStream_t stream) {
  const u16* x = (const u16*)d_in[0]; const u16* kc = (const u16*)d_in[1]; const u16* vc = (const u16*)d_in[2];
  const u16* wq = (const u16*)d_in[3]; const u16* wo = (const u16*)d_in[4];
  const u16* wg = (const u16*)d_in[6]; const u16* wu = (const u16*)d_in[7]; const u16* wd = (const u16*)d_in[8];
  const u16* c = (const u16*)d_in[9]; const u16* wm = (const u16*)d_in[10];
  char* ws = (char*)d_ws;
  float* mod = (float*)ws;
  u16* h1t = (u16*)(ws + (1 << 20));
  u16* x1 = (u16*)(ws + (4 << 20));
  u16* h2t = (u16*)(ws + (8 << 20));
  u16* qt = (u16*)(ws + (16 << 20));
  u16* ot = (u16*)(ws + (48 << 20));
  u16* hm = (u16*)(ws + (80 << 20));
  float* part = (float*)(ws + (112 << 20));
  k_mod<<<108, 576, 0, stream>>>(c, wm, mod);
  k_ln<1, true><<<S / 16, 384, 0, stream>>>(x, nullptr, mod, nullptr, h1t);
  k_gemm<0><<<dim3(HD / 32, S / 32), 64, 0, stream>>>(h1t, wq, nullptr, qt, D, HD);
  k_attn<<<dim3(S / 32, H), 64, 0, stream>>>(qt, kc, vc, ot);
  k_gemm<1><<<dim3(D / 32, S / 32), 64, 0, stream>>>(ot, wo, nullptr, part, HD, D);
  k_ln<1, false><<<S / 16, 384, 0, stream>>>(x, part, mod, x1, h2t);
  k_gemm<2><<<dim3(F / 32, S / 32), 64, 0, stream>>>(h2t, wg, wu, hm, D, F);
  k_gemm<1><<<dim3(D / 32, S / 32), 64, 0, stream>>>(hm, wd, nullptr, part, F, D);
  k_final<1><<<S * D / 8 / 256, 256, 0, stream>>>(x1, part, mod, (u16*)d_out);
}
```

```cpp
#include <hip/hip_runtime.h>
#include <hip/hip_bf16.h>
#include <stdint.h>

typedef unsigned short u16;
typedef __attribute__((ext_vector_type(4))) unsigned u32x4;
typedef __attribute__((ext_vector_type(2))) unsigned u32x2;
typedef __attribute__((ext_vector_type(4))) float f32x4;
#define DI __device__ __forceinline__

constexpr int S = 2048, D = 576, H = 32, DH = 128, HD = 4096, F = 4608;
constexpr int NSPLIT = 8;

DI float bf2f(u16 x) { return __uint_as_float(((unsigned)x) << 16); }
DI u16 f2bf(float x) { __hip_bfloat16 b = __float2bfloat16(x); return *reinterpret_cast<u16*>(&b); }
DI float rbf(float x) { return bf2f(f2bf(x)); }
DI float blo(unsigned u) { return __uint_as_float(u << 16); }
DI float bhi(unsigned u) { return __uint_as_float(u & 0xffff0000u); }
DI unsigned pk(float lo, float hi) { return (__float_as_uint(lo) >> 16) | (__float_as_uint(hi) & 0xffff0000u); }


DI unsigned tid(unsigned wv) { unsigned l; asm volatile("v_mbcnt_lo_u32_b32 %0, -1, 0\n\tv_mbcnt_hi_u32_b32 %0, -1, %0" : "=v"(l)); return wv * 64u + l; }

DI void publish_flag(unsigned* flag, unsigned tag, unsigned wv) {
  asm volatile("s_waitcnt vmcnt(0)" ::: "memory");
  __syncthreads();
  if (tid(wv) == 0) __hip_atomic_store(flag, tag, __ATOMIC_RELAXED, __HIP_MEMORY_SCOPE_AGENT);
}
DI void wait_flags(unsigned* flags, unsigned n, unsigned tag, unsigned wv) {
  if (wv == 0) {
    unsigned* p = flags + (tid(wv) & (n - 1u));
    for (unsigned spins = 0; spins < (1u << 18); ++spins) {
      const unsigned v = __hip_atomic_load(p, __ATOMIC_RELAXED, __HIP_MEMORY_SCOPE_AGENT);
      if (__all(v == tag)) break;
      __builtin_amdgcn_s_sleep(1);
    }
  }
  asm volatile("" ::: "memory");
  __builtin_amdgcn_s_barrier();
  asm volatile("" ::: "memory");
}
template <int NP>
DI void sweep_mod(const char* gran, float* dst, unsigned tag, unsigned wv) {
  const unsigned t = tid(wv);
  const auto rg = __builtin_amdgcn_make_buffer_rsrc((void*)gran, 0, NP * 16, 0x20000);
  constexpr int NI = (NP + 255) / 256;
  for (unsigned spins = 0; spins < (1u << 18); ++spins) {
    u32x4 g[NI]; bool ok = true;
#pragma unroll
    for (int i = 0; i < NI; ++i) { const unsigned p = min(t + 256u * i, (unsigned)NP - 1u); g[i] = __builtin_amdgcn_raw_buffer_load_b128(rg, p * 16u, 0, 16); }
#pragma unroll
    for (int i = 0; i < NI; ++i) {
      const unsigned p = min(t + 256u * i, (unsigned)NP - 1u);
      ok = ok && g[i][1] == tag && g[i][3] == tag;
      *(u32x2*)(dst + 2u * p) = u32x2{g[i][0], g[i][2]};
    }
    if (__all(ok)) break;
    __builtin_amdgcn_s_sleep(1);
  }
  __syncthreads();
}
typedef __attribute__((ext_vector_type(2))) __bf16 bf16x2;
typedef __attribute__((ext_vector_type(2))) float f32x2;
DI unsigned cvtpk(float lo, float hi) { f32x2 v = {lo, hi}; return __builtin_bit_cast(unsigned, __builtin_convertvector(v, bf16x2)); }
DI float addsel(unsigned u, unsigned sel, float a) { return __builtin_amdgcn_fdot2_f32_bf16(__builtin_bit_cast(bf16x2, u), __builtin_bit_cast(bf16x2, sel), a, false); }
DI float xsum(float s) {
  s += __builtin_bit_cast(float, __builtin_amdgcn_update_dpp(0, __builtin_bit_cast(int, s), 0x128  , 0xf, 0xf, false));
  auto a = __builtin_amdgcn_permlane16_swap(__float_as_uint(s), __float_as_uint(s), false, false); s = __uint_as_float(a[0]) + __uint_as_float(a[1]);
  auto b = __builtin_amdgcn_permlane32_swap(__float_as_uint(s), __float_as_uint(s), false, false); s = __uint_as_float(b[0]) + __uint_as_float(b[1]);
  return s;
}
template <int CTRL> DI float dppadd(float s) { return s + __builtin_bit_cast(float, __builtin_amdgcn_update_dpp(0, __builtin_bit_cast(int, s), CTRL, 0xf, 0xf, false)); }
struct ModSlot {
  u32x4 w[5];
  DI void partial(const float (&s)[5], float* red, unsigned t, unsigned wv) {
    float a[8];
#pragma unroll
    for (int j = 0; j < 4; ++j) {
      a[2 * j] = s[0] * blo(w[0][j]); a[2 * j + 1] = s[0] * bhi(w[0][j]);
#pragma unroll
      for (int i = 1; i < 5; ++i) { a[2 * j] = fmaf(s[i], blo(w[i][j]), a[2 * j]); a[2 * j + 1] = fmaf(s[i], bhi(w[i][j]), a[2 * j + 1]); }
    }
    float b[4], d[2];
#pragma unroll
    for (int j = 0; j < 4; ++j) { auto r = __builtin_amdgcn_permlane32_swap(__float_as_uint(a[j]), __float_as_uint(a[j + 4]), false, false); b[j] = __uint_as_float(r[0]) + __uint_as_float(r[1]); }
#pragma unroll
    for (int j = 0; j < 2; ++j) { auto r = __builtin_amdgcn_permlane16_swap(__float_as_uint(b[j]), __float_as_uint(b[j + 2]), false, false); d[j] = __uint_as_float(r[0]) + __uint_as_float(r[1]); }
#pragma unroll
    for (int j = 0; j < 2; ++j) { d[j] = dppadd<0x128>(d[j]); d[j] = dppadd<0x124>(d[j]); d[j] = dppadd<0x4E>(d[j]); }
    const unsigned l = t & 63u;
    if ((l & 14u) == 0u) *(f32x2*)(red + wv * 16u + (l & 1u) * 8u + (l >> 5) * 4u + ((l >> 4) & 1u) * 2u) = f32x2{d[0], d[1]};
  }
  static DI void emit(const float* red, char* gran, unsigned col0, unsigned tag, unsigned j) {
    const unsigned gc = col0 + j, seg = gc / (unsigned)D;
    float r = rbf((red[j] + red[16 + j]) + (red[32 + j] + red[48 + j])); if (seg == 1u || seg == 4u) r = rbf(1.f + r);
    __hip_atomic_store((unsigned long long*)(gran + gc * 8u), ((unsigned long long)tag << 32) | __float_as_uint(r), __ATOMIC_RELAXED, __HIP_MEMORY_SCOPE_AGENT);
  }
};
template <int MODE, class Hook>
DI void row_phase(Hook hook, const u32x2* xpre, const u16* __restrict__ x, const u16* part, const float* sh, const float* scp, const float* gt, u32x4* x1s, float* red, u16* outp, unsigned row0, unsigned wv) {
  const unsigned t = tid(wv), w = wv, tok = t & 7u, g = (t >> 3) & 3u;
  const unsigned m = row0 + tok;
  const unsigned lane16 = ((m & 15u) + 16u * g) * 16u, tbase = (m >> 4) * 18u * 1024u;
  const unsigned pbase = ((m >> 8) * 128u + ((m >> 4) & 15u)) * 18432u;
  const auto rp = __builtin_amdgcn_make_buffer_rsrc((void*)part, 0, NSPLIT * S * D * 2, 0x20000);
  const auto ro = __builtin_amdgcn_make_buffer_rsrc((void*)outp, 0, S * D * 2, 0x20000);
  unsigned klo = 0x00003f80u, khi = 0x3f800000u; asm volatile("" : "+s"(klo), "+s"(khi));
  const int np = (w == 0) ? 3 : 2;
  float v[3][8];
  u32x4 pv[3][NSPLIT]; u32x2 xa[3], xb[3]; u32x4 xs[3];
  f32x4 g0[3], g1[3];
#pragma unroll
  for (int i = 0; i < 3; ++i) {
    if (i < np) {
      const unsigned c = (t >> 5) + 8u * i, k0 = 32u * c + 4u * g;
      if (MODE != 0) {
#pragma unroll
        for (int p = 0; p < NSPLIT; ++p) pv[i][p] = __builtin_amdgcn_raw_buffer_load_b128(rp, pbase + c * 1024u + lane16, p * 294912, 16);
        g0[i] = *(const f32x4*)(gt + k0); g1[i] = *(const f32x4*)(gt + k0 + 16);
      }
      if (MODE == 2) xs[i] = x1s[c * 32u + (t & 31u)];
      else {
        if (MODE == 0) { xa[i] = xpre[2 * i]; xb[i] = xpre[2 * i + 1]; }
        else { xa[i] = *(const u32x2*)(x + (size_t)m * D + k0); xb[i] = *(const u32x2*)(x + (size_t)m * D + k0 + 16); }
      }
    }
  }
  hook(0);
  float s = 0;
#pragma unroll
  for (int i = 0; i < 3; ++i) {
    if (i < np) {
      const unsigned c = (t >> 5) + 8u * i, k0 = 32u * c + 4u * g;
      if (MODE == 2) {
#pragma unroll
        for (int j = 0; j < 4; ++j) { v[i][2 * j] = blo(xs[i][j]); v[i][2 * j + 1] = bhi(xs[i][j]); }
      } else {
        v[i][0] = blo(xa[i][0]); v[i][1] = bhi(xa[i][0]); v[i][2] = blo(xa[i][1]); v[i][3] = bhi(xa[i][1]);
        v[i][4] = blo(xb[i][0]); v[i][5] = bhi(xb[i][0]); v[i][6] = blo(xb[i][1]); v[i][7] = bhi(xb[i][1]);
      }
      if (MODE != 0) {
        float a[8];
#pragma unroll
        for (int j = 0; j < 4; ++j) { a[2 * j] = blo(pv[i][0][j]); a[2 * j + 1] = bhi(pv[i][0][j]); }
#pragma unroll
        for (int p = 1; p < NSPLIT; ++p)
#pragma unroll
          for (int j = 0; j < 4; ++j) { a[2 * j] = addsel(pv[i][p][j], klo, a[2 * j]); a[2 * j + 1] = addsel(pv[i][p][j], khi, a[2 * j + 1]); }
        u32x4 o;
#pragma unroll
        for (int j = 0; j < 4; ++j) {
          const float ga = j < 2 ? g0[i][2 * j] : g1[i][2 * j - 4], gb = j < 2 ? g0[i][2 * j + 1] : g1[i][2 * j - 3];
          unsigned r = cvtpk(a[2 * j], a[2 * j + 1]);
          r = cvtpk(ga * blo(r), gb * bhi(r));
          o[j] = cvtpk(v[i][2 * j] + blo(r), v[i][2 * j + 1] + bhi(r));
        }
        if (MODE == 1) {
          x1s[c * 32u + (t & 31u)] = o;
#pragma unroll
          for (int j = 0; j < 4; ++j) { v[i][2 * j] = blo(o[j]); v[i][2 * j + 1] = bhi(o[j]); }
        } else {
          u32x2 oa = {o[0], o[1]}, ob = {o[2], o[3]};
          *(u32x2*)(outp + (size_t)m * D + k0) = oa; *(u32x2*)(outp + (size_t)m * D + k0 + 16) = ob;
        }
      }
#pragma unroll
      for (int j = 0; j < 8; ++j) s += v[i][j];
    }
  }
  if (MODE == 2) return;
  s = xsum(s);
  if ((t & 63u) < 8u) red[w * 8 + tok] = s;
  __syncthreads();
  const float mean = (red[tok] + red[8 + tok] + red[16 + tok] + red[24 + tok]) * (1.f / D);
  float q = 0;
#pragma unroll
  for (int i = 0; i < 3; ++i)
    if (i < np) {
#pragma unroll
      for (int j = 0; j < 8; ++j) { const float d = v[i][j] - mean; q = fmaf(d, d, q); }
    }
  q = xsum(q);
  if ((t & 63u) < 8u) red[32 + w * 8 + tok] = q;
  __syncthreads();
  const float var = rbf((red[32 + tok] + red[40 + tok] + red[48 + tok] + red[56 + tok]) * (1.f / D));
  const float mb = rbf(mean);
  const float rs = 1.f / rbf(sqrtf(rbf(var + 1e-5f)));
  hook(1);
#pragma unroll
  for (int i = 0; i < 3; ++i) {
    if (i < np) {
      const unsigned c = (t >> 5) + 8u * i, k0 = 32u * c + 4u * g;
      const f32x4 c0 = *(const f32x4*)(scp + k0), c1 = *(const f32x4*)(scp + k0 + 16), s0 = *(const f32x4*)(sh + k0), s1 = *(const f32x4*)(sh + k0 + 16);
      u32x4 o;
#pragma unroll
      for (int j = 0; j < 4; ++j) {
        const float ca = j < 2 ? c0[2 * j] : c1[2 * j - 4], cb = j < 2 ? c0[2 * j + 1] : c1[2 * j - 3];
        const float sa = j < 2 ? s0[2 * j] : s1[2 * j - 4], sb = j < 2 ? s0[2 * j + 1] : s1[2 * j - 3];
        unsigned r = cvtpk(v[i][2 * j] - mb, v[i][2 * j + 1] - mb);
        r = cvtpk(blo(r) * rs, bhi(r) * rs);
        r = cvtpk(blo(r) * ca, bhi(r) * cb);
        o[j] = cvtpk(blo(r) + sa, bhi(r) + sb);
      }
      __builtin_amdgcn_raw_buffer_store_b128(o, ro, tbase + c * 1024u + lane16, 0, 16);
    }
  }
}

__global__ __launch_bounds__(256, 1) void k_mega(const u16* __restrict__ x, const u16* __restrict__ kc, const u16* __restrict__ vc, const u16* __restrict__ wq,
    const u16* __restrict__ wo, const u16* __restrict__ wg, const u16* __restrict__ wu, const u16* __restrict__ wd,
    const u16* __restrict__ cvec, const u16* __restrict__ wm, char* ws, u16* outp) {
  __shared__ __attribute__((aligned(16))) char lds[163840];
  const unsigned ldsb = (unsigned)(size_t)lds;
  const unsigned wv = __builtin_amdgcn_readfirstlane(threadIdx.x >> 6);
  const unsigned bid = blockIdx.x;
  const unsigned xx = bid & 7u, rt = (bid >> 3) & 7u, ii = bid >> 6, mi = xx * 4u + ii;
  const unsigned tb0 = rt * 16u + wv * 4u, row0 = rt * 256u + mi * 8u;
  const unsigned ww = (rt * 4u + ii) * 4u + wv, grow = min(ww * 9u / 2u, 568u);
  unsigned* fl = (unsigned*)(ws + (64 << 10)) + rt * 2048u + 1024u;
  char* gran = ws + (256 << 10);
  unsigned* epoch = (unsigned*)ws;
  u16* h1t = (u16*)(ws + (1 << 20)); u16* h2t = (u16*)(ws + (8 << 20)); u16* ot = (u16*)(ws + (16 << 20)); u16* hm = (u16*)(ws + (48 << 20));
  u16* part = (u16*)(ws + (80 << 20)); u16* part2 = (u16*)(ws + (112 << 20));
  u32x4* x1s = (u32x4*)(lds + 147456); float* red = (float*)(lds + 163840 - 256);
  float* mred = (float*)(lds + 69632); float* modl = (float*)(lds + 156672);
  unsigned tag;
  {
    const unsigned t = tid(wv);
    const unsigned lj = bid >> 3, colA = ((xx + 8u * (lj >> 2)) * 4u + (lj & 3u)) * 16u;
    const bool hasA = colA < 3456u;
    ModSlot ma; u32x2 xp[6]; unsigned cc[5], ep;
    asm volatile(
    "LP_start:\n"
    "v_mov_b32 v245, 0\n"
    "global_load_dword %16, v245, %25 sc1\n"
    "s_mov_b64 s[56:57], %26\n"
    "s_and_b32 s57, s57, 0xffff\n"
    "s_mov_b32 s58, 1152\n"
    "s_mov_b32 s59, 0x20000\n"
    "s_mov_b64 s[48:49], %27\n"
    "s_and_b32 s49, s49, 0xffff\n"
    "s_mov_b32 s50, 3981312\n"
    "s_mov_b32 s51, 0x20000\n"
    "s_mov_b64 s[52:53], %28\n"
    "s_and_b32 s53, s53, 0xffff\n"
    "s_mov_b32 s54, 2359296\n"
    "s_mov_b32 s55, 0x20000\n"
    "s_add_u32 s61, %29, 0\n"
    "buffer_load_dwordx4 %0, %17, s[48:51], s61 offen\n"
    "s_add_u32 s61, %29, 884736\n"
    "buffer_load_dwordx4 %1, %17, s[48:51], s61 offen\n"
    "s_add_u32 s61, %29, 1769472\n"
    "buffer_load_dwordx4 %2, %17, s[48:51], s61 offen\n"
    "s_add_u32 s61, %29, 2654208\n"
    "buffer_load_dwordx4 %3, %17, s[48:51], s61 offen\n"
    "s_add_u32 s61, %29, 3538944\n"
    "buffer_load_dwordx4 %4, %17, s[48:51], s61 offen\n"
    "buffer_load_ushort %11, %19, s[56:59], 0 offen offset:0\n"
    "buffer_load_ushort %12, %19, s[56:59], 0 offen offset:256\n"
    "buffer_load_ushort %13, %19, s[56:59], 0 offen offset:512\n"
    "buffer_load_ushort %14, %19, s[56:59], 0 offen offset:768\n"
    "buffer_load_ushort %15, %19, s[56:59], 0 offen offset:1024\n"
    "buffer_load_dwordx2 %5, %18, s[52:55], 0 offen offset:0\n"
    "buffer_load_dwordx2 %6, %18, s[52:55], 0 offen offset:32\n"
    "buffer_load_dwordx2 %7, %18, s[52:55], 0 offen offset:512\n"
    "buffer_load_dwordx2 %8, %18, s[52:55], 0 offen offset:544\n"
    "buffer_load_dwordx2 %9, %18, s[52:55], 0 offen offset:1024\n"
    "buffer_load_dwordx2 %10, %18, s[52:55], 0 offen offset:1056\n"
    "s_mov_b64 s[44:45], %20\n"
    "s_and_b32 s45, s45, 0xffff\n"
    "s_mov_b32 s46, 4710656\n"
    "s_mov_b32 s47, 0x20000\n"
    "s_mul_i32 s61, %21, 131072\n"
    "s_lshl_b32 s62, %21, 12\n"
    "s_add_u32 s62, s62, %22\n"
    "v_mbcnt_lo_u32_b32 v244, -1, 0\n"
    "v_mbcnt_hi_u32_b32 v244, -1, v244\n"
    "v_bfe_u32 v245, v244, 2, 2\n"
    "v_mul_u32_u24 v245, 8192, v245\n"
    "v_lshrrev_b32 v246, 4, v244\n"
    "v_lshl_add_u32 v245, v246, 6, v245\n"
    "v_and_b32 v246, 3, v244\n"
    "v_xor_b32 v247, 2, v246\n"
    "v_lshl_add_u32 v246, v246, 4, v245\n"
    "v_lshl_add_u32 v247, v247, 4, v245\n"
    "v_add_u32 v240, 0, v246\n"
    "v_add_u32 v241, 31744, v247\n"
    "v_add_u32 v242, 63488, v246\n"
    "v_add_u32 v243, 95232, v247\n"
    "s_add_u32 m0, s62, 98304\n"
    "s_nop 0\n"
    "buffer_load_dwordx4 v240, s[44:47], s61 offen offset:0 lds\n"
    "buffer_load_dwordx4 v241, s[44:47], s61 offen offset:1024 lds\n"
    "buffer_load_dwordx4 v242, s[44:47], s61 offen offset:2048 lds\n"
    "buffer_load_dwordx4 v243, s[44:47], s61 offen offset:3072 lds\n"
    "s_add_u32 s61, s61, 524288\n"
    "s_add_u32 m0, s62, 114688\n"
    "s_nop 0\n"
    "buffer_load_dwordx4 v240, s[44:47], s61 offen offset:0 lds\n"
    "buffer_load_dwordx4 v241, s[44:47], s61 offen offset:1024 lds\n"
    "buffer_load_dwordx4 v242, s[44:47], s61 offen offset:2048 lds\n"
    "buffer_load_dwordx4 v243, s[44:47], s61 offen offset:3072 lds\n"
    "s_add_u32 s61, s61, 524288\n"
    "v_min_u32 v245, 27, v244\n"
    "v_lshrrev_b32 v246, 1, v245\n"
    "v_and_b32 v245, 1, v245\n"
    "v_lshlrev_b32 v245, 7, v245\n"
    "v_lshl_add_u32 v245, v246, 13, v245\n"
    "s_mov_b32 m0, s62\n"
    "s_nop 0\n"
    "global_load_lds_dword v245, %23\n"
    "s_getpc_b64 s[64:65]\n"
    "LP_pc:\n"
    "v_lshlrev_b32 v245, 7, v244\n"
    "v_add_u32 v245, %24, v245\n"
    "s_mov_b32 s66, LB_end-LP_pc-4\n"
    "v_min_u32 v245, s66, v245\n"
    "v_and_b32 v245, -4, v245\n"
    "global_load_lds_dword v245, s[64:65]\n"
    "s_waitcnt vmcnt(10)\n"
    : "=&v"(ma.w[0]), "=&v"(ma.w[1]), "=&v"(ma.w[2]), "=&v"(ma.w[3]), "=&v"(ma.w[4]), "=&v"(xp[0]), "=&v"(xp[1]), "=&v"(xp[2]), "=&v"(xp[3]), "=&v"(xp[4]), "=&v"(xp[5]), "=&v"(cc[0]), "=&v"(cc[1]), "=&v"(cc[2]), "=&v"(cc[3]), "=&v"(cc[4]), "=&v"(ep)
    : "v"((t >> 1) * 6912u + (t & 1u) * 16u), "v"((row0 + (t & 7u)) * 1152u + (t >> 5) * 64u + ((t >> 3) & 3u) * 8u), "v"((t >> 1) * 2u), "s"((const char*)wq + mi * 256u), "s"(wv), "s"(ldsb), "s"((const char*)wq + mi * 256u + (size_t)(128u + (rt * 4u + wv) * 14u) * 8192u), "s"((ww & 15u) * 8192u), "s"(epoch), "s"(cvec), "s"(wm), "s"(x), "s"(hasA ? colA * 2u : 0x7ff00000u)
    : "v240","v241","v242","v243","v244","v245","v246","v247","s44","s45","s46","s47","s48","s49","s50","s51","s52","s53","s54","s55","s56","s57","s58","s59","s60","s61","s62","s63","s64","s65","s66","s67","scc","memory");

    row_phase<0>([&](int st) {
      if (st == 0) {
        tag = __builtin_amdgcn_readfirstlane(ep) + 1u;
        auto silu = [](unsigned v) { const float xv = blo(v); return rbf(xv / (1.f + __expf(-xv))); };
        const float sv[5] = {silu(cc[0]), silu(cc[1]), silu(cc[2]), silu(cc[3]), silu(cc[4])};

        ma.partial(sv, mred, t, wv);
        __syncthreads();
        if (hasA) { if (t < 16u) ModSlot::emit(mred, gran, colA, tag, t); }
        else if (t == 0u) ModSlot::emit(mred, gran, 3456u + (colA - 3456u) / 16u, tag, 0u);

      } else {


        sweep_mod<576>(gran, modl, tag, wv);

      }
    }, xp, x, nullptr, modl, modl + D, nullptr, nullptr, red, h1t, row0, wv);
    publish_flag(fl + 0 * 128 + mi, tag, wv);
  }
  asm volatile(
    "s_mov_b64 s[40:41], %0\n"
    "s_and_b32 s41, s41, 0xffff\n"
    "s_mov_b32 s42, %1\n"
    "s_mov_b32 s43, 0x20000\n"
    "v_mbcnt_lo_u32_b32 v237, -1, 0\n"
    "v_mbcnt_hi_u32_b32 v237, -1, v237\n"
    "s_mov_b32 s66, 0x7ff00000\n"
    "s_mov_b32 s60, 0\n"
    "s_mov_b32 s67, 9\n"
    "v_lshlrev_b32 v236, 4, v237\n"
    "v_add_u32 v216, 0, v236\n"
    "v_add_u32 v217, 18432, v236\n"
    "v_add_u32 v218, 36864, v236\n"
    "v_add_u32 v219, 55296, v236\n"
    "s_lshl_b32 s68, %6, 5\n"
    "v_and_b32 v244, 7, v237\n"
    "v_lshlrev_b32 v244, 2, v244\n"
    "v_add_u32 v244, s68, v244\n"
    "s_mov_b32 s69, 0x40000\n"
    "PLA0:\n"
    "global_load_dword v245, v244, %13 offset:0 sc1\n"
    "s_waitcnt vmcnt(0)\n"
    "v_cmp_eq_u32 vcc, %27, v245\n"
    "s_cmp_eq_u64 vcc, -1\n"
    "s_cbranch_scc1 PDA0\n"
    "s_sleep 1\n"
    "s_sub_u32 s69, s69, 1\n"
    "s_cmp_lg_u32 s69, 0\n"
    "s_cbranch_scc1 PLA0\n"
    "PDA0:\n"
    "buffer_load_dwordx4 a[0:3], v216, s[40:43], s60 offen offset:0 sc1\n"
    "buffer_load_dwordx4 a[4:7], v216, s[40:43], s60 offen offset:1024 sc1\n"
    "buffer_load_dwordx4 a[8:11], v217, s[40:43], s60 offen offset:0 sc1\n"
    "buffer_load_dwordx4 a[12:15], v217, s[40:43], s60 offen offset:1024 sc1\n"
    "buffer_load_dwordx4 a[16:19], v218, s[40:43], s60 offen offset:0 sc1\n"
    "buffer_load_dwordx4 a[20:23], v218, s[40:43], s60 offen offset:1024 sc1\n"
    "buffer_load_dwordx4 a[24:27], v219, s[40:43], s60 offen offset:0 sc1\n"
    "buffer_load_dwordx4 a[28:31], v219, s[40:43], s60 offen offset:1024 sc1\n"
    "s_add_u32 s60, s60, 2048\n"
    "s_sub_u32 s67, s67, 1\n"
    "s_cmp_gt_i32 s67, 0\n"
    "s_cselect_b32 s60, s60, s66\n"
    "buffer_load_dwordx4 a[32:35], v216, s[40:43], s60 offen offset:0 sc1\n"
    "buffer_load_dwordx4 a[36:39], v216, s[40:43], s60 offen offset:1024 sc1\n"
    "buffer_load_dwordx4 a[40:43], v217, s[40:43], s60 offen offset:0 sc1\n"
    "buffer_load_dwordx4 a[44:47], v217, s[40:43], s60 offen offset:1024 sc1\n"
    "buffer_load_dwordx4 a[48:51], v218, s[40:43], s60 offen offset:0 sc1\n"
    "buffer_load_dwordx4 a[52:55], v218, s[40:43], s60 offen offset:1024 sc1\n"
    "buffer_load_dwordx4 a[56:59], v219, s[40:43], s60 offen offset:0 sc1\n"
    "buffer_load_dwordx4 a[60:63], v219, s[40:43], s60 offen offset:1024 sc1\n"
    "s_add_u32 s60, s60, 2048\n"
    "s_sub_u32 s67, s67, 1\n"
    "s_cmp_gt_i32 s67, 0\n"
    "s_cselect_b32 s60, s60, s66\n"
    "s_mov_b64 s[44:45], %2\n"
    "s_and_b32 s45, s45, 0xffff\n"
    "s_mov_b32 s46, 4710656\n"
    "s_mov_b32 s47, 0x20000\n"
    "s_mov_b64 s[48:49], %3\n"
    "s_and_b32 s49, s49, 0xffff\n"
    "s_mov_b32 s50, 524288\n"
    "s_mov_b32 s51, 0x20000\n"
    "s_mov_b64 s[52:53], %4\n"
    "s_and_b32 s53, s53, 0xffff\n"
    "s_mov_b32 s54, 524288\n"
    "s_mov_b32 s55, 0x20000\n"
    "s_mov_b64 s[56:57], %5\n"
    "s_and_b32 s57, s57, 0xffff\n"
    "s_mov_b32 s58, 397312\n"
    "s_mov_b32 s59, 0x20000\n"
    "s_mov_b32 s74, 1317011456\n"
    "s_mul_i32 s61, %6, 131072\n"
    "s_add_u32 s61, s61, 1048576\n"
    "s_lshl_b32 s65, %6, 12\n"
    "s_add_u32 s72, s65, %7\n"
    "s_add_u32 s73, s72, 65536\n"
    "s_add_u32 s62, s72, 131072\n"
    "s_mov_b32 s70, s65\n"
    "s_mov_b32 s71, s65\n"
    "s_add_u32 s63, %7, 98304\n"
    "s_mov_b32 s64, 9\n"
    "s_add_u32 s65, %7, 163840\n"
    "v_bfe_u32 v244, v237, 2, 2\n"
    "v_mul_u32_u24 v244, 8192, v244\n"
    "v_lshrrev_b32 v245, 4, v237\n"
    "v_lshl_add_u32 v244, v245, 6, v244\n"
    "v_and_b32 v245, 3, v237\n"
    "v_xor_b32 v246, 2, v245\n"
    "v_lshl_add_u32 v245, v245, 4, v244\n"
    "v_lshl_add_u32 v246, v246, 4, v244\n"
    "v_add_u32 v212, 0, v245\n"
    "v_add_u32 v213, 31744, v246\n"
    "v_add_u32 v214, 63488, v245\n"
    "v_add_u32 v215, 95232, v246\n"
    "v_lshrrev_b32 v244, 4, v237\n"
    "v_and_b32 v245, 15, v237\n"
    "v_xor_b32 v245, v245, v244\n"
    "v_xor_b32 v246, 0, v245\n"
    "v_lshlrev_b32 v246, 4, v246\n"
    "v_lshl_add_u32 v230, v244, 8, v246\n"
    "v_xor_b32 v246, 4, v245\n"
    "v_lshlrev_b32 v246, 4, v246\n"
    "v_lshl_add_u32 v231, v244, 8, v246\n"
    "v_xor_b32 v246, 8, v245\n"
    "v_lshlrev_b32 v246, 4, v246\n"
    "v_lshl_add_u32 v232, v244, 8, v246\n"
    "v_xor_b32 v246, 12, v245\n"
    "v_lshlrev_b32 v246, 4, v246\n"
    "v_lshl_add_u32 v233, v244, 8, v246\n"
    "v_bfe_u32 v245, v237, 2, 2\n"
    "v_lshlrev_b32 v245, 8, v245\n"
    "v_lshl_add_u32 v245, v244, 6, v245\n"
    "v_and_b32 v246, 3, v237\n"
    "v_lshl_add_u32 v234, v246, 4, v245\n"
    "v_xor_b32 v235, 32, v234\n"
    "v_and_b32 v245, 15, v237\n"
    "v_xor_b32 v246, v244, v245\n"
    "v_lshlrev_b32 v245, 8, v245\n"
    "v_add_u32 v245, %7, v245\n"
    "v_xor_b32 v247, 0, v246\n"
    "v_lshl_add_u32 v224, v247, 4, v245\n"
    "v_xor_b32 v247, 4, v246\n"
    "v_lshl_add_u32 v225, v247, 4, v245\n"
    "v_xor_b32 v247, 8, v246\n"
    "v_lshl_add_u32 v226, v247, 4, v245\n"
    "v_xor_b32 v247, 12, v246\n"
    "v_lshl_add_u32 v227, v247, 4, v245\n"
    "v_bfe_u32 v245, v237, 2, 2\n"
    "v_lshlrev_b32 v245, 6, v245\n"
    "v_lshl_add_u32 v245, v244, 10, v245\n"
    "v_and_b32 v246, 3, v237\n"
    "v_and_b32 v247, 1, v244\n"
    "v_lshl_add_u32 v228, v246, 3, v245\n"
    "v_lshl_add_u32 v228, v247, 5, v228\n"
    "v_add_u32 v228, %7, v228\n"
    "v_add_u32 v228, 65536, v228\n"
    "v_xor_b32 v229, 32, v228\n"
    "v_lshlrev_b32 v247, 1, v247\n"
    "v_xor_b32 v246, v246, v247\n"
    "v_lshl_add_u32 v210, v246, 4, v245\n"
    "v_mov_b32 v0, 0\n"
    "v_mov_b32 v1, 0\n"
    "v_mov_b32 v2, 0\n"
    "v_mov_b32 v3, 0\n"
    "v_mov_b32 v4, 0\n"
    "v_mov_b32 v5, 0\n"
    "v_mov_b32 v6, 0\n"
    "v_mov_b32 v7, 0\n"
    "v_mov_b32 v8, 0\n"
    "v_mov_b32 v9, 0\n"
    "v_mov_b32 v10, 0\n"
    "v_mov_b32 v11, 0\n"
    "v_mov_b32 v12, 0\n"
    "v_mov_b32 v13, 0\n"
    "v_mov_b32 v14, 0\n"
    "v_mov_b32 v15, 0\n"
    "v_mov_b32 v16, 0\n"
    "v_mov_b32 v17, 0\n"
    "v_mov_b32 v18, 0\n"
    "v_mov_b32 v19, 0\n"
    "v_mov_b32 v20, 0\n"
    "v_mov_b32 v21, 0\n"
    "v_mov_b32 v22, 0\n"
    "v_mov_b32 v23, 0\n"
    "v_mov_b32 v24, 0\n"
    "v_mov_b32 v25, 0\n"
    "v_mov_b32 v26, 0\n"
    "v_mov_b32 v27, 0\n"
    "v_mov_b32 v28, 0\n"
    "v_mov_b32 v29, 0\n"
    "v_mov_b32 v30, 0\n"
    "v_mov_b32 v31, 0\n"
    "v_mov_b32 v32, 0\n"
    "v_mov_b32 v33, 0\n"
    "v_mov_b32 v34, 0\n"
    "v_mov_b32 v35, 0\n"
    "v_mov_b32 v36, 0\n"
    "v_mov_b32 v37, 0\n"
    "v_mov_b32 v38, 0\n"
    "v_mov_b32 v39, 0\n"
    "v_mov_b32 v40, 0\n"
    "v_mov_b32 v41, 0\n"
    "v_mov_b32 v42, 0\n"
    "v_mov_b32 v43, 0\n"
    "v_mov_b32 v44, 0\n"
    "v_mov_b32 v45, 0\n"
    "v_mov_b32 v46, 0\n"
    "v_mov_b32 v47, 0\n"
    "v_mov_b32 v48, 0\n"
    "v_mov_b32 v49, 0\n"
    "v_mov_b32 v50, 0\n"
    "v_mov_b32 v51, 0\n"
    "v_mov_b32 v52, 0\n"
    "v_mov_b32 v53, 0\n"
    "v_mov_b32 v54, 0\n"
    "v_mov_b32 v55, 0\n"
    "v_mov_b32 v56, 0\n"
    "v_mov_b32 v57, 0\n"
    "v_mov_b32 v58, 0\n"
    "v_mov_b32 v59, 0\n"
    "v_mov_b32 v60, 0\n"
    "v_mov_b32 v61, 0\n"
    "v_mov_b32 v62, 0\n"
    "v_mov_b32 v63, 0\n"
    "v_mov_b32 v64, 0\n"
    "v_mov_b32 v65, 0\n"
    "v_mov_b32 v66, 0\n"
    "v_mov_b32 v67, 0\n"
    "v_mov_b32 v68, 0\n"
    "v_mov_b32 v69, 0\n"
    "v_mov_b32 v70, 0\n"
    "v_mov_b32 v71, 0\n"
    "v_mov_b32 v72, 0\n"
    "v_mov_b32 v73, 0\n"
    "v_mov_b32 v74, 0\n"
    "v_mov_b32 v75, 0\n"
    "v_mov_b32 v76, 0\n"
    "v_mov_b32 v77, 0\n"
    "v_mov_b32 v78, 0\n"
    "v_mov_b32 v79, 0\n"
    "v_mov_b32 v80, 0\n"
    "v_mov_b32 v81, 0\n"
    "v_mov_b32 v82, 0\n"
    "v_mov_b32 v83, 0\n"
    "v_mov_b32 v84, 0\n"
    "v_mov_b32 v85, 0\n"
    "v_mov_b32 v86, 0\n"
    "v_mov_b32 v87, 0\n"
    "v_mov_b32 v88, 0\n"
    "v_mov_b32 v89, 0\n"
    "v_mov_b32 v90, 0\n"
    "v_mov_b32 v91, 0\n"
    "v_mov_b32 v92, 0\n"
    "v_mov_b32 v93, 0\n"
    "v_mov_b32 v94, 0\n"
    "v_mov_b32 v95, 0\n"
    "v_mov_b32 v96, 0\n"
    "v_mov_b32 v97, 0\n"
    "v_mov_b32 v98, 0\n"
    "v_mov_b32 v99, 0\n"
    "v_mov_b32 v100, 0\n"
    "v_mov_b32 v101, 0\n"
    "v_mov_b32 v102, 0\n"
    "v_mov_b32 v103, 0\n"
    "v_mov_b32 v104, 0\n"
    "v_mov_b32 v105, 0\n"
    "v_mov_b32 v106, 0\n"
    "v_mov_b32 v107, 0\n"
    "v_mov_b32 v108, 0\n"
    "v_mov_b32 v109, 0\n"
    "v_mov_b32 v110, 0\n"
    "v_mov_b32 v111, 0\n"
    "v_mov_b32 v112, 0\n"
    "v_mov_b32 v113, 0\n"
    "v_mov_b32 v114, 0\n"
    "v_mov_b32 v115, 0\n"
    "v_mov_b32 v116, 0\n"
    "v_mov_b32 v117, 0\n"
    "v_mov_b32 v118, 0\n"
    "v_mov_b32 v119, 0\n"
    "v_mov_b32 v120, 0\n"
    "v_mov_b32 v121, 0\n"
    "v_mov_b32 v122, 0\n"
    "v_mov_b32 v123, 0\n"
    "v_mov_b32 v124, 0\n"
    "v_mov_b32 v125, 0\n"
    "v_mov_b32 v126, 0\n"
    "v_mov_b32 v127, 0\n"
    "v_mov_b32 v128, 0\n"
    "v_mov_b32 v129, 0\n"
    "v_mov_b32 v130, 0\n"
    "v_mov_b32 v131, 0\n"
    "v_mov_b32 v132, 0\n"
    "v_mov_b32 v133, 0\n"
    "v_mov_b32 v134, 0\n"
    "v_mov_b32 v135, 0\n"
    "v_mov_b32 v136, 0\n"
    "v_mov_b32 v137, 0\n"
    "v_mov_b32 v138, 0\n"
    "v_mov_b32 v139, 0\n"
    "v_mov_b32 v140, 0\n"
    "v_mov_b32 v141, 0\n"
    "v_mov_b32 v142, 0\n"
    "v_mov_b32 v143, 0\n"
    "v_mov_b32 v144, 0\n"
    "v_mov_b32 v145, 0\n"
    "v_mov_b32 v146, 0\n"
    "v_mov_b32 v147, 0\n"
    "v_mov_b32 v148, 0\n"
    "v_mov_b32 v149, 0\n"
    "v_mov_b32 v150, 0\n"
    "v_mov_b32 v151, 0\n"
    "v_mov_b32 v152, 0\n"
    "v_mov_b32 v153, 0\n"
    "v_mov_b32 v154, 0\n"
    "v_mov_b32 v155, 0\n"
    "v_mov_b32 v156, 0\n"
    "v_mov_b32 v157, 0\n"
    "v_mov_b32 v158, 0\n"
    "v_mov_b32 v159, 0\n"
    "v_mov_b32 v160, 0\n"
    "v_mov_b32 v161, 0\n"
    "v_mov_b32 v162, 0\n"
    "v_mov_b32 v163, 0\n"
    "v_mov_b32 v164, 0\n"
    "v_mov_b32 v165, 0\n"
    "v_mov_b32 v166, 0\n"
    "v_mov_b32 v167, 0\n"
    "v_mov_b32 v168, 0\n"
    "v_mov_b32 v169, 0\n"
    "v_mov_b32 v170, 0\n"
    "v_mov_b32 v171, 0\n"
    "v_mov_b32 v172, 0\n"
    "v_mov_b32 v173, 0\n"
    "v_mov_b32 v174, 0\n"
    "v_mov_b32 v175, 0\n"
    "v_mov_b32 v176, 0\n"
    "v_mov_b32 v177, 0\n"
    "v_mov_b32 v178, 0\n"
    "v_mov_b32 v179, 0\n"
    "v_mov_b32 v180, 0\n"
    "v_mov_b32 v181, 0\n"
    "v_mov_b32 v182, 0\n"
    "v_mov_b32 v183, 0\n"
    "v_mov_b32 v184, 0\n"
    "v_mov_b32 v185, 0\n"
    "v_mov_b32 v186, 0\n"
    "v_mov_b32 v187, 0\n"
    "v_mov_b32 v188, 0\n"
    "v_mov_b32 v189, 0\n"
    "v_mov_b32 v190, 0\n"
    "v_mov_b32 v191, 0\n"
    "v_accvgpr_write_b32 a128, 0\n"
    "v_accvgpr_write_b32 a129, 0\n"
    "v_accvgpr_write_b32 a130, 0\n"
    "v_accvgpr_write_b32 a131, 0\n"
    "v_accvgpr_write_b32 a132, 0\n"
    "v_accvgpr_write_b32 a133, 0\n"
    "v_accvgpr_write_b32 a134, 0\n"
    "v_accvgpr_write_b32 a135, 0\n"
    "v_accvgpr_write_b32 a136, 0\n"
    "v_accvgpr_write_b32 a137, 0\n"
    "v_accvgpr_write_b32 a138, 0\n"
    "v_accvgpr_write_b32 a139, 0\n"
    "v_accvgpr_write_b32 a140, 0\n"
    "v_accvgpr_write_b32 a141, 0\n"
    "v_accvgpr_write_b32 a142, 0\n"
    "v_accvgpr_write_b32 a143, 0\n"
    "v_accvgpr_write_b32 a144, 0\n"
    "v_accvgpr_write_b32 a145, 0\n"
    "v_accvgpr_write_b32 a146, 0\n"
    "v_accvgpr_write_b32 a147, 0\n"
    "v_accvgpr_write_b32 a148, 0\n"
    "v_accvgpr_write_b32 a149, 0\n"
    "v_accvgpr_write_b32 a150, 0\n"
    "v_accvgpr_write_b32 a151, 0\n"
    "v_accvgpr_write_b32 a152, 0\n"
    "v_accvgpr_write_b32 a153, 0\n"
    "v_accvgpr_write_b32 a154, 0\n"
    "v_accvgpr_write_b32 a155, 0\n"
    "v_accvgpr_write_b32 a156, 0\n"
    "v_accvgpr_write_b32 a157, 0\n"
    "v_accvgpr_write_b32 a158, 0\n"
    "v_accvgpr_write_b32 a159, 0\n"
    "v_accvgpr_write_b32 a160, 0\n"
    "v_accvgpr_write_b32 a161, 0\n"
    "v_accvgpr_write_b32 a162, 0\n"
    "v_accvgpr_write_b32 a163, 0\n"
    "v_accvgpr_write_b32 a164, 0\n"
    "v_accvgpr_write_b32 a165, 0\n"
    "v_accvgpr_write_b32 a166, 0\n"
    "v_accvgpr_write_b32 a167, 0\n"
    "v_accvgpr_write_b32 a168, 0\n"
    "v_accvgpr_write_b32 a169, 0\n"
    "v_accvgpr_write_b32 a170, 0\n"
    "v_accvgpr_write_b32 a171, 0\n"
    "v_accvgpr_write_b32 a172, 0\n"
    "v_accvgpr_write_b32 a173, 0\n"
    "v_accvgpr_write_b32 a174, 0\n"
    "v_accvgpr_write_b32 a175, 0\n"
    "v_accvgpr_write_b32 a176, 0\n"
    "v_accvgpr_write_b32 a177, 0\n"
    "v_accvgpr_write_b32 a178, 0\n"
    "v_accvgpr_write_b32 a179, 0\n"
    "v_accvgpr_write_b32 a180, 0\n"
    "v_accvgpr_write_b32 a181, 0\n"
    "v_accvgpr_write_b32 a182, 0\n"
    "v_accvgpr_write_b32 a183, 0\n"
    "v_accvgpr_write_b32 a184, 0\n"
    "v_accvgpr_write_b32 a185, 0\n"
    "v_accvgpr_write_b32 a186, 0\n"
    "v_accvgpr_write_b32 a187, 0\n"
    "v_accvgpr_write_b32 a188, 0\n"
    "v_accvgpr_write_b32 a189, 0\n"
    "v_accvgpr_write_b32 a190, 0\n"
    "v_accvgpr_write_b32 a191, 0\n"
    "v_add_u32 v211, s63, v210\n"
    "s_add_u32 s63, s63, 16384\n"
    "s_cmp_ge_u32 s63, s65\n"
    "s_cselect_b32 s69, 65536, 0\n"
    "s_sub_u32 s63, s63, s69\n"
    "s_lshl_b32 s68, %6, 5\n"
    "s_add_u32 s69, s68, %14\n"
    "s_lshl_b32 s69, s69, 2\n"
    "v_mov_b32 v243, s69\n"
    "ds_read_b64_tr_b16 a[192:193], v211 offset:0\n"
    "ds_read_b64_tr_b16 a[194:195], v211 offset:4096\n"
    "ds_read_b64_tr_b16 a[196:197], v211 offset:8192\n"
    "ds_read_b64_tr_b16 a[198:199], v211 offset:12288\n"
    "ds_read_b64_tr_b16 a[200:201], v211 offset:8\n"
    "ds_read_b64_tr_b16 a[202:203], v211 offset:4104\n"
    "ds_read_b64_tr_b16 a[204:205], v211 offset:8200\n"
    "ds_read_b64_tr_b16 a[206:207], v211 offset:12296\n"
    "ds_read_b64_tr_b16 a[208:209], v211 offset:256\n"
    "ds_read_b64_tr_b16 a[210:211], v211 offset:4352\n"
    "ds_read_b64_tr_b16 a[212:213], v211 offset:8448\n"
    "ds_read_b64_tr_b16 a[214:215], v211 offset:12544\n"
    "s_add_u32 m0, s72, 0\n"
    "s_nop 0\n"
    "buffer_load_dwordx4 v230, s[48:51], s70 offen offset:0 lds\n"
    "buffer_load_dwordx4 v231, s[48:51], s70 offen offset:1024 lds\n"
    "buffer_load_dwordx4 v232, s[48:51], s70 offen offset:2048 lds\n"
    "buffer_load_dwordx4 v233, s[48:51], s70 offen offset:3072 lds\n"
    "s_add_u32 s70, s70, 16384\n"
    "s_add_u32 m0, s72, 16384\n"
    "s_nop 0\n"
    "buffer_load_dwordx4 v230, s[48:51], s70 offen offset:0 lds\n"
    "buffer_load_dwordx4 v231, s[48:51], s70 offen offset:1024 lds\n"
    "buffer_load_dwordx4 v232, s[48:51], s70 offen offset:2048 lds\n"
    "buffer_load_dwordx4 v233, s[48:51], s70 offen offset:3072 lds\n"
    "s_add_u32 s70, s70, 16384\n"
    "s_add_u32 m0, s73, 0\n"
    "s_nop 0\n"
    "buffer_load_dwordx4 v234, s[52:55], s71 offen offset:0 lds\n"
    "buffer_load_dwordx4 v235, s[52:55], s71 offen offset:1024 lds\n"
    "buffer_load_dwordx4 v234, s[52:55], s71 offen offset:2048 lds\n"
    "buffer_load_dwordx4 v235, s[52:55], s71 offen offset:3072 lds\n"
    "s_add_u32 s71, s71, 16384\n"
    "s_mov_b32 s69, %26\n"
    "LA_stag:\n"
    "s_cmp_eq_u32 s69, 0\n"
    "s_cbranch_scc1 LA_stagd\n"
    "s_sleep 16\n"
    "s_sub_u32 s69, s69, 1\n"
    "s_branch LA_stag\n"
    "LA_stagd:\n"
    "s_waitcnt vmcnt(20)\n"
    "s_waitcnt lgkmcnt(8)\n"
    "LA_qloop:\n"
    "v_mfma_f32_16x16x32_bf16 v[64:67], a[192:195], a[0:3], v[64:67]\n"
    "ds_read_b64_tr_b16 a[216:217], v211 offset:264\n"
    "v_mfma_f32_16x16x32_bf16 v[64:67], a[196:199], a[4:7], v[64:67]\n"
    "buffer_load_dwordx4 a[64:67], v216, s[40:43], s60 offen offset:0 sc1\n"
    "v_mfma_f32_16x16x32_bf16 v[68:71], a[192:195], a[8:11], v[68:71]\n"
    "ds_read_b64_tr_b16 a[218:219], v211 offset:4360\n"
    "v_mfma_f32_16x16x32_bf16 v[68:71], a[196:199], a[12:15], v[68:71]\n"
    "v_mfma_f32_16x16x32_bf16 v[72:75], a[192:195], a[16:19], v[72:75]\n"
    "ds_read_b64_tr_b16 a[220:221], v211 offset:8456\n"
    "v_mfma_f32_16x16x32_bf16 v[72:75], a[196:199], a[20:23], v[72:75]\n"
    "buffer_load_dwordx4 a[68:71], v216, s[40:43], s60 offen offset:1024 sc1\n"
    "v_mfma_f32_16x16x32_bf16 v[76:79], a[192:195], a[24:27], v[76:79]\n"
    "ds_read_b64_tr_b16 a[222:223], v211 offset:12552\n"
    "v_mfma_f32_16x16x32_bf16 v[76:79], a[196:199], a[28:31], v[76:79]\n"
    "s_waitcnt lgkmcnt(8)\n"
    "v_mfma_f32_16x16x32_bf16 v[80:83], a[200:203], a[0:3], v[80:83]\n"
    "ds_read_b64_tr_b16 a[224:225], v211 offset:512\n"
    "v_mfma_f32_16x16x32_bf16 v[80:83], a[204:207], a[4:7], v[80:83]\n"
    "buffer_load_dwordx4 a[72:75], v217, s[40:43], s60 offen offset:0 sc1\n"
    "v_mfma_f32_16x16x32_bf16 v[84:87], a[200:203], a[8:11], v[84:87]\n"
    "ds_read_b64_tr_b16 a[226:227], v211 offset:4608\n"
    "v_mfma_f32_16x16x32_bf16 v[84:87], a[204:207], a[12:15], v[84:87]\n"
    "v_mfma_f32_16x16x32_bf16 v[88:91], a[200:203], a[16:19], v[88:91]\n"
    "ds_read_b64_tr_b16 a[228:229], v211 offset:8704\n"
    "v_mfma_f32_16x16x32_bf16 v[88:91], a[204:207], a[20:23], v[88:91]\n"
    "buffer_load_dwordx4 a[76:79], v217, s[40:43], s60 offen offset:1024 sc1\n"
    "v_mfma_f32_16x16x32_bf16 v[92:95], a[200:203], a[24:27], v[92:95]\n"
    "ds_read_b64_tr_b16 a[230:231], v211 offset:12800\n"
    "v_mfma_f32_16x16x32_bf16 v[92:95], a[204:207], a[28:31], v[92:95]\n"
    "s_waitcnt lgkmcnt(8)\n"
    "v_mfma_f32_16x16x32_bf16 v[96:99], a[208:211], a[0:3], v[96:99]\n"
    "ds_read_b64_tr_b16 a[232:233], v211 offset:520\n"
    "v_mfma_f32_16x16x32_bf16 v[96:99], a[212:215], a[4:7], v[96:99]\n"
    "buffer_load_dwordx4 a[80:83], v218, s[40:43], s60 offen offset:0 sc1\n"
    "v_mfma_f32_16x16x32_bf16 v[100:103], a[208:211], a[8:11], v[100:103]\n"
    "ds_read_b64_tr_b16 a[234:235], v211 offset:4616\n"
    "v_mfma_f32_16x16x32_bf16 v[100:103], a[212:215], a[12:15], v[100:103]\n"
    "v_mfma_f32_16x16x32_bf16 v[104:107], a[208:211], a[16:19], v[104:107]\n"
    "ds_read_b64_tr_b16 a[236:237], v211 offset:8712\n"
    "v_mfma_f32_16x16x32_bf16 v[104:107], a[212:215], a[20:23], v[104:107]\n"
    "buffer_load_dwordx4 a[84:87], v218, s[40:43], s60 offen offset:1024 sc1\n"
    "v_mfma_f32_16x16x32_bf16 v[108:111], a[208:211], a[24:27], v[108:111]\n"
    "ds_read_b64_tr_b16 a[238:239], v211 offset:12808\n"
    "v_mfma_f32_16x16x32_bf16 v[108:111], a[212:215], a[28:31], v[108:111]\n"
    "s_waitcnt lgkmcnt(8)\n"
    "v_mfma_f32_16x16x32_bf16 v[112:115], a[216:219], a[0:3], v[112:115]\n"
    "ds_read_b64_tr_b16 a[240:241], v211 offset:768\n"
    "v_mfma_f32_16x16x32_bf16 v[112:115], a[220:223], a[4:7], v[112:115]\n"
    "buffer_load_dwordx4 a[88:91], v219, s[40:43], s60 offen offset:0 sc1\n"
    "v_mfma_f32_16x16x32_bf16 v[116:119], a[216:219], a[8:11], v[116:119]\n"
    "ds_read_b64_tr_b16 a[242:243], v211 offset:4864\n"
    "v_mfma_f32_16x16x32_bf16 v[116:119], a[220:223], a[12:15], v[116:119]\n"
    "v_mfma_f32_16x16x32_bf16 v[120:123], a[216:219], a[16:19], v[120:123]\n"
    "ds_read_b64_tr_b16 a[244:245], v211 offset:8960\n"
    "v_mfma_f32_16x16x32_bf16 v[120:123], a[220:223], a[20:23], v[120:123]\n"
    "buffer_load_dwordx4 a[92:95], v219, s[40:43], s60 offen offset:1024 sc1\n"
    "v_mfma_f32_16x16x32_bf16 v[124:127], a[216:219], a[24:27], v[124:127]\n"
    "s_add_u32 s60, s60, 2048\n"
    "ds_read_b64_tr_b16 a[246:247], v211 offset:13056\n"
    "v_mfma_f32_16x16x32_bf16 v[124:127], a[220:223], a[28:31], v[124:127]\n"
    "s_waitcnt lgkmcnt(8)\n"
    "s_sub_u32 s67, s67, 1\n"
    "v_mfma_f32_16x16x32_bf16 v[128:131], a[224:227], a[0:3], v[128:131]\n"
    "s_cmp_gt_i32 s67, 0\n"
    "s_cselect_b32 s60, s60, s66\n"
    "ds_read_b64_tr_b16 a[248:249], v211 offset:776\n"
    "v_mfma_f32_16x16x32_bf16 v[128:131], a[228:231], a[4:7], v[128:131]\n"
    "s_mov_b32 m0, s62\n"
    "s_nop 0\n"
    "buffer_load_dwordx4 v212, s[44:47], s61 offen offset:0 lds\n"
    "v_mfma_f32_16x16x32_bf16 v[132:135], a[224:227], a[8:11], v[132:135]\n"
    "ds_read_b64_tr_b16 a[250:251], v211 offset:4872\n"
    "v_mfma_f32_16x16x32_bf16 v[132:135], a[228:231], a[12:15], v[132:135]\n"
    "v_mfma_f32_16x16x32_bf16 v[136:139], a[224:227], a[16:19], v[136:139]\n"
    "ds_read_b64_tr_b16 a[252:253], v211 offset:8968\n"
    "v_mfma_f32_16x16x32_bf16 v[136:139], a[228:231], a[20:23], v[136:139]\n"
    "buffer_load_dwordx4 v213, s[44:47], s61 offen offset:1024 lds\n"
    "v_mfma_f32_16x16x32_bf16 v[140:143], a[224:227], a[24:27], v[140:143]\n"
    "s_waitcnt vmcnt(10)\n"
    "ds_read_b64_tr_b16 a[254:255], v211 offset:13064\n"
    "v_mfma_f32_16x16x32_bf16 v[140:143], a[228:231], a[28:31], v[140:143]\n"
    "s_waitcnt lgkmcnt(8)\n"
    "s_barrier\n"
    "v_add_u32 v211, s63, v210\n"
    "v_mfma_f32_16x16x32_bf16 v[144:147], a[232:235], a[0:3], v[144:147]\n"
    "s_add_u32 s63, s63, 16384\n"
    "ds_read_b64_tr_b16 a[192:193], v211 offset:0\n"
    "v_mfma_f32_16x16x32_bf16 v[144:147], a[236:239], a[4:7], v[144:147]\n"
    "s_cmp_ge_u32 s63, s65\n"
    "s_cselect_b32 s69, 65536, 0\n"
    "buffer_load_dwordx4 v214, s[44:47], s61 offen offset:2048 lds\n"
    "v_mfma_f32_16x16x32_bf16 v[148:151], a[232:235], a[8:11], v[148:151]\n"
    "s_sub_u32 s63, s63, s69\n"
    "ds_read_b64_tr_b16 a[194:195], v211 offset:4096\n"
    "v_mfma_f32_16x16x32_bf16 v[148:151], a[236:239], a[12:15], v[148:151]\n"
    "s_sub_u32 s64, s64, 1\n"
    "v_mfma_f32_16x16x32_bf16 v[152:155], a[232:235], a[16:19], v[152:155]\n"
    "ds_read_b64_tr_b16 a[196:197], v211 offset:8192\n"
    "v_mfma_f32_16x16x32_bf16 v[152:155], a[236:239], a[20:23], v[152:155]\n"
    "buffer_load_dwordx4 v215, s[44:47], s61 offen offset:3072 lds\n"
    "v_mfma_f32_16x16x32_bf16 v[156:159], a[232:235], a[24:27], v[156:159]\n"
    "s_add_u32 s61, s61, 524288\n"
    "ds_read_b64_tr_b16 a[198:199], v211 offset:12288\n"
    "v_mfma_f32_16x16x32_bf16 v[156:159], a[236:239], a[28:31], v[156:159]\n"
    "s_waitcnt lgkmcnt(8)\n"
    "s_add_u32 s62, s62, 16384\n"
    "v_mfma_f32_16x16x32_bf16 v[160:163], a[240:243], a[0:3], v[160:163]\n"
    "s_cmp_ge_u32 s62, s65\n"
    "s_cselect_b32 s68, 65536, 0\n"
    "ds_read_b64_tr_b16 a[200:201], v211 offset:8\n"
    "v_mfma_f32_16x16x32_bf16 v[160:163], a[244:247], a[4:7], v[160:163]\n"
    "s_sub_u32 s62, s62, s68\n"
    "v_mfma_f32_16x16x32_bf16 v[164:167], a[240:243], a[8:11], v[164:167]\n"
    "ds_read_b64_tr_b16 a[202:203], v211 offset:4104\n"
    "v_mfma_f32_16x16x32_bf16 v[164:167], a[244:247], a[12:15], v[164:167]\n"
    "v_mfma_f32_16x16x32_bf16 v[168:171], a[240:243], a[16:19], v[168:171]\n"
    "ds_read_b64_tr_b16 a[204:205], v211 offset:8200\n"
    "v_mfma_f32_16x16x32_bf16 v[168:171], a[244:247], a[20:23], v[168:171]\n"
    "v_mfma_f32_16x16x32_bf16 v[172:175], a[240:243], a[24:27], v[172:175]\n"
    "ds_read_b64_tr_b16 a[206:207], v211 offset:12296\n"
    "v_mfma_f32_16x16x32_bf16 v[172:175], a[244:247], a[28:31], v[172:175]\n"
    "s_waitcnt lgkmcnt(8)\n"
    "v_mfma_f32_16x16x32_bf16 v[176:179], a[248:251], a[0:3], v[176:179]\n"
    "ds_read_b64_tr_b16 a[208:209], v211 offset:256\n"
    "v_mfma_f32_16x16x32_bf16 v[176:179], a[252:255], a[4:7], v[176:179]\n"
    "v_mfma_f32_16x16x32_bf16 v[180:183], a[248:251], a[8:11], v[180:183]\n"
    "ds_read_b64_tr_b16 a[210:211], v211 offset:4352\n"
    "v_mfma_f32_16x16x32_bf16 v[180:183], a[252:255], a[12:15], v[180:183]\n"
    "v_mfma_f32_16x16x32_bf16 v[184:187], a[248:251], a[16:19], v[184:187]\n"
    "ds_read_b64_tr_b16 a[212:213], v211 offset:8448\n"
    "v_mfma_f32_16x16x32_bf16 v[184:187], a[252:255], a[20:23], v[184:187]\n"
    "v_mfma_f32_16x16x32_bf16 v[188:191], a[248:251], a[24:27], v[188:191]\n"
    "ds_read_b64_tr_b16 a[214:215], v211 offset:12544\n"
    "v_mfma_f32_16x16x32_bf16 v[188:191], a[252:255], a[28:31], v[188:191]\n"
    "s_waitcnt lgkmcnt(8)\n"
    "s_waitcnt vmcnt(16)\n"
    "v_mfma_f32_16x16x32_bf16 v[64:67], a[192:195], a[32:35], v[64:67]\n"
    "ds_read_b64_tr_b16 a[216:217], v211 offset:264\n"
    "v_mfma_f32_16x16x32_bf16 v[64:67], a[196:199], a[36:39], v[64:67]\n"
    "buffer_load_dwordx4 a[0:3], v216, s[40:43], s60 offen offset:0 sc1\n"
    "v_mfma_f32_16x16x32_bf16 v[68:71], a[192:195], a[40:43], v[68:71]\n"
    "ds_read_b64_tr_b16 a[218:219], v211 offset:4360\n"
    "v_mfma_f32_16x16x32_bf16 v[68:71], a[196:199], a[44:47], v[68:71]\n"
    "v_mfma_f32_16x16x32_bf16 v[72:75], a[192:195], a[48:51], v[72:75]\n"
    "ds_read_b64_tr_b16 a[220:221], v211 offset:8456\n"
    "v_mfma_f32_16x16x32_bf16 v[72:75], a[196:199], a[52:55], v[72:75]\n"
    "buffer_load_dwordx4 a[4:7], v216, s[40:43], s60 offen offset:1024 sc1\n"
    "v_mfma_f32_16x16x32_bf16 v[76:79], a[192:195], a[56:59], v[76:79]\n"
    "ds_read_b64_tr_b16 a[222:223], v211 offset:12552\n"
    "v_mfma_f32_16x16x32_bf16 v[76:79], a[196:199], a[60:63], v[76:79]\n"
    "s_waitcnt lgkmcnt(8)\n"
    "v_mfma_f32_16x16x32_bf16 v[80:83], a[200:203], a[32:35], v[80:83]\n"
    "ds_read_b64_tr_b16 a[224:225], v211 offset:512\n"
    "v_mfma_f32_16x16x32_bf16 v[80:83], a[204:207], a[36:39], v[80:83]\n"
    "buffer_load_dwordx4 a[8:11], v217, s[40:43], s60 offen offset:0 sc1\n"
    "v_mfma_f32_16x16x32_bf16 v[84:87], a[200:203], a[40:43], v[84:87]\n"
    "ds_read_b64_tr_b16 a[226:227], v211 offset:4608\n"
    "v_mfma_f32_16x16x32_bf16 v[84:87], a[204:207], a[44:47], v[84:87]\n"
    "v_mfma_f32_16x16x32_bf16 v[88:91], a[200:203], a[48:51], v[88:91]\n"
    "ds_read_b64_tr_b16 a[228:229], v211 offset:8704\n"
    "v_mfma_f32_16x16x32_bf16 v[88:91], a[204:207], a[52:55], v[88:91]\n"
    "buffer_load_dwordx4 a[12:15], v217, s[40:43], s60 offen offset:1024 sc1\n"
    "v_mfma_f32_16x16x32_bf16 v[92:95], a[200:203], a[56:59], v[92:95]\n"
    "ds_read_b64_tr_b16 a[230:231], v211 offset:12800\n"
    "v_mfma_f32_16x16x32_bf16 v[92:95], a[204:207], a[60:63], v[92:95]\n"
    "s_waitcnt lgkmcnt(8)\n"
    "v_mfma_f32_16x16x32_bf16 v[96:99], a[208:211], a[32:35], v[96:99]\n"
    "ds_read_b64_tr_b16 a[232:233], v211 offset:520\n"
    "v_mfma_f32_16x16x32_bf16 v[96:99], a[212:215], a[36:39], v[96:99]\n"
    "buffer_load_dwordx4 a[16:19], v218, s[40:43], s60 offen offset:0 sc1\n"
    "v_mfma_f32_16x16x32_bf16 v[100:103], a[208:211], a[40:43], v[100:103]\n"
    "ds_read_b64_tr_b16 a[234:235], v211 offset:4616\n"
    "v_mfma_f32_16x16x32_bf16 v[100:103], a[212:215], a[44:47], v[100:103]\n"
    "v_mfma_f32_16x16x32_bf16 v[104:107], a[208:211], a[48:51], v[104:107]\n"
    "ds_read_b64_tr_b16 a[236:237], v211 offset:8712\n"
    "v_mfma_f32_16x16x32_bf16 v[104:107], a[212:215], a[52:55], v[104:107]\n"
    "buffer_load_dwordx4 a[20:23], v218, s[40:43], s60 offen offset:1024 sc1\n"
    "v_mfma_f32_16x16x32_bf16 v[108:111], a[208:211], a[56:59], v[108:111]\n"
    "ds_read_b64_tr_b16 a[238:239], v211 offset:12808\n"
    "v_mfma_f32_16x16x32_bf16 v[108:111], a[212:215], a[60:63], v[108:111]\n"
    "s_waitcnt lgkmcnt(8)\n"
    "v_mfma_f32_16x16x32_bf16 v[112:115], a[216:219], a[32:35], v[112:115]\n"
    "ds_read_b64_tr_b16 a[240:241], v211 offset:768\n"
    "v_mfma_f32_16x16x32_bf16 v[112:115], a[220:223], a[36:39], v[112:115]\n"
    "buffer_load_dwordx4 a[24:27], v219, s[40:43], s60 offen offset:0 sc1\n"
    "v_mfma_f32_16x16x32_bf16 v[116:119], a[216:219], a[40:43], v[116:119]\n"
    "ds_read_b64_tr_b16 a[242:243], v211 offset:4864\n"
    "v_mfma_f32_16x16x32_bf16 v[116:119], a[220:223], a[44:47], v[116:119]\n"
    "v_mfma_f32_16x16x32_bf16 v[120:123], a[216:219], a[48:51], v[120:123]\n"
    "ds_read_b64_tr_b16 a[244:245], v211 offset:8960\n"
    "v_mfma_f32_16x16x32_bf16 v[120:123], a[220:223], a[52:55], v[120:123]\n"
    "buffer_load_dwordx4 a[28:31], v219, s[40:43], s60 offen offset:1024 sc1\n"
    "v_mfma_f32_16x16x32_bf16 v[124:127], a[216:219], a[56:59], v[124:127]\n"
    "s_add_u32 s60, s60, 2048\n"
    "ds_read_b64_tr_b16 a[246:247], v211 offset:13056\n"
    "v_mfma_f32_16x16x32_bf16 v[124:127], a[220:223], a[60:63], v[124:127]\n"
    "s_waitcnt lgkmcnt(8)\n"
    "s_sub_u32 s67, s67, 1\n"
    "v_mfma_f32_16x16x32_bf16 v[128:131], a[224:227], a[32:35], v[128:131]\n"
    "s_cmp_gt_i32 s67, 0\n"
    "s_cselect_b32 s60, s60, s66\n"
    "ds_read_b64_tr_b16 a[248:249], v211 offset:776\n"
    "v_mfma_f32_16x16x32_bf16 v[128:131], a[228:231], a[36:39], v[128:131]\n"
    "s_mov_b32 m0, s62\n"
    "s_nop 0\n"
    "buffer_load_dwordx4 v212, s[44:47], s61 offen offset:0 lds\n"
    "v_mfma_f32_16x16x32_bf16 v[132:135], a[224:227], a[40:43], v[132:135]\n"
    "ds_read_b64_tr_b16 a[250:251], v211 offset:4872\n"
    "v_mfma_f32_16x16x32_bf16 v[132:135], a[228:231], a[44:47], v[132:135]\n"
    "v_mfma_f32_16x16x32_bf16 v[136:139], a[224:227], a[48:51], v[136:139]\n"
    "ds_read_b64_tr_b16 a[252:253], v211 offset:8968\n"
    "v_mfma_f32_16x16x32_bf16 v[136:139], a[228:231], a[52:55], v[136:139]\n"
    "buffer_load_dwordx4 v213, s[44:47], s61 offen offset:1024 lds\n"
    "v_mfma_f32_16x16x32_bf16 v[140:143], a[224:227], a[56:59], v[140:143]\n"
    "s_waitcnt vmcnt(10)\n"
    "ds_read_b64_tr_b16 a[254:255], v211 offset:13064\n"
    "v_mfma_f32_16x16x32_bf16 v[140:143], a[228:231], a[60:63], v[140:143]\n"
    "s_waitcnt lgkmcnt(8)\n"
    "s_barrier\n"
    "v_add_u32 v211, s63, v210\n"
    "v_mfma_f32_16x16x32_bf16 v[144:147], a[232:235], a[32:35], v[144:147]\n"
    "s_add_u32 s63, s63, 16384\n"
    "ds_read_b64_tr_b16 a[192:193], v211 offset:0\n"
    "v_mfma_f32_16x16x32_bf16 v[144:147], a[236:239], a[36:39], v[144:147]\n"
    "s_cmp_ge_u32 s63, s65\n"
    "s_cselect_b32 s69, 65536, 0\n"
    "buffer_load_dwordx4 v214, s[44:47], s61 offen offset:2048 lds\n"
    "v_mfma_f32_16x16x32_bf16 v[148:151], a[232:235], a[40:43], v[148:151]\n"
    "s_sub_u32 s63, s63, s69\n"
    "ds_read_b64_tr_b16 a[194:195], v211 offset:4096\n"
    "v_mfma_f32_16x16x32_bf16 v[148:151], a[236:239], a[44:47], v[148:151]\n"
    "s_sub_u32 s64, s64, 1\n"
    "v_mfma_f32_16x16x32_bf16 v[152:155], a[232:235], a[48:51], v[152:155]\n"
    "ds_read_b64_tr_b16 a[196:197], v211 offset:8192\n"
    "v_mfma_f32_16x16x32_bf16 v[152:155], a[236:239], a[52:55], v[152:155]\n"
    "buffer_load_dwordx4 v215, s[44:47], s61 offen offset:3072 lds\n"
    "v_mfma_f32_16x16x32_bf16 v[156:159], a[232:235], a[56:59], v[156:159]\n"
    "s_add_u32 s61, s61, 524288\n"
    "ds_read_b64_tr_b16 a[198:199], v211 offset:12288\n"
    "v_mfma_f32_16x16x32_bf16 v[156:159], a[236:239], a[60:63], v[156:159]\n"
    "s_waitcnt lgkmcnt(8)\n"
    "s_add_u32 s62, s62, 16384\n"
    "v_mfma_f32_16x16x32_bf16 v[160:163], a[240:243], a[32:35], v[160:163]\n"
    "s_cmp_ge_u32 s62, s65\n"
    "s_cselect_b32 s68, 65536, 0\n"
    "ds_read_b64_tr_b16 a[200:201], v211 offset:8\n"
    "v_mfma_f32_16x16x32_bf16 v[160:163], a[244:247], a[36:39], v[160:163]\n"
    "s_sub_u32 s62, s62, s68\n"
    "v_mfma_f32_16x16x32_bf16 v[164:167], a[240:243], a[40:43], v[164:167]\n"
    "ds_read_b64_tr_b16 a[202:203], v211 offset:4104\n"
    "v_mfma_f32_16x16x32_bf16 v[164:167], a[244:247], a[44:47], v[164:167]\n"
    "v_mfma_f32_16x16x32_bf16 v[168:171], a[240:243], a[48:51], v[168:171]\n"
    "ds_read_b64_tr_b16 a[204:205], v211 offset:8200\n"
    "v_mfma_f32_16x16x32_bf16 v[168:171], a[244:247], a[52:55], v[168:171]\n"
    "v_mfma_f32_16x16x32_bf16 v[172:175], a[240:243], a[56:59], v[172:175]\n"
    "ds_read_b64_tr_b16 a[206:207], v211 offset:12296\n"
    "v_mfma_f32_16x16x32_bf16 v[172:175], a[244:247], a[60:63], v[172:175]\n"
    "s_waitcnt lgkmcnt(8)\n"
    "v_mfma_f32_16x16x32_bf16 v[176:179], a[248:251], a[32:35], v[176:179]\n"
    "ds_read_b64_tr_b16 a[208:209], v211 offset:256\n"
    "v_mfma_f32_16x16x32_bf16 v[176:179], a[252:255], a[36:39], v[176:179]\n"
    "v_mfma_f32_16x16x32_bf16 v[180:183], a[248:251], a[40:43], v[180:183]\n"
    "ds_read_b64_tr_b16 a[210:211], v211 offset:4352\n"
    "v_mfma_f32_16x16x32_bf16 v[180:183], a[252:255], a[44:47], v[180:183]\n"
    "v_mfma_f32_16x16x32_bf16 v[184:187], a[248:251], a[48:51], v[184:187]\n"
    "ds_read_b64_tr_b16 a[212:213], v211 offset:8448\n"
    "v_mfma_f32_16x16x32_bf16 v[184:187], a[252:255], a[52:55], v[184:187]\n"
    "v_mfma_f32_16x16x32_bf16 v[188:191], a[248:251], a[56:59], v[188:191]\n"
    "ds_read_b64_tr_b16 a[214:215], v211 offset:12544\n"
    "v_mfma_f32_16x16x32_bf16 v[188:191], a[252:255], a[60:63], v[188:191]\n"
    "s_waitcnt lgkmcnt(8)\n"
    "s_waitcnt vmcnt(16)\n"
    "v_mfma_f32_16x16x32_bf16 v[64:67], a[192:195], a[64:67], v[64:67]\n"
    "ds_read_b64_tr_b16 a[216:217], v211 offset:264\n"
    "v_mfma_f32_16x16x32_bf16 v[64:67], a[196:199], a[68:71], v[64:67]\n"
    "buffer_load_dwordx4 a[32:35], v216, s[40:43], s60 offen offset:0 sc1\n"
    "v_mfma_f32_16x16x32_bf16 v[68:71], a[192:195], a[72:75], v[68:71]\n"
    "ds_read_b64_tr_b16 a[218:219], v211 offset:4360\n"
    "v_mfma_f32_16x16x32_bf16 v[68:71], a[196:199], a[76:79], v[68:71]\n"
    "v_mfma_f32_16x16x32_bf16 v[72:75], a[192:195], a[80:83], v[72:75]\n"
    "ds_read_b64_tr_b16 a[220:221], v211 offset:8456\n"
    "v_mfma_f32_16x16x32_bf16 v[72:75], a[196:199], a[84:87], v[72:75]\n"
    "buffer_load_dwordx4 a[36:39], v216, s[40:43], s60 offen offset:1024 sc1\n"
    "v_mfma_f32_16x16x32_bf16 v[76:79], a[192:195], a[88:91], v[76:79]\n"
    "ds_read_b64_tr_b16 a[222:223], v211 offset:12552\n"
    "v_mfma_f32_16x16x32_bf16 v[76:79], a[196:199], a[92:95], v[76:79]\n"
    "s_waitcnt lgkmcnt(8)\n"
    "v_mfma_f32_16x16x32_bf16 v[80:83], a[200:203], a[64:67], v[80:83]\n"
    "ds_read_b64_tr_b16 a[224:225], v211 offset:512\n"
    "v_mfma_f32_16x16x32_bf16 v[80:83], a[204:207], a[68:71], v[80:83]\n"
    "buffer_load_dwordx4 a[40:43], v217, s[40:43], s60 offen offset:0 sc1\n"
    "v_mfma_f32_16x16x32_bf16 v[84:87], a[200:203], a[72:75], v[84:87]\n"
    "ds_read_b64_tr_b16 a[226:227], v211 offset:4608\n"
    "v_mfma_f32_16x16x32_bf16 v[84:87], a[204:207], a[76:79], v[84:87]\n"
    "v_mfma_f32_16x16x32_bf16 v[88:91], a[200:203], a[80:83], v[88:91]\n"
    "ds_read_b64_tr_b16 a[228:229], v211 offset:8704\n"
    "v_mfma_f32_16x16x32_bf16 v[88:91], a[204:207], a[84:87], v[88:91]\n"
    "buffer_load_dwordx4 a[44:47], v217, s[40:43], s60 offen offset:1024 sc1\n"
    "v_mfma_f32_16x16x32_bf16 v[92:95], a[200:203], a[88:91], v[92:95]\n"
    "ds_read_b64_tr_b16 a[230:231], v211 offset:12800\n"
    "v_mfma_f32_16x16x32_bf16 v[92:95], a[204:207], a[92:95], v[92:95]\n"
    "s_waitcnt lgkmcnt(8)\n"
    "v_mfma_f32_16x16x32_bf16 v[96:99], a[208:211], a[64:67], v[96:99]\n"
    "ds_read_b64_tr_b16 a[232:233], v211 offset:520\n"
    "v_mfma_f32_16x16x32_bf16 v[96:99], a[212:215], a[68:71], v[96:99]\n"
    "buffer_load_dwordx4 a[48:51], v218, s[40:43], s60 offen offset:0 sc1\n"
    "v_mfma_f32_16x16x32_bf16 v[100:103], a[208:211], a[72:75], v[100:103]\n"
    "ds_read_b64_tr_b16 a[234:235], v211 offset:4616\n"
    "v_mfma_f32_16x16x32_bf16 v[100:103], a[212:215], a[76:79], v[100:103]\n"
    "v_mfma_f32_16x16x32_bf16 v[104:107], a[208:211], a[80:83], v[104:107]\n"
    "ds_read_b64_tr_b16 a[236:237], v211 offset:8712\n"
    "v_mfma_f32_16x16x32_bf16 v[104:107], a[212:215], a[84:87], v[104:107]\n"
    "buffer_load_dwordx4 a[52:55], v218, s[40:43], s60 offen offset:1024 sc1\n"
    "v_mfma_f32_16x16x32_bf16 v[108:111], a[208:211], a[88:91], v[108:111]\n"
    "ds_read_b64_tr_b16 a[238:239], v211 offset:12808\n"
    "v_mfma_f32_16x16x32_bf16 v[108:111], a[212:215], a[92:95], v[108:111]\n"
    "s_waitcnt lgkmcnt(8)\n"
    "v_mfma_f32_16x16x32_bf16 v[112:115], a[216:219], a[64:67], v[112:115]\n"
    "ds_read_b64_tr_b16 a[240:241], v211 offset:768\n"
    "v_mfma_f32_16x16x32_bf16 v[112:115], a[220:223], a[68:71], v[112:115]\n"
    "buffer_load_dwordx4 a[56:59], v219, s[40:43], s60 offen offset:0 sc1\n"
    "v_mfma_f32_16x16x32_bf16 v[116:119], a[216:219], a[72:75], v[116:119]\n"
    "ds_read_b64_tr_b16 a[242:243], v211 offset:4864\n"
    "v_mfma_f32_16x16x32_bf16 v[116:119], a[220:223], a[76:79], v[116:119]\n"
    "v_mfma_f32_16x16x32_bf16 v[120:123], a[216:219], a[80:83], v[120:123]\n"
    "ds_read_b64_tr_b16 a[244:245], v211 offset:8960\n"
    "v_mfma_f32_16x16x32_bf16 v[120:123], a[220:223], a[84:87], v[120:123]\n"
    "buffer_load_dwordx4 a[60:63], v219, s[40:43], s60 offen offset:1024 sc1\n"
    "v_mfma_f32_16x16x32_bf16 v[124:127], a[216:219], a[88:91], v[124:127]\n"
    "s_add_u32 s60, s60, 2048\n"
    "ds_read_b64_tr_b16 a[246:247], v211 offset:13056\n"
    "v_mfma_f32_16x16x32_bf16 v[124:127], a[220:223], a[92:95], v[124:127]\n"
    "s_waitcnt lgkmcnt(8)\n"
    "s_sub_u32 s67, s67, 1\n"
    "v_mfma_f32_16x16x32_bf16 v[128:131], a[224:227], a[64:67], v[128:131]\n"
    "s_cmp_gt_i32 s67, 0\n"
    "s_cselect_b32 s60, s60, s66\n"
    "ds_read_b64_tr_b16 a[248:249], v211 offset:776\n"
    "v_mfma_f32_16x16x32_bf16 v[128:131], a[228:231], a[68:71], v[128:131]\n"
    "s_mov_b32 m0, s62\n"
    "s_nop 0\n"
    "buffer_load_dwordx4 v212, s[44:47], s61 offen offset:0 lds\n"
    "v_mfma_f32_16x16x32_bf16 v[132:135], a[224:227], a[72:75], v[132:135]\n"
    "ds_read_b64_tr_b16 a[250:251], v211 offset:4872\n"
    "v_mfma_f32_16x16x32_bf16 v[132:135], a[228:231], a[76:79], v[132:135]\n"
    "v_mfma_f32_16x16x32_bf16 v[136:139], a[224:227], a[80:83], v[136:139]\n"
    "ds_read_b64_tr_b16 a[252:253], v211 offset:8968\n"
    "v_mfma_f32_16x16x32_bf16 v[136:139], a[228:231], a[84:87], v[136:139]\n"
    "buffer_load_dwordx4 v213, s[44:47], s61 offen offset:1024 lds\n"
    "v_mfma_f32_16x16x32_bf16 v[140:143], a[224:227], a[88:91], v[140:143]\n"
    "s_waitcnt vmcnt(10)\n"
    "ds_read_b64_tr_b16 a[254:255], v211 offset:13064\n"
    "v_mfma_f32_16x16x32_bf16 v[140:143], a[228:231], a[92:95], v[140:143]\n"
    "s_waitcnt lgkmcnt(8)\n"
    "s_barrier\n"
    "v_add_u32 v211, s63, v210\n"
    "v_mfma_f32_16x16x32_bf16 v[144:147], a[232:235], a[64:67], v[144:147]\n"
    "s_add_u32 s63, s63, 16384\n"
    "ds_read_b64_tr_b16 a[192:193], v211 offset:0\n"
    "v_mfma_f32_16x16x32_bf16 v[144:147], a[236:239], a[68:71], v[144:147]\n"
    "s_cmp_ge_u32 s63, s65\n"
    "s_cselect_b32 s69, 65536, 0\n"
    "buffer_load_dwordx4 v214, s[44:47], s61 offen offset:2048 lds\n"
    "v_mfma_f32_16x16x32_bf16 v[148:151], a[232:235], a[72:75], v[148:151]\n"
    "s_sub_u32 s63, s63, s69\n"
    "ds_read_b64_tr_b16 a[194:195], v211 offset:4096\n"
    "v_mfma_f32_16x16x32_bf16 v[148:151], a[236:239], a[76:79], v[148:151]\n"
    "s_sub_u32 s64, s64, 1\n"
    "v_mfma_f32_16x16x32_bf16 v[152:155], a[232:235], a[80:83], v[152:155]\n"
    "ds_read_b64_tr_b16 a[196:197], v211 offset:8192\n"
    "v_mfma_f32_16x16x32_bf16 v[152:155], a[236:239], a[84:87], v[152:155]\n"
    "buffer_load_dwordx4 v215, s[44:47], s61 offen offset:3072 lds\n"
    "v_mfma_f32_16x16x32_bf16 v[156:159], a[232:235], a[88:91], v[156:159]\n"
    "s_add_u32 s61, s61, 524288\n"
    "ds_read_b64_tr_b16 a[198:199], v211 offset:12288\n"
    "v_mfma_f32_16x16x32_bf16 v[156:159], a[236:239], a[92:95], v[156:159]\n"
    "s_waitcnt lgkmcnt(8)\n"
    "s_add_u32 s62, s62, 16384\n"
    "v_mfma_f32_16x16x32_bf16 v[160:163], a[240:243], a[64:67], v[160:163]\n"
    "s_cmp_ge_u32 s62, s65\n"
    "s_cselect_b32 s68, 65536, 0\n"
    "ds_read_b64_tr_b16 a[200:201], v211 offset:8\n"
    "v_mfma_f32_16x16x32_bf16 v[160:163], a[244:247], a[68:71], v[160:163]\n"
    "s_sub_u32 s62, s62, s68\n"
    "v_mfma_f32_16x16x32_bf16 v[164:167], a[240:243], a[72:75], v[164:167]\n"
    "ds_read_b64_tr_b16 a[202:203], v211 offset:4104\n"
    "v_mfma_f32_16x16x32_bf16 v[164:167], a[244:247], a[76:79], v[164:167]\n"
    "v_mfma_f32_16x16x32_bf16 v[168:171], a[240:243], a[80:83], v[168:171]\n"
    "ds_read_b64_tr_b16 a[204:205], v211 offset:8200\n"
    "v_mfma_f32_16x16x32_bf16 v[168:171], a[244:247], a[84:87], v[168:171]\n"
    "v_mfma_f32_16x16x32_bf16 v[172:175], a[240:243], a[88:91], v[172:175]\n"
    "ds_read_b64_tr_b16 a[206:207], v211 offset:12296\n"
    "v_mfma_f32_16x16x32_bf16 v[172:175], a[244:247], a[92:95], v[172:175]\n"
    "s_waitcnt lgkmcnt(8)\n"
    "v_mfma_f32_16x16x32_bf16 v[176:179], a[248:251], a[64:67], v[176:179]\n"
    "ds_read_b64_tr_b16 a[208:209], v211 offset:256\n"
    "v_mfma_f32_16x16x32_bf16 v[176:179], a[252:255], a[68:71], v[176:179]\n"
    "v_mfma_f32_16x16x32_bf16 v[180:183], a[248:251], a[72:75], v[180:183]\n"
    "ds_read_b64_tr_b16 a[210:211], v211 offset:4352\n"
    "v_mfma_f32_16x16x32_bf16 v[180:183], a[252:255], a[76:79], v[180:183]\n"
    "v_mfma_f32_16x16x32_bf16 v[184:187], a[248:251], a[80:83], v[184:187]\n"
    "ds_read_b64_tr_b16 a[212:213], v211 offset:8448\n"
    "v_mfma_f32_16x16x32_bf16 v[184:187], a[252:255], a[84:87], v[184:187]\n"
    "v_mfma_f32_16x16x32_bf16 v[188:191], a[248:251], a[88:91], v[188:191]\n"
    "ds_read_b64_tr_b16 a[214:215], v211 offset:12544\n"
    "v_mfma_f32_16x16x32_bf16 v[188:191], a[252:255], a[92:95], v[188:191]\n"
    "s_waitcnt lgkmcnt(8)\n"
    "s_waitcnt vmcnt(16)\n"
    "s_cmp_eq_u32 s64, 0\n"
    "s_cbranch_scc0 LA_qloop\n"
    "s_add_u32 m0, s72, 32768\n"
    "s_nop 0\n"
    "buffer_load_dwordx4 v230, s[48:51], s70 offen offset:0 lds\n"
    "buffer_load_dwordx4 v231, s[48:51], s70 offen offset:1024 lds\n"
    "buffer_load_dwordx4 v232, s[48:51], s70 offen offset:2048 lds\n"
    "buffer_load_dwordx4 v233, s[48:51], s70 offen offset:3072 lds\n"
    "s_add_u32 s70, s70, 16384\n"
    "s_add_u32 m0, s73, 16384\n"
    "s_nop 0\n"
    "buffer_load_dwordx4 v234, s[52:55], s71 offen offset:0 lds\n"
    "buffer_load_dwordx4 v235, s[52:55], s71 offen offset:1024 lds\n"
    "buffer_load_dwordx4 v234, s[52:55], s71 offen offset:2048 lds\n"
    "buffer_load_dwordx4 v235, s[52:55], s71 offen offset:3072 lds\n"
    "s_add_u32 s71, s71, 16384\n"
    "s_mov_b64 s[80:81], %8\n"
    "s_mov_b64 s[82:83], %9\n"
    "s_mov_b64 s[84:85], %10\n"
    "s_mov_b64 s[86:87], %11\n"
    "v_min_u32 v242, 8, v237\n"
    "v_lshlrev_b32 v242, 7, v242\n"
    "s_mov_b32 s78, 0x3e0293ee\n"
    "s_mov_b32 s79, 0x3e0293ee\n"
    "s_mov_b64 s[44:45], %16\n"
    "s_and_b32 s45, s45, 0xffff\n"
    "s_mov_b32 s46, 589824\n"
    "s_mov_b32 s47, 0x20000\n"
    "s_lshr_b32 s68, %6, 1\n"
    "s_mul_i32 s63, s68, 9216\n"
    "s_add_u32 s63, s63, %7\n"
    "s_add_u32 s68, s68, 0\n"
    "s_mul_i32 s61, s68, 36864\n"
    "s_and_b32 s68, %6, 1\n"
    "s_mul_i32 s65, s68, 18432\n"
    "s_add_u32 s61, s61, s65\n"
    "s_lshl_b32 s65, s68, 12\n"
    "s_add_u32 s63, s63, s65\n"
    "s_sub_u32 s65, 8192, s65\n"
    "s_add_u32 s65, s65, s63\n"
    "s_cmp_eq_u32 s68, 0\n"
    "s_cselect_b32 s66, -1, 0\n"
    "s_cselect_b32 s67, 0, -1\n"
    "s_mov_b32 s69, s61\n"
    "v_bfe_u32 v244, v237, 2, 2\n"
    "v_mul_u32_u24 v244, 1152, v244\n"
    "v_lshrrev_b32 v245, 4, v237\n"
    "v_lshl_add_u32 v244, v245, 6, v244\n"
    "v_add_u32 v244, %17, v244\n"
    "v_and_b32 v245, 3, v237\n"
    "v_xor_b32 v246, 2, v245\n"
    "v_lshl_add_u32 v245, v245, 4, v244\n"
    "v_lshl_add_u32 v246, v246, 4, v244\n"
    "v_add_u32 v220, 0, v245\n"
    "v_add_u32 v221, 3584, v246\n"
    "v_add_u32 v222, 7168, v245\n"
    "v_add_u32 v223, 10752, v246\n"
    "v_bfe_u32 v244, v237, 1, 4\n"
    "v_mul_u32_u24 v244, 1152, v244\n"
    "v_and_b32 v245, 1, v237\n"
    "v_lshl_add_u32 v244, v245, 4, v244\n"
    "v_add_u32 v250, %18, v244\n"
    "s_waitcnt vmcnt(12)\n"
    "v_pk_mul_f32 v[64:65], v[64:65], s[78:79]\n"
    "v_pk_mul_f32 v[66:67], v[66:67], s[78:79]\n"
    "v_cvt_pk_bf16_f32 v64, v64, v65\n"
    "v_cvt_pk_bf16_f32 v66, v66, v67\n"
    "v_accvgpr_write_b32 a0, v64\n"
    "v_accvgpr_write_b32 a1, v66\n"
    "v_pk_mul_f32 v[80:81], v[80:81], s[78:79]\n"
    "v_pk_mul_f32 v[82:83], v[82:83], s[78:79]\n"
    "v_cvt_pk_bf16_f32 v80, v80, v81\n"
    "v_cvt_pk_bf16_f32 v82, v82, v83\n"
    "v_accvgpr_write_b32 a2, v80\n"
    "v_accvgpr_write_b32 a3, v82\n"
    "v_pk_mul_f32 v[96:97], v[96:97], s[78:79]\n"
    "v_pk_mul_f32 v[98:99], v[98:99], s[78:79]\n"
    "v_cvt_pk_bf16_f32 v96, v96, v97\n"
    "v_cvt_pk_bf16_f32 v98, v98, v99\n"
    "v_accvgpr_write_b32 a4, v96\n"
    "v_accvgpr_write_b32 a5, v98\n"
    "v_pk_mul_f32 v[112:113], v[112:113], s[78:79]\n"
    "v_pk_mul_f32 v[114:115], v[114:115], s[78:79]\n"
    "v_cvt_pk_bf16_f32 v112, v112, v113\n"
    "v_cvt_pk_bf16_f32 v114, v114, v115\n"
    "v_accvgpr_write_b32 a6, v112\n"
    "v_accvgpr_write_b32 a7, v114\n"
    "v_pk_mul_f32 v[128:129], v[128:129], s[78:79]\n"
    "v_pk_mul_f32 v[130:131], v[130:131], s[78:79]\n"
    "v_cvt_pk_bf16_f32 v128, v128, v129\n"
    "v_cvt_pk_bf16_f32 v130, v130, v131\n"
    "v_accvgpr_write_b32 a8, v128\n"
    "v_accvgpr_write_b32 a9, v130\n"
    "v_pk_mul_f32 v[144:145], v[144:145], s[78:79]\n"
    "v_pk_mul_f32 v[146:147], v[146:147], s[78:79]\n"
    "v_cvt_pk_bf16_f32 v144, v144, v145\n"
    "v_cvt_pk_bf16_f32 v146, v146, v147\n"
    "v_accvgpr_write_b32 a10, v144\n"
    "v_accvgpr_write_b32 a11, v146\n"
    "v_pk_mul_f32 v[160:161], v[160:161], s[78:79]\n"
    "v_pk_mul_f32 v[162:163], v[162:163], s[78:79]\n"
    "v_cvt_pk_bf16_f32 v160, v160, v161\n"
    "v_cvt_pk_bf16_f32 v162, v162, v163\n"
    "v_accvgpr_write_b32 a12, v160\n"
    "v_accvgpr_write_b32 a13, v162\n"
    "v_pk_mul_f32 v[176:177], v[176:177], s[78:79]\n"
    "v_pk_mul_f32 v[178:179], v[178:179], s[78:79]\n"
    "v_cvt_pk_bf16_f32 v176, v176, v177\n"
    "v_cvt_pk_bf16_f32 v178, v178, v179\n"
    "v_accvgpr_write_b32 a14, v176\n"
    "v_accvgpr_write_b32 a15, v178\n"
    "v_pk_mul_f32 v[68:69], v[68:69], s[78:79]\n"
    "v_pk_mul_f32 v[70:71], v[70:71], s[78:79]\n"
    "v_cvt_pk_bf16_f32 v68, v68, v69\n"
    "v_cvt_pk_bf16_f32 v70, v70, v71\n"
    "v_accvgpr_write_b32 a16, v68\n"
    "v_accvgpr_write_b32 a17, v70\n"
    "v_pk_mul_f32 v[84:85], v[84:85], s[78:79]\n"
    "v_pk_mul_f32 v[86:87], v[86:87], s[78:79]\n"
    "v_cvt_pk_bf16_f32 v84, v84, v85\n"
    "v_cvt_pk_bf16_f32 v86, v86, v87\n"
    "v_accvgpr_write_b32 a18, v84\n"
    "v_accvgpr_write_b32 a19, v86\n"
    "v_pk_mul_f32 v[100:101], v[100:101], s[78:79]\n"
    "v_pk_mul_f32 v[102:103], v[102:103], s[78:79]\n"
    "v_cvt_pk_bf16_f32 v100, v100, v101\n"
    "v_cvt_pk_bf16_f32 v102, v102, v103\n"
    "v_accvgpr_write_b32 a20, v100\n"
    "v_accvgpr_write_b32 a21, v102\n"
    "v_pk_mul_f32 v[116:117], v[116:117], s[78:79]\n"
    "v_pk_mul_f32 v[118:119], v[118:119], s[78:79]\n"
    "v_cvt_pk_bf16_f32 v116, v116, v117\n"
    "v_cvt_pk_bf16_f32 v118, v118, v119\n"
    "v_accvgpr_write_b32 a22, v116\n"
    "v_accvgpr_write_b32 a23, v118\n"
    "v_pk_mul_f32 v[132:133], v[132:133], s[78:79]\n"
    "v_pk_mul_f32 v[134:135], v[134:135], s[78:79]\n"
    "v_cvt_pk_bf16_f32 v132, v132, v133\n"
    "v_cvt_pk_bf16_f32 v134, v134, v135\n"
    "v_accvgpr_write_b32 a24, v132\n"
    "v_accvgpr_write_b32 a25, v134\n"
    "v_pk_mul_f32 v[148:149], v[148:149], s[78:79]\n"
    "v_pk_mul_f32 v[150:151], v[150:151], s[78:79]\n"
    "v_cvt_pk_bf16_f32 v148, v148, v149\n"
    "v_cvt_pk_bf16_f32 v150, v150, v151\n"
    "v_accvgpr_write_b32 a26, v148\n"
    "v_accvgpr_write_b32 a27, v150\n"
    "v_pk_mul_f32 v[164:165], v[164:165], s[78:79]\n"
    "v_pk_mul_f32 v[166:167], v[166:167], s[78:79]\n"
    "v_cvt_pk_bf16_f32 v164, v164, v165\n"
    "v_cvt_pk_bf16_f32 v166, v166, v167\n"
    "v_accvgpr_write_b32 a28, v164\n"
    "v_accvgpr_write_b32 a29, v166\n"
    "v_pk_mul_f32 v[180:181], v[180:181], s[78:79]\n"
    "v_pk_mul_f32 v[182:183], v[182:183], s[78:79]\n"
    "v_cvt_pk_bf16_f32 v180, v180, v181\n"
    "v_cvt_pk_bf16_f32 v182, v182, v183\n"
    "v_accvgpr_write_b32 a30, v180\n"
    "v_accvgpr_write_b32 a31, v182\n"
    "v_pk_mul_f32 v[72:73], v[72:73], s[78:79]\n"
    "v_pk_mul_f32 v[74:75], v[74:75], s[78:79]\n"
    "v_cvt_pk_bf16_f32 v72, v72, v73\n"
    "v_cvt_pk_bf16_f32 v74, v74, v75\n"
    "v_accvgpr_write_b32 a32, v72\n"
    "v_accvgpr_write_b32 a33, v74\n"
    "v_pk_mul_f32 v[88:89], v[88:89], s[78:79]\n"
    "v_pk_mul_f32 v[90:91], v[90:91], s[78:79]\n"
    "v_cvt_pk_bf16_f32 v88, v88, v89\n"
    "v_cvt_pk_bf16_f32 v90, v90, v91\n"
    "v_accvgpr_write_b32 a34, v88\n"
    "v_accvgpr_write_b32 a35, v90\n"
    "v_pk_mul_f32 v[104:105], v[104:105], s[78:79]\n"
    "v_pk_mul_f32 v[106:107], v[106:107], s[78:79]\n"
    "v_cvt_pk_bf16_f32 v104, v104, v105\n"
    "v_cvt_pk_bf16_f32 v106, v106, v107\n"
    "v_accvgpr_write_b32 a36, v104\n"
    "v_accvgpr_write_b32 a37, v106\n"
    "v_pk_mul_f32 v[120:121], v[120:121], s[78:79]\n"
    "v_pk_mul_f32 v[122:123], v[122:123], s[78:79]\n"
    "v_cvt_pk_bf16_f32 v120, v120, v121\n"
    "v_cvt_pk_bf16_f32 v122, v122, v123\n"
    "v_accvgpr_write_b32 a38, v120\n"
    "v_accvgpr_write_b32 a39, v122\n"
    "v_pk_mul_f32 v[136:137], v[136:137], s[78:79]\n"
    "v_pk_mul_f32 v[138:139], v[138:139], s[78:79]\n"
    "v_cvt_pk_bf16_f32 v136, v136, v137\n"
    "v_cvt_pk_bf16_f32 v138, v138, v139\n"
    "v_accvgpr_write_b32 a40, v136\n"
    "v_accvgpr_write_b32 a41, v138\n"
    "v_pk_mul_f32 v[152:153], v[152:153], s[78:79]\n"
    "v_pk_mul_f32 v[154:155], v[154:155], s[78:79]\n"
    "v_cvt_pk_bf16_f32 v152, v152, v153\n"
    "v_cvt_pk_bf16_f32 v154, v154, v155\n"
    "v_accvgpr_write_b32 a42, v152\n"
    "v_accvgpr_write_b32 a43, v154\n"
    "v_pk_mul_f32 v[168:169], v[168:169], s[78:79]\n"
    "v_pk_mul_f32 v[170:171], v[170:171], s[78:79]\n"
    "v_cvt_pk_bf16_f32 v168, v168, v169\n"
    "v_cvt_pk_bf16_f32 v170, v170, v171\n"
    "v_accvgpr_write_b32 a44, v168\n"
    "v_accvgpr_write_b32 a45, v170\n"
    "v_pk_mul_f32 v[184:185], v[184:185], s[78:79]\n"
    "v_pk_mul_f32 v[186:187], v[186:187], s[78:79]\n"
    "v_cvt_pk_bf16_f32 v184, v184, v185\n"
    "v_cvt_pk_bf16_f32 v186, v186, v187\n"
    "v_accvgpr_write_b32 a46, v184\n"
    "v_accvgpr_write_b32 a47, v186\n"
    "v_pk_mul_f32 v[76:77], v[76:77], s[78:79]\n"
    "v_pk_mul_f32 v[78:79], v[78:79], s[78:79]\n"
    "v_cvt_pk_bf16_f32 v76, v76, v77\n"
    "v_cvt_pk_bf16_f32 v78, v78, v79\n"
    "v_accvgpr_write_b32 a48, v76\n"
    "v_accvgpr_write_b32 a49, v78\n"
    "v_pk_mul_f32 v[92:93], v[92:93], s[78:79]\n"
    "v_pk_mul_f32 v[94:95], v[94:95], s[78:79]\n"
    "v_cvt_pk_bf16_f32 v92, v92, v93\n"
    "v_cvt_pk_bf16_f32 v94, v94, v95\n"
    "v_accvgpr_write_b32 a50, v92\n"
    "v_accvgpr_write_b32 a51, v94\n"
    "v_pk_mul_f32 v[108:109], v[108:109], s[78:79]\n"
    "v_pk_mul_f32 v[110:111], v[110:111], s[78:79]\n"
    "v_cvt_pk_bf16_f32 v108, v108, v109\n"
    "v_cvt_pk_bf16_f32 v110, v110, v111\n"
    "v_accvgpr_write_b32 a52, v108\n"
    "v_accvgpr_write_b32 a53, v110\n"
    "v_pk_mul_f32 v[124:125], v[124:125], s[78:79]\n"
    "v_pk_mul_f32 v[126:127], v[126:127], s[78:79]\n"
    "v_cvt_pk_bf16_f32 v124, v124, v125\n"
    "v_cvt_pk_bf16_f32 v126, v126, v127\n"
    "v_accvgpr_write_b32 a54, v124\n"
    "v_accvgpr_write_b32 a55, v126\n"
    "v_pk_mul_f32 v[140:141], v[140:141], s[78:79]\n"
    "v_pk_mul_f32 v[142:143], v[142:143], s[78:79]\n"
    "v_cvt_pk_bf16_f32 v140, v140, v141\n"
    "v_cvt_pk_bf16_f32 v142, v142, v143\n"
    "v_accvgpr_write_b32 a56, v140\n"
    "v_accvgpr_write_b32 a57, v142\n"
    "v_pk_mul_f32 v[156:157], v[156:157], s[78:79]\n"
    "v_pk_mul_f32 v[158:159], v[158:159], s[78:79]\n"
    "v_cvt_pk_bf16_f32 v156, v156, v157\n"
    "v_cvt_pk_bf16_f32 v158, v158, v159\n"
    "v_accvgpr_write_b32 a58, v156\n"
    "v_accvgpr_write_b32 a59, v158\n"
    "v_pk_mul_f32 v[172:173], v[172:173], s[78:79]\n"
    "v_pk_mul_f32 v[174:175], v[174:175], s[78:79]\n"
    "v_cvt_pk_bf16_f32 v172, v172, v173\n"
    "v_cvt_pk_bf16_f32 v174, v174, v175\n"
    "v_accvgpr_write_b32 a60, v172\n"
    "v_accvgpr_write_b32 a61, v174\n"
    "v_pk_mul_f32 v[188:189], v[188:189], s[78:79]\n"
    "v_pk_mul_f32 v[190:191], v[190:191], s[78:79]\n"
    "v_cvt_pk_bf16_f32 v188, v188, v189\n"
    "v_cvt_pk_bf16_f32 v190, v190, v191\n"
    "v_accvgpr_write_b32 a62, v188\n"
    "v_accvgpr_write_b32 a63, v190\n"
    "s_waitcnt vmcnt(8)\n"
    "s_waitcnt lgkmcnt(0)\n"
    "s_barrier\n"
    "ds_read_b128 a[64:67], v224 offset:0\n"
    "ds_read_b128 a[68:71], v225 offset:0\n"
    "ds_read_b128 a[72:75], v226 offset:0\n"
    "ds_read_b128 a[76:79], v227 offset:0\n"
    "s_add_u32 s61, %22, 0\n"
    "s_and_b32 s61, s61, 15\n"
    "s_mul_i32 s61, s61, 36864\n"
    "s_add_u32 s61, s61, s69\n"
    "s_add_u32 m0, s63, 145408\n"
    "s_nop 0\n"
    "buffer_load_dwordx4 v220, s[44:47], s61 offen offset:0 lds\n"
    "buffer_load_dwordx4 v221, s[44:47], s61 offen offset:1024 lds\n"
    "buffer_load_dwordx4 v222, s[44:47], s61 offen offset:2048 lds\n"
    "buffer_load_dwordx4 v223, s[44:47], s61 offen offset:3072 lds\n"
    "s_add_u32 m0, s65, 145408\n"
    "s_mov_b64 exec, s[66:67]\n"
    "buffer_load_dwordx4 v250, s[44:47], s61 offen lds\n"
    "s_mov_b64 exec, -1\n"
    "v_mov_b32 v244, 0x3f803f80\n"
    "v_accvgpr_write_b32 a192, v244\n"
    "v_mov_b32 v238, 0\n"
    "v_accvgpr_write_b32 a193, v244\n"
    "v_mov_b32 v239, 0\n"
    "v_accvgpr_write_b32 a194, v244\n"
    "v_mov_b32 v240, 0\n"
    "v_accvgpr_write_b32 a195, v244\n"
    "v_mov_b32 v241, 0\n"
    "s_mov_b32 s64, 30\n"
    "s_getpc_b64 s[78:79]\n"
    "LA_pc:\n"
    "s_add_u32 s78, s78, LA_exit-LA_pc\n"
    "s_addc_u32 s79, s79, 0\n"
    "s_lshl_b32 s68, %26, 2\n"
    "s_add_u32 s68, s68, %6\n"
    "s_and_b32 s68, s68, 15\n"
    "s_lshl_b32 s68, s68, 13\n"
    "v_lshlrev_b32 v207, 7, v237\n"
    "v_add_u32 v207, s68, v207\n"
    "s_mov_b32 s68, LB_end-LA_exit-4\n"
    "v_min_u32 v207, s68, v207\n"
    "v_and_b32 v207, -4, v207\n"
    "s_waitcnt lgkmcnt(0)\n"
    "v_mfma_f32_16x16x32_bf16 v[128:131], a[64:67], a[0:3], 0\n"
    "v_mfma_f32_16x16x32_bf16 v[128:131], a[68:71], a[4:7], v[128:131]\n"
    "v_mfma_f32_16x16x32_bf16 v[128:131], a[72:75], a[8:11], v[128:131]\n"
    "v_mfma_f32_16x16x32_bf16 v[128:131], a[76:79], a[12:15], v[128:131]\n"
    "v_mfma_f32_16x16x32_bf16 v[132:135], a[64:67], a[16:19], 0\n"
    "v_mfma_f32_16x16x32_bf16 v[132:135], a[68:71], a[20:23], v[132:135]\n"
    "ds_read_b128 a[80:83], v224 offset:4096\n"
    "v_mfma_f32_16x16x32_bf16 v[132:135], a[72:75], a[24:27], v[132:135]\n"
    "v_mfma_f32_16x16x32_bf16 v[132:135], a[76:79], a[28:31], v[132:135]\n"
    "ds_read_b128 a[84:87], v225 offset:4096\n"
    "v_mfma_f32_16x16x32_bf16 v[136:139], a[64:67], a[32:35], 0\n"
    "v_mfma_f32_16x16x32_bf16 v[136:139], a[68:71], a[36:39], v[136:139]\n"
    "ds_read_b128 a[88:91], v226 offset:4096\n"
    "v_mfma_f32_16x16x32_bf16 v[136:139], a[72:75], a[40:43], v[136:139]\n"
    "v_mfma_f32_16x16x32_bf16 v[136:139], a[76:79], a[44:47], v[136:139]\n"
    "v_mfma_f32_16x16x32_bf16 v[140:143], a[64:67], a[48:51], 0\n"
    "v_mfma_f32_16x16x32_bf16 v[140:143], a[68:71], a[52:55], v[140:143]\n"
    "ds_read_b128 a[92:95], v227 offset:4096\n"
    "v_mfma_f32_16x16x32_bf16 v[140:143], a[72:75], a[56:59], v[140:143]\n"
    "v_mfma_f32_16x16x32_bf16 v[140:143], a[76:79], a[60:63], v[140:143]\n"
    "s_waitcnt lgkmcnt(0)\n"
    "v_max3_f32 v160, v128, v129, v130\n"
    "v_max_f32 v160, v160, v131\n"
    "v_max3_f32 v164, v132, v133, v134\n"
    "v_max_f32 v164, v164, v135\n"
    "v_max3_f32 v168, v136, v137, v138\n"
    "v_max_f32 v168, v168, v139\n"
    "s_nop 0\n"
    "v_max3_f32 v172, v140, v141, v142\n"
    "v_max_f32 v172, v172, v143\n"
    "v_mov_b32 v161, v160\n"
    "v_mov_b32 v165, v164\n"
    "v_mov_b32 v169, v168\n"
    "v_mov_b32 v173, v172\n"
    "v_permlane32_swap_b32 v160, v161\n"
    "v_permlane32_swap_b32 v164, v165\n"
    "v_permlane32_swap_b32 v168, v169\n"
    "v_permlane32_swap_b32 v172, v173\n"
    "v_max_f32 v160, v160, v161\n"
    "v_max_f32 v164, v164, v165\n"
    "v_max_f32 v168, v168, v169\n"
    "v_max_f32 v172, v172, v173\n"
    "v_mov_b32 v161, v160\n"
    "v_mov_b32 v165, v164\n"
    "v_mov_b32 v169, v168\n"
    "v_mov_b32 v173, v172\n"
    "v_permlane16_swap_b32 v160, v161\n"
    "v_permlane16_swap_b32 v164, v165\n"
    "v_permlane16_swap_b32 v168, v169\n"
    "v_permlane16_swap_b32 v172, v173\n"
    "v_max_f32 v160, v160, v161\n"
    "v_max_f32 v164, v164, v165\n"
    "v_max_f32 v168, v168, v169\n"
    "v_max_f32 v172, v172, v173\n"
    "v_sub_f32 v128, v128, v160\n"
    "v_sub_f32 v129, v129, v160\n"
    "v_sub_f32 v130, v130, v160\n"
    "v_sub_f32 v131, v131, v160\n"
    "v_sub_f32 v160, 0, v160\n"
    "v_mov_b32 v161, v160\n"
    "v_mov_b32 v162, v160\n"
    "v_mov_b32 v163, v160\n"
    "v_sub_f32 v132, v132, v164\n"
    "v_sub_f32 v133, v133, v164\n"
    "v_sub_f32 v134, v134, v164\n"
    "v_sub_f32 v135, v135, v164\n"
    "v_sub_f32 v164, 0, v164\n"
    "v_mov_b32 v165, v164\n"
    "v_mov_b32 v166, v164\n"
    "v_mov_b32 v167, v164\n"
    "v_sub_f32 v136, v136, v168\n"
    "v_sub_f32 v137, v137, v168\n"
    "v_sub_f32 v138, v138, v168\n"
    "v_sub_f32 v139, v139, v168\n"
    "v_sub_f32 v168, 0, v168\n"
    "v_mov_b32 v169, v168\n"
    "v_mov_b32 v170, v168\n"
    "v_mov_b32 v171, v168\n"
    "v_sub_f32 v140, v140, v172\n"
    "v_sub_f32 v141, v141, v172\n"
    "v_sub_f32 v142, v142, v172\n"
    "v_sub_f32 v143, v143, v172\n"
    "v_sub_f32 v172, 0, v172\n"
    "v_mov_b32 v173, v172\n"
    "v_mov_b32 v174, v172\n"
    "v_mov_b32 v175, v172\n"
    "v_exp_f32 v128, v128\n"
    "v_exp_f32 v129, v129\n"
    "s_nop 0\n"
    "v_cvt_pk_bf16_f32 v64, v128, v129\n"
    "v_exp_f32 v130, v130\n"
    "v_exp_f32 v131, v131\n"
    "s_nop 0\n"
    "v_cvt_pk_bf16_f32 v65, v130, v131\n"
    "v_exp_f32 v132, v132\n"
    "v_exp_f32 v133, v133\n"
    "s_nop 0\n"
    "v_cvt_pk_bf16_f32 v72, v132, v133\n"
    "v_exp_f32 v134, v134\n"
    "v_exp_f32 v135, v135\n"
    "s_nop 0\n"
    "v_cvt_pk_bf16_f32 v73, v134, v135\n"
    "v_exp_f32 v136, v136\n"
    "v_exp_f32 v137, v137\n"
    "s_nop 0\n"
    "v_cvt_pk_bf16_f32 v80, v136, v137\n"
    "v_exp_f32 v138, v138\n"
    "v_exp_f32 v139, v139\n"
    "s_nop 0\n"
    "v_cvt_pk_bf16_f32 v81, v138, v139\n"
    "v_exp_f32 v140, v140\n"
    "v_mfma_f32_16x16x32_bf16 v[144:147], a[80:83], a[0:3], v[160:163]\n"
    "v_exp_f32 v141, v141\n"
    "v_mfma_f32_16x16x32_bf16 v[144:147], a[84:87], a[4:7], v[144:147]\n"
    "ds_read_b128 a[64:67], v224 offset:8192\n"
    "v_cvt_pk_bf16_f32 v88, v140, v141\n"
    "v_mfma_f32_16x16x32_bf16 v[144:147], a[88:91], a[8:11], v[144:147]\n"
    "v_exp_f32 v142, v142\n"
    "v_mfma_f32_16x16x32_bf16 v[144:147], a[92:95], a[12:15], v[144:147]\n"
    "v_mfma_f32_16x16x32_bf16 v[148:151], a[80:83], a[16:19], v[164:167]\n"
    "v_exp_f32 v143, v143\n"
    "v_mfma_f32_16x16x32_bf16 v[148:151], a[84:87], a[20:23], v[148:151]\n"
    "ds_read_b128 a[68:71], v225 offset:8192\n"
    "v_cvt_pk_bf16_f32 v89, v142, v143\n"
    "v_mfma_f32_16x16x32_bf16 v[148:151], a[88:91], a[24:27], v[148:151]\n"
    "v_exp_f32 v144, v144\n"
    "v_mfma_f32_16x16x32_bf16 v[148:151], a[92:95], a[28:31], v[148:151]\n"
    "v_mfma_f32_16x16x32_bf16 v[152:155], a[80:83], a[32:35], v[168:171]\n"
    "v_exp_f32 v145, v145\n"
    "v_mfma_f32_16x16x32_bf16 v[152:155], a[84:87], a[36:39], v[152:155]\n"
    "ds_read_b128 a[72:75], v226 offset:8192\n"
    "v_cvt_pk_bf16_f32 v66, v144, v145\n"
    "v_mfma_f32_16x16x32_bf16 v[152:155], a[88:91], a[40:43], v[152:155]\n"
    "v_exp_f32 v146, v146\n"
    "v_mfma_f32_16x16x32_bf16 v[152:155], a[92:95], a[44:47], v[152:155]\n"
    "v_mfma_f32_16x16x32_bf16 v[156:159], a[80:83], a[48:51], v[172:175]\n"
    "v_exp_f32 v147, v147\n"
    "v_mfma_f32_16x16x32_bf16 v[156:159], a[84:87], a[52:55], v[156:159]\n"
    "ds_read_b128 a[76:79], v227 offset:8192\n"
    "v_cvt_pk_bf16_f32 v67, v146, v147\n"
    "v_mfma_f32_16x16x32_bf16 v[156:159], a[88:91], a[56:59], v[156:159]\n"
    "v_exp_f32 v148, v148\n"
    "v_mfma_f32_16x16x32_bf16 v[156:159], a[92:95], a[60:63], v[156:159]\n"
    "s_waitcnt lgkmcnt(0)\n"
    "v_exp_f32 v149, v149\n"
    "s_nop 0\n"
    "v_cvt_pk_bf16_f32 v74, v148, v149\n"
    "v_exp_f32 v150, v150\n"
    "v_exp_f32 v151, v151\n"
    "s_nop 0\n"
    "v_cvt_pk_bf16_f32 v75, v150, v151\n"
    "v_exp_f32 v152, v152\n"
    "v_exp_f32 v153, v153\n"
    "s_nop 0\n"
    "v_cvt_pk_bf16_f32 v82, v152, v153\n"
    "v_exp_f32 v154, v154\n"
    "v_exp_f32 v155, v155\n"
    "s_nop 0\n"
    "v_cvt_pk_bf16_f32 v83, v154, v155\n"
    "v_exp_f32 v156, v156\n"
    "v_mfma_f32_16x16x32_bf16 v[128:131], a[64:67], a[0:3], v[160:163]\n"
    "v_exp_f32 v157, v157\n"
    "v_mfma_f32_16x16x32_bf16 v[128:131], a[68:71], a[4:7], v[128:131]\n"
    "ds_read_b128 a[80:83], v224 offset:12288\n"
    "v_cvt_pk_bf16_f32 v90, v156, v157\n"
    "v_mfma_f32_16x16x32_bf16 v[128:131], a[72:75], a[8:11], v[128:131]\n"
    "v_exp_f32 v158, v158\n"
    "v_mfma_f32_16x16x32_bf16 v[128:131], a[76:79], a[12:15], v[128:131]\n"
    "v_mfma_f32_16x16x32_bf16 v[132:135], a[64:67], a[16:19], v[164:167]\n"
    "v_exp_f32 v159, v159\n"
    "v_mfma_f32_16x16x32_bf16 v[132:135], a[68:71], a[20:23], v[132:135]\n"
    "ds_read_b128 a[84:87], v225 offset:12288\n"
    "v_cvt_pk_bf16_f32 v91, v158, v159\n"
    "v_mfma_f32_16x16x32_bf16 v[132:135], a[72:75], a[24:27], v[132:135]\n"
    "v_exp_f32 v128, v128\n"
    "v_mfma_f32_16x16x32_bf16 v[132:135], a[76:79], a[28:31], v[132:135]\n"
    "v_mfma_f32_16x16x32_bf16 v[136:139], a[64:67], a[32:35], v[168:171]\n"
    "v_exp_f32 v129, v129\n"
    "v_mfma_f32_16x16x32_bf16 v[136:139], a[68:71], a[36:39], v[136:139]\n"
    "ds_read_b128 a[88:91], v226 offset:12288\n"
    "v_cvt_pk_bf16_f32 v68, v128, v129\n"
    "v_mfma_f32_16x16x32_bf16 v[136:139], a[72:75], a[40:43], v[136:139]\n"
    "v_exp_f32 v130, v130\n"
    "v_mfma_f32_16x16x32_bf16 v[136:139], a[76:79], a[44:47], v[136:139]\n"
    "v_mfma_f32_16x16x32_bf16 v[140:143], a[64:67], a[48:51], v[172:175]\n"
    "v_exp_f32 v131, v131\n"
    "v_mfma_f32_16x16x32_bf16 v[140:143], a[68:71], a[52:55], v[140:143]\n"
    "ds_read_b128 a[92:95], v227 offset:12288\n"
    "v_cvt_pk_bf16_f32 v69, v130, v131\n"
    "v_mfma_f32_16x16x32_bf16 v[140:143], a[72:75], a[56:59], v[140:143]\n"
    "v_exp_f32 v132, v132\n"
    "v_mfma_f32_16x16x32_bf16 v[140:143], a[76:79], a[60:63], v[140:143]\n"
    "s_waitcnt lgkmcnt(0)\n"
    "v_exp_f32 v133, v133\n"
    "s_nop 0\n"
    "v_cvt_pk_bf16_f32 v76, v132, v133\n"
    "v_exp_f32 v134, v134\n"
    "v_exp_f32 v135, v135\n"
    "s_nop 0\n"
    "v_cvt_pk_bf16_f32 v77, v134, v135\n"
    "v_exp_f32 v136, v136\n"
    "v_exp_f32 v137, v137\n"
    "s_nop 0\n"
    "v_cvt_pk_bf16_f32 v84, v136, v137\n"
    "v_exp_f32 v138, v138\n"
    "v_exp_f32 v139, v139\n"
    "s_nop 0\n"
    "v_cvt_pk_bf16_f32 v85, v138, v139\n"
    "v_exp_f32 v140, v140\n"
    "s_waitcnt vmcnt(63)\n"
    "v_mfma_f32_16x16x32_bf16 v[144:147], a[80:83], a[0:3], v[160:163]\n"
    "s_barrier\n"
    "v_exp_f32 v141, v141\n"
    "v_mfma_f32_16x16x32_bf16 v[144:147], a[84:87], a[4:7], v[144:147]\n"
    "ds_read_b128 a[64:67], v224 offset:16384\n"
    "v_cvt_pk_bf16_f32 v92, v140, v141\n"
    "v_mfma_f32_16x16x32_bf16 v[144:147], a[88:91], a[8:11], v[144:147]\n"
    "s_add_u32 m0, s72, 49152\n"
    "v_exp_f32 v142, v142\n"
    "v_mfma_f32_16x16x32_bf16 v[144:147], a[92:95], a[12:15], v[144:147]\n"
    "buffer_load_dwordx4 v230, s[48:51], s70 offen offset:0 lds\n"
    "v_mfma_f32_16x16x32_bf16 v[148:151], a[80:83], a[16:19], v[164:167]\n"
    "v_exp_f32 v143, v143\n"
    "v_mfma_f32_16x16x32_bf16 v[148:151], a[84:87], a[20:23], v[148:151]\n"
    "ds_read_b128 a[68:71], v225 offset:16384\n"
    "v_cvt_pk_bf16_f32 v93, v142, v143\n"
    "v_mfma_f32_16x16x32_bf16 v[148:151], a[88:91], a[24:27], v[148:151]\n"
    "v_exp_f32 v144, v144\n"
    "v_mfma_f32_16x16x32_bf16 v[148:151], a[92:95], a[28:31], v[148:151]\n"
    "buffer_load_dwordx4 v231, s[48:51], s70 offen offset:1024 lds\n"
    "v_mfma_f32_16x16x32_bf16 v[152:155], a[80:83], a[32:35], v[168:171]\n"
    "v_exp_f32 v145, v145\n"
    "v_mfma_f32_16x16x32_bf16 v[152:155], a[84:87], a[36:39], v[152:155]\n"
    "ds_read_b128 a[72:75], v226 offset:16384\n"
    "v_cvt_pk_bf16_f32 v70, v144, v145\n"
    "v_mfma_f32_16x16x32_bf16 v[152:155], a[88:91], a[40:43], v[152:155]\n"
    "v_exp_f32 v146, v146\n"
    "v_mfma_f32_16x16x32_bf16 v[152:155], a[92:95], a[44:47], v[152:155]\n"
    "v_mfma_f32_16x16x32_bf16 v[156:159], a[80:83], a[48:51], v[172:175]\n"
    "v_exp_f32 v147, v147\n"
    "v_mfma_f32_16x16x32_bf16 v[156:159], a[84:87], a[52:55], v[156:159]\n"
    "ds_read_b128 a[76:79], v227 offset:16384\n"
    "v_cvt_pk_bf16_f32 v71, v146, v147\n"
    "v_mfma_f32_16x16x32_bf16 v[156:159], a[88:91], a[56:59], v[156:159]\n"
    "v_exp_f32 v148, v148\n"
    "v_mfma_f32_16x16x32_bf16 v[156:159], a[92:95], a[60:63], v[156:159]\n"
    "ds_read_b64_tr_b16 a[96:97], v228 offset:0\n"
    "v_mfma_f32_16x16x32_bf16 v[176:179], a[192:195], v[64:67], 0\n"
    "ds_read_b64_tr_b16 a[98:99], v228 offset:4096\n"
    "v_mfma_f32_16x16x32_bf16 v[176:179], a[192:195], v[68:71], v[176:179]\n"
    "ds_read_b64_tr_b16 a[100:101], v228 offset:8192\n"
    "v_exp_f32 v149, v149\n"
    "s_nop 0\n"
    "v_cvt_pk_bf16_f32 v78, v148, v149\n"
    "s_nop 3\n"
    "v_cmp_nge_f32 vcc, s74, v176\n"
    "v_exp_f32 v150, v150\n"
    "s_cbranch_vccnz SA_0_0_X0\n"
    "RA_0_0_X0:\n"
    "v_exp_f32 v151, v151\n"
    "ds_read_b64_tr_b16 a[102:103], v228 offset:12288\n"
    "v_cvt_pk_bf16_f32 v79, v150, v151\n"
    "v_exp_f32 v152, v152\n"
    "v_add_f32 v238, v238, v176\n"
    "v_mfma_f32_16x16x32_bf16 v[180:183], a[192:195], v[72:75], 0\n"
    "ds_read_b64_tr_b16 a[104:105], v229 offset:0\n"
    "v_mfma_f32_16x16x32_bf16 v[180:183], a[192:195], v[76:79], v[180:183]\n"
    "ds_read_b64_tr_b16 a[106:107], v229 offset:4096\n"
    "v_exp_f32 v153, v153\n"
    "s_nop 0\n"
    "v_cvt_pk_bf16_f32 v86, v152, v153\n"
    "s_nop 3\n"
    "v_cmp_nge_f32 vcc, s74, v180\n"
    "v_exp_f32 v154, v154\n"
    "s_cbranch_vccnz SA_0_1_X0\n"
    "RA_0_1_X0:\n"
    "v_exp_f32 v155, v155\n"
    "ds_read_b64_tr_b16 a[108:109], v229 offset:8192\n"
    "v_cvt_pk_bf16_f32 v87, v154, v155\n"
    "v_exp_f32 v156, v156\n"
    "ds_read_b64_tr_b16 a[110:111], v229 offset:12288\n"
    "v_add_f32 v239, v239, v180\n"
    "v_mfma_f32_16x16x32_bf16 v[176:179], a[192:195], v[80:83], 0\n"
    "v_mfma_f32_16x16x32_bf16 v[176:179], a[192:195], v[84:87], v[176:179]\n"
    "LA_loop:\n"
    "s_waitcnt lgkmcnt(8)\n"
    "v_mfma_f32_16x16x32_bf16 v[128:131], a[64:67], a[0:3], v[160:163]\n"
    "v_exp_f32 v157, v157\n"
    "v_mfma_f32_16x16x32_bf16 v[128:131], a[68:71], a[4:7], v[128:131]\n"
    "v_cvt_pk_bf16_f32 v94, v156, v157\n"
    "v_cmp_nge_f32 vcc, s74, v176\n"
    "v_mfma_f32_16x16x32_bf16 v[128:131], a[72:75], a[8:11], v[128:131]\n"
    "v_exp_f32 v158, v158\n"
    "v_mfma_f32_16x16x32_bf16 v[128:131], a[76:79], a[12:15], v[128:131]\n"
    "s_cbranch_vccnz SA_0_2_L\n"
    "RA_0_2_L:\n"
    "v_mfma_f32_16x16x32_bf16 v[132:135], a[64:67], a[16:19], v[164:167]\n"
    "v_exp_f32 v159, v159\n"
    "v_mfma_f32_16x16x32_bf16 v[132:135], a[68:71], a[20:23], v[132:135]\n"
    "ds_read_b128 a[80:83], v224 offset:20480\n"
    "v_cvt_pk_bf16_f32 v95, v158, v159\n"
    "v_mfma_f32_16x16x32_bf16 v[132:135], a[72:75], a[24:27], v[132:135]\n"
    "v_exp_f32 v128, v128\n"
    "v_mfma_f32_16x16x32_bf16 v[132:135], a[76:79], a[28:31], v[132:135]\n"
    "ds_read_b128 a[84:87], v225 offset:20480\n"
    "v_add_f32 v240, v240, v176\n"
    "v_mfma_f32_16x16x32_bf16 v[180:183], a[192:195], v[88:91], 0\n"
    "ds_read_b128 a[88:91], v226 offset:20480\n"
    "v_mfma_f32_16x16x32_bf16 v[180:183], a[192:195], v[92:95], v[180:183]\n"
    "buffer_load_dwordx4 v232, s[48:51], s70 offen offset:2048 lds\n"
    "v_mfma_f32_16x16x32_bf16 v[136:139], a[64:67], a[32:35], v[168:171]\n"
    "v_exp_f32 v129, v129\n"
    "v_mfma_f32_16x16x32_bf16 v[136:139], a[68:71], a[36:39], v[136:139]\n"
    "v_cmp_nge_f32 vcc, s74, v180\n"
    "v_cvt_pk_bf16_f32 v96, v128, v129\n"
    "v_mfma_f32_16x16x32_bf16 v[136:139], a[72:75], a[40:43], v[136:139]\n"
    "v_exp_f32 v130, v130\n"
    "v_mfma_f32_16x16x32_bf16 v[136:139], a[76:79], a[44:47], v[136:139]\n"
    "s_cbranch_vccnz SA_0_3_L\n"
    "RA_0_3_L:\n"
    "v_add_f32 v241, v241, v180\n"
    "v_mfma_f32_16x16x32_bf16 v[140:143], a[64:67], a[48:51], v[172:175]\n"
    "v_exp_f32 v131, v131\n"
    "v_mfma_f32_16x16x32_bf16 v[140:143], a[68:71], a[52:55], v[140:143]\n"
    "ds_read_b128 a[92:95], v227 offset:20480\n"
    "v_cvt_pk_bf16_f32 v97, v130, v131\n"
    "v_mfma_f32_16x16x32_bf16 v[140:143], a[72:75], a[56:59], v[140:143]\n"
    "v_exp_f32 v132, v132\n"
    "v_mfma_f32_16x16x32_bf16 v[140:143], a[76:79], a[60:63], v[140:143]\n"
    "s_waitcnt lgkmcnt(4)\n"
    "ds_read_b64_tr_b16 a[112:113], v228 offset:256\n"
    "v_mfma_f32_16x16x32_bf16 a[128:131], a[96:99], v[64:67], a[128:131]\n"
    "v_exp_f32 v133, v133\n"
    "v_mfma_f32_16x16x32_bf16 a[128:131], a[100:103], v[68:71], a[128:131]\n"
    "ds_read_b64_tr_b16 a[114:115], v228 offset:4352\n"
    "v_cvt_pk_bf16_f32 v104, v132, v133\n"
    "v_mfma_f32_16x16x32_bf16 a[144:147], a[104:107], v[64:67], a[144:147]\n"
    "v_exp_f32 v134, v134\n"
    "v_mfma_f32_16x16x32_bf16 a[144:147], a[108:111], v[68:71], a[144:147]\n"
    "ds_read_b64_tr_b16 a[116:117], v228 offset:8448\n"
    "v_mfma_f32_16x16x32_bf16 a[132:135], a[96:99], v[72:75], a[132:135]\n"
    "v_exp_f32 v135, v135\n"
    "v_mfma_f32_16x16x32_bf16 a[132:135], a[100:103], v[76:79], a[132:135]\n"
    "ds_read_b64_tr_b16 a[118:119], v228 offset:12544\n"
    "v_cvt_pk_bf16_f32 v105, v134, v135\n"
    "v_mfma_f32_16x16x32_bf16 a[148:151], a[104:107], v[72:75], a[148:151]\n"
    "v_exp_f32 v136, v136\n"
    "v_mfma_f32_16x16x32_bf16 a[148:151], a[108:111], v[76:79], a[148:151]\n"
    "buffer_load_dwordx4 v233, s[48:51], s70 offen offset:3072 lds\n"
    "v_mfma_f32_16x16x32_bf16 a[136:139], a[96:99], v[80:83], a[136:139]\n"
    "s_add_u32 s70, s70, 16384\n"
    "v_exp_f32 v137, v137\n"
    "v_mfma_f32_16x16x32_bf16 a[136:139], a[100:103], v[84:87], a[136:139]\n"
    "ds_read_b64_tr_b16 a[120:121], v229 offset:256\n"
    "v_cvt_pk_bf16_f32 v112, v136, v137\n"
    "v_mfma_f32_16x16x32_bf16 a[152:155], a[104:107], v[80:83], a[152:155]\n"
    "v_exp_f32 v138, v138\n"
    "v_mfma_f32_16x16x32_bf16 a[152:155], a[108:111], v[84:87], a[152:155]\n"
    "ds_read_b64_tr_b16 a[122:123], v229 offset:4352\n"
    "v_mfma_f32_16x16x32_bf16 a[140:143], a[96:99], v[88:91], a[140:143]\n"
    "v_exp_f32 v139, v139\n"
    "v_mfma_f32_16x16x32_bf16 a[140:143], a[100:103], v[92:95], a[140:143]\n"
    "ds_read_b64_tr_b16 a[124:125], v229 offset:8448\n"
    "v_cvt_pk_bf16_f32 v113, v138, v139\n"
    "v_mfma_f32_16x16x32_bf16 a[156:159], a[104:107], v[88:91], a[156:159]\n"
    "v_exp_f32 v140, v140\n"
    "v_mfma_f32_16x16x32_bf16 a[156:159], a[108:111], v[92:95], a[156:159]\n"
    "s_waitcnt lgkmcnt(7)\n"
    "ds_read_b64_tr_b16 a[126:127], v229 offset:12544\n"
    "v_mfma_f32_16x16x32_bf16 v[144:147], a[80:83], a[0:3], v[160:163]\n"
    "v_exp_f32 v141, v141\n"
    "v_mfma_f32_16x16x32_bf16 v[144:147], a[84:87], a[4:7], v[144:147]\n"
    "ds_read_b128 a[64:67], v224 offset:24576\n"
    "v_cvt_pk_bf16_f32 v120, v140, v141\n"
    "v_mfma_f32_16x16x32_bf16 v[144:147], a[88:91], a[8:11], v[144:147]\n"
    "s_add_u32 m0, s73, 32768\n"
    "v_exp_f32 v142, v142\n"
    "v_mfma_f32_16x16x32_bf16 v[144:147], a[92:95], a[12:15], v[144:147]\n"
    "buffer_load_dwordx4 v234, s[52:55], s71 offen offset:0 lds\n"
    "v_mfma_f32_16x16x32_bf16 v[148:151], a[80:83], a[16:19], v[164:167]\n"
    "v_exp_f32 v143, v143\n"
    "v_mfma_f32_16x16x32_bf16 v[148:151], a[84:87], a[20:23], v[148:151]\n"
    "ds_read_b128 a[68:71], v225 offset:24576\n"
    "v_cvt_pk_bf16_f32 v121, v142, v143\n"
    "v_mfma_f32_16x16x32_bf16 v[148:151], a[88:91], a[24:27], v[148:151]\n"
    "v_exp_f32 v144, v144\n"
    "v_mfma_f32_16x16x32_bf16 v[148:151], a[92:95], a[28:31], v[148:151]\n"
    "buffer_load_dwordx4 v235, s[52:55], s71 offen offset:1024 lds\n"
    "v_mfma_f32_16x16x32_bf16 v[152:155], a[80:83], a[32:35], v[168:171]\n"
    "v_exp_f32 v145, v145\n"
    "v_mfma_f32_16x16x32_bf16 v[152:155], a[84:87], a[36:39], v[152:155]\n"
    "ds_read_b128 a[72:75], v226 offset:24576\n"
    "v_cvt_pk_bf16_f32 v98, v144, v145\n"
    "v_mfma_f32_16x16x32_bf16 v[152:155], a[88:91], a[40:43], v[152:155]\n"
    "v_exp_f32 v146, v146\n"
    "v_mfma_f32_16x16x32_bf16 v[152:155], a[92:95], a[44:47], v[152:155]\n"
    "v_mfma_f32_16x16x32_bf16 v[156:159], a[80:83], a[48:51], v[172:175]\n"
    "v_exp_f32 v147, v147\n"
    "v_mfma_f32_16x16x32_bf16 v[156:159], a[84:87], a[52:55], v[156:159]\n"
    "ds_read_b128 a[76:79], v227 offset:24576\n"
    "v_cvt_pk_bf16_f32 v99, v146, v147\n"
    "v_mfma_f32_16x16x32_bf16 v[156:159], a[88:91], a[56:59], v[156:159]\n"
    "v_exp_f32 v148, v148\n"
    "v_mfma_f32_16x16x32_bf16 v[156:159], a[92:95], a[60:63], v[156:159]\n"
    "s_waitcnt lgkmcnt(4)\n"
    "ds_read_b64_tr_b16 a[96:97], v228 offset:512\n"
    "v_mfma_f32_16x16x32_bf16 a[160:163], a[112:115], v[64:67], a[160:163]\n"
    "v_exp_f32 v149, v149\n"
    "v_mfma_f32_16x16x32_bf16 a[160:163], a[116:119], v[68:71], a[160:163]\n"
    "ds_read_b64_tr_b16 a[98:99], v228 offset:4608\n"
    "v_cvt_pk_bf16_f32 v106, v148, v149\n"
    "v_mfma_f32_16x16x32_bf16 a[176:179], a[120:123], v[64:67], a[176:179]\n"
    "v_exp_f32 v150, v150\n"
    "v_mfma_f32_16x16x32_bf16 a[176:179], a[124:127], v[68:71], a[176:179]\n"
    "ds_read_b64_tr_b16 a[100:101], v228 offset:8704\n"
    "v_mfma_f32_16x16x32_bf16 a[164:167], a[112:115], v[72:75], a[164:167]\n"
    "v_exp_f32 v151, v151\n"
    "v_mfma_f32_16x16x32_bf16 a[164:167], a[116:119], v[76:79], a[164:167]\n"
    "ds_read_b64_tr_b16 a[102:103], v228 offset:12800\n"
    "v_cvt_pk_bf16_f32 v107, v150, v151\n"
    "v_mfma_f32_16x16x32_bf16 a[180:183], a[120:123], v[72:75], a[180:183]\n"
    "v_exp_f32 v152, v152\n"
    "v_mfma_f32_16x16x32_bf16 a[180:183], a[124:127], v[76:79], a[180:183]\n"
    "v_mfma_f32_16x16x32_bf16 a[168:171], a[112:115], v[80:83], a[168:171]\n"
    "v_exp_f32 v153, v153\n"
    "v_mfma_f32_16x16x32_bf16 a[168:171], a[116:119], v[84:87], a[168:171]\n"
    "ds_read_b64_tr_b16 a[104:105], v229 offset:512\n"
    "v_cvt_pk_bf16_f32 v114, v152, v153\n"
    "v_mfma_f32_16x16x32_bf16 a[184:187], a[120:123], v[80:83], a[184:187]\n"
    "v_exp_f32 v154, v154\n"
    "v_mfma_f32_16x16x32_bf16 a[184:187], a[124:127], v[84:87], a[184:187]\n"
    "ds_read_b64_tr_b16 a[106:107], v229 offset:4608\n"
    "v_mfma_f32_16x16x32_bf16 a[172:175], a[112:115], v[88:91], a[172:175]\n"
    "v_exp_f32 v155, v155\n"
    "v_mfma_f32_16x16x32_bf16 a[172:175], a[116:119], v[92:95], a[172:175]\n"
    "ds_read_b64_tr_b16 a[108:109], v229 offset:8704\n"
    "v_cvt_pk_bf16_f32 v115, v154, v155\n"
    "v_mfma_f32_16x16x32_bf16 a[188:191], a[120:123], v[88:91], a[188:191]\n"
    "v_exp_f32 v156, v156\n"
    "v_mfma_f32_16x16x32_bf16 a[188:191], a[124:127], v[92:95], a[188:191]\n"
    "s_waitcnt lgkmcnt(7)\n"
    "ds_read_b64_tr_b16 a[110:111], v229 offset:12800\n"
    "v_mfma_f32_16x16x32_bf16 v[128:131], a[64:67], a[0:3], v[160:163]\n"
    "v_exp_f32 v157, v157\n"
    "v_mfma_f32_16x16x32_bf16 v[128:131], a[68:71], a[4:7], v[128:131]\n"
    "ds_read_b128 a[80:83], v224 offset:28672\n"
    "v_cvt_pk_bf16_f32 v122, v156, v157\n"
    "v_mfma_f32_16x16x32_bf16 v[128:131], a[72:75], a[8:11], v[128:131]\n"
    "v_exp_f32 v158, v158\n"
    "v_mfma_f32_16x16x32_bf16 v[128:131], a[76:79], a[12:15], v[128:131]\n"
    "buffer_load_dwordx4 v234, s[52:55], s71 offen offset:2048 lds\n"
    "v_mfma_f32_16x16x32_bf16 v[132:135], a[64:67], a[16:19], v[164:167]\n"
    "v_exp_f32 v159, v159\n"
    "v_mfma_f32_16x16x32_bf16 v[132:135], a[68:71], a[20:23], v[132:135]\n"
    "ds_read_b128 a[84:87], v225 offset:28672\n"
    "v_cvt_pk_bf16_f32 v123, v158, v159\n"
    "v_mfma_f32_16x16x32_bf16 v[132:135], a[72:75], a[24:27], v[132:135]\n"
    "v_exp_f32 v128, v128\n"
    "v_mfma_f32_16x16x32_bf16 v[132:135], a[76:79], a[28:31], v[132:135]\n"
    "buffer_load_dwordx4 v235, s[52:55], s71 offen offset:3072 lds\n"
    "v_mfma_f32_16x16x32_bf16 v[136:139], a[64:67], a[32:35], v[168:171]\n"
    "s_add_u32 s71, s71, 16384\n"
    "v_exp_f32 v129, v129\n"
    "v_mfma_f32_16x16x32_bf16 v[136:139], a[68:71], a[36:39], v[136:139]\n"
    "ds_read_b128 a[88:91], v226 offset:28672\n"
    "v_cvt_pk_bf16_f32 v100, v128, v129\n"
    "v_mfma_f32_16x16x32_bf16 v[136:139], a[72:75], a[40:43], v[136:139]\n"
    "v_exp_f32 v130, v130\n"
    "v_mfma_f32_16x16x32_bf16 v[136:139], a[76:79], a[44:47], v[136:139]\n"
    "global_load_dword a212, v242, s[82:83]\n"
    "v_mfma_f32_16x16x32_bf16 v[140:143], a[64:67], a[48:51], v[172:175]\n"
    "v_exp_f32 v131, v131\n"
    "v_mfma_f32_16x16x32_bf16 v[140:143], a[68:71], a[52:55], v[140:143]\n"
    "ds_read_b128 a[92:95], v227 offset:28672\n"
    "v_cvt_pk_bf16_f32 v101, v130, v131\n"
    "v_mfma_f32_16x16x32_bf16 v[140:143], a[72:75], a[56:59], v[140:143]\n"
    "v_exp_f32 v132, v132\n"
    "v_mfma_f32_16x16x32_bf16 v[140:143], a[76:79], a[60:63], v[140:143]\n"
    "s_waitcnt lgkmcnt(4)\n"
    "ds_read_b64_tr_b16 a[112:113], v228 offset:768\n"
    "v_mfma_f32_16x16x32_bf16 v[0:3], a[96:99], v[64:67], v[0:3]\n"
    "v_exp_f32 v133, v133\n"
    "v_mfma_f32_16x16x32_bf16 v[0:3], a[100:103], v[68:71], v[0:3]\n"
    "ds_read_b64_tr_b16 a[114:115], v228 offset:4864\n"
    "v_cvt_pk_bf16_f32 v108, v132, v133\n"
    "v_mfma_f32_16x16x32_bf16 v[16:19], a[104:107], v[64:67], v[16:19]\n"
    "v_exp_f32 v134, v134\n"
    "v_mfma_f32_16x16x32_bf16 v[16:19], a[108:111], v[68:71], v[16:19]\n"
    "ds_read_b64_tr_b16 a[116:117], v228 offset:8960\n"
    "v_mfma_f32_16x16x32_bf16 v[4:7], a[96:99], v[72:75], v[4:7]\n"
    "v_exp_f32 v135, v135\n"
    "v_mfma_f32_16x16x32_bf16 v[4:7], a[100:103], v[76:79], v[4:7]\n"
    "ds_read_b64_tr_b16 a[118:119], v228 offset:13056\n"
    "v_cvt_pk_bf16_f32 v109, v134, v135\n"
    "v_mfma_f32_16x16x32_bf16 v[20:23], a[104:107], v[72:75], v[20:23]\n"
    "v_exp_f32 v136, v136\n"
    "v_mfma_f32_16x16x32_bf16 v[20:23], a[108:111], v[76:79], v[20:23]\n"
    "s_add_u32 s82, s82, 9216\n"
    "s_addc_u32 s83, s83, 0\n"
    "v_mfma_f32_16x16x32_bf16 v[8:11], a[96:99], v[80:83], v[8:11]\n"
    "v_exp_f32 v137, v137\n"
    "v_mfma_f32_16x16x32_bf16 v[8:11], a[100:103], v[84:87], v[8:11]\n"
    "ds_read_b64_tr_b16 a[120:121], v229 offset:768\n"
    "v_cvt_pk_bf16_f32 v116, v136, v137\n"
    "v_mfma_f32_16x16x32_bf16 v[24:27], a[104:107], v[80:83], v[24:27]\n"
    "v_exp_f32 v138, v138\n"
    "v_mfma_f32_16x16x32_bf16 v[24:27], a[108:111], v[84:87], v[24:27]\n"
    "ds_read_b64_tr_b16 a[122:123], v229 offset:4864\n"
    "v_mfma_f32_16x16x32_bf16 v[12:15], a[96:99], v[88:91], v[12:15]\n"
    "v_exp_f32 v139, v139\n"
    "v_mfma_f32_16x16x32_bf16 v[12:15], a[100:103], v[92:95], v[12:15]\n"
    "ds_read_b64_tr_b16 a[124:125], v229 offset:8960\n"
    "v_cvt_pk_bf16_f32 v117, v138, v139\n"
    "v_mfma_f32_16x16x32_bf16 v[28:31], a[104:107], v[88:91], v[28:31]\n"
    "v_exp_f32 v140, v140\n"
    "v_mfma_f32_16x16x32_bf16 v[28:31], a[108:111], v[92:95], v[28:31]\n"
    "s_waitcnt lgkmcnt(7)\n"
    "s_waitcnt vmcnt(10)\n"
    "ds_read_b64_tr_b16 a[126:127], v229 offset:13056\n"
    "v_mfma_f32_16x16x32_bf16 v[144:147], a[80:83], a[0:3], v[160:163]\n"
    "s_barrier\n"
    "v_exp_f32 v141, v141\n"
    "v_mfma_f32_16x16x32_bf16 v[144:147], a[84:87], a[4:7], v[144:147]\n"
    "ds_read_b128 a[64:67], v224 offset:32768\n"
    "v_cvt_pk_bf16_f32 v124, v140, v141\n"
    "v_mfma_f32_16x16x32_bf16 v[144:147], a[88:91], a[8:11], v[144:147]\n"
    "s_add_u32 m0, s72, 0\n"
    "v_exp_f32 v142, v142\n"
    "v_mfma_f32_16x16x32_bf16 v[144:147], a[92:95], a[12:15], v[144:147]\n"
    "buffer_load_dwordx4 v230, s[48:51], s70 offen offset:0 lds\n"
    "v_mfma_f32_16x16x32_bf16 v[148:151], a[80:83], a[16:19], v[164:167]\n"
    "s_sub_u32 s64, s64, 1\n"
    "v_exp_f32 v143, v143\n"
    "v_mfma_f32_16x16x32_bf16 v[148:151], a[84:87], a[20:23], v[148:151]\n"
    "ds_read_b128 a[68:71], v225 offset:32768\n"
    "v_cvt_pk_bf16_f32 v125, v142, v143\n"
    "v_mfma_f32_16x16x32_bf16 v[148:151], a[88:91], a[24:27], v[148:151]\n"
    "v_exp_f32 v144, v144\n"
    "v_mfma_f32_16x16x32_bf16 v[148:151], a[92:95], a[28:31], v[148:151]\n"
    "buffer_load_dwordx4 v231, s[48:51], s70 offen offset:1024 lds\n"
    "v_mfma_f32_16x16x32_bf16 v[152:155], a[80:83], a[32:35], v[168:171]\n"
    "v_exp_f32 v145, v145\n"
    "v_mfma_f32_16x16x32_bf16 v[152:155], a[84:87], a[36:39], v[152:155]\n"
    "ds_read_b128 a[72:75], v226 offset:32768\n"
    "v_cvt_pk_bf16_f32 v102, v144, v145\n"
    "v_mfma_f32_16x16x32_bf16 v[152:155], a[88:91], a[40:43], v[152:155]\n"
    "v_exp_f32 v146, v146\n"
    "v_mfma_f32_16x16x32_bf16 v[152:155], a[92:95], a[44:47], v[152:155]\n"
    "v_mfma_f32_16x16x32_bf16 v[156:159], a[80:83], a[48:51], v[172:175]\n"
    "v_exp_f32 v147, v147\n"
    "v_mfma_f32_16x16x32_bf16 v[156:159], a[84:87], a[52:55], v[156:159]\n"
    "ds_read_b128 a[76:79], v227 offset:32768\n"
    "v_cvt_pk_bf16_f32 v103, v146, v147\n"
    "v_mfma_f32_16x16x32_bf16 v[156:159], a[88:91], a[56:59], v[156:159]\n"
    "v_exp_f32 v148, v148\n"
    "v_mfma_f32_16x16x32_bf16 v[156:159], a[92:95], a[60:63], v[156:159]\n"
    "ds_read_b64_tr_b16 a[96:97], v228 offset:16384\n"
    "v_mfma_f32_16x16x32_bf16 v[176:179], a[192:195], v[96:99], 0\n"
    "ds_read_b64_tr_b16 a[98:99], v228 offset:20480\n"
    "v_mfma_f32_16x16x32_bf16 v[176:179], a[192:195], v[100:103], v[176:179]\n"
    "s_waitcnt lgkmcnt(6)\n"
    "ds_read_b64_tr_b16 a[100:101], v228 offset:24576\n"
    "v_mfma_f32_16x16x32_bf16 v[32:35], a[112:115], v[64:67], v[32:35]\n"
    "v_exp_f32 v149, v149\n"
    "v_mfma_f32_16x16x32_bf16 v[32:35], a[116:119], v[68:71], v[32:35]\n"
    "v_cvt_pk_bf16_f32 v110, v148, v149\n"
    "v_cmp_nge_f32 vcc, s74, v176\n"
    "v_mfma_f32_16x16x32_bf16 v[48:51], a[120:123], v[64:67], v[48:51]\n"
    "v_exp_f32 v150, v150\n"
    "v_mfma_f32_16x16x32_bf16 v[48:51], a[124:127], v[68:71], v[48:51]\n"
    "s_cbranch_vccnz SA_1_0_L\n"
    "RA_1_0_L:\n"
    "v_mfma_f32_16x16x32_bf16 v[36:39], a[112:115], v[72:75], v[36:39]\n"
    "v_exp_f32 v151, v151\n"
    "v_mfma_f32_16x16x32_bf16 v[36:39], a[116:119], v[76:79], v[36:39]\n"
    "ds_read_b64_tr_b16 a[102:103], v228 offset:28672\n"
    "v_cvt_pk_bf16_f32 v111, v150, v151\n"
    "v_mfma_f32_16x16x32_bf16 v[52:55], a[120:123], v[72:75], v[52:55]\n"
    "v_exp_f32 v152, v152\n"
    "v_mfma_f32_16x16x32_bf16 v[52:55], a[124:127], v[76:79], v[52:55]\n"
    "v_add_f32 v238, v238, v176\n"
    "v_mfma_f32_16x16x32_bf16 v[180:183], a[192:195], v[104:107], 0\n"
    "ds_read_b64_tr_b16 a[104:105], v229 offset:16384\n"
    "v_mfma_f32_16x16x32_bf16 v[180:183], a[192:195], v[108:111], v[180:183]\n"
    "ds_read_b64_tr_b16 a[106:107], v229 offset:20480\n"
    "v_mfma_f32_16x16x32_bf16 v[40:43], a[112:115], v[80:83], v[40:43]\n"
    "v_exp_f32 v153, v153\n"
    "v_mfma_f32_16x16x32_bf16 v[40:43], a[116:119], v[84:87], v[40:43]\n"
    "v_cvt_pk_bf16_f32 v118, v152, v153\n"
    "v_cmp_nge_f32 vcc, s74, v180\n"
    "v_mfma_f32_16x16x32_bf16 v[56:59], a[120:123], v[80:83], v[56:59]\n"
    "v_exp_f32 v154, v154\n"
    "v_mfma_f32_16x16x32_bf16 v[56:59], a[124:127], v[84:87], v[56:59]\n"
    "s_cbranch_vccnz SA_1_1_L\n"
    "RA_1_1_L:\n"
    "v_mfma_f32_16x16x32_bf16 v[44:47], a[112:115], v[88:91], v[44:47]\n"
    "v_exp_f32 v155, v155\n"
    "v_mfma_f32_16x16x32_bf16 v[44:47], a[116:119], v[92:95], v[44:47]\n"
    "ds_read_b64_tr_b16 a[108:109], v229 offset:24576\n"
    "v_cvt_pk_bf16_f32 v119, v154, v155\n"
    "v_mfma_f32_16x16x32_bf16 v[60:63], a[120:123], v[88:91], v[60:63]\n"
    "v_exp_f32 v156, v156\n"
    "v_mfma_f32_16x16x32_bf16 v[60:63], a[124:127], v[92:95], v[60:63]\n"
    "ds_read_b64_tr_b16 a[110:111], v229 offset:28672\n"
    "v_add_f32 v239, v239, v180\n"
    "v_mfma_f32_16x16x32_bf16 v[176:179], a[192:195], v[112:115], 0\n"
    "v_mfma_f32_16x16x32_bf16 v[176:179], a[192:195], v[116:119], v[176:179]\n"
    "s_waitcnt lgkmcnt(8)\n"
    "v_mfma_f32_16x16x32_bf16 v[128:131], a[64:67], a[0:3], v[160:163]\n"
    "v_exp_f32 v157, v157\n"
    "v_mfma_f32_16x16x32_bf16 v[128:131], a[68:71], a[4:7], v[128:131]\n"
    "v_cvt_pk_bf16_f32 v126, v156, v157\n"
    "v_cmp_nge_f32 vcc, s74, v176\n"
    "v_mfma_f32_16x16x32_bf16 v[128:131], a[72:75], a[8:11], v[128:131]\n"
    "v_exp_f32 v158, v158\n"
    "v_mfma_f32_16x16x32_bf16 v[128:131], a[76:79], a[12:15], v[128:131]\n"
    "s_cbranch_vccnz SA_1_2_L\n"
    "RA_1_2_L:\n"
    "v_mfma_f32_16x16x32_bf16 v[132:135], a[64:67], a[16:19], v[164:167]\n"
    "v_exp_f32 v159, v159\n"
    "v_mfma_f32_16x16x32_bf16 v[132:135], a[68:71], a[20:23], v[132:135]\n"
    "ds_read_b128 a[80:83], v224 offset:36864\n"
    "v_cvt_pk_bf16_f32 v127, v158, v159\n"
    "v_mfma_f32_16x16x32_bf16 v[132:135], a[72:75], a[24:27], v[132:135]\n"
    "v_exp_f32 v128, v128\n"
    "v_mfma_f32_16x16x32_bf16 v[132:135], a[76:79], a[28:31], v[132:135]\n"
    "ds_read_b128 a[84:87], v225 offset:36864\n"
    "v_add_f32 v240, v240, v176\n"
    "v_mfma_f32_16x16x32_bf16 v[180:183], a[192:195], v[120:123], 0\n"
    "ds_read_b128 a[88:91], v226 offset:36864\n"
    "v_mfma_f32_16x16x32_bf16 v[180:183], a[192:195], v[124:127], v[180:183]\n"
    "buffer_load_dwordx4 v232, s[48:51], s70 offen offset:2048 lds\n"
    "v_mfma_f32_16x16x32_bf16 v[136:139], a[64:67], a[32:35], v[168:171]\n"
    "v_exp_f32 v129, v129\n"
    "v_mfma_f32_16x16x32_bf16 v[136:139], a[68:71], a[36:39], v[136:139]\n"
    "v_cmp_nge_f32 vcc, s74, v180\n"
    "v_cvt_pk_bf16_f32 v64, v128, v129\n"
    "v_mfma_f32_16x16x32_bf16 v[136:139], a[72:75], a[40:43], v[136:139]\n"
    "v_exp_f32 v130, v130\n"
    "v_mfma_f32_16x16x32_bf16 v[136:139], a[76:79], a[44:47], v[136:139]\n"
    "s_cbranch_vccnz SA_1_3_L\n"
    "RA_1_3_L:\n"
    "v_add_f32 v241, v241, v180\n"
    "v_mfma_f32_16x16x32_bf16 v[140:143], a[64:67], a[48:51], v[172:175]\n"
    "v_exp_f32 v131, v131\n"
    "v_mfma_f32_16x16x32_bf16 v[140:143], a[68:71], a[52:55], v[140:143]\n"
    "ds_read_b128 a[92:95], v227 offset:36864\n"
    "v_cvt_pk_bf16_f32 v65, v130, v131\n"
    "v_mfma_f32_16x16x32_bf16 v[140:143], a[72:75], a[56:59], v[140:143]\n"
    "v_exp_f32 v132, v132\n"
    "v_mfma_f32_16x16x32_bf16 v[140:143], a[76:79], a[60:63], v[140:143]\n"
    "s_waitcnt lgkmcnt(4)\n"
    "ds_read_b64_tr_b16 a[112:113], v228 offset:16640\n"
    "v_mfma_f32_16x16x32_bf16 a[128:131], a[96:99], v[96:99], a[128:131]\n"
    "v_exp_f32 v133, v133\n"
    "v_mfma_f32_16x16x32_bf16 a[128:131], a[100:103], v[100:103], a[128:131]\n"
    "ds_read_b64_tr_b16 a[114:115], v228 offset:20736\n"
    "v_cvt_pk_bf16_f32 v72, v132, v133\n"
    "v_mfma_f32_16x16x32_bf16 a[144:147], a[104:107], v[96:99], a[144:147]\n"
    "v_exp_f32 v134, v134\n"
    "v_mfma_f32_16x16x32_bf16 a[144:147], a[108:111], v[100:103], a[144:147]\n"
    "ds_read_b64_tr_b16 a[116:117], v228 offset:24832\n"
    "v_mfma_f32_16x16x32_bf16 a[132:135], a[96:99], v[104:107], a[132:135]\n"
    "v_exp_f32 v135, v135\n"
    "v_mfma_f32_16x16x32_bf16 a[132:135], a[100:103], v[108:111], a[132:135]\n"
    "ds_read_b64_tr_b16 a[118:119], v228 offset:28928\n"
    "v_cvt_pk_bf16_f32 v73, v134, v135\n"
    "v_mfma_f32_16x16x32_bf16 a[148:151], a[104:107], v[104:107], a[148:151]\n"
    "v_exp_f32 v136, v136\n"
    "v_mfma_f32_16x16x32_bf16 a[148:151], a[108:111], v[108:111], a[148:151]\n"
    "buffer_load_dwordx4 v233, s[48:51], s70 offen offset:3072 lds\n"
    "v_mfma_f32_16x16x32_bf16 a[136:139], a[96:99], v[112:115], a[136:139]\n"
    "s_add_u32 s70, s70, 16384\n"
    "v_exp_f32 v137, v137\n"
    "v_mfma_f32_16x16x32_bf16 a[136:139], a[100:103], v[116:119], a[136:139]\n"
    "ds_read_b64_tr_b16 a[120:121], v229 offset:16640\n"
    "v_cvt_pk_bf16_f32 v80, v136, v137\n"
    "v_mfma_f32_16x16x32_bf16 a[152:155], a[104:107], v[112:115], a[152:155]\n"
    "v_exp_f32 v138, v138\n"
    "v_mfma_f32_16x16x32_bf16 a[152:155], a[108:111], v[116:119], a[152:155]\n"
    "ds_read_b64_tr_b16 a[122:123], v229 offset:20736\n"
    "v_mfma_f32_16x16x32_bf16 a[140:143], a[96:99], v[120:123], a[140:143]\n"
    "v_exp_f32 v139, v139\n"
    "v_mfma_f32_16x16x32_bf16 a[140:143], a[100:103], v[124:127], a[140:143]\n"
    "ds_read_b64_tr_b16 a[124:125], v229 offset:24832\n"
    "v_cvt_pk_bf16_f32 v81, v138, v139\n"
    "v_mfma_f32_16x16x32_bf16 a[156:159], a[104:107], v[120:123], a[156:159]\n"
    "v_exp_f32 v140, v140\n"
    "v_mfma_f32_16x16x32_bf16 a[156:159], a[108:111], v[124:127], a[156:159]\n"
    "s_waitcnt lgkmcnt(7)\n"
    "ds_read_b64_tr_b16 a[126:127], v229 offset:28928\n"
    "v_mfma_f32_16x16x32_bf16 v[144:147], a[80:83], a[0:3], v[160:163]\n"
    "v_exp_f32 v141, v141\n"
    "v_mfma_f32_16x16x32_bf16 v[144:147], a[84:87], a[4:7], v[144:147]\n"
    "ds_read_b128 a[64:67], v224 offset:40960\n"
    "v_cvt_pk_bf16_f32 v88, v140, v141\n"
    "v_mfma_f32_16x16x32_bf16 v[144:147], a[88:91], a[8:11], v[144:147]\n"
    "s_add_u32 m0, s73, 49152\n"
    "v_exp_f32 v142, v142\n"
    "v_mfma_f32_16x16x32_bf16 v[144:147], a[92:95], a[12:15], v[144:147]\n"
    "buffer_load_dwordx4 v234, s[52:55], s71 offen offset:0 lds\n"
    "v_mfma_f32_16x16x32_bf16 v[148:151], a[80:83], a[16:19], v[164:167]\n"
    "v_exp_f32 v143, v143\n"
    "v_mfma_f32_16x16x32_bf16 v[148:151], a[84:87], a[20:23], v[148:151]\n"
    "ds_read_b128 a[68:71], v225 offset:40960\n"
    "v_cvt_pk_bf16_f32 v89, v142, v143\n"
    "v_mfma_f32_16x16x32_bf16 v[148:151], a[88:91], a[24:27], v[148:151]\n"
    "v_exp_f32 v144, v144\n"
    "v_mfma_f32_16x16x32_bf16 v[148:151], a[92:95], a[28:31], v[148:151]\n"
    "buffer_load_dwordx4 v235, s[52:55], s71 offen offset:1024 lds\n"
    "v_mfma_f32_16x16x32_bf16 v[152:155], a[80:83], a[32:35], v[168:171]\n"
    "v_exp_f32 v145, v145\n"
    "v_mfma_f32_16x16x32_bf16 v[152:155], a[84:87], a[36:39], v[152:155]\n"
    "ds_read_b128 a[72:75], v226 offset:40960\n"
    "v_cvt_pk_bf16_f32 v66, v144, v145\n"
    "v_mfma_f32_16x16x32_bf16 v[152:155], a[88:91], a[40:43], v[152:155]\n"
    "v_exp_f32 v146, v146\n"
    "v_mfma_f32_16x16x32_bf16 v[152:155], a[92:95], a[44:47], v[152:155]\n"
    "v_mfma_f32_16x16x32_bf16 v[156:159], a[80:83], a[48:51], v[172:175]\n"
    "v_exp_f32 v147, v147\n"
    "v_mfma_f32_16x16x32_bf16 v[156:159], a[84:87], a[52:55], v[156:159]\n"
    "ds_read_b128 a[76:79], v227 offset:40960\n"
    "v_cvt_pk_bf16_f32 v67, v146, v147\n"
    "v_mfma_f32_16x16x32_bf16 v[156:159], a[88:91], a[56:59], v[156:159]\n"
    "v_exp_f32 v148, v148\n"
    "v_mfma_f32_16x16x32_bf16 v[156:159], a[92:95], a[60:63], v[156:159]\n"
    "s_waitcnt lgkmcnt(4)\n"
    "ds_read_b64_tr_b16 a[96:97], v228 offset:16896\n"
    "v_mfma_f32_16x16x32_bf16 a[160:163], a[112:115], v[96:99], a[160:163]\n"
    "v_exp_f32 v149, v149\n"
    "v_mfma_f32_16x16x32_bf16 a[160:163], a[116:119], v[100:103], a[160:163]\n"
    "ds_read_b64_tr_b16 a[98:99], v228 offset:20992\n"
    "v_cvt_pk_bf16_f32 v74, v148, v149\n"
    "v_mfma_f32_16x16x32_bf16 a[176:179], a[120:123], v[96:99], a[176:179]\n"
    "v_exp_f32 v150, v150\n"
    "v_mfma_f32_16x16x32_bf16 a[176:179], a[124:127], v[100:103], a[176:179]\n"
    "ds_read_b64_tr_b16 a[100:101], v228 offset:25088\n"
    "v_mfma_f32_16x16x32_bf16 a[164:167], a[112:115], v[104:107], a[164:167]\n"
    "v_exp_f32 v151, v151\n"
    "v_mfma_f32_16x16x32_bf16 a[164:167], a[116:119], v[108:111], a[164:167]\n"
    "ds_read_b64_tr_b16 a[102:103], v228 offset:29184\n"
    "v_cvt_pk_bf16_f32 v75, v150, v151\n"
    "v_mfma_f32_16x16x32_bf16 a[180:183], a[120:123], v[104:107], a[180:183]\n"
    "v_exp_f32 v152, v152\n"
    "v_mfma_f32_16x16x32_bf16 a[180:183], a[124:127], v[108:111], a[180:183]\n"
    "v_mfma_f32_16x16x32_bf16 a[168:171], a[112:115], v[112:115], a[168:171]\n"
    "v_exp_f32 v153, v153\n"
    "v_mfma_f32_16x16x32_bf16 a[168:171], a[116:119], v[116:119], a[168:171]\n"
    "ds_read_b64_tr_b16 a[104:105], v229 offset:16896\n"
    "v_cvt_pk_bf16_f32 v82, v152, v153\n"
    "v_mfma_f32_16x16x32_bf16 a[184:187], a[120:123], v[112:115], a[184:187]\n"
    "v_exp_f32 v154, v154\n"
    "v_mfma_f32_16x16x32_bf16 a[184:187], a[124:127], v[116:119], a[184:187]\n"
    "ds_read_b64_tr_b16 a[106:107], v229 offset:20992\n"
    "v_mfma_f32_16x16x32_bf16 a[172:175], a[112:115], v[120:123], a[172:175]\n"
    "v_exp_f32 v155, v155\n"
    "v_mfma_f32_16x16x32_bf16 a[172:175], a[116:119], v[124:127], a[172:175]\n"
    "ds_read_b64_tr_b16 a[108:109], v229 offset:25088\n"
    "v_cvt_pk_bf16_f32 v83, v154, v155\n"
    "v_mfma_f32_16x16x32_bf16 a[188:191], a[120:123], v[120:123], a[188:191]\n"
    "v_exp_f32 v156, v156\n"
    "v_mfma_f32_16x16x32_bf16 a[188:191], a[124:127], v[124:127], a[188:191]\n"
    "s_waitcnt lgkmcnt(7)\n"
    "ds_read_b64_tr_b16 a[110:111], v229 offset:29184\n"
    "v_mfma_f32_16x16x32_bf16 v[128:131], a[64:67], a[0:3], v[160:163]\n"
    "v_exp_f32 v157, v157\n"
    "v_mfma_f32_16x16x32_bf16 v[128:131], a[68:71], a[4:7], v[128:131]\n"
    "ds_read_b128 a[80:83], v224 offset:45056\n"
    "v_cvt_pk_bf16_f32 v90, v156, v157\n"
    "v_mfma_f32_16x16x32_bf16 v[128:131], a[72:75], a[8:11], v[128:131]\n"
    "v_exp_f32 v158, v158\n"
    "v_mfma_f32_16x16x32_bf16 v[128:131], a[76:79], a[12:15], v[128:131]\n"
    "buffer_load_dwordx4 v234, s[52:55], s71 offen offset:2048 lds\n"
    "v_mfma_f32_16x16x32_bf16 v[132:135], a[64:67], a[16:19], v[164:167]\n"
    "v_exp_f32 v159, v159\n"
    "v_mfma_f32_16x16x32_bf16 v[132:135], a[68:71], a[20:23], v[132:135]\n"
    "ds_read_b128 a[84:87], v225 offset:45056\n"
    "v_cvt_pk_bf16_f32 v91, v158, v159\n"
    "v_mfma_f32_16x16x32_bf16 v[132:135], a[72:75], a[24:27], v[132:135]\n"
    "v_exp_f32 v128, v128\n"
    "v_mfma_f32_16x16x32_bf16 v[132:135], a[76:79], a[28:31], v[132:135]\n"
    "buffer_load_dwordx4 v235, s[52:55], s71 offen offset:3072 lds\n"
    "v_mfma_f32_16x16x32_bf16 v[136:139], a[64:67], a[32:35], v[168:171]\n"
    "s_add_u32 s71, s71, 16384\n"
    "v_exp_f32 v129, v129\n"
    "v_mfma_f32_16x16x32_bf16 v[136:139], a[68:71], a[36:39], v[136:139]\n"
    "ds_read_b128 a[88:91], v226 offset:45056\n"
    "v_cvt_pk_bf16_f32 v68, v128, v129\n"
    "v_mfma_f32_16x16x32_bf16 v[136:139], a[72:75], a[40:43], v[136:139]\n"
    "v_exp_f32 v130, v130\n"
    "v_mfma_f32_16x16x32_bf16 v[136:139], a[76:79], a[44:47], v[136:139]\n"
    "global_load_dword a212, v242, s[84:85]\n"
    "v_mfma_f32_16x16x32_bf16 v[140:143], a[64:67], a[48:51], v[172:175]\n"
    "v_exp_f32 v131, v131\n"
    "v_mfma_f32_16x16x32_bf16 v[140:143], a[68:71], a[52:55], v[140:143]\n"
    "ds_read_b128 a[92:95], v227 offset:45056\n"
    "v_cvt_pk_bf16_f32 v69, v130, v131\n"
    "v_mfma_f32_16x16x32_bf16 v[140:143], a[72:75], a[56:59], v[140:143]\n"
    "v_exp_f32 v132, v132\n"
    "v_mfma_f32_16x16x32_bf16 v[140:143], a[76:79], a[60:63], v[140:143]\n"
    "s_waitcnt lgkmcnt(4)\n"
    "ds_read_b64_tr_b16 a[112:113], v228 offset:17152\n"
    "v_mfma_f32_16x16x32_bf16 v[0:3], a[96:99], v[96:99], v[0:3]\n"
    "v_exp_f32 v133, v133\n"
    "v_mfma_f32_16x16x32_bf16 v[0:3], a[100:103], v[100:103], v[0:3]\n"
    "ds_read_b64_tr_b16 a[114:115], v228 offset:21248\n"
    "v_cvt_pk_bf16_f32 v76, v132, v133\n"
    "v_mfma_f32_16x16x32_bf16 v[16:19], a[104:107], v[96:99], v[16:19]\n"
    "v_exp_f32 v134, v134\n"
    "v_mfma_f32_16x16x32_bf16 v[16:19], a[108:111], v[100:103], v[16:19]\n"
    "ds_read_b64_tr_b16 a[116:117], v228 offset:25344\n"
    "v_mfma_f32_16x16x32_bf16 v[4:7], a[96:99], v[104:107], v[4:7]\n"
    "v_exp_f32 v135, v135\n"
    "v_mfma_f32_16x16x32_bf16 v[4:7], a[100:103], v[108:111], v[4:7]\n"
    "ds_read_b64_tr_b16 a[118:119], v228 offset:29440\n"
    "v_cvt_pk_bf16_f32 v77, v134, v135\n"
    "v_mfma_f32_16x16x32_bf16 v[20:23], a[104:107], v[104:107], v[20:23]\n"
    "v_exp_f32 v136, v136\n"
    "v_mfma_f32_16x16x32_bf16 v[20:23], a[108:111], v[108:111], v[20:23]\n"
    "s_add_u32 s84, s84, 9216\n"
    "s_addc_u32 s85, s85, 0\n"
    "v_mfma_f32_16x16x32_bf16 v[8:11], a[96:99], v[112:115], v[8:11]\n"
    "v_exp_f32 v137, v137\n"
    "v_mfma_f32_16x16x32_bf16 v[8:11], a[100:103], v[116:119], v[8:11]\n"
    "ds_read_b64_tr_b16 a[120:121], v229 offset:17152\n"
    "v_cvt_pk_bf16_f32 v84, v136, v137\n"
    "v_mfma_f32_16x16x32_bf16 v[24:27], a[104:107], v[112:115], v[24:27]\n"
    "v_exp_f32 v138, v138\n"
    "v_mfma_f32_16x16x32_bf16 v[24:27], a[108:111], v[116:119], v[24:27]\n"
    "ds_read_b64_tr_b16 a[122:123], v229 offset:21248\n"
    "v_mfma_f32_16x16x32_bf16 v[12:15], a[96:99], v[120:123], v[12:15]\n"
    "v_exp_f32 v139, v139\n"
    "v_mfma_f32_16x16x32_bf16 v[12:15], a[100:103], v[124:127], v[12:15]\n"
    "ds_read_b64_tr_b16 a[124:125], v229 offset:25344\n"
    "v_cvt_pk_bf16_f32 v85, v138, v139\n"
    "v_mfma_f32_16x16x32_bf16 v[28:31], a[104:107], v[120:123], v[28:31]\n"
    "v_exp_f32 v140, v140\n"
    "v_mfma_f32_16x16x32_bf16 v[28:31], a[108:111], v[124:127], v[28:31]\n"
    "s_waitcnt lgkmcnt(7)\n"
    "s_waitcnt vmcnt(10)\n"
    "ds_read_b64_tr_b16 a[126:127], v229 offset:29440\n"
    "v_mfma_f32_16x16x32_bf16 v[144:147], a[80:83], a[0:3], v[160:163]\n"
    "s_barrier\n"
    "v_exp_f32 v141, v141\n"
    "v_mfma_f32_16x16x32_bf16 v[144:147], a[84:87], a[4:7], v[144:147]\n"
    "ds_read_b128 a[64:67], v224 offset:49152\n"
    "v_cvt_pk_bf16_f32 v92, v140, v141\n"
    "v_mfma_f32_16x16x32_bf16 v[144:147], a[88:91], a[8:11], v[144:147]\n"
    "s_add_u32 m0, s72, 16384\n"
    "v_exp_f32 v142, v142\n"
    "v_mfma_f32_16x16x32_bf16 v[144:147], a[92:95], a[12:15], v[144:147]\n"
    "buffer_load_dwordx4 v230, s[48:51], s70 offen offset:0 lds\n"
    "v_mfma_f32_16x16x32_bf16 v[148:151], a[80:83], a[16:19], v[164:167]\n"
    "s_sub_u32 s64, s64, 1\n"
    "v_exp_f32 v143, v143\n"
    "v_mfma_f32_16x16x32_bf16 v[148:151], a[84:87], a[20:23], v[148:151]\n"
    "ds_read_b128 a[68:71], v225 offset:49152\n"
    "v_cvt_pk_bf16_f32 v93, v142, v143\n"
    "v_mfma_f32_16x16x32_bf16 v[148:151], a[88:91], a[24:27], v[148:151]\n"
    "v_exp_f32 v144, v144\n"
    "v_mfma_f32_16x16x32_bf16 v[148:151], a[92:95], a[28:31], v[148:151]\n"
    "buffer_load_dwordx4 v231, s[48:51], s70 offen offset:1024 lds\n"
    "v_mfma_f32_16x16x32_bf16 v[152:155], a[80:83], a[32:35], v[168:171]\n"
    "v_exp_f32 v145, v145\n"
    "v_mfma_f32_16x16x32_bf16 v[152:155], a[84:87], a[36:39], v[152:155]\n"
    "ds_read_b128 a[72:75], v226 offset:49152\n"
    "v_cvt_pk_bf16_f32 v70, v144, v145\n"
    "v_mfma_f32_16x16x32_bf16 v[152:155], a[88:91], a[40:43], v[152:155]\n"
    "v_exp_f32 v146, v146\n"
    "v_mfma_f32_16x16x32_bf16 v[152:155], a[92:95], a[44:47], v[152:155]\n"
    "v_mfma_f32_16x16x32_bf16 v[156:159], a[80:83], a[48:51], v[172:175]\n"
    "v_exp_f32 v147, v147\n"
    "v_mfma_f32_16x16x32_bf16 v[156:159], a[84:87], a[52:55], v[156:159]\n"
    "ds_read_b128 a[76:79], v227 offset:49152\n"
    "v_cvt_pk_bf16_f32 v71, v146, v147\n"
    "v_mfma_f32_16x16x32_bf16 v[156:159], a[88:91], a[56:59], v[156:159]\n"
    "v_exp_f32 v148, v148\n"
    "v_mfma_f32_16x16x32_bf16 v[156:159], a[92:95], a[60:63], v[156:159]\n"
    "ds_read_b64_tr_b16 a[96:97], v228 offset:32768\n"
    "v_mfma_f32_16x16x32_bf16 v[176:179], a[192:195], v[64:67], 0\n"
    "ds_read_b64_tr_b16 a[98:99], v228 offset:36864\n"
    "v_mfma_f32_16x16x32_bf16 v[176:179], a[192:195], v[68:71], v[176:179]\n"
    "s_waitcnt lgkmcnt(6)\n"
    "ds_read_b64_tr_b16 a[100:101], v228 offset:40960\n"
    "v_mfma_f32_16x16x32_bf16 v[32:35], a[112:115], v[96:99], v[32:35]\n"
    "v_exp_f32 v149, v149\n"
    "v_mfma_f32_16x16x32_bf16 v[32:35], a[116:119], v[100:103], v[32:35]\n"
    "v_cvt_pk_bf16_f32 v78, v148, v149\n"
    "v_cmp_nge_f32 vcc, s74, v176\n"
    "v_mfma_f32_16x16x32_bf16 v[48:51], a[120:123], v[96:99], v[48:51]\n"
    "v_exp_f32 v150, v150\n"
    "v_mfma_f32_16x16x32_bf16 v[48:51], a[124:127], v[100:103], v[48:51]\n"
    "s_cbranch_vccnz SA_2_0_L\n"
    "RA_2_0_L:\n"
    "v_mfma_f32_16x16x32_bf16 v[36:39], a[112:115], v[104:107], v[36:39]\n"
    "v_exp_f32 v151, v151\n"
    "v_mfma_f32_16x16x32_bf16 v[36:39], a[116:119], v[108:111], v[36:39]\n"
    "ds_read_b64_tr_b16 a[102:103], v228 offset:45056\n"
    "v_cvt_pk_bf16_f32 v79, v150, v151\n"
    "v_mfma_f32_16x16x32_bf16 v[52:55], a[120:123], v[104:107], v[52:55]\n"
    "v_exp_f32 v152, v152\n"
    "v_mfma_f32_16x16x32_bf16 v[52:55], a[124:127], v[108:111], v[52:55]\n"
    "v_add_f32 v238, v238, v176\n"
    "v_mfma_f32_16x16x32_bf16 v[180:183], a[192:195], v[72:75], 0\n"
    "ds_read_b64_tr_b16 a[104:105], v229 offset:32768\n"
    "v_mfma_f32_16x16x32_bf16 v[180:183], a[192:195], v[76:79], v[180:183]\n"
    "ds_read_b64_tr_b16 a[106:107], v229 offset:36864\n"
    "v_mfma_f32_16x16x32_bf16 v[40:43], a[112:115], v[112:115], v[40:43]\n"
    "v_exp_f32 v153, v153\n"
    "v_mfma_f32_16x16x32_bf16 v[40:43], a[116:119], v[116:119], v[40:43]\n"
    "v_cvt_pk_bf16_f32 v86, v152, v153\n"
    "v_cmp_nge_f32 vcc, s74, v180\n"
    "v_mfma_f32_16x16x32_bf16 v[56:59], a[120:123], v[112:115], v[56:59]\n"
    "v_exp_f32 v154, v154\n"
    "v_mfma_f32_16x16x32_bf16 v[56:59], a[124:127], v[116:119], v[56:59]\n"
    "s_cbranch_vccnz SA_2_1_L\n"
    "RA_2_1_L:\n"
    "v_mfma_f32_16x16x32_bf16 v[44:47], a[112:115], v[120:123], v[44:47]\n"
    "v_exp_f32 v155, v155\n"
    "v_mfma_f32_16x16x32_bf16 v[44:47], a[116:119], v[124:127], v[44:47]\n"
    "ds_read_b64_tr_b16 a[108:109], v229 offset:40960\n"
    "v_cvt_pk_bf16_f32 v87, v154, v155\n"
    "v_mfma_f32_16x16x32_bf16 v[60:63], a[120:123], v[120:123], v[60:63]\n"
    "v_exp_f32 v156, v156\n"
    "v_mfma_f32_16x16x32_bf16 v[60:63], a[124:127], v[124:127], v[60:63]\n"
    "ds_read_b64_tr_b16 a[110:111], v229 offset:45056\n"
    "v_add_f32 v239, v239, v180\n"
    "v_mfma_f32_16x16x32_bf16 v[176:179], a[192:195], v[80:83], 0\n"
    "v_mfma_f32_16x16x32_bf16 v[176:179], a[192:195], v[84:87], v[176:179]\n"
    "s_cmp_eq_u32 s64, 0\n"
    "s_cbranch_scc1 LA_exit\n"
    "s_waitcnt lgkmcnt(8)\n"
    "v_mfma_f32_16x16x32_bf16 v[128:131], a[64:67], a[0:3], v[160:163]\n"
    "v_exp_f32 v157, v157\n"
    "v_mfma_f32_16x16x32_bf16 v[128:131], a[68:71], a[4:7], v[128:131]\n"
    "v_cvt_pk_bf16_f32 v94, v156, v157\n"
    "v_cmp_nge_f32 vcc, s74, v176\n"
    "v_mfma_f32_16x16x32_bf16 v[128:131], a[72:75], a[8:11], v[128:131]\n"
    "v_exp_f32 v158, v158\n"
    "v_mfma_f32_16x16x32_bf16 v[128:131], a[76:79], a[12:15], v[128:131]\n"
    "s_cbranch_vccnz SA_2_2_L\n"
    "RA_2_2_L:\n"
    "v_mfma_f32_16x16x32_bf16 v[132:135], a[64:67], a[16:19], v[164:167]\n"
    "v_exp_f32 v159, v159\n"
    "v_mfma_f32_16x16x32_bf16 v[132:135], a[68:71], a[20:23], v[132:135]\n"
    "ds_read_b128 a[80:83], v224 offset:53248\n"
    "v_cvt_pk_bf16_f32 v95, v158, v159\n"
    "v_mfma_f32_16x16x32_bf16 v[132:135], a[72:75], a[24:27], v[132:135]\n"
    "v_exp_f32 v128, v128\n"
    "v_mfma_f32_16x16x32_bf16 v[132:135], a[76:79], a[28:31], v[132:135]\n"
    "ds_read_b128 a[84:87], v225 offset:53248\n"
    "v_add_f32 v240, v240, v176\n"
    "v_mfma_f32_16x16x32_bf16 v[180:183], a[192:195], v[88:91], 0\n"
    "ds_read_b128 a[88:91], v226 offset:53248\n"
    "v_mfma_f32_16x16x32_bf16 v[180:183], a[192:195], v[92:95], v[180:183]\n"
    "buffer_load_dwordx4 v232, s[48:51], s70 offen offset:2048 lds\n"
    "v_mfma_f32_16x16x32_bf16 v[136:139], a[64:67], a[32:35], v[168:171]\n"
    "v_exp_f32 v129, v129\n"
    "v_mfma_f32_16x16x32_bf16 v[136:139], a[68:71], a[36:39], v[136:139]\n"
    "v_cmp_nge_f32 vcc, s74, v180\n"
    "v_cvt_pk_bf16_f32 v96, v128, v129\n"
    "v_mfma_f32_16x16x32_bf16 v[136:139], a[72:75], a[40:43], v[136:139]\n"
    "v_exp_f32 v130, v130\n"
    "v_mfma_f32_16x16x32_bf16 v[136:139], a[76:79], a[44:47], v[136:139]\n"
    "s_cbranch_vccnz SA_2_3_L\n"
    "RA_2_3_L:\n"
    "v_add_f32 v241, v241, v180\n"
    "v_mfma_f32_16x16x32_bf16 v[140:143], a[64:67], a[48:51], v[172:175]\n"
    "v_exp_f32 v131, v131\n"
    "v_mfma_f32_16x16x32_bf16 v[140:143], a[68:71], a[52:55], v[140:143]\n"
    "ds_read_b128 a[92:95], v227 offset:53248\n"
    "v_cvt_pk_bf16_f32 v97, v130, v131\n"
    "v_mfma_f32_16x16x32_bf16 v[140:143], a[72:75], a[56:59], v[140:143]\n"
    "v_exp_f32 v132, v132\n"
    "v_mfma_f32_16x16x32_bf16 v[140:143], a[76:79], a[60:63], v[140:143]\n"
    "s_waitcnt lgkmcnt(4)\n"
    "ds_read_b64_tr_b16 a[112:113], v228 offset:33024\n"
    "v_mfma_f32_16x16x32_bf16 a[128:131], a[96:99], v[64:67], a[128:131]\n"
    "v_exp_f32 v133, v133\n"
    "v_mfma_f32_16x16x32_bf16 a[128:131], a[100:103], v[68:71], a[128:131]\n"
    "ds_read_b64_tr_b16 a[114:115], v228 offset:37120\n"
    "v_cvt_pk_bf16_f32 v104, v132, v133\n"
    "v_mfma_f32_16x16x32_bf16 a[144:147], a[104:107], v[64:67], a[144:147]\n"
    "v_exp_f32 v134, v134\n"
    "v_mfma_f32_16x16x32_bf16 a[144:147], a[108:111], v[68:71], a[144:147]\n"
    "ds_read_b64_tr_b16 a[116:117], v228 offset:41216\n"
    "v_mfma_f32_16x16x32_bf16 a[132:135], a[96:99], v[72:75], a[132:135]\n"
    "v_exp_f32 v135, v135\n"
    "v_mfma_f32_16x16x32_bf16 a[132:135], a[100:103], v[76:79], a[132:135]\n"
    "ds_read_b64_tr_b16 a[118:119], v228 offset:45312\n"
    "v_cvt_pk_bf16_f32 v105, v134, v135\n"
    "v_mfma_f32_16x16x32_bf16 a[148:151], a[104:107], v[72:75], a[148:151]\n"
    "v_exp_f32 v136, v136\n"
    "v_mfma_f32_16x16x32_bf16 a[148:151], a[108:111], v[76:79], a[148:151]\n"
    "buffer_load_dwordx4 v233, s[48:51], s70 offen offset:3072 lds\n"
    "v_mfma_f32_16x16x32_bf16 a[136:139], a[96:99], v[80:83], a[136:139]\n"
    "s_add_u32 s70, s70, 16384\n"
    "v_exp_f32 v137, v137\n"
    "v_mfma_f32_16x16x32_bf16 a[136:139], a[100:103], v[84:87], a[136:139]\n"
    "ds_read_b64_tr_b16 a[120:121], v229 offset:33024\n"
    "v_cvt_pk_bf16_f32 v112, v136, v137\n"
    "v_mfma_f32_16x16x32_bf16 a[152:155], a[104:107], v[80:83], a[152:155]\n"
    "v_exp_f32 v138, v138\n"
    "v_mfma_f32_16x16x32_bf16 a[152:155], a[108:111], v[84:87], a[152:155]\n"
    "ds_read_b64_tr_b16 a[122:123], v229 offset:37120\n"
    "v_mfma_f32_16x16x32_bf16 a[140:143], a[96:99], v[88:91], a[140:143]\n"
    "v_exp_f32 v139, v139\n"
    "v_mfma_f32_16x16x32_bf16 a[140:143], a[100:103], v[92:95], a[140:143]\n"
    "ds_read_b64_tr_b16 a[124:125], v229 offset:41216\n"
    "v_cvt_pk_bf16_f32 v113, v138, v139\n"
    "v_mfma_f32_16x16x32_bf16 a[156:159], a[104:107], v[88:91], a[156:159]\n"
    "v_exp_f32 v140, v140\n"
    "v_mfma_f32_16x16x32_bf16 a[156:159], a[108:111], v[92:95], a[156:159]\n"
    "s_waitcnt lgkmcnt(7)\n"
    "ds_read_b64_tr_b16 a[126:127], v229 offset:45312\n"
    "v_mfma_f32_16x16x32_bf16 v[144:147], a[80:83], a[0:3], v[160:163]\n"
    "v_exp_f32 v141, v141\n"
    "v_mfma_f32_16x16x32_bf16 v[144:147], a[84:87], a[4:7], v[144:147]\n"
    "ds_read_b128 a[64:67], v224 offset:57344\n"
    "v_cvt_pk_bf16_f32 v120, v140, v141\n"
    "v_mfma_f32_16x16x32_bf16 v[144:147], a[88:91], a[8:11], v[144:147]\n"
    "s_add_u32 m0, s73, 0\n"
    "v_exp_f32 v142, v142\n"
    "v_mfma_f32_16x16x32_bf16 v[144:147], a[92:95], a[12:15], v[144:147]\n"
    "buffer_load_dwordx4 v234, s[52:55], s71 offen offset:0 lds\n"
    "v_mfma_f32_16x16x32_bf16 v[148:151], a[80:83], a[16:19], v[164:167]\n"
    "v_exp_f32 v143, v143\n"
    "v_mfma_f32_16x16x32_bf16 v[148:151], a[84:87], a[20:23], v[148:151]\n"
    "ds_read_b128 a[68:71], v225 offset:57344\n"
    "v_cvt_pk_bf16_f32 v121, v142, v143\n"
    "v_mfma_f32_16x16x32_bf16 v[148:151], a[88:91], a[24:27], v[148:151]\n"
    "v_exp_f32 v144, v144\n"
    "v_mfma_f32_16x16x32_bf16 v[148:151], a[92:95], a[28:31], v[148:151]\n"
    "buffer_load_dwordx4 v235, s[52:55], s71 offen offset:1024 lds\n"
    "v_mfma_f32_16x16x32_bf16 v[152:155], a[80:83], a[32:35], v[168:171]\n"
    "v_exp_f32 v145, v145\n"
    "v_mfma_f32_16x16x32_bf16 v[152:155], a[84:87], a[36:39], v[152:155]\n"
    "ds_read_b128 a[72:75], v226 offset:57344\n"
    "v_cvt_pk_bf16_f32 v98, v144, v145\n"
    "v_mfma_f32_16x16x32_bf16 v[152:155], a[88:91], a[40:43], v[152:155]\n"
    "v_exp_f32 v146, v146\n"
    "v_mfma_f32_16x16x32_bf16 v[152:155], a[92:95], a[44:47], v[152:155]\n"
    "v_mfma_f32_16x16x32_bf16 v[156:159], a[80:83], a[48:51], v[172:175]\n"
    "v_exp_f32 v147, v147\n"
    "v_mfma_f32_16x16x32_bf16 v[156:159], a[84:87], a[52:55], v[156:159]\n"
    "ds_read_b128 a[76:79], v227 offset:57344\n"
    "v_cvt_pk_bf16_f32 v99, v146, v147\n"
    "v_mfma_f32_16x16x32_bf16 v[156:159], a[88:91], a[56:59], v[156:159]\n"
    "v_exp_f32 v148, v148\n"
    "v_mfma_f32_16x16x32_bf16 v[156:159], a[92:95], a[60:63], v[156:159]\n"
    "s_waitcnt lgkmcnt(4)\n"
    "ds_read_b64_tr_b16 a[96:97], v228 offset:33280\n"
    "v_mfma_f32_16x16x32_bf16 a[160:163], a[112:115], v[64:67], a[160:163]\n"
    "v_exp_f32 v149, v149\n"
    "v_mfma_f32_16x16x32_bf16 a[160:163], a[116:119], v[68:71], a[160:163]\n"
    "ds_read_b64_tr_b16 a[98:99], v228 offset:37376\n"
    "v_cvt_pk_bf16_f32 v106, v148, v149\n"
    "v_mfma_f32_16x16x32_bf16 a[176:179], a[120:123], v[64:67], a[176:179]\n"
    "v_exp_f32 v150, v150\n"
    "v_mfma_f32_16x16x32_bf16 a[176:179], a[124:127], v[68:71], a[176:179]\n"
    "ds_read_b64_tr_b16 a[100:101], v228 offset:41472\n"
    "v_mfma_f32_16x16x32_bf16 a[164:167], a[112:115], v[72:75], a[164:167]\n"
    "v_exp_f32 v151, v151\n"
    "v_mfma_f32_16x16x32_bf16 a[164:167], a[116:119], v[76:79], a[164:167]\n"
    "ds_read_b64_tr_b16 a[102:103], v228 offset:45568\n"
    "v_cvt_pk_bf16_f32 v107, v150, v151\n"
    "v_mfma_f32_16x16x32_bf16 a[180:183], a[120:123], v[72:75], a[180:183]\n"
    "v_exp_f32 v152, v152\n"
    "v_mfma_f32_16x16x32_bf16 a[180:183], a[124:127], v[76:79], a[180:183]\n"
    "v_mfma_f32_16x16x32_bf16 a[168:171], a[112:115], v[80:83], a[168:171]\n"
    "v_exp_f32 v153, v153\n"
    "v_mfma_f32_16x16x32_bf16 a[168:171], a[116:119], v[84:87], a[168:171]\n"
    "ds_read_b64_tr_b16 a[104:105], v229 offset:33280\n"
    "v_cvt_pk_bf16_f32 v114, v152, v153\n"
    "v_mfma_f32_16x16x32_bf16 a[184:187], a[120:123], v[80:83], a[184:187]\n"
    "v_exp_f32 v154, v154\n"
    "v_mfma_f32_16x16x32_bf16 a[184:187], a[124:127], v[84:87], a[184:187]\n"
    "ds_read_b64_tr_b16 a[106:107], v229 offset:37376\n"
    "v_mfma_f32_16x16x32_bf16 a[172:175], a[112:115], v[88:91], a[172:175]\n"
    "v_exp_f32 v155, v155\n"
    "v_mfma_f32_16x16x32_bf16 a[172:175], a[116:119], v[92:95], a[172:175]\n"
    "ds_read_b64_tr_b16 a[108:109], v229 offset:41472\n"
    "v_cvt_pk_bf16_f32 v115, v154, v155\n"
    "v_mfma_f32_16x16x32_bf16 a[188:191], a[120:123], v[88:91], a[188:191]\n"
    "v_exp_f32 v156, v156\n"
    "v_mfma_f32_16x16x32_bf16 a[188:191], a[124:127], v[92:95], a[188:191]\n"
    "s_waitcnt lgkmcnt(7)\n"
    "ds_read_b64_tr_b16 a[110:111], v229 offset:45568\n"
    "v_mfma_f32_16x16x32_bf16 v[128:131], a[64:67], a[0:3], v[160:163]\n"
    "v_exp_f32 v157, v157\n"
    "v_mfma_f32_16x16x32_bf16 v[128:131], a[68:71], a[4:7], v[128:131]\n"
    "ds_read_b128 a[80:83], v224 offset:61440\n"
    "v_cvt_pk_bf16_f32 v122, v156, v157\n"
    "v_mfma_f32_16x16x32_bf16 v[128:131], a[72:75], a[8:11], v[128:131]\n"
    "v_exp_f32 v158, v158\n"
    "v_mfma_f32_16x16x32_bf16 v[128:131], a[76:79], a[12:15], v[128:131]\n"
    "buffer_load_dwordx4 v234, s[52:55], s71 offen offset:2048 lds\n"
    "v_mfma_f32_16x16x32_bf16 v[132:135], a[64:67], a[16:19], v[164:167]\n"
    "v_exp_f32 v159, v159\n"
    "v_mfma_f32_16x16x32_bf16 v[132:135], a[68:71], a[20:23], v[132:135]\n"
    "ds_read_b128 a[84:87], v225 offset:61440\n"
    "v_cvt_pk_bf16_f32 v123, v158, v159\n"
    "v_mfma_f32_16x16x32_bf16 v[132:135], a[72:75], a[24:27], v[132:135]\n"
    "v_exp_f32 v128, v128\n"
    "v_mfma_f32_16x16x32_bf16 v[132:135], a[76:79], a[28:31], v[132:135]\n"
    "buffer_load_dwordx4 v235, s[52:55], s71 offen offset:3072 lds\n"
    "v_mfma_f32_16x16x32_bf16 v[136:139], a[64:67], a[32:35], v[168:171]\n"
    "s_add_u32 s71, s71, 16384\n"
    "v_exp_f32 v129, v129\n"
    "v_mfma_f32_16x16x32_bf16 v[136:139], a[68:71], a[36:39], v[136:139]\n"
    "ds_read_b128 a[88:91], v226 offset:61440\n"
    "v_cvt_pk_bf16_f32 v100, v128, v129\n"
    "v_mfma_f32_16x16x32_bf16 v[136:139], a[72:75], a[40:43], v[136:139]\n"
    "v_exp_f32 v130, v130\n"
    "v_mfma_f32_16x16x32_bf16 v[136:139], a[76:79], a[44:47], v[136:139]\n"
    "global_load_dword a212, v242, s[86:87]\n"
    "v_mfma_f32_16x16x32_bf16 v[140:143], a[64:67], a[48:51], v[172:175]\n"
    "v_exp_f32 v131, v131\n"
    "v_mfma_f32_16x16x32_bf16 v[140:143], a[68:71], a[52:55], v[140:143]\n"
    "ds_read_b128 a[92:95], v227 offset:61440\n"
    "v_cvt_pk_bf16_f32 v101, v130, v131\n"
    "v_mfma_f32_16x16x32_bf16 v[140:143], a[72:75], a[56:59], v[140:143]\n"
    "v_exp_f32 v132, v132\n"
    "v_mfma_f32_16x16x32_bf16 v[140:143], a[76:79], a[60:63], v[140:143]\n"
    "s_waitcnt lgkmcnt(4)\n"
    "ds_read_b64_tr_b16 a[112:113], v228 offset:33536\n"
    "v_mfma_f32_16x16x32_bf16 v[0:3], a[96:99], v[64:67], v[0:3]\n"
    "v_exp_f32 v133, v133\n"
    "v_mfma_f32_16x16x32_bf16 v[0:3], a[100:103], v[68:71], v[0:3]\n"
    "ds_read_b64_tr_b16 a[114:115], v228 offset:37632\n"
    "v_cvt_pk_bf16_f32 v108, v132, v133\n"
    "v_mfma_f32_16x16x32_bf16 v[16:19], a[104:107], v[64:67], v[16:19]\n"
    "v_exp_f32 v134, v134\n"
    "v_mfma_f32_16x16x32_bf16 v[16:19], a[108:111], v[68:71], v[16:19]\n"
    "ds_read_b64_tr_b16 a[116:117], v228 offset:41728\n"
    "v_mfma_f32_16x16x32_bf16 v[4:7], a[96:99], v[72:75], v[4:7]\n"
    "v_exp_f32 v135, v135\n"
    "v_mfma_f32_16x16x32_bf16 v[4:7], a[100:103], v[76:79], v[4:7]\n"
    "ds_read_b64_tr_b16 a[118:119], v228 offset:45824\n"
    "v_cvt_pk_bf16_f32 v109, v134, v135\n"
    "v_mfma_f32_16x16x32_bf16 v[20:23], a[104:107], v[72:75], v[20:23]\n"
    "v_exp_f32 v136, v136\n"
    "v_mfma_f32_16x16x32_bf16 v[20:23], a[108:111], v[76:79], v[20:23]\n"
    "s_add_u32 s86, s86, 1152\n"
    "s_addc_u32 s87, s87, 0\n"
    "v_mfma_f32_16x16x32_bf16 v[8:11], a[96:99], v[80:83], v[8:11]\n"
    "v_exp_f32 v137, v137\n"
    "v_mfma_f32_16x16x32_bf16 v[8:11], a[100:103], v[84:87], v[8:11]\n"
    "ds_read_b64_tr_b16 a[120:121], v229 offset:33536\n"
    "v_cvt_pk_bf16_f32 v116, v136, v137\n"
    "v_mfma_f32_16x16x32_bf16 v[24:27], a[104:107], v[80:83], v[24:27]\n"
    "v_exp_f32 v138, v138\n"
    "v_mfma_f32_16x16x32_bf16 v[24:27], a[108:111], v[84:87], v[24:27]\n"
    "ds_read_b64_tr_b16 a[122:123], v229 offset:37632\n"
    "v_mfma_f32_16x16x32_bf16 v[12:15], a[96:99], v[88:91], v[12:15]\n"
    "v_exp_f32 v139, v139\n"
    "v_mfma_f32_16x16x32_bf16 v[12:15], a[100:103], v[92:95], v[12:15]\n"
    "ds_read_b64_tr_b16 a[124:125], v229 offset:41728\n"
    "v_cvt_pk_bf16_f32 v117, v138, v139\n"
    "v_mfma_f32_16x16x32_bf16 v[28:31], a[104:107], v[88:91], v[28:31]\n"
    "v_exp_f32 v140, v140\n"
    "v_mfma_f32_16x16x32_bf16 v[28:31], a[108:111], v[92:95], v[28:31]\n"
    "s_waitcnt lgkmcnt(7)\n"
    "s_waitcnt vmcnt(10)\n"
    "ds_read_b64_tr_b16 a[126:127], v229 offset:45824\n"
    "v_mfma_f32_16x16x32_bf16 v[144:147], a[80:83], a[0:3], v[160:163]\n"
    "s_barrier\n"
    "v_exp_f32 v141, v141\n"
    "v_mfma_f32_16x16x32_bf16 v[144:147], a[84:87], a[4:7], v[144:147]\n"
    "ds_read_b128 a[64:67], v224 offset:0\n"
    "v_cvt_pk_bf16_f32 v124, v140, v141\n"
    "v_mfma_f32_16x16x32_bf16 v[144:147], a[88:91], a[8:11], v[144:147]\n"
    "s_add_u32 m0, s72, 32768\n"
    "v_exp_f32 v142, v142\n"
    "v_mfma_f32_16x16x32_bf16 v[144:147], a[92:95], a[12:15], v[144:147]\n"
    "buffer_load_dwordx4 v230, s[48:51], s70 offen offset:0 lds\n"
    "v_mfma_f32_16x16x32_bf16 v[148:151], a[80:83], a[16:19], v[164:167]\n"
    "s_sub_u32 s64, s64, 1\n"
    "v_exp_f32 v143, v143\n"
    "v_mfma_f32_16x16x32_bf16 v[148:151], a[84:87], a[20:23], v[148:151]\n"
    "ds_read_b128 a[68:71], v225 offset:0\n"
    "v_cvt_pk_bf16_f32 v125, v142, v143\n"
    "v_mfma_f32_16x16x32_bf16 v[148:151], a[88:91], a[24:27], v[148:151]\n"
    "v_exp_f32 v144, v144\n"
    "v_mfma_f32_16x16x32_bf16 v[148:151], a[92:95], a[28:31], v[148:151]\n"
    "buffer_load_dwordx4 v231, s[48:51], s70 offen offset:1024 lds\n"
    "v_mfma_f32_16x16x32_bf16 v[152:155], a[80:83], a[32:35], v[168:171]\n"
    "v_exp_f32 v145, v145\n"
    "v_mfma_f32_16x16x32_bf16 v[152:155], a[84:87], a[36:39], v[152:155]\n"
    "ds_read_b128 a[72:75], v226 offset:0\n"
    "v_cvt_pk_bf16_f32 v102, v144, v145\n"
    "v_mfma_f32_16x16x32_bf16 v[152:155], a[88:91], a[40:43], v[152:155]\n"
    "v_exp_f32 v146, v146\n"
    "v_mfma_f32_16x16x32_bf16 v[152:155], a[92:95], a[44:47], v[152:155]\n"
    "v_mfma_f32_16x16x32_bf16 v[156:159], a[80:83], a[48:51], v[172:175]\n"
    "v_exp_f32 v147, v147\n"
    "v_mfma_f32_16x16x32_bf16 v[156:159], a[84:87], a[52:55], v[156:159]\n"
    "ds_read_b128 a[76:79], v227 offset:0\n"
    "v_cvt_pk_bf16_f32 v103, v146, v147\n"
    "v_mfma_f32_16x16x32_bf16 v[156:159], a[88:91], a[56:59], v[156:159]\n"
    "v_exp_f32 v148, v148\n"
    "v_mfma_f32_16x16x32_bf16 v[156:159], a[92:95], a[60:63], v[156:159]\n"
    "ds_read_b64_tr_b16 a[96:97], v228 offset:49152\n"
    "v_mfma_f32_16x16x32_bf16 v[176:179], a[192:195], v[96:99], 0\n"
    "ds_read_b64_tr_b16 a[98:99], v228 offset:53248\n"
    "v_mfma_f32_16x16x32_bf16 v[176:179], a[192:195], v[100:103], v[176:179]\n"
    "s_waitcnt lgkmcnt(6)\n"
    "ds_read_b64_tr_b16 a[100:101], v228 offset:57344\n"
    "v_mfma_f32_16x16x32_bf16 v[32:35], a[112:115], v[64:67], v[32:35]\n"
    "v_exp_f32 v149, v149\n"
    "v_mfma_f32_16x16x32_bf16 v[32:35], a[116:119], v[68:71], v[32:35]\n"
    "v_cvt_pk_bf16_f32 v110, v148, v149\n"
    "v_cmp_nge_f32 vcc, s74, v176\n"
    "v_mfma_f32_16x16x32_bf16 v[48:51], a[120:123], v[64:67], v[48:51]\n"
    "v_exp_f32 v150, v150\n"
    "v_mfma_f32_16x16x32_bf16 v[48:51], a[124:127], v[68:71], v[48:51]\n"
    "s_cbranch_vccnz SA_3_0_L\n"
    "RA_3_0_L:\n"
    "v_mfma_f32_16x16x32_bf16 v[36:39], a[112:115], v[72:75], v[36:39]\n"
    "v_exp_f32 v151, v151\n"
    "v_mfma_f32_16x16x32_bf16 v[36:39], a[116:119], v[76:79], v[36:39]\n"
    "ds_read_b64_tr_b16 a[102:103], v228 offset:61440\n"
    "v_cvt_pk_bf16_f32 v111, v150, v151\n"
    "v_mfma_f32_16x16x32_bf16 v[52:55], a[120:123], v[72:75], v[52:55]\n"
    "v_exp_f32 v152, v152\n"
    "v_mfma_f32_16x16x32_bf16 v[52:55], a[124:127], v[76:79], v[52:55]\n"
    "v_add_f32 v238, v238, v176\n"
    "v_mfma_f32_16x16x32_bf16 v[180:183], a[192:195], v[104:107], 0\n"
    "ds_read_b64_tr_b16 a[104:105], v229 offset:49152\n"
    "v_mfma_f32_16x16x32_bf16 v[180:183], a[192:195], v[108:111], v[180:183]\n"
    "ds_read_b64_tr_b16 a[106:107], v229 offset:53248\n"
    "v_mfma_f32_16x16x32_bf16 v[40:43], a[112:115], v[80:83], v[40:43]\n"
    "v_exp_f32 v153, v153\n"
    "v_mfma_f32_16x16x32_bf16 v[40:43], a[116:119], v[84:87], v[40:43]\n"
    "v_cvt_pk_bf16_f32 v118, v152, v153\n"
    "v_cmp_nge_f32 vcc, s74, v180\n"
    "v_mfma_f32_16x16x32_bf16 v[56:59], a[120:123], v[80:83], v[56:59]\n"
    "v_exp_f32 v154, v154\n"
    "v_mfma_f32_16x16x32_bf16 v[56:59], a[124:127], v[84:87], v[56:59]\n"
    "s_cbranch_vccnz SA_3_1_L\n"
    "RA_3_1_L:\n"
    "v_mfma_f32_16x16x32_bf16 v[44:47], a[112:115], v[88:91], v[44:47]\n"
    "v_exp_f32 v155, v155\n"
    "v_mfma_f32_16x16x32_bf16 v[44:47], a[116:119], v[92:95], v[44:47]\n"
    "ds_read_b64_tr_b16 a[108:109], v229 offset:57344\n"
    "v_cvt_pk_bf16_f32 v119, v154, v155\n"
    "v_mfma_f32_16x16x32_bf16 v[60:63], a[120:123], v[88:91], v[60:63]\n"
    "v_exp_f32 v156, v156\n"
    "v_mfma_f32_16x16x32_bf16 v[60:63], a[124:127], v[92:95], v[60:63]\n"
    "ds_read_b64_tr_b16 a[110:111], v229 offset:61440\n"
    "v_add_f32 v239, v239, v180\n"
    "v_mfma_f32_16x16x32_bf16 v[176:179], a[192:195], v[112:115], 0\n"
    "v_mfma_f32_16x16x32_bf16 v[176:179], a[192:195], v[116:119], v[176:179]\n"
    "s_waitcnt lgkmcnt(8)\n"
    "v_mfma_f32_16x16x32_bf16 v[128:131], a[64:67], a[0:3], v[160:163]\n"
    "v_exp_f32 v157, v157\n"
    "v_mfma_f32_16x16x32_bf16 v[128:131], a[68:71], a[4:7], v[128:131]\n"
    "v_cvt_pk_bf16_f32 v126, v156, v157\n"
    "v_cmp_nge_f32 vcc, s74, v176\n"
    "v_mfma_f32_16x16x32_bf16 v[128:131], a[72:75], a[8:11], v[128:131]\n"
    "v_exp_f32 v158, v158\n"
    "v_mfma_f32_16x16x32_bf16 v[128:131], a[76:79], a[12:15], v[128:131]\n"
    "s_cbranch_vccnz SA_3_2_L\n"
    "RA_3_2_L:\n"
    "v_mfma_f32_16x16x32_bf16 v[132:135], a[64:67], a[16:19], v[164:167]\n"
    "v_exp_f32 v159, v159\n"
    "v_mfma_f32_16x16x32_bf16 v[132:135], a[68:71], a[20:23], v[132:135]\n"
    "ds_read_b128 a[80:83], v224 offset:4096\n"
    "v_cvt_pk_bf16_f32 v127, v158, v159\n"
    "v_mfma_f32_16x16x32_bf16 v[132:135], a[72:75], a[24:27], v[132:135]\n"
    "v_exp_f32 v128, v128\n"
    "v_mfma_f32_16x16x32_bf16 v[132:135], a[76:79], a[28:31], v[132:135]\n"
    "ds_read_b128 a[84:87], v225 offset:4096\n"
    "v_add_f32 v240, v240, v176\n"
    "v_mfma_f32_16x16x32_bf16 v[180:183], a[192:195], v[120:123], 0\n"
    "ds_read_b128 a[88:91], v226 offset:4096\n"
    "v_mfma_f32_16x16x32_bf16 v[180:183], a[192:195], v[124:127], v[180:183]\n"
    "buffer_load_dwordx4 v232, s[48:51], s70 offen offset:2048 lds\n"
    "v_mfma_f32_16x16x32_bf16 v[136:139], a[64:67], a[32:35], v[168:171]\n"
    "v_exp_f32 v129, v129\n"
    "v_mfma_f32_16x16x32_bf16 v[136:139], a[68:71], a[36:39], v[136:139]\n"
    "v_cmp_nge_f32 vcc, s74, v180\n"
    "v_cvt_pk_bf16_f32 v64, v128, v129\n"
    "v_mfma_f32_16x16x32_bf16 v[136:139], a[72:75], a[40:43], v[136:139]\n"
    "v_exp_f32 v130, v130\n"
    "v_mfma_f32_16x16x32_bf16 v[136:139], a[76:79], a[44:47], v[136:139]\n"
    "s_cbranch_vccnz SA_3_3_L\n"
    "RA_3_3_L:\n"
    "v_add_f32 v241, v241, v180\n"
    "v_mfma_f32_16x16x32_bf16 v[140:143], a[64:67], a[48:51], v[172:175]\n"
    "v_exp_f32 v131, v131\n"
    "v_mfma_f32_16x16x32_bf16 v[140:143], a[68:71], a[52:55], v[140:143]\n"
    "ds_read_b128 a[92:95], v227 offset:4096\n"
    "v_cvt_pk_bf16_f32 v65, v130, v131\n"
    "v_mfma_f32_16x16x32_bf16 v[140:143], a[72:75], a[56:59], v[140:143]\n"
    "v_exp_f32 v132, v132\n"
    "v_mfma_f32_16x16x32_bf16 v[140:143], a[76:79], a[60:63], v[140:143]\n"
    "s_waitcnt lgkmcnt(4)\n"
    "ds_read_b64_tr_b16 a[112:113], v228 offset:49408\n"
    "v_mfma_f32_16x16x32_bf16 a[128:131], a[96:99], v[96:99], a[128:131]\n"
    "v_exp_f32 v133, v133\n"
    "v_mfma_f32_16x16x32_bf16 a[128:131], a[100:103], v[100:103], a[128:131]\n"
    "ds_read_b64_tr_b16 a[114:115], v228 offset:53504\n"
    "v_cvt_pk_bf16_f32 v72, v132, v133\n"
    "v_mfma_f32_16x16x32_bf16 a[144:147], a[104:107], v[96:99], a[144:147]\n"
    "v_exp_f32 v134, v134\n"
    "v_mfma_f32_16x16x32_bf16 a[144:147], a[108:111], v[100:103], a[144:147]\n"
    "ds_read_b64_tr_b16 a[116:117], v228 offset:57600\n"
    "v_mfma_f32_16x16x32_bf16 a[132:135], a[96:99], v[104:107], a[132:135]\n"
    "v_exp_f32 v135, v135\n"
    "v_mfma_f32_16x16x32_bf16 a[132:135], a[100:103], v[108:111], a[132:135]\n"
    "ds_read_b64_tr_b16 a[118:119], v228 offset:61696\n"
    "v_cvt_pk_bf16_f32 v73, v134, v135\n"
    "v_mfma_f32_16x16x32_bf16 a[148:151], a[104:107], v[104:107], a[148:151]\n"
    "v_exp_f32 v136, v136\n"
    "v_mfma_f32_16x16x32_bf16 a[148:151], a[108:111], v[108:111], a[148:151]\n"
    "buffer_load_dwordx4 v233, s[48:51], s70 offen offset:3072 lds\n"
    "v_mfma_f32_16x16x32_bf16 a[136:139], a[96:99], v[112:115], a[136:139]\n"
    "s_add_u32 s70, s70, 16384\n"
    "v_exp_f32 v137, v137\n"
    "v_mfma_f32_16x16x32_bf16 a[136:139], a[100:103], v[116:119], a[136:139]\n"
    "ds_read_b64_tr_b16 a[120:121], v229 offset:49408\n"
    "v_cvt_pk_bf16_f32 v80, v136, v137\n"
    "v_mfma_f32_16x16x32_bf16 a[152:155], a[104:107], v[112:115], a[152:155]\n"
    "v_exp_f32 v138, v138\n"
    "v_mfma_f32_16x16x32_bf16 a[152:155], a[108:111], v[116:119], a[152:155]\n"
    "ds_read_b64_tr_b16 a[122:123], v229 offset:53504\n"
    "v_mfma_f32_16x16x32_bf16 a[140:143], a[96:99], v[120:123], a[140:143]\n"
    "v_exp_f32 v139, v139\n"
    "v_mfma_f32_16x16x32_bf16 a[140:143], a[100:103], v[124:127], a[140:143]\n"
    "ds_read_b64_tr_b16 a[124:125], v229 offset:57600\n"
    "v_cvt_pk_bf16_f32 v81, v138, v139\n"
    "v_mfma_f32_16x16x32_bf16 a[156:159], a[104:107], v[120:123], a[156:159]\n"
    "v_exp_f32 v140, v140\n"
    "v_mfma_f32_16x16x32_bf16 a[156:159], a[108:111], v[124:127], a[156:159]\n"
    "s_waitcnt lgkmcnt(7)\n"
    "ds_read_b64_tr_b16 a[126:127], v229 offset:61696\n"
    "v_mfma_f32_16x16x32_bf16 v[144:147], a[80:83], a[0:3], v[160:163]\n"
    "v_exp_f32 v141, v141\n"
    "v_mfma_f32_16x16x32_bf16 v[144:147], a[84:87], a[4:7], v[144:147]\n"
    "ds_read_b128 a[64:67], v224 offset:8192\n"
    "v_cvt_pk_bf16_f32 v88, v140, v141\n"
    "v_mfma_f32_16x16x32_bf16 v[144:147], a[88:91], a[8:11], v[144:147]\n"
    "s_add_u32 m0, s73, 16384\n"
    "v_exp_f32 v142, v142\n"
    "v_mfma_f32_16x16x32_bf16 v[144:147], a[92:95], a[12:15], v[144:147]\n"
    "buffer_load_dwordx4 v234, s[52:55], s71 offen offset:0 lds\n"
    "v_mfma_f32_16x16x32_bf16 v[148:151], a[80:83], a[16:19], v[164:167]\n"
    "v_exp_f32 v143, v143\n"
    "v_mfma_f32_16x16x32_bf16 v[148:151], a[84:87], a[20:23], v[148:151]\n"
    "ds_read_b128 a[68:71], v225 offset:8192\n"
    "v_cvt_pk_bf16_f32 v89, v142, v143\n"
    "v_mfma_f32_16x16x32_bf16 v[148:151], a[88:91], a[24:27], v[148:151]\n"
    "v_exp_f32 v144, v144\n"
    "v_mfma_f32_16x16x32_bf16 v[148:151], a[92:95], a[28:31], v[148:151]\n"
    "buffer_load_dwordx4 v235, s[52:55], s71 offen offset:1024 lds\n"
    "v_mfma_f32_16x16x32_bf16 v[152:155], a[80:83], a[32:35], v[168:171]\n"
    "v_exp_f32 v145, v145\n"
    "v_mfma_f32_16x16x32_bf16 v[152:155], a[84:87], a[36:39], v[152:155]\n"
    "ds_read_b128 a[72:75], v226 offset:8192\n"
    "v_cvt_pk_bf16_f32 v66, v144, v145\n"
    "v_mfma_f32_16x16x32_bf16 v[152:155], a[88:91], a[40:43], v[152:155]\n"
    "v_exp_f32 v146, v146\n"
    "v_mfma_f32_16x16x32_bf16 v[152:155], a[92:95], a[44:47], v[152:155]\n"
    "global_load_dword a213, v207, s[78:79]\n"
    "v_mfma_f32_16x16x32_bf16 v[156:159], a[80:83], a[48:51], v[172:175]\n"
    "v_exp_f32 v147, v147\n"
    "v_mfma_f32_16x16x32_bf16 v[156:159], a[84:87], a[52:55], v[156:159]\n"
    "ds_read_b128 a[76:79], v227 offset:8192\n"
    "v_cvt_pk_bf16_f32 v67, v146, v147\n"
    "v_mfma_f32_16x16x32_bf16 v[156:159], a[88:91], a[56:59], v[156:159]\n"
    "v_exp_f32 v148, v148\n"
    "v_mfma_f32_16x16x32_bf16 v[156:159], a[92:95], a[60:63], v[156:159]\n"
    "s_waitcnt lgkmcnt(4)\n"
    "ds_read_b64_tr_b16 a[96:97], v228 offset:49664\n"
    "v_mfma_f32_16x16x32_bf16 a[160:163], a[112:115], v[96:99], a[160:163]\n"
    "v_exp_f32 v149, v149\n"
    "v_mfma_f32_16x16x32_bf16 a[160:163], a[116:119], v[100:103], a[160:163]\n"
    "ds_read_b64_tr_b16 a[98:99], v228 offset:53760\n"
    "v_cvt_pk_bf16_f32 v74, v148, v149\n"
    "v_mfma_f32_16x16x32_bf16 a[176:179], a[120:123], v[96:99], a[176:179]\n"
    "v_exp_f32 v150, v150\n"
    "v_mfma_f32_16x16x32_bf16 a[176:179], a[124:127], v[100:103], a[176:179]\n"
    "ds_read_b64_tr_b16 a[100:101], v228 offset:57856\n"
    "v_mfma_f32_16x16x32_bf16 a[164:167], a[112:115], v[104:107], a[164:167]\n"
    "v_exp_f32 v151, v151\n"
    "v_mfma_f32_16x16x32_bf16 a[164:167], a[116:119], v[108:111], a[164:167]\n"
    "ds_read_b64_tr_b16 a[102:103], v228 offset:61952\n"
    "v_cvt_pk_bf16_f32 v75, v150, v151\n"
    "v_mfma_f32_16x16x32_bf16 a[180:183], a[120:123], v[104:107], a[180:183]\n"
    "v_exp_f32 v152, v152\n"
    "v_mfma_f32_16x16x32_bf16 a[180:183], a[124:127], v[108:111], a[180:183]\n"
    "v_mfma_f32_16x16x32_bf16 a[168:171], a[112:115], v[112:115], a[168:171]\n"
    "v_exp_f32 v153, v153\n"
    "v_mfma_f32_16x16x32_bf16 a[168:171], a[116:119], v[116:119], a[168:171]\n"
    "ds_read_b64_tr_b16 a[104:105], v229 offset:49664\n"
    "v_cvt_pk_bf16_f32 v82, v152, v153\n"
    "v_mfma_f32_16x16x32_bf16 a[184:187], a[120:123], v[112:115], a[184:187]\n"
    "v_exp_f32 v154, v154\n"
    "v_mfma_f32_16x16x32_bf16 a[184:187], a[124:127], v[116:119], a[184:187]\n"
    "ds_read_b64_tr_b16 a[106:107], v229 offset:53760\n"
    "v_mfma_f32_16x16x32_bf16 a[172:175], a[112:115], v[120:123], a[172:175]\n"
    "v_exp_f32 v155, v155\n"
    "v_mfma_f32_16x16x32_bf16 a[172:175], a[116:119], v[124:127], a[172:175]\n"
    "ds_read_b64_tr_b16 a[108:109], v229 offset:57856\n"
    "v_cvt_pk_bf16_f32 v83, v154, v155\n"
    "v_mfma_f32_16x16x32_bf16 a[188:191], a[120:123], v[120:123], a[188:191]\n"
    "v_exp_f32 v156, v156\n"
    "v_mfma_f32_16x16x32_bf16 a[188:191], a[124:127], v[124:127], a[188:191]\n"
    "s_waitcnt lgkmcnt(7)\n"
    "ds_read_b64_tr_b16 a[110:111], v229 offset:61952\n"
    "v_mfma_f32_16x16x32_bf16 v[128:131], a[64:67], a[0:3], v[160:163]\n"
    "v_exp_f32 v157, v157\n"
    "v_mfma_f32_16x16x32_bf16 v[128:131], a[68:71], a[4:7], v[128:131]\n"
    "ds_read_b128 a[80:83], v224 offset:12288\n"
    "v_cvt_pk_bf16_f32 v90, v156, v157\n"
    "v_mfma_f32_16x16x32_bf16 v[128:131], a[72:75], a[8:11], v[128:131]\n"
    "v_exp_f32 v158, v158\n"
    "v_mfma_f32_16x16x32_bf16 v[128:131], a[76:79], a[12:15], v[128:131]\n"
    "buffer_load_dwordx4 v234, s[52:55], s71 offen offset:2048 lds\n"
    "v_mfma_f32_16x16x32_bf16 v[132:135], a[64:67], a[16:19], v[164:167]\n"
    "v_exp_f32 v159, v159\n"
    "v_mfma_f32_16x16x32_bf16 v[132:135], a[68:71], a[20:23], v[132:135]\n"
    "ds_read_b128 a[84:87], v225 offset:12288\n"
    "v_cvt_pk_bf16_f32 v91, v158, v159\n"
    "v_mfma_f32_16x16x32_bf16 v[132:135], a[72:75], a[24:27], v[132:135]\n"
    "v_exp_f32 v128, v128\n"
    "v_mfma_f32_16x16x32_bf16 v[132:135], a[76:79], a[28:31], v[132:135]\n"
    "buffer_load_dwordx4 v235, s[52:55], s71 offen offset:3072 lds\n"
    "v_mfma_f32_16x16x32_bf16 v[136:139], a[64:67], a[32:35], v[168:171]\n"
    "s_add_u32 s71, s71, 16384\n"
    "v_exp_f32 v129, v129\n"
    "v_mfma_f32_16x16x32_bf16 v[136:139], a[68:71], a[36:39], v[136:139]\n"
    "ds_read_b128 a[88:91], v226 offset:12288\n"
    "v_cvt_pk_bf16_f32 v68, v128, v129\n"
    "v_mfma_f32_16x16x32_bf16 v[136:139], a[72:75], a[40:43], v[136:139]\n"
    "v_exp_f32 v130, v130\n"
    "v_mfma_f32_16x16x32_bf16 v[136:139], a[76:79], a[44:47], v[136:139]\n"
    "global_load_dword a212, v242, s[80:81]\n"
    "v_mfma_f32_16x16x32_bf16 v[140:143], a[64:67], a[48:51], v[172:175]\n"
    "v_exp_f32 v131, v131\n"
    "v_mfma_f32_16x16x32_bf16 v[140:143], a[68:71], a[52:55], v[140:143]\n"
    "ds_read_b128 a[92:95], v227 offset:12288\n"
    "v_cvt_pk_bf16_f32 v69, v130, v131\n"
    "v_mfma_f32_16x16x32_bf16 v[140:143], a[72:75], a[56:59], v[140:143]\n"
    "v_exp_f32 v132, v132\n"
    "v_mfma_f32_16x16x32_bf16 v[140:143], a[76:79], a[60:63], v[140:143]\n"
    "s_waitcnt lgkmcnt(4)\n"
    "ds_read_b64_tr_b16 a[112:113], v228 offset:49920\n"
    "v_mfma_f32_16x16x32_bf16 v[0:3], a[96:99], v[96:99], v[0:3]\n"
    "v_exp_f32 v133, v133\n"
    "v_mfma_f32_16x16x32_bf16 v[0:3], a[100:103], v[100:103], v[0:3]\n"
    "ds_read_b64_tr_b16 a[114:115], v228 offset:54016\n"
    "v_cvt_pk_bf16_f32 v76, v132, v133\n"
    "v_mfma_f32_16x16x32_bf16 v[16:19], a[104:107], v[96:99], v[16:19]\n"
    "v_exp_f32 v134, v134\n"
    "v_mfma_f32_16x16x32_bf16 v[16:19], a[108:111], v[100:103], v[16:19]\n"
    "ds_read_b64_tr_b16 a[116:117], v228 offset:58112\n"
    "v_mfma_f32_16x16x32_bf16 v[4:7], a[96:99], v[104:107], v[4:7]\n"
    "v_exp_f32 v135, v135\n"
    "v_mfma_f32_16x16x32_bf16 v[4:7], a[100:103], v[108:111], v[4:7]\n"
    "ds_read_b64_tr_b16 a[118:119], v228 offset:62208\n"
    "v_cvt_pk_bf16_f32 v77, v134, v135\n"
    "v_mfma_f32_16x16x32_bf16 v[20:23], a[104:107], v[104:107], v[20:23]\n"
    "v_exp_f32 v136, v136\n"
    "v_mfma_f32_16x16x32_bf16 v[20:23], a[108:111], v[108:111], v[20:23]\n"
    "s_add_u32 s80, s80, 1152\n"
    "s_addc_u32 s81, s81, 0\n"
    "v_mfma_f32_16x16x32_bf16 v[8:11], a[96:99], v[112:115], v[8:11]\n"
    "v_exp_f32 v137, v137\n"
    "v_mfma_f32_16x16x32_bf16 v[8:11], a[100:103], v[116:119], v[8:11]\n"
    "ds_read_b64_tr_b16 a[120:121], v229 offset:49920\n"
    "v_cvt_pk_bf16_f32 v84, v136, v137\n"
    "v_mfma_f32_16x16x32_bf16 v[24:27], a[104:107], v[112:115], v[24:27]\n"
    "v_exp_f32 v138, v138\n"
    "v_mfma_f32_16x16x32_bf16 v[24:27], a[108:111], v[116:119], v[24:27]\n"
    "ds_read_b64_tr_b16 a[122:123], v229 offset:54016\n"
    "v_mfma_f32_16x16x32_bf16 v[12:15], a[96:99], v[120:123], v[12:15]\n"
    "v_exp_f32 v139, v139\n"
    "v_mfma_f32_16x16x32_bf16 v[12:15], a[100:103], v[124:127], v[12:15]\n"
    "ds_read_b64_tr_b16 a[124:125], v229 offset:58112\n"
    "v_cvt_pk_bf16_f32 v85, v138, v139\n"
    "v_mfma_f32_16x16x32_bf16 v[28:31], a[104:107], v[120:123], v[28:31]\n"
    "v_exp_f32 v140, v140\n"
    "v_mfma_f32_16x16x32_bf16 v[28:31], a[108:111], v[124:127], v[28:31]\n"
    "s_waitcnt lgkmcnt(7)\n"
    "s_waitcnt vmcnt(11)\n"
    "ds_read_b64_tr_b16 a[126:127], v229 offset:62208\n"
    "v_mfma_f32_16x16x32_bf16 v[144:147], a[80:83], a[0:3], v[160:163]\n"
    "s_barrier\n"
    "v_exp_f32 v141, v141\n"
    "v_mfma_f32_16x16x32_bf16 v[144:147], a[84:87], a[4:7], v[144:147]\n"
    "ds_read_b128 a[64:67], v224 offset:16384\n"
    "v_cvt_pk_bf16_f32 v92, v140, v141\n"
    "v_mfma_f32_16x16x32_bf16 v[144:147], a[88:91], a[8:11], v[144:147]\n"
    "s_add_u32 m0, s72, 49152\n"
    "v_exp_f32 v142, v142\n"
    "v_mfma_f32_16x16x32_bf16 v[144:147], a[92:95], a[12:15], v[144:147]\n"
    "buffer_load_dwordx4 v230, s[48:51], s70 offen offset:0 lds\n"
    "v_mfma_f32_16x16x32_bf16 v[148:151], a[80:83], a[16:19], v[164:167]\n"
    "s_sub_u32 s64, s64, 1\n"
    "v_exp_f32 v143, v143\n"
    "v_mfma_f32_16x16x32_bf16 v[148:151], a[84:87], a[20:23], v[148:151]\n"
    "ds_read_b128 a[68:71], v225 offset:16384\n"
    "v_cvt_pk_bf16_f32 v93, v142, v143\n"
    "v_mfma_f32_16x16x32_bf16 v[148:151], a[88:91], a[24:27], v[148:151]\n"
    "v_exp_f32 v144, v144\n"
    "v_mfma_f32_16x16x32_bf16 v[148:151], a[92:95], a[28:31], v[148:151]\n"
    "buffer_load_dwordx4 v231, s[48:51], s70 offen offset:1024 lds\n"
    "v_mfma_f32_16x16x32_bf16 v[152:155], a[80:83], a[32:35], v[168:171]\n"
    "v_exp_f32 v145, v145\n"
    "v_mfma_f32_16x16x32_bf16 v[152:155], a[84:87], a[36:39], v[152:155]\n"
    "ds_read_b128 a[72:75], v226 offset:16384\n"
    "v_cvt_pk_bf16_f32 v70, v144, v145\n"
    "v_mfma_f32_16x16x32_bf16 v[152:155], a[88:91], a[40:43], v[152:155]\n"
    "v_exp_f32 v146, v146\n"
    "v_mfma_f32_16x16x32_bf16 v[152:155], a[92:95], a[44:47], v[152:155]\n"
    "v_mfma_f32_16x16x32_bf16 v[156:159], a[80:83], a[48:51], v[172:175]\n"
    "v_exp_f32 v147, v147\n"
    "v_mfma_f32_16x16x32_bf16 v[156:159], a[84:87], a[52:55], v[156:159]\n"
    "ds_read_b128 a[76:79], v227 offset:16384\n"
    "v_cvt_pk_bf16_f32 v71, v146, v147\n"
    "v_mfma_f32_16x16x32_bf16 v[156:159], a[88:91], a[56:59], v[156:159]\n"
    "v_exp_f32 v148, v148\n"
    "v_mfma_f32_16x16x32_bf16 v[156:159], a[92:95], a[60:63], v[156:159]\n"
    "ds_read_b64_tr_b16 a[96:97], v228 offset:0\n"
    "v_mfma_f32_16x16x32_bf16 v[176:179], a[192:195], v[64:67], 0\n"
    "ds_read_b64_tr_b16 a[98:99], v228 offset:4096\n"
    "v_mfma_f32_16x16x32_bf16 v[176:179], a[192:195], v[68:71], v[176:179]\n"
    "s_waitcnt lgkmcnt(6)\n"
    "ds_read_b64_tr_b16 a[100:101], v228 offset:8192\n"
    "v_mfma_f32_16x16x32_bf16 v[32:35], a[112:115], v[96:99], v[32:35]\n"
    "v_exp_f32 v149, v149\n"
    "v_mfma_f32_16x16x32_bf16 v[32:35], a[116:119], v[100:103], v[32:35]\n"
    "v_cvt_pk_bf16_f32 v78, v148, v149\n"
    "v_cmp_nge_f32 vcc, s74, v176\n"
    "v_mfma_f32_16x16x32_bf16 v[48:51], a[120:123], v[96:99], v[48:51]\n"
    "v_exp_f32 v150, v150\n"
    "v_mfma_f32_16x16x32_bf16 v[48:51], a[124:127], v[100:103], v[48:51]\n"
    "s_cbranch_vccnz SA_0_0_L\n"
    "RA_0_0_L:\n"
    "v_mfma_f32_16x16x32_bf16 v[36:39], a[112:115], v[104:107], v[36:39]\n"
    "v_exp_f32 v151, v151\n"
    "v_mfma_f32_16x16x32_bf16 v[36:39], a[116:119], v[108:111], v[36:39]\n"
    "ds_read_b64_tr_b16 a[102:103], v228 offset:12288\n"
    "v_cvt_pk_bf16_f32 v79, v150, v151\n"
    "v_mfma_f32_16x16x32_bf16 v[52:55], a[120:123], v[104:107], v[52:55]\n"
    "v_exp_f32 v152, v152\n"
    "v_mfma_f32_16x16x32_bf16 v[52:55], a[124:127], v[108:111], v[52:55]\n"
    "v_add_f32 v238, v238, v176\n"
    "v_mfma_f32_16x16x32_bf16 v[180:183], a[192:195], v[72:75], 0\n"
    "ds_read_b64_tr_b16 a[104:105], v229 offset:0\n"
    "v_mfma_f32_16x16x32_bf16 v[180:183], a[192:195], v[76:79], v[180:183]\n"
    "ds_read_b64_tr_b16 a[106:107], v229 offset:4096\n"
    "v_mfma_f32_16x16x32_bf16 v[40:43], a[112:115], v[112:115], v[40:43]\n"
    "v_exp_f32 v153, v153\n"
    "v_mfma_f32_16x16x32_bf16 v[40:43], a[116:119], v[116:119], v[40:43]\n"
    "v_cvt_pk_bf16_f32 v86, v152, v153\n"
    "v_cmp_nge_f32 vcc, s74, v180\n"
    "v_mfma_f32_16x16x32_bf16 v[56:59], a[120:123], v[112:115], v[56:59]\n"
    "v_exp_f32 v154, v154\n"
    "v_mfma_f32_16x16x32_bf16 v[56:59], a[124:127], v[116:119], v[56:59]\n"
    "s_cbranch_vccnz SA_0_1_L\n"
    "RA_0_1_L:\n"
    "v_mfma_f32_16x16x32_bf16 v[44:47], a[112:115], v[120:123], v[44:47]\n"
    "v_exp_f32 v155, v155\n"
    "v_mfma_f32_16x16x32_bf16 v[44:47], a[116:119], v[124:127], v[44:47]\n"
    "ds_read_b64_tr_b16 a[108:109], v229 offset:8192\n"
    "v_cvt_pk_bf16_f32 v87, v154, v155\n"
    "v_mfma_f32_16x16x32_bf16 v[60:63], a[120:123], v[120:123], v[60:63]\n"
    "v_exp_f32 v156, v156\n"
    "v_mfma_f32_16x16x32_bf16 v[60:63], a[124:127], v[124:127], v[60:63]\n"
    "ds_read_b64_tr_b16 a[110:111], v229 offset:12288\n"
    "v_add_f32 v239, v239, v180\n"
    "v_mfma_f32_16x16x32_bf16 v[176:179], a[192:195], v[80:83], 0\n"
    "v_mfma_f32_16x16x32_bf16 v[176:179], a[192:195], v[84:87], v[176:179]\n"
    "s_branch LA_loop\n"
    "LA_exit:\n"
    "s_waitcnt lgkmcnt(8)\n"
    "v_mfma_f32_16x16x32_bf16 v[128:131], a[64:67], a[0:3], v[160:163]\n"
    "v_exp_f32 v157, v157\n"
    "v_mfma_f32_16x16x32_bf16 v[128:131], a[68:71], a[4:7], v[128:131]\n"
    "v_cvt_pk_bf16_f32 v94, v156, v157\n"
    "v_cmp_nge_f32 vcc, s74, v176\n"
    "v_mfma_f32_16x16x32_bf16 v[128:131], a[72:75], a[8:11], v[128:131]\n"
    "v_exp_f32 v158, v158\n"
    "v_mfma_f32_16x16x32_bf16 v[128:131], a[76:79], a[12:15], v[128:131]\n"
    "s_cbranch_vccnz SA_2_2_X31\n"
    "RA_2_2_X31:\n"
    "v_mfma_f32_16x16x32_bf16 v[132:135], a[64:67], a[16:19], v[164:167]\n"
    "v_exp_f32 v159, v159\n"
    "v_mfma_f32_16x16x32_bf16 v[132:135], a[68:71], a[20:23], v[132:135]\n"
    "ds_read_b128 a[80:83], v224 offset:53248\n"
    "v_cvt_pk_bf16_f32 v95, v158, v159\n"
    "v_mfma_f32_16x16x32_bf16 v[132:135], a[72:75], a[24:27], v[132:135]\n"
    "v_exp_f32 v128, v128\n"
    "v_mfma_f32_16x16x32_bf16 v[132:135], a[76:79], a[28:31], v[132:135]\n"
    "ds_read_b128 a[84:87], v225 offset:53248\n"
    "v_add_f32 v240, v240, v176\n"
    "v_mfma_f32_16x16x32_bf16 v[180:183], a[192:195], v[88:91], 0\n"
    "ds_read_b128 a[88:91], v226 offset:53248\n"
    "v_mfma_f32_16x16x32_bf16 v[180:183], a[192:195], v[92:95], v[180:183]\n"
    "v_mfma_f32_16x16x32_bf16 v[136:139], a[64:67], a[32:35], v[168:171]\n"
    "v_exp_f32 v129, v129\n"
    "v_mfma_f32_16x16x32_bf16 v[136:139], a[68:71], a[36:39], v[136:139]\n"
    "v_cmp_nge_f32 vcc, s74, v180\n"
    "v_cvt_pk_bf16_f32 v96, v128, v129\n"
    "v_mfma_f32_16x16x32_bf16 v[136:139], a[72:75], a[40:43], v[136:139]\n"
    "v_exp_f32 v130, v130\n"
    "v_mfma_f32_16x16x32_bf16 v[136:139], a[76:79], a[44:47], v[136:139]\n"
    "s_cbranch_vccnz SA_2_3_X31\n"
    "RA_2_3_X31:\n"
    "v_add_f32 v241, v241, v180\n"
    "v_mfma_f32_16x16x32_bf16 v[140:143], a[64:67], a[48:51], v[172:175]\n"
    "v_exp_f32 v131, v131\n"
    "v_mfma_f32_16x16x32_bf16 v[140:143], a[68:71], a[52:55], v[140:143]\n"
    "s_waitcnt vmcnt(0)\n"
    "ds_read_b128 a[92:95], v227 offset:53248\n"
    "v_cvt_pk_bf16_f32 v97, v130, v131\n"
    "v_mfma_f32_16x16x32_bf16 v[140:143], a[72:75], a[56:59], v[140:143]\n"
    "s_barrier\n"
    "v_exp_f32 v132, v132\n"
    "v_mfma_f32_16x16x32_bf16 v[140:143], a[76:79], a[60:63], v[140:143]\n"
    "s_waitcnt lgkmcnt(4)\n"
    "ds_read_b64_tr_b16 a[112:113], v228 offset:33024\n"
    "v_mfma_f32_16x16x32_bf16 a[128:131], a[96:99], v[64:67], a[128:131]\n"
    "s_add_u32 s61, %22, 2\n"
    "v_exp_f32 v133, v133\n"
    "v_mfma_f32_16x16x32_bf16 a[128:131], a[100:103], v[68:71], a[128:131]\n"
    "s_and_b32 s61, s61, 15\n"
    "ds_read_b64_tr_b16 a[114:115], v228 offset:37120\n"
    "v_cvt_pk_bf16_f32 v104, v132, v133\n"
    "v_mfma_f32_16x16x32_bf16 a[144:147], a[104:107], v[64:67], a[144:147]\n"
    "s_mul_i32 s61, s61, 36864\n"
    "v_exp_f32 v134, v134\n"
    "v_mfma_f32_16x16x32_bf16 a[144:147], a[108:111], v[68:71], a[144:147]\n"
    "s_add_u32 s61, s61, s69\n"
    "ds_read_b64_tr_b16 a[116:117], v228 offset:41216\n"
    "v_mfma_f32_16x16x32_bf16 a[132:135], a[96:99], v[72:75], a[132:135]\n"
    "s_add_u32 m0, s63, 0\n"
    "v_exp_f32 v135, v135\n"
    "v_mfma_f32_16x16x32_bf16 a[132:135], a[100:103], v[76:79], a[132:135]\n"
    "ds_read_b64_tr_b16 a[118:119], v228 offset:45312\n"
    "v_cvt_pk_bf16_f32 v105, v134, v135\n"
    "v_mfma_f32_16x16x32_bf16 a[148:151], a[104:107], v[72:75], a[148:151]\n"
    "v_exp_f32 v136, v136\n"
    "v_mfma_f32_16x16x32_bf16 a[148:151], a[108:111], v[76:79], a[148:151]\n"
    "buffer_load_dwordx4 v220, s[44:47], s61 offen offset:0 lds\n"
    "v_mfma_f32_16x16x32_bf16 a[136:139], a[96:99], v[80:83], a[136:139]\n"
    "v_exp_f32 v137, v137\n"
    "v_mfma_f32_16x16x32_bf16 a[136:139], a[100:103], v[84:87], a[136:139]\n"
    "ds_read_b64_tr_b16 a[120:121], v229 offset:33024\n"
    "v_cvt_pk_bf16_f32 v112, v136, v137\n"
    "v_mfma_f32_16x16x32_bf16 a[152:155], a[104:107], v[80:83], a[152:155]\n"
    "v_exp_f32 v138, v138\n"
    "v_mfma_f32_16x16x32_bf16 a[152:155], a[108:111], v[84:87], a[152:155]\n"
    "ds_read_b64_tr_b16 a[122:123], v229 offset:37120\n"
    "v_mfma_f32_16x16x32_bf16 a[140:143], a[96:99], v[88:91], a[140:143]\n"
    "v_exp_f32 v139, v139\n"
    "v_mfma_f32_16x16x32_bf16 a[140:143], a[100:103], v[92:95], a[140:143]\n"
    "ds_read_b64_tr_b16 a[124:125], v229 offset:41216\n"
    "v_cvt_pk_bf16_f32 v113, v138, v139\n"
    "v_mfma_f32_16x16x32_bf16 a[156:159], a[104:107], v[88:91], a[156:159]\n"
    "v_exp_f32 v140, v140\n"
    "v_mfma_f32_16x16x32_bf16 a[156:159], a[108:111], v[92:95], a[156:159]\n"
    "s_waitcnt lgkmcnt(7)\n"
    "ds_read_b64_tr_b16 a[126:127], v229 offset:45312\n"
    "v_mfma_f32_16x16x32_bf16 v[144:147], a[80:83], a[0:3], v[160:163]\n"
    "v_exp_f32 v141, v141\n"
    "v_mfma_f32_16x16x32_bf16 v[144:147], a[84:87], a[4:7], v[144:147]\n"
    "ds_read_b128 a[64:67], v224 offset:57344\n"
    "v_cvt_pk_bf16_f32 v120, v140, v141\n"
    "v_mfma_f32_16x16x32_bf16 v[144:147], a[88:91], a[8:11], v[144:147]\n"
    "v_exp_f32 v142, v142\n"
    "v_mfma_f32_16x16x32_bf16 v[144:147], a[92:95], a[12:15], v[144:147]\n"
    "buffer_load_dwordx4 v221, s[44:47], s61 offen offset:1024 lds\n"
    "v_mfma_f32_16x16x32_bf16 v[148:151], a[80:83], a[16:19], v[164:167]\n"
    "v_exp_f32 v143, v143\n"
    "v_mfma_f32_16x16x32_bf16 v[148:151], a[84:87], a[20:23], v[148:151]\n"
    "ds_read_b128 a[68:71], v225 offset:57344\n"
    "v_cvt_pk_bf16_f32 v121, v142, v143\n"
    "v_mfma_f32_16x16x32_bf16 v[148:151], a[88:91], a[24:27], v[148:151]\n"
    "v_exp_f32 v144, v144\n"
    "v_mfma_f32_16x16x32_bf16 v[148:151], a[92:95], a[28:31], v[148:151]\n"
    "buffer_load_dwordx4 v222, s[44:47], s61 offen offset:2048 lds\n"
    "v_mfma_f32_16x16x32_bf16 v[152:155], a[80:83], a[32:35], v[168:171]\n"
    "v_exp_f32 v145, v145\n"
    "v_mfma_f32_16x16x32_bf16 v[152:155], a[84:87], a[36:39], v[152:155]\n"
    "ds_read_b128 a[72:75], v226 offset:57344\n"
    "v_cvt_pk_bf16_f32 v98, v144, v145\n"
    "v_mfma_f32_16x16x32_bf16 v[152:155], a[88:91], a[40:43], v[152:155]\n"
    "v_exp_f32 v146, v146\n"
    "v_mfma_f32_16x16x32_bf16 v[152:155], a[92:95], a[44:47], v[152:155]\n"
    "buffer_load_dwordx4 v223, s[44:47], s61 offen offset:3072 lds\n"
    "v_mfma_f32_16x16x32_bf16 v[156:159], a[80:83], a[48:51], v[172:175]\n"
    "s_add_u32 m0, s65, 0\n"
    "v_exp_f32 v147, v147\n"
    "v_mfma_f32_16x16x32_bf16 v[156:159], a[84:87], a[52:55], v[156:159]\n"
    "ds_read_b128 a[76:79], v227 offset:57344\n"
    "v_cvt_pk_bf16_f32 v99, v146, v147\n"
    "v_mfma_f32_16x16x32_bf16 v[156:159], a[88:91], a[56:59], v[156:159]\n"
    "v_exp_f32 v148, v148\n"
    "v_mfma_f32_16x16x32_bf16 v[156:159], a[92:95], a[60:63], v[156:159]\n"
    "s_waitcnt lgkmcnt(4)\n"
    "ds_read_b64_tr_b16 a[96:97], v228 offset:33280\n"
    "v_mfma_f32_16x16x32_bf16 a[160:163], a[112:115], v[64:67], a[160:163]\n"
    "v_exp_f32 v149, v149\n"
    "v_mfma_f32_16x16x32_bf16 a[160:163], a[116:119], v[68:71], a[160:163]\n"
    "ds_read_b64_tr_b16 a[98:99], v228 offset:37376\n"
    "v_cvt_pk_bf16_f32 v106, v148, v149\n"
    "v_mfma_f32_16x16x32_bf16 a[176:179], a[120:123], v[64:67], a[176:179]\n"
    "v_exp_f32 v150, v150\n"
    "v_mfma_f32_16x16x32_bf16 a[176:179], a[124:127], v[68:71], a[176:179]\n"
    "ds_read_b64_tr_b16 a[100:101], v228 offset:41472\n"
    "v_mfma_f32_16x16x32_bf16 a[164:167], a[112:115], v[72:75], a[164:167]\n"
    "v_exp_f32 v151, v151\n"
    "v_mfma_f32_16x16x32_bf16 a[164:167], a[116:119], v[76:79], a[164:167]\n"
    "ds_read_b64_tr_b16 a[102:103], v228 offset:45568\n"
    "v_cvt_pk_bf16_f32 v107, v150, v151\n"
    "v_mfma_f32_16x16x32_bf16 a[180:183], a[120:123], v[72:75], a[180:183]\n"
    "v_exp_f32 v152, v152\n"
    "v_mfma_f32_16x16x32_bf16 a[180:183], a[124:127], v[76:79], a[180:183]\n"
    "s_mov_b64 exec, s[66:67]\n"
    "buffer_load_dwordx4 v250, s[44:47], s61 offen lds\n"
    "s_mov_b64 exec, -1\n"
    "v_mfma_f32_16x16x32_bf16 a[168:171], a[112:115], v[80:83], a[168:171]\n"
    "s_add_u32 s61, %22, 4\n"
    "v_exp_f32 v153, v153\n"
    "v_mfma_f32_16x16x32_bf16 a[168:171], a[116:119], v[84:87], a[168:171]\n"
    "s_and_b32 s61, s61, 15\n"
    "ds_read_b64_tr_b16 a[104:105], v229 offset:33280\n"
    "v_cvt_pk_bf16_f32 v114, v152, v153\n"
    "v_mfma_f32_16x16x32_bf16 a[184:187], a[120:123], v[80:83], a[184:187]\n"
    "s_mul_i32 s61, s61, 36864\n"
    "v_exp_f32 v154, v154\n"
    "v_mfma_f32_16x16x32_bf16 a[184:187], a[124:127], v[84:87], a[184:187]\n"
    "s_add_u32 s61, s61, s69\n"
    "ds_read_b64_tr_b16 a[106:107], v229 offset:37376\n"
    "v_mfma_f32_16x16x32_bf16 a[172:175], a[112:115], v[88:91], a[172:175]\n"
    "s_add_u32 m0, s63, 18432\n"
    "v_exp_f32 v155, v155\n"
    "v_mfma_f32_16x16x32_bf16 a[172:175], a[116:119], v[92:95], a[172:175]\n"
    "ds_read_b64_tr_b16 a[108:109], v229 offset:41472\n"
    "v_cvt_pk_bf16_f32 v115, v154, v155\n"
    "v_mfma_f32_16x16x32_bf16 a[188:191], a[120:123], v[88:91], a[188:191]\n"
    "v_exp_f32 v156, v156\n"
    "v_mfma_f32_16x16x32_bf16 a[188:191], a[124:127], v[92:95], a[188:191]\n"
    "s_waitcnt lgkmcnt(7)\n"
    "ds_read_b64_tr_b16 a[110:111], v229 offset:45568\n"
    "v_mfma_f32_16x16x32_bf16 v[128:131], a[64:67], a[0:3], v[160:163]\n"
    "v_exp_f32 v157, v157\n"
    "v_mfma_f32_16x16x32_bf16 v[128:131], a[68:71], a[4:7], v[128:131]\n"
    "ds_read_b128 a[80:83], v224 offset:61440\n"
    "v_cvt_pk_bf16_f32 v122, v156, v157\n"
    "v_mfma_f32_16x16x32_bf16 v[128:131], a[72:75], a[8:11], v[128:131]\n"
    "v_exp_f32 v158, v158\n"
    "v_mfma_f32_16x16x32_bf16 v[128:131], a[76:79], a[12:15], v[128:131]\n"
    "buffer_load_dwordx4 v220, s[44:47], s61 offen offset:0 lds\n"
    "v_mfma_f32_16x16x32_bf16 v[132:135], a[64:67], a[16:19], v[164:167]\n"
    "v_exp_f32 v159, v159\n"
    "v_mfma_f32_16x16x32_bf16 v[132:135], a[68:71], a[20:23], v[132:135]\n"
    "ds_read_b128 a[84:87], v225 offset:61440\n"
    "v_cvt_pk_bf16_f32 v123, v158, v159\n"
    "v_mfma_f32_16x16x32_bf16 v[132:135], a[72:75], a[24:27], v[132:135]\n"
    "v_exp_f32 v128, v128\n"
    "v_mfma_f32_16x16x32_bf16 v[132:135], a[76:79], a[28:31], v[132:135]\n"
    "buffer_load_dwordx4 v221, s[44:47], s61 offen offset:1024 lds\n"
    "v_mfma_f32_16x16x32_bf16 v[136:139], a[64:67], a[32:35], v[168:171]\n"
    "v_exp_f32 v129, v129\n"
    "v_mfma_f32_16x16x32_bf16 v[136:139], a[68:71], a[36:39], v[136:139]\n"
    "ds_read_b128 a[88:91], v226 offset:61440\n"
    "v_cvt_pk_bf16_f32 v100, v128, v129\n"
    "v_mfma_f32_16x16x32_bf16 v[136:139], a[72:75], a[40:43], v[136:139]\n"
    "v_exp_f32 v130, v130\n"
    "v_mfma_f32_16x16x32_bf16 v[136:139], a[76:79], a[44:47], v[136:139]\n"
    "buffer_load_dwordx4 v222, s[44:47], s61 offen offset:2048 lds\n"
    "v_mfma_f32_16x16x32_bf16 v[140:143], a[64:67], a[48:51], v[172:175]\n"
    "v_exp_f32 v131, v131\n"
    "v_mfma_f32_16x16x32_bf16 v[140:143], a[68:71], a[52:55], v[140:143]\n"
    "ds_read_b128 a[92:95], v227 offset:61440\n"
    "v_cvt_pk_bf16_f32 v101, v130, v131\n"
    "v_mfma_f32_16x16x32_bf16 v[140:143], a[72:75], a[56:59], v[140:143]\n"
    "v_exp_f32 v132, v132\n"
    "v_mfma_f32_16x16x32_bf16 v[140:143], a[76:79], a[60:63], v[140:143]\n"
    "s_waitcnt lgkmcnt(4)\n"
    "ds_read_b64_tr_b16 a[112:113], v228 offset:33536\n"
    "v_mfma_f32_16x16x32_bf16 v[0:3], a[96:99], v[64:67], v[0:3]\n"
    "v_exp_f32 v133, v133\n"
    "v_mfma_f32_16x16x32_bf16 v[0:3], a[100:103], v[68:71], v[0:3]\n"
    "ds_read_b64_tr_b16 a[114:115], v228 offset:37632\n"
    "v_cvt_pk_bf16_f32 v108, v132, v133\n"
    "v_mfma_f32_16x16x32_bf16 v[16:19], a[104:107], v[64:67], v[16:19]\n"
    "v_exp_f32 v134, v134\n"
    "v_mfma_f32_16x16x32_bf16 v[16:19], a[108:111], v[68:71], v[16:19]\n"
    "ds_read_b64_tr_b16 a[116:117], v228 offset:41728\n"
    "v_mfma_f32_16x16x32_bf16 v[4:7], a[96:99], v[72:75], v[4:7]\n"
    "v_exp_f32 v135, v135\n"
    "v_mfma_f32_16x16x32_bf16 v[4:7], a[100:103], v[76:79], v[4:7]\n"
    "ds_read_b64_tr_b16 a[118:119], v228 offset:45824\n"
    "v_cvt_pk_bf16_f32 v109, v134, v135\n"
    "v_mfma_f32_16x16x32_bf16 v[20:23], a[104:107], v[72:75], v[20:23]\n"
    "v_exp_f32 v136, v136\n"
    "v_mfma_f32_16x16x32_bf16 v[20:23], a[108:111], v[76:79], v[20:23]\n"
    "buffer_load_dwordx4 v223, s[44:47], s61 offen offset:3072 lds\n"
    "v_mfma_f32_16x16x32_bf16 v[8:11], a[96:99], v[80:83], v[8:11]\n"
    "s_add_u32 m0, s65, 18432\n"
    "v_exp_f32 v137, v137\n"
    "v_mfma_f32_16x16x32_bf16 v[8:11], a[100:103], v[84:87], v[8:11]\n"
    "ds_read_b64_tr_b16 a[120:121], v229 offset:33536\n"
    "v_cvt_pk_bf16_f32 v116, v136, v137\n"
    "v_mfma_f32_16x16x32_bf16 v[24:27], a[104:107], v[80:83], v[24:27]\n"
    "v_exp_f32 v138, v138\n"
    "v_mfma_f32_16x16x32_bf16 v[24:27], a[108:111], v[84:87], v[24:27]\n"
    "ds_read_b64_tr_b16 a[122:123], v229 offset:37632\n"
    "v_mfma_f32_16x16x32_bf16 v[12:15], a[96:99], v[88:91], v[12:15]\n"
    "v_exp_f32 v139, v139\n"
    "v_mfma_f32_16x16x32_bf16 v[12:15], a[100:103], v[92:95], v[12:15]\n"
    "ds_read_b64_tr_b16 a[124:125], v229 offset:41728\n"
    "v_cvt_pk_bf16_f32 v117, v138, v139\n"
    "v_mfma_f32_16x16x32_bf16 v[28:31], a[104:107], v[88:91], v[28:31]\n"
    "v_exp_f32 v140, v140\n"
    "v_mfma_f32_16x16x32_bf16 v[28:31], a[108:111], v[92:95], v[28:31]\n"
    "s_waitcnt lgkmcnt(7)\n"
    "s_waitcnt vmcnt(12)\n"
    "ds_read_b64_tr_b16 a[126:127], v229 offset:45824\n"
    "v_mfma_f32_16x16x32_bf16 v[144:147], a[80:83], a[0:3], v[160:163]\n"
    "s_barrier\n"
    "v_exp_f32 v141, v141\n"
    "v_mfma_f32_16x16x32_bf16 v[144:147], a[84:87], a[4:7], v[144:147]\n"
    "s_mov_b64 exec, s[66:67]\n"
    "buffer_load_dwordx4 v250, s[44:47], s61 offen lds\n"
    "s_mov_b64 exec, -1\n"
    "v_cvt_pk_bf16_f32 v124, v140, v141\n"
    "v_mfma_f32_16x16x32_bf16 v[144:147], a[88:91], a[8:11], v[144:147]\n"
    "s_add_u32 s61, %22, 8\n"
    "v_exp_f32 v142, v142\n"
    "v_mfma_f32_16x16x32_bf16 v[144:147], a[92:95], a[12:15], v[144:147]\n"
    "s_and_b32 s61, s61, 15\n"
    "v_mfma_f32_16x16x32_bf16 v[148:151], a[80:83], a[16:19], v[164:167]\n"
    "s_mul_i32 s61, s61, 36864\n"
    "v_exp_f32 v143, v143\n"
    "v_mfma_f32_16x16x32_bf16 v[148:151], a[84:87], a[20:23], v[148:151]\n"
    "s_add_u32 s61, s61, s69\n"
    "v_cvt_pk_bf16_f32 v125, v142, v143\n"
    "v_mfma_f32_16x16x32_bf16 v[148:151], a[88:91], a[24:27], v[148:151]\n"
    "s_add_u32 m0, s63, 73728\n"
    "v_exp_f32 v144, v144\n"
    "v_mfma_f32_16x16x32_bf16 v[148:151], a[92:95], a[28:31], v[148:151]\n"
    "buffer_load_dwordx4 v220, s[44:47], s61 offen offset:0 lds\n"
    "v_mfma_f32_16x16x32_bf16 v[152:155], a[80:83], a[32:35], v[168:171]\n"
    "v_exp_f32 v145, v145\n"
    "v_mfma_f32_16x16x32_bf16 v[152:155], a[84:87], a[36:39], v[152:155]\n"
    "buffer_load_dwordx4 v221, s[44:47], s61 offen offset:1024 lds\n"
    "v_cvt_pk_bf16_f32 v102, v144, v145\n"
    "v_mfma_f32_16x16x32_bf16 v[152:155], a[88:91], a[40:43], v[152:155]\n"
    "v_exp_f32 v146, v146\n"
    "v_mfma_f32_16x16x32_bf16 v[152:155], a[92:95], a[44:47], v[152:155]\n"
    "buffer_load_dwordx4 v222, s[44:47], s61 offen offset:2048 lds\n"
    "v_mfma_f32_16x16x32_bf16 v[156:159], a[80:83], a[48:51], v[172:175]\n"
    "v_exp_f32 v147, v147\n"
    "v_mfma_f32_16x16x32_bf16 v[156:159], a[84:87], a[52:55], v[156:159]\n"
    "buffer_load_dwordx4 v223, s[44:47], s61 offen offset:3072 lds\n"
    "v_cvt_pk_bf16_f32 v103, v146, v147\n"
    "v_mfma_f32_16x16x32_bf16 v[156:159], a[88:91], a[56:59], v[156:159]\n"
    "s_add_u32 m0, s65, 73728\n"
    "v_exp_f32 v148, v148\n"
    "v_mfma_f32_16x16x32_bf16 v[156:159], a[92:95], a[60:63], v[156:159]\n"
    "ds_read_b64_tr_b16 a[96:97], v228 offset:49152\n"
    "v_mfma_f32_16x16x32_bf16 v[176:179], a[192:195], v[96:99], 0\n"
    "ds_read_b64_tr_b16 a[98:99], v228 offset:53248\n"
    "v_mfma_f32_16x16x32_bf16 v[176:179], a[192:195], v[100:103], v[176:179]\n"
    "s_waitcnt lgkmcnt(2)\n"
    "ds_read_b64_tr_b16 a[100:101], v228 offset:57344\n"
    "v_mfma_f32_16x16x32_bf16 v[32:35], a[112:115], v[64:67], v[32:35]\n"
    "v_exp_f32 v149, v149\n"
    "v_mfma_f32_16x16x32_bf16 v[32:35], a[116:119], v[68:71], v[32:35]\n"
    "v_cvt_pk_bf16_f32 v110, v148, v149\n"
    "v_cmp_nge_f32 vcc, s74, v176\n"
    "v_mfma_f32_16x16x32_bf16 v[48:51], a[120:123], v[64:67], v[48:51]\n"
    "v_exp_f32 v150, v150\n"
    "v_mfma_f32_16x16x32_bf16 v[48:51], a[124:127], v[68:71], v[48:51]\n"
    "s_cbranch_vccnz SA_3_0_X31\n"
    "RA_3_0_X31:\n"
    "v_mfma_f32_16x16x32_bf16 v[36:39], a[112:115], v[72:75], v[36:39]\n"
    "v_exp_f32 v151, v151\n"
    "v_mfma_f32_16x16x32_bf16 v[36:39], a[116:119], v[76:79], v[36:39]\n"
    "ds_read_b64_tr_b16 a[102:103], v228 offset:61440\n"
    "v_cvt_pk_bf16_f32 v111, v150, v151\n"
    "v_mfma_f32_16x16x32_bf16 v[52:55], a[120:123], v[72:75], v[52:55]\n"
    "v_exp_f32 v152, v152\n"
    "v_mfma_f32_16x16x32_bf16 v[52:55], a[124:127], v[76:79], v[52:55]\n"
    "s_mov_b64 exec, s[66:67]\n"
    "buffer_load_dwordx4 v250, s[44:47], s61 offen lds\n"
    "s_mov_b64 exec, -1\n"
    "v_add_f32 v238, v238, v176\n"
    "v_mfma_f32_16x16x32_bf16 v[180:183], a[192:195], v[104:107], 0\n"
    "ds_read_b64_tr_b16 a[104:105], v229 offset:49152\n"
    "v_mfma_f32_16x16x32_bf16 v[180:183], a[192:195], v[108:111], v[180:183]\n"
    "ds_read_b64_tr_b16 a[106:107], v229 offset:53248\n"
    "v_mfma_f32_16x16x32_bf16 v[40:43], a[112:115], v[80:83], v[40:43]\n"
    "v_exp_f32 v153, v153\n"
    "v_mfma_f32_16x16x32_bf16 v[40:43], a[116:119], v[84:87], v[40:43]\n"
    "v_cvt_pk_bf16_f32 v118, v152, v153\n"
    "v_cmp_nge_f32 vcc, s74, v180\n"
    "v_mfma_f32_16x16x32_bf16 v[56:59], a[120:123], v[80:83], v[56:59]\n"
    "v_exp_f32 v154, v154\n"
    "v_mfma_f32_16x16x32_bf16 v[56:59], a[124:127], v[84:87], v[56:59]\n"
    "s_cbranch_vccnz SA_3_1_X31\n"
    "RA_3_1_X31:\n"
    "v_mfma_f32_16x16x32_bf16 v[44:47], a[112:115], v[88:91], v[44:47]\n"
    "v_exp_f32 v155, v155\n"
    "v_mfma_f32_16x16x32_bf16 v[44:47], a[116:119], v[92:95], v[44:47]\n"
    "ds_read_b64_tr_b16 a[108:109], v229 offset:57344\n"
    "v_cvt_pk_bf16_f32 v119, v154, v155\n"
    "v_mfma_f32_16x16x32_bf16 v[60:63], a[120:123], v[88:91], v[60:63]\n"
    "v_exp_f32 v156, v156\n"
    "v_mfma_f32_16x16x32_bf16 v[60:63], a[124:127], v[92:95], v[60:63]\n"
    "ds_read_b64_tr_b16 a[110:111], v229 offset:61440\n"
    "v_add_f32 v239, v239, v180\n"
    "v_mfma_f32_16x16x32_bf16 v[176:179], a[192:195], v[112:115], 0\n"
    "v_mfma_f32_16x16x32_bf16 v[176:179], a[192:195], v[116:119], v[176:179]\n"
    "ds_read_b64_tr_b16 a[112:113], v228 offset:49408\n"
    "ds_read_b64_tr_b16 a[114:115], v228 offset:53504\n"
    "ds_read_b64_tr_b16 a[116:117], v228 offset:57600\n"
    "ds_read_b64_tr_b16 a[118:119], v228 offset:61696\n"
    "ds_read_b64_tr_b16 a[120:121], v229 offset:49408\n"
    "ds_read_b64_tr_b16 a[122:123], v229 offset:53504\n"
    "ds_read_b64_tr_b16 a[124:125], v229 offset:57600\n"
    "ds_read_b64_tr_b16 a[126:127], v229 offset:61696\n"
    "v_exp_f32 v157, v157\n"
    "s_nop 0\n"
    "v_cvt_pk_bf16_f32 v126, v156, v157\n"
    "v_cmp_nge_f32 vcc, s74, v176\n"
    "v_exp_f32 v158, v158\n"
    "s_cbranch_vccnz SA_3_2_X32\n"
    "RA_3_2_X32:\n"
    "v_exp_f32 v159, v159\n"
    "s_nop 0\n"
    "v_cvt_pk_bf16_f32 v127, v158, v159\n"
    "v_add_f32 v240, v240, v176\n"
    "v_mfma_f32_16x16x32_bf16 v[180:183], a[192:195], v[120:123], 0\n"
    "v_mfma_f32_16x16x32_bf16 v[180:183], a[192:195], v[124:127], v[180:183]\n"
    "s_nop 7\n"
    "v_cmp_nge_f32 vcc, s74, v180\n"
    "s_cbranch_vccnz SA_3_3_X32\n"
    "RA_3_3_X32:\n"
    "v_add_f32 v241, v241, v180\n"
    "v_rcp_f32 v238, v238\n"
    "v_rcp_f32 v239, v239\n"
    "v_rcp_f32 v240, v240\n"
    "v_rcp_f32 v241, v241\n"
    "s_waitcnt vmcnt(63)\n"
    "s_barrier\n"
    "s_waitcnt lgkmcnt(8)\n"
    "v_mfma_f32_16x16x32_bf16 a[128:131], a[96:99], v[96:99], a[128:131]\n"
    "v_lshrrev_b32 v244, 4, v237\n"
    "v_bfe_u32 v245, v237, 2, 2\n"
    "v_mfma_f32_16x16x32_bf16 a[128:131], a[100:103], v[100:103], a[128:131]\n"
    "v_and_b32 v246, 3, v237\n"
    "v_lshlrev_b32 v246, 3, v246\n"
    "v_mfma_f32_16x16x32_bf16 a[144:147], a[104:107], v[96:99], a[144:147]\n"
    "v_add_u32 v246, %7, v246\n"
    "v_lshl_add_u32 v242, v244, 7, v246\n"
    "v_mfma_f32_16x16x32_bf16 a[144:147], a[108:111], v[100:103], a[144:147]\n"
    "v_lshl_add_u32 v242, v245, 5, v242\n"
    "v_lshl_add_u32 v234, v244, 10, v246\n"
    "v_mfma_f32_16x16x32_bf16 a[132:135], a[96:99], v[104:107], a[132:135]\n"
    "v_lshl_add_u32 v234, v245, 6, v234\n"
    "v_and_b32 v244, 1, v244\n"
    "v_mfma_f32_16x16x32_bf16 a[132:135], a[100:103], v[108:111], a[132:135]\n"
    "v_lshl_add_u32 v234, v244, 5, v234\n"
    "v_xor_b32 v235, 32, v234\n"
    "v_accvgpr_read_b32 v208, a128\n"
    "v_accvgpr_read_b32 v209, a129\n"
    "v_accvgpr_read_b32 v210, a130\n"
    "v_mfma_f32_16x16x32_bf16 a[148:151], a[104:107], v[104:107], a[148:151]\n"
    "v_add_u32 v224, 145408, v234\n"
    "v_add_u32 v225, 145408, v235\n"
    "v_accvgpr_read_b32 v211, a131\n"
    "v_mul_f32 v208, v238, v208\n"
    "v_mul_f32 v209, v238, v209\n"
    "v_mfma_f32_16x16x32_bf16 a[148:151], a[108:111], v[108:111], a[148:151]\n"
    "v_add_u32 v226, 145408, v242\n"
    "v_mul_f32 v210, v238, v210\n"
    "v_mul_f32 v211, v238, v211\n"
    "v_cvt_pk_bf16_f32 v144, v208, v209\n"
    "v_cvt_pk_bf16_f32 v145, v210, v211\n"
    "v_mfma_f32_16x16x32_bf16 a[136:139], a[96:99], v[112:115], a[136:139]\n"
    "v_accvgpr_read_b32 v212, a144\n"
    "v_accvgpr_read_b32 v213, a145\n"
    "v_accvgpr_read_b32 v214, a146\n"
    "v_accvgpr_read_b32 v215, a147\n"
    "v_mul_f32 v212, v238, v212\n"
    "v_mfma_f32_16x16x32_bf16 a[136:139], a[100:103], v[116:119], a[136:139]\n"
    "v_mul_f32 v213, v238, v213\n"
    "v_mul_f32 v214, v238, v214\n"
    "v_mul_f32 v215, v238, v215\n"
    "v_cvt_pk_bf16_f32 v146, v212, v213\n"
    "v_cvt_pk_bf16_f32 v147, v214, v215\n"
    "v_mfma_f32_16x16x32_bf16 a[152:155], a[104:107], v[112:115], a[152:155]\n"
    "s_mov_b32 s74, 0\n"
    "buffer_store_dwordx4 v[144:147], v236, s[56:59], s74 offen offset:0 sc1\n"
    "v_accvgpr_read_b32 v208, a132\n"
    "v_accvgpr_read_b32 v209, a133\n"
    "v_accvgpr_read_b32 v210, a134\n"
    "v_accvgpr_read_b32 v211, a135\n"
    "v_mul_f32 v208, v239, v208\n"
    "v_mfma_f32_16x16x32_bf16 a[152:155], a[108:111], v[116:119], a[152:155]\n"
    "v_mul_f32 v209, v239, v209\n"
    "v_mul_f32 v210, v239, v210\n"
    "v_mul_f32 v211, v239, v211\n"
    "v_cvt_pk_bf16_f32 v160, v208, v209\n"
    "v_cvt_pk_bf16_f32 v161, v210, v211\n"
    "v_mfma_f32_16x16x32_bf16 a[140:143], a[96:99], v[120:123], a[140:143]\n"
    "v_accvgpr_read_b32 v212, a148\n"
    "v_accvgpr_read_b32 v213, a149\n"
    "v_accvgpr_read_b32 v214, a150\n"
    "v_accvgpr_read_b32 v215, a151\n"
    "v_mul_f32 v212, v239, v212\n"
    "v_mfma_f32_16x16x32_bf16 a[140:143], a[100:103], v[124:127], a[140:143]\n"
    "v_mul_f32 v213, v239, v213\n"
    "v_mul_f32 v214, v239, v214\n"
    "v_mul_f32 v215, v239, v215\n"
    "v_cvt_pk_bf16_f32 v162, v212, v213\n"
    "v_cvt_pk_bf16_f32 v163, v214, v215\n"
    "v_mfma_f32_16x16x32_bf16 a[156:159], a[104:107], v[120:123], a[156:159]\n"
    "s_mov_b32 s74, 131072\n"
    "buffer_store_dwordx4 v[160:163], v236, s[56:59], s74 offen offset:0 sc1\n"
    "v_accvgpr_read_b32 v208, a136\n"
    "v_accvgpr_read_b32 v209, a137\n"
    "v_accvgpr_read_b32 v210, a138\n"
    "v_accvgpr_read_b32 v211, a139\n"
    "v_mul_f32 v208, v240, v208\n"
    "v_mfma_f32_16x16x32_bf16 a[156:159], a[108:111], v[124:127], a[156:159]\n"
    "s_waitcnt lgkmcnt(0)\n"
    "v_mul_f32 v209, v240, v209\n"
    "v_mul_f32 v210, v240, v210\n"
    "v_mul_f32 v211, v240, v211\n"
    "v_cvt_pk_bf16_f32 v176, v208, v209\n"
    "v_cvt_pk_bf16_f32 v177, v210, v211\n"
    "v_mfma_f32_16x16x32_bf16 a[160:163], a[112:115], v[96:99], a[160:163]\n"
    "ds_read_b64_tr_b16 a[96:97], v228 offset:49664\n"
    "v_accvgpr_read_b32 v212, a152\n"
    "v_accvgpr_read_b32 v213, a153\n"
    "v_accvgpr_read_b32 v214, a154\n"
    "v_accvgpr_read_b32 v215, a155\n"
    "v_mfma_f32_16x16x32_bf16 a[160:163], a[116:119], v[100:103], a[160:163]\n"
    "ds_read_b64_tr_b16 a[98:99], v228 offset:53760\n"
    "v_mul_f32 v212, v240, v212\n"
    "v_mul_f32 v213, v240, v213\n"
    "v_mul_f32 v214, v240, v214\n"
    "v_mul_f32 v215, v240, v215\n"
    "v_mfma_f32_16x16x32_bf16 a[176:179], a[120:123], v[96:99], a[176:179]\n"
    "ds_read_b64_tr_b16 a[100:101], v228 offset:57856\n"
    "v_cvt_pk_bf16_f32 v178, v212, v213\n"
    "v_cvt_pk_bf16_f32 v179, v214, v215\n"
    "v_accvgpr_read_b32 v208, a140\n"
    "v_accvgpr_read_b32 v209, a141\n"
    "v_mfma_f32_16x16x32_bf16 a[176:179], a[124:127], v[100:103], a[176:179]\n"
    "ds_read_b64_tr_b16 a[102:103], v228 offset:61952\n"
    "v_accvgpr_read_b32 v210, a142\n"
    "v_accvgpr_read_b32 v211, a143\n"
    "v_mul_f32 v208, v241, v208\n"
    "v_mul_f32 v209, v241, v209\n"
    "v_mfma_f32_16x16x32_bf16 a[164:167], a[112:115], v[104:107], a[164:167]\n"
    "ds_read_b64_tr_b16 a[104:105], v229 offset:49664\n"
    "v_mul_f32 v210, v241, v210\n"
    "v_mul_f32 v211, v241, v211\n"
    "v_cvt_pk_bf16_f32 v192, v208, v209\n"
    "v_cvt_pk_bf16_f32 v193, v210, v211\n"
    "v_mfma_f32_16x16x32_bf16 a[164:167], a[116:119], v[108:111], a[164:167]\n"
    "ds_read_b64_tr_b16 a[106:107], v229 offset:53760\n"
    "v_accvgpr_read_b32 v212, a156\n"
    "v_accvgpr_read_b32 v213, a157\n"
    "v_accvgpr_read_b32 v214, a158\n"
    "v_accvgpr_read_b32 v215, a159\n"
    "v_mfma_f32_16x16x32_bf16 a[180:183], a[120:123], v[104:107], a[180:183]\n"
    "ds_read_b64_tr_b16 a[108:109], v229 offset:57856\n"
    "v_mul_f32 v212, v241, v212\n"
    "v_mul_f32 v213, v241, v213\n"
    "v_mul_f32 v214, v241, v214\n"
    "v_mul_f32 v215, v241, v215\n"
    "v_mfma_f32_16x16x32_bf16 a[180:183], a[124:127], v[108:111], a[180:183]\n"
    "ds_read_b64_tr_b16 a[110:111], v229 offset:61952\n"
    "v_cvt_pk_bf16_f32 v194, v212, v213\n"
    "v_cvt_pk_bf16_f32 v195, v214, v215\n"
    "v_accvgpr_read_b32 v208, a160\n"
    "v_accvgpr_read_b32 v209, a161\n"
    "v_mfma_f32_16x16x32_bf16 a[168:171], a[112:115], v[112:115], a[168:171]\n"
    "s_mov_b32 s74, 262144\n"
    "buffer_store_dwordx4 v[176:179], v236, s[56:59], s74 offen offset:0 sc1\n"
    "v_accvgpr_read_b32 v210, a162\n"
    "v_accvgpr_read_b32 v211, a163\n"
    "v_mul_f32 v208, v238, v208\n"
    "v_mul_f32 v209, v238, v209\n"
    "v_mul_f32 v210, v238, v210\n"
    "v_mfma_f32_16x16x32_bf16 a[168:171], a[116:119], v[116:119], a[168:171]\n"
    "v_mul_f32 v211, v238, v211\n"
    "v_cvt_pk_bf16_f32 v148, v208, v209\n"
    "v_cvt_pk_bf16_f32 v149, v210, v211\n"
    "v_accvgpr_read_b32 v212, a176\n"
    "v_accvgpr_read_b32 v213, a177\n"
    "v_mfma_f32_16x16x32_bf16 a[184:187], a[120:123], v[112:115], a[184:187]\n"
    "s_mov_b32 s74, 393216\n"
    "buffer_store_dwordx4 v[192:195], v236, s[56:59], s74 offen offset:0 sc1\n"
    "v_accvgpr_read_b32 v214, a178\n"
    "v_accvgpr_read_b32 v215, a179\n"
    "v_mul_f32 v212, v238, v212\n"
    "v_mul_f32 v213, v238, v213\n"
    "v_mul_f32 v214, v238, v214\n"
    "v_mfma_f32_16x16x32_bf16 a[184:187], a[124:127], v[116:119], a[184:187]\n"
    "v_mul_f32 v215, v238, v215\n"
    "v_cvt_pk_bf16_f32 v150, v212, v213\n"
    "v_cvt_pk_bf16_f32 v151, v214, v215\n"
    "v_accvgpr_read_b32 v208, a164\n"
    "v_accvgpr_read_b32 v209, a165\n"
    "v_mfma_f32_16x16x32_bf16 a[172:175], a[112:115], v[120:123], a[172:175]\n"
    "s_mov_b32 s74, 0\n"
    "buffer_store_dwordx4 v[148:151], v236, s[56:59], s74 offen offset:1024 sc1\n"
    "v_accvgpr_read_b32 v210, a166\n"
    "v_accvgpr_read_b32 v211, a167\n"
    "v_mul_f32 v208, v239, v208\n"
    "v_mul_f32 v209, v239, v209\n"
    "v_mul_f32 v210, v239, v210\n"
    "v_mfma_f32_16x16x32_bf16 a[172:175], a[116:119], v[124:127], a[172:175]\n"
    "v_mul_f32 v211, v239, v211\n"
    "v_cvt_pk_bf16_f32 v164, v208, v209\n"
    "v_cvt_pk_bf16_f32 v165, v210, v211\n"
    "v_accvgpr_read_b32 v212, a180\n"
    "v_accvgpr_read_b32 v213, a181\n"
    "v_mfma_f32_16x16x32_bf16 a[188:191], a[120:123], v[120:123], a[188:191]\n"
    "v_accvgpr_read_b32 v214, a182\n"
    "v_accvgpr_read_b32 v215, a183\n"
    "v_mul_f32 v212, v239, v212\n"
    "v_mul_f32 v213, v239, v213\n"
    "v_mul_f32 v214, v239, v214\n"
    "v_mfma_f32_16x16x32_bf16 a[188:191], a[124:127], v[124:127], a[188:191]\n"
    "s_waitcnt lgkmcnt(0)\n"
    "v_mul_f32 v215, v239, v215\n"
    "v_cvt_pk_bf16_f32 v166, v212, v213\n"
    "v_cvt_pk_bf16_f32 v167, v214, v215\n"
    "v_accvgpr_read_b32 v208, a168\n"
    "v_accvgpr_read_b32 v209, a169\n"
    "v_mfma_f32_16x16x32_bf16 v[0:3], a[96:99], v[96:99], v[0:3]\n"
    "ds_read_b64_tr_b16 a[112:113], v228 offset:49920\n"
    "v_accvgpr_read_b32 v210, a170\n"
    "v_accvgpr_read_b32 v211, a171\n"
    "v_mul_f32 v208, v240, v208\n"
    "v_mul_f32 v209, v240, v209\n"
    "v_mfma_f32_16x16x32_bf16 v[0:3], a[100:103], v[100:103], v[0:3]\n"
    "ds_read_b64_tr_b16 a[114:115], v228 offset:54016\n"
    "v_mul_f32 v210, v240, v210\n"
    "v_mul_f32 v211, v240, v211\n"
    "v_cvt_pk_bf16_f32 v180, v208, v209\n"
    "v_cvt_pk_bf16_f32 v181, v210, v211\n"
    "v_mfma_f32_16x16x32_bf16 v[16:19], a[104:107], v[96:99], v[16:19]\n"
    "ds_read_b64_tr_b16 a[116:117], v228 offset:58112\n"
    "v_accvgpr_read_b32 v212, a184\n"
    "v_accvgpr_read_b32 v213, a185\n"
    "v_accvgpr_read_b32 v214, a186\n"
    "v_accvgpr_read_b32 v215, a187\n"
    "v_mfma_f32_16x16x32_bf16 v[16:19], a[108:111], v[100:103], v[16:19]\n"
    "ds_read_b64_tr_b16 a[118:119], v228 offset:62208\n"
    "v_mul_f32 v212, v240, v212\n"
    "v_mul_f32 v213, v240, v213\n"
    "v_mul_f32 v214, v240, v214\n"
    "v_mul_f32 v215, v240, v215\n"
    "v_mfma_f32_16x16x32_bf16 v[4:7], a[96:99], v[104:107], v[4:7]\n"
    "ds_read_b64_tr_b16 a[120:121], v229 offset:49920\n"
    "v_cvt_pk_bf16_f32 v182, v212, v213\n"
    "v_cvt_pk_bf16_f32 v183, v214, v215\n"
    "v_accvgpr_read_b32 v208, a172\n"
    "v_accvgpr_read_b32 v209, a173\n"
    "v_mfma_f32_16x16x32_bf16 v[4:7], a[100:103], v[108:111], v[4:7]\n"
    "ds_read_b64_tr_b16 a[122:123], v229 offset:54016\n"
    "v_accvgpr_read_b32 v210, a174\n"
    "v_accvgpr_read_b32 v211, a175\n"
    "v_mul_f32 v208, v241, v208\n"
    "v_mul_f32 v209, v241, v209\n"
    "v_mfma_f32_16x16x32_bf16 v[20:23], a[104:107], v[104:107], v[20:23]\n"
    "ds_read_b64_tr_b16 a[124:125], v229 offset:58112\n"
    "v_mul_f32 v210, v241, v210\n"
    "v_mul_f32 v211, v241, v211\n"
    "v_cvt_pk_bf16_f32 v196, v208, v209\n"
    "v_cvt_pk_bf16_f32 v197, v210, v211\n"
    "v_mfma_f32_16x16x32_bf16 v[20:23], a[108:111], v[108:111], v[20:23]\n"
    "ds_read_b64_tr_b16 a[126:127], v229 offset:62208\n"
    "v_accvgpr_read_b32 v212, a188\n"
    "v_accvgpr_read_b32 v213, a189\n"
    "v_accvgpr_read_b32 v214, a190\n"
    "v_accvgpr_read_b32 v215, a191\n"
    "v_mfma_f32_16x16x32_bf16 v[8:11], a[96:99], v[112:115], v[8:11]\n"
    "ds_read_b64_tr_b16 a[216:217], v224 offset:0\n"
    "v_mul_f32 v212, v241, v212\n"
    "v_mul_f32 v213, v241, v213\n"
    "v_mul_f32 v214, v241, v214\n"
    "v_mul_f32 v215, v241, v215\n"
    "v_mfma_f32_16x16x32_bf16 v[8:11], a[100:103], v[116:119], v[8:11]\n"
    "ds_read_b64_tr_b16 a[218:219], v224 offset:4096\n"
    "v_cvt_pk_bf16_f32 v198, v212, v213\n"
    "v_cvt_pk_bf16_f32 v199, v214, v215\n"
    "v_mul_f32 v0, v238, v0\n"
    "v_mul_f32 v1, v238, v1\n"
    "v_mfma_f32_16x16x32_bf16 v[24:27], a[104:107], v[112:115], v[24:27]\n"
    "ds_read_b64_tr_b16 a[220:221], v224 offset:9216\n"
    "v_mul_f32 v2, v238, v2\n"
    "v_mul_f32 v3, v238, v3\n"
    "v_cvt_pk_bf16_f32 v152, v0, v1\n"
    "v_cvt_pk_bf16_f32 v153, v2, v3\n"
    "v_mfma_f32_16x16x32_bf16 v[24:27], a[108:111], v[116:119], v[24:27]\n"
    "ds_read_b64_tr_b16 a[222:223], v224 offset:13312\n"
    "v_mul_f32 v16, v238, v16\n"
    "v_mul_f32 v17, v238, v17\n"
    "v_mul_f32 v18, v238, v18\n"
    "v_mul_f32 v19, v238, v19\n"
    "v_mfma_f32_16x16x32_bf16 v[12:15], a[96:99], v[120:123], v[12:15]\n"
    "ds_read_b64_tr_b16 a[224:225], v225 offset:0\n"
    "v_cvt_pk_bf16_f32 v154, v16, v17\n"
    "v_cvt_pk_bf16_f32 v155, v18, v19\n"
    "v_mul_f32 v4, v239, v4\n"
    "v_mul_f32 v5, v239, v5\n"
    "v_mfma_f32_16x16x32_bf16 v[12:15], a[100:103], v[124:127], v[12:15]\n"
    "ds_read_b64_tr_b16 a[226:227], v225 offset:4096\n"
    "v_mul_f32 v6, v239, v6\n"
    "v_mul_f32 v7, v239, v7\n"
    "v_cvt_pk_bf16_f32 v168, v4, v5\n"
    "v_cvt_pk_bf16_f32 v169, v6, v7\n"
    "v_mfma_f32_16x16x32_bf16 v[28:31], a[104:107], v[120:123], v[28:31]\n"
    "ds_read_b64_tr_b16 a[228:229], v225 offset:9216\n"
    "v_mul_f32 v20, v239, v20\n"
    "v_mul_f32 v21, v239, v21\n"
    "v_mul_f32 v22, v239, v22\n"
    "v_mul_f32 v23, v239, v23\n"
    "v_mfma_f32_16x16x32_bf16 v[28:31], a[108:111], v[124:127], v[28:31]\n"
    "s_waitcnt lgkmcnt(7)\n"
    "ds_read_b64_tr_b16 a[230:231], v225 offset:13312\n"
    "v_cvt_pk_bf16_f32 v170, v20, v21\n"
    "v_cvt_pk_bf16_f32 v171, v22, v23\n"
    "v_mul_f32 v8, v240, v8\n"
    "v_mul_f32 v9, v240, v9\n"
    "v_mfma_f32_16x16x32_bf16 v[32:35], a[112:115], v[96:99], v[32:35]\n"
    "ds_read_b64_tr_b16 a[232:233], v224 offset:256\n"
    "v_mul_f32 v10, v240, v10\n"
    "v_mul_f32 v11, v240, v11\n"
    "v_cvt_pk_bf16_f32 v184, v8, v9\n"
    "v_cvt_pk_bf16_f32 v185, v10, v11\n"
    "v_mfma_f32_16x16x32_bf16 v[32:35], a[116:119], v[100:103], v[32:35]\n"
    "ds_read_b64_tr_b16 a[234:235], v224 offset:4352\n"
    "v_mul_f32 v24, v240, v24\n"
    "v_mul_f32 v25, v240, v25\n"
    "v_mul_f32 v26, v240, v26\n"
    "v_mul_f32 v27, v240, v27\n"
    "v_mfma_f32_16x16x32_bf16 v[48:51], a[120:123], v[96:99], v[48:51]\n"
    "ds_read_b64_tr_b16 a[236:237], v224 offset:9472\n"
    "v_cvt_pk_bf16_f32 v186, v24, v25\n"
    "v_cvt_pk_bf16_f32 v187, v26, v27\n"
    "v_mul_f32 v12, v241, v12\n"
    "v_mul_f32 v13, v241, v13\n"
    "v_mfma_f32_16x16x32_bf16 v[48:51], a[124:127], v[100:103], v[48:51]\n"
    "ds_read_b64_tr_b16 a[238:239], v224 offset:13568\n"
    "v_mul_f32 v14, v241, v14\n"
    "v_mul_f32 v15, v241, v15\n"
    "v_cvt_pk_bf16_f32 v200, v12, v13\n"
    "v_cvt_pk_bf16_f32 v201, v14, v15\n"
    "v_mfma_f32_16x16x32_bf16 v[36:39], a[112:115], v[104:107], v[36:39]\n"
    "s_mov_b32 s74, 131072\n"
    "buffer_store_dwordx4 v[164:167], v236, s[56:59], s74 offen offset:1024 sc1\n"
    "v_mul_f32 v28, v241, v28\n"
    "v_mul_f32 v29, v241, v29\n"
    "v_mul_f32 v30, v241, v30\n"
    "v_mul_f32 v31, v241, v31\n"
    "v_cvt_pk_bf16_f32 v202, v28, v29\n"
    "v_mfma_f32_16x16x32_bf16 v[36:39], a[116:119], v[108:111], v[36:39]\n"
    "v_cvt_pk_bf16_f32 v203, v30, v31\n"
    "v_mul_f32 v32, v238, v32\n"
    "v_mul_f32 v33, v238, v33\n"
    "v_mul_f32 v34, v238, v34\n"
    "v_mul_f32 v35, v238, v35\n"
    "v_mfma_f32_16x16x32_bf16 v[52:55], a[120:123], v[104:107], v[52:55]\n"
    "s_mov_b32 s74, 262144\n"
    "buffer_store_dwordx4 v[180:183], v236, s[56:59], s74 offen offset:1024 sc1\n"
    "v_cvt_pk_bf16_f32 v156, v32, v33\n"
    "v_cvt_pk_bf16_f32 v157, v34, v35\n"
    "v_mul_f32 v48, v238, v48\n"
    "v_mul_f32 v49, v238, v49\n"
    "v_mul_f32 v50, v238, v50\n"
    "v_mfma_f32_16x16x32_bf16 v[52:55], a[124:127], v[108:111], v[52:55]\n"
    "v_mul_f32 v51, v238, v51\n"
    "v_cvt_pk_bf16_f32 v158, v48, v49\n"
    "v_cvt_pk_bf16_f32 v159, v50, v51\n"
    "v_mfma_f32_16x16x32_bf16 v[40:43], a[112:115], v[112:115], v[40:43]\n"
    "s_mov_b32 s74, 393216\n"
    "buffer_store_dwordx4 v[196:199], v236, s[56:59], s74 offen offset:1024 sc1\n"
    "v_mfma_f32_16x16x32_bf16 v[40:43], a[116:119], v[116:119], v[40:43]\n"
    "v_mul_f32 v36, v239, v36\n"
    "v_mul_f32 v37, v239, v37\n"
    "v_mul_f32 v38, v239, v38\n"
    "v_mul_f32 v39, v239, v39\n"
    "v_cvt_pk_bf16_f32 v172, v36, v37\n"
    "v_mfma_f32_16x16x32_bf16 v[56:59], a[120:123], v[112:115], v[56:59]\n"
    "s_mov_b32 s74, 0\n"
    "buffer_store_dwordx4 v[152:155], v236, s[56:59], s74 offen offset:2048 sc1\n"
    "v_cvt_pk_bf16_f32 v173, v38, v39\n"
    "v_mul_f32 v52, v239, v52\n"
    "v_mul_f32 v53, v239, v53\n"
    "v_mul_f32 v54, v239, v54\n"
    "v_mul_f32 v55, v239, v55\n"
    "v_mfma_f32_16x16x32_bf16 v[56:59], a[124:127], v[116:119], v[56:59]\n"
    "v_cvt_pk_bf16_f32 v174, v52, v53\n"
    "v_cvt_pk_bf16_f32 v175, v54, v55\n"
    "v_mfma_f32_16x16x32_bf16 v[44:47], a[112:115], v[120:123], v[44:47]\n"
    "s_mov_b32 s74, 131072\n"
    "buffer_store_dwordx4 v[168:171], v236, s[56:59], s74 offen offset:2048 sc1\n"
    "v_mfma_f32_16x16x32_bf16 v[44:47], a[116:119], v[124:127], v[44:47]\n"
    "v_mul_f32 v40, v240, v40\n"
    "v_mul_f32 v41, v240, v41\n"
    "v_mul_f32 v42, v240, v42\n"
    "v_mul_f32 v43, v240, v43\n"
    "v_cvt_pk_bf16_f32 v188, v40, v41\n"
    "v_mfma_f32_16x16x32_bf16 v[60:63], a[120:123], v[120:123], v[60:63]\n"
    "s_mov_b32 s74, 262144\n"
    "buffer_store_dwordx4 v[184:187], v236, s[56:59], s74 offen offset:2048 sc1\n"
    "v_cvt_pk_bf16_f32 v189, v42, v43\n"
    "v_mul_f32 v56, v240, v56\n"
    "v_mul_f32 v57, v240, v57\n"
    "v_mul_f32 v58, v240, v58\n"
    "v_mul_f32 v59, v240, v59\n"
    "v_mfma_f32_16x16x32_bf16 v[60:63], a[124:127], v[124:127], v[60:63]\n"
    "s_waitcnt lgkmcnt(8)\n"
    "v_cvt_pk_bf16_f32 v190, v56, v57\n"
    "v_cvt_pk_bf16_f32 v191, v58, v59\n"
    "v_mfma_f32_16x16x32_bf16 v[64:67], a[216:219], v[144:147], 0\n"
    "s_mov_b64 s[40:41], %15\n"
    "ds_read_b64_tr_b16 a[240:241], v225 offset:256\n"
    "v_mfma_f32_16x16x32_bf16 v[64:67], a[220:223], v[148:151], v[64:67]\n"
    "s_and_b32 s41, s41, 0xffff\n"
    "s_mov_b32 s74, 393216\n"
    "buffer_store_dwordx4 v[200:203], v236, s[56:59], s74 offen offset:2048 sc1\n"
    "v_mul_f32 v44, v241, v44\n"
    "v_mul_f32 v45, v241, v45\n"
    "v_mul_f32 v46, v241, v46\n"
    "v_mul_f32 v47, v241, v47\n"
    "v_cvt_pk_bf16_f32 v204, v44, v45\n"
    "v_mfma_f32_16x16x32_bf16 v[68:71], a[216:219], v[160:163], 0\n"
    "s_mov_b32 s42, 409600\n"
    "ds_read_b64_tr_b16 a[242:243], v225 offset:4352\n"
    "v_cvt_pk_bf16_f32 v205, v46, v47\n"
    "v_mul_f32 v60, v241, v60\n"
    "v_mul_f32 v61, v241, v61\n"
    "v_mul_f32 v62, v241, v62\n"
    "v_mfma_f32_16x16x32_bf16 v[68:71], a[220:223], v[164:167], v[68:71]\n"
    "s_mov_b32 s43, 0x20000\n"
    "s_mov_b32 s74, 0\n"
    "buffer_store_dwordx4 v[156:159], v236, s[56:59], s74 offen offset:3072 sc1\n"
    "v_mul_f32 v63, v241, v63\n"
    "v_cvt_pk_bf16_f32 v206, v60, v61\n"
    "v_cvt_pk_bf16_f32 v207, v62, v63\n"
    "v_mfma_f32_16x16x32_bf16 v[72:75], a[216:219], v[176:179], 0\n"
    "ds_read_b64_tr_b16 a[244:245], v225 offset:9472\n"
    "v_add_u32 v230, 0, v236\n"
    "v_mfma_f32_16x16x32_bf16 v[72:75], a[220:223], v[180:183], v[72:75]\n"
    "s_mov_b32 s74, 131072\n"
    "buffer_store_dwordx4 v[172:175], v236, s[56:59], s74 offen offset:3072 sc1\n"
    "v_add_u32 v231, 131072, v236\n"
    "v_mfma_f32_16x16x32_bf16 v[76:79], a[216:219], v[192:195], 0\n"
    "ds_read_b64_tr_b16 a[246:247], v225 offset:13568\n"
    "v_add_u32 v232, 262144, v236\n"
    "v_mfma_f32_16x16x32_bf16 v[76:79], a[220:223], v[196:199], v[76:79]\n"
    "s_waitcnt lgkmcnt(8)\n"
    "s_mov_b32 s74, 262144\n"
    "buffer_store_dwordx4 v[188:191], v236, s[56:59], s74 offen offset:3072 sc1\n"
    "v_add_u32 v233, 393216, v236\n"
    "v_mfma_f32_16x16x32_bf16 v[80:83], a[224:227], v[144:147], 0\n"
    "s_mov_b64 s[52:53], %19\n"
    "ds_read_b64_tr_b16 a[248:249], v224 offset:512\n"
    "v_mfma_f32_16x16x32_bf16 v[80:83], a[228:231], v[148:151], v[80:83]\n"
    "s_and_b32 s53, s53, 0xffff\n"
    "s_mov_b32 s74, 393216\n"
    "buffer_store_dwordx4 v[204:207], v236, s[56:59], s74 offen offset:3072 sc1\n"
    "v_mfma_f32_16x16x32_bf16 v[84:87], a[224:227], v[160:163], 0\n"
    "s_mov_b32 s54, 73728\n"
    "ds_read_b64_tr_b16 a[250:251], v224 offset:4608\n"
    "v_mfma_f32_16x16x32_bf16 v[84:87], a[228:231], v[164:167], v[84:87]\n"
    "s_mov_b32 s55, 0x20000\n"
    "v_mfma_f32_16x16x32_bf16 v[88:91], a[224:227], v[176:179], 0\n"
    "s_mov_b32 s70, %20\n"
    "ds_read_b64_tr_b16 a[252:253], v224 offset:9728\n"
    "v_mfma_f32_16x16x32_bf16 v[88:91], a[228:231], v[180:183], v[88:91]\n"
    "s_mov_b32 s71, %21\n"
    "v_and_b32 v244, 3, v237\n"
    "v_and_b32 v245, -16, v243\n"
    "v_mfma_f32_16x16x32_bf16 v[92:95], a[224:227], v[192:195], 0\n"
    "s_add_u32 s61, %22, 12\n"
    "ds_read_b64_tr_b16 a[254:255], v224 offset:13824\n"
    "v_lshl_add_u32 v244, v244, 2, v245\n"
    "v_mfma_f32_16x16x32_bf16 v[92:95], a[228:231], v[196:199], v[92:95]\n"
    "s_waitcnt lgkmcnt(8)\n"
    "s_and_b32 s61, s61, 15\n"
    "v_mfma_f32_16x16x32_bf16 v[96:99], a[232:235], v[144:147], 0\n"
    "s_mul_i32 s61, s61, 36864\n"
    "ds_read_b64_tr_b16 a[216:217], v225 offset:512\n"
    "v_mfma_f32_16x16x32_bf16 v[96:99], a[236:239], v[148:151], v[96:99]\n"
    "s_add_u32 s61, s61, s69\n"
    "v_mfma_f32_16x16x32_bf16 v[100:103], a[232:235], v[160:163], 0\n"
    "s_add_u32 m0, s63, 92160\n"
    "ds_read_b64_tr_b16 a[218:219], v225 offset:4608\n"
    "v_mfma_f32_16x16x32_bf16 v[100:103], a[236:239], v[164:167], v[100:103]\n"
    "buffer_load_dwordx4 v220, s[44:47], s61 offen offset:0 lds\n"
    "v_mfma_f32_16x16x32_bf16 v[104:107], a[232:235], v[176:179], 0\n"
    "ds_read_b64_tr_b16 a[220:221], v225 offset:9728\n"
    "v_mfma_f32_16x16x32_bf16 v[104:107], a[236:239], v[180:183], v[104:107]\n"
    "v_mfma_f32_16x16x32_bf16 v[108:111], a[232:235], v[192:195], 0\n"
    "ds_read_b64_tr_b16 a[222:223], v225 offset:13824\n"
    "v_mfma_f32_16x16x32_bf16 v[108:111], a[236:239], v[196:199], v[108:111]\n"
    "s_waitcnt lgkmcnt(8)\n"
    "buffer_load_dwordx4 v221, s[44:47], s61 offen offset:1024 lds\n"
    "v_mfma_f32_16x16x32_bf16 v[112:115], a[240:243], v[144:147], 0\n"
    "ds_read_b64_tr_b16 a[224:225], v224 offset:768\n"
    "v_mfma_f32_16x16x32_bf16 v[112:115], a[244:247], v[148:151], v[112:115]\n"
    "v_mfma_f32_16x16x32_bf16 v[116:119], a[240:243], v[160:163], 0\n"
    "ds_read_b64_tr_b16 a[226:227], v224 offset:4864\n"
    "v_mfma_f32_16x16x32_bf16 v[116:119], a[244:247], v[164:167], v[116:119]\n"
    "buffer_load_dwordx4 v222, s[44:47], s61 offen offset:2048 lds\n"
    "v_mfma_f32_16x16x32_bf16 v[120:123], a[240:243], v[176:179], 0\n"
    "ds_read_b64_tr_b16 a[228:229], v224 offset:9984\n"
    "v_mfma_f32_16x16x32_bf16 v[120:123], a[244:247], v[180:183], v[120:123]\n"
    "v_mfma_f32_16x16x32_bf16 v[124:127], a[240:243], v[192:195], 0\n"
    "ds_read_b64_tr_b16 a[230:231], v224 offset:14080\n"
    "v_mfma_f32_16x16x32_bf16 v[124:127], a[244:247], v[196:199], v[124:127]\n"
    "s_waitcnt lgkmcnt(8)\n"
    "buffer_load_dwordx4 v223, s[44:47], s61 offen offset:3072 lds\n"
    "v_add_u32 v224, 0, v234\n"
    "v_mfma_f32_16x16x32_bf16 v[128:131], a[248:251], v[144:147], 0\n"
    "s_add_u32 m0, s65, 92160\n"
    "ds_read_b64_tr_b16 a[232:233], v225 offset:768\n"
    "v_mfma_f32_16x16x32_bf16 v[128:131], a[252:255], v[148:151], v[128:131]\n"
    "v_mfma_f32_16x16x32_bf16 v[132:135], a[248:251], v[160:163], 0\n"
    "ds_read_b64_tr_b16 a[234:235], v225 offset:4864\n"
    "v_mfma_f32_16x16x32_bf16 v[132:135], a[252:255], v[164:167], v[132:135]\n"
    "s_mov_b64 exec, s[66:67]\n"
    "buffer_load_dwordx4 v250, s[44:47], s61 offen lds\n"
    "s_mov_b64 exec, -1\n"
    "v_mfma_f32_16x16x32_bf16 v[136:139], a[248:251], v[176:179], 0\n"
    "ds_read_b64_tr_b16 a[236:237], v225 offset:9984\n"
    "v_mfma_f32_16x16x32_bf16 v[136:139], a[252:255], v[180:183], v[136:139]\n"
    "v_mfma_f32_16x16x32_bf16 v[140:143], a[248:251], v[192:195], 0\n"
    "ds_read_b64_tr_b16 a[238:239], v225 offset:14080\n"
    "v_mfma_f32_16x16x32_bf16 v[140:143], a[252:255], v[196:199], v[140:143]\n"
    "s_waitcnt lgkmcnt(8)\n"
    "v_add_u32 v225, 0, v235\n"
    "v_mfma_f32_16x16x32_bf16 v[0:3], a[216:219], v[144:147], 0\n"
    "ds_read_b64_tr_b16 a[240:241], v226 offset:8192\n"
    "v_mfma_f32_16x16x32_bf16 v[0:3], a[220:223], v[148:151], v[0:3]\n"
    "v_mfma_f32_16x16x32_bf16 v[4:7], a[216:219], v[160:163], 0\n"
    "ds_read_b64_tr_b16 a[242:243], v226 offset:8704\n"
    "v_mfma_f32_16x16x32_bf16 v[4:7], a[220:223], v[164:167], v[4:7]\n"
    "v_mfma_f32_16x16x32_bf16 v[8:11], a[216:219], v[176:179], 0\n"
    "ds_read_b64_tr_b16 a[244:245], v226 offset:17408\n"
    "v_mfma_f32_16x16x32_bf16 v[8:11], a[220:223], v[180:183], v[8:11]\n"
    "v_mfma_f32_16x16x32_bf16 v[12:15], a[216:219], v[192:195], 0\n"
    "s_waitcnt vmcnt(31)\n"
    "ds_read_b64_tr_b16 a[246:247], v226 offset:17920\n"
    "v_mfma_f32_16x16x32_bf16 v[12:15], a[220:223], v[196:199], v[12:15]\n"
    "s_waitcnt lgkmcnt(8)\n"
    "s_barrier\n"
    "v_add_u32 v226, 0, v242\n"
    "v_mfma_f32_16x16x32_bf16 v[16:19], a[224:227], v[144:147], 0\n"
    "s_add_u32 s61, %22, 6\n"
    "ds_read_b64_tr_b16 a[248:249], v224 offset:0\n"
    "v_mfma_f32_16x16x32_bf16 v[16:19], a[228:231], v[148:151], v[16:19]\n"
    "s_and_b32 s61, s61, 15\n"
    "v_mfma_f32_16x16x32_bf16 v[20:23], a[224:227], v[160:163], 0\n"
    "s_mul_i32 s61, s61, 36864\n"
    "ds_read_b64_tr_b16 a[250:251], v224 offset:4096\n"
    "v_mfma_f32_16x16x32_bf16 v[20:23], a[228:231], v[164:167], v[20:23]\n"
    "s_add_u32 s61, s61, s69\n"
    "v_mfma_f32_16x16x32_bf16 v[24:27], a[224:227], v[176:179], 0\n"
    "s_add_u32 m0, s63, 110592\n"
    "ds_read_b64_tr_b16 a[252:253], v224 offset:9216\n"
    "v_mfma_f32_16x16x32_bf16 v[24:27], a[228:231], v[180:183], v[24:27]\n"
    "s_waitcnt vmcnt(13)\n"
    "v_mov_b32 v245, %27\n"
    "s_mov_b64 exec, 1\n"
    "global_store_dword v243, v245, %13 offset:512 sc1\n"
    "s_mov_b64 exec, -1\n"
    "v_mfma_f32_16x16x32_bf16 v[28:31], a[224:227], v[192:195], 0\n"
    "ds_read_b64_tr_b16 a[254:255], v224 offset:13312\n"
    "v_mfma_f32_16x16x32_bf16 v[28:31], a[228:231], v[196:199], v[28:31]\n"
    "s_waitcnt lgkmcnt(8)\n"
    "buffer_load_dwordx4 v220, s[44:47], s61 offen offset:0 lds\n"
    "v_mfma_f32_16x16x32_bf16 v[32:35], a[232:235], v[144:147], 0\n"
    "ds_read_b64_tr_b16 a[216:217], v225 offset:0\n"
    "v_mfma_f32_16x16x32_bf16 v[32:35], a[236:239], v[148:151], v[32:35]\n"
    "v_mfma_f32_16x16x32_bf16 v[36:39], a[232:235], v[160:163], 0\n"
    "ds_read_b64_tr_b16 a[218:219], v225 offset:4096\n"
    "v_mfma_f32_16x16x32_bf16 v[36:39], a[236:239], v[164:167], v[36:39]\n"
    "buffer_load_dwordx4 v221, s[44:47], s61 offen offset:1024 lds\n"
    "v_mfma_f32_16x16x32_bf16 v[40:43], a[232:235], v[176:179], 0\n"
    "ds_read_b64_tr_b16 a[220:221], v225 offset:9216\n"
    "v_mfma_f32_16x16x32_bf16 v[40:43], a[236:239], v[180:183], v[40:43]\n"
    "v_mfma_f32_16x16x32_bf16 v[44:47], a[232:235], v[192:195], 0\n"
    "ds_read_b64_tr_b16 a[222:223], v225 offset:13312\n"
    "v_mfma_f32_16x16x32_bf16 v[44:47], a[236:239], v[196:199], v[44:47]\n"
    "s_waitcnt lgkmcnt(8)\n"
    "buffer_load_dwordx4 v222, s[44:47], s61 offen offset:2048 lds\n"
    "v_mfma_f32_16x16x32_bf16 v[48:51], a[240:243], v[144:147], 0\n"
    "ds_read_b64_tr_b16 a[224:225], v224 offset:256\n"
    "v_mfma_f32_16x16x32_bf16 v[48:51], a[244:247], v[148:151], v[48:51]\n"
    "v_mfma_f32_16x16x32_bf16 v[52:55], a[240:243], v[160:163], 0\n"
    "ds_read_b64_tr_b16 a[226:227], v224 offset:4352\n"
    "v_mfma_f32_16x16x32_bf16 v[52:55], a[244:247], v[164:167], v[52:55]\n"
    "v_mfma_f32_16x16x32_bf16 v[56:59], a[240:243], v[176:179], 0\n"
    "ds_read_b64_tr_b16 a[228:229], v224 offset:9472\n"
    "v_mfma_f32_16x16x32_bf16 v[56:59], a[244:247], v[180:183], v[56:59]\n"
    "s_waitcnt vmcnt(9)\n"
    "v_mov_b32 v245, %27\n"
    "s_mov_b64 exec, 1\n"
    "global_store_dword v243, v245, %13 offset:-512 sc1\n"
    "s_mov_b64 exec, -1\n"
    "v_mfma_f32_16x16x32_bf16 v[60:63], a[240:243], v[192:195], 0\n"
    "ds_read_b64_tr_b16 a[230:231], v224 offset:13568\n"
    "v_mfma_f32_16x16x32_bf16 v[60:63], a[244:247], v[196:199], v[60:63]\n"
    "s_waitcnt lgkmcnt(8)\n"
    "v_mfma_f32_16x16x32_bf16 v[64:67], a[248:251], v[152:155], v[64:67]\n"
    "ds_read_b64_tr_b16 a[232:233], v225 offset:256\n"
    "v_mfma_f32_16x16x32_bf16 v[64:67], a[252:255], v[156:159], v[64:67]\n"
    "global_load_dword v246, v244, %13 offset:512 sc1\n"
    "v_mfma_f32_16x16x32_bf16 v[68:71], a[248:251], v[168:171], v[68:71]\n"
    "ds_read_b64_tr_b16 a[234:235], v225 offset:4352\n"
    "v_mfma_f32_16x16x32_bf16 v[68:71], a[252:255], v[172:175], v[68:71]\n"
    "buffer_load_dwordx4 v223, s[44:47], s61 offen offset:3072 lds\n"
    "v_mfma_f32_16x16x32_bf16 v[72:75], a[248:251], v[184:187], v[72:75]\n"
    "s_add_u32 m0, s65, 110592\n"
    "ds_read_b64_tr_b16 a[236:237], v225 offset:9472\n"
    "v_mfma_f32_16x16x32_bf16 v[72:75], a[252:255], v[188:191], v[72:75]\n"
    "v_mfma_f32_16x16x32_bf16 v[76:79], a[248:251], v[200:203], v[76:79]\n"
    "ds_read_b64_tr_b16 a[238:239], v225 offset:13568\n"
    "v_mfma_f32_16x16x32_bf16 v[76:79], a[252:255], v[204:207], v[76:79]\n"
    "s_waitcnt lgkmcnt(8)\n"
    "s_mov_b64 exec, s[66:67]\n"
    "buffer_load_dwordx4 v250, s[44:47], s61 offen lds\n"
    "s_mov_b64 exec, -1\n"
    "v_mfma_f32_16x16x32_bf16 v[80:83], a[216:219], v[152:155], v[80:83]\n"
    "s_add_u32 s61, %22, 10\n"
    "ds_read_b64_tr_b16 a[240:241], v224 offset:512\n"
    "v_mfma_f32_16x16x32_bf16 v[80:83], a[220:223], v[156:159], v[80:83]\n"
    "s_and_b32 s61, s61, 15\n"
    "v_mfma_f32_16x16x32_bf16 v[84:87], a[216:219], v[168:171], v[84:87]\n"
    "s_mul_i32 s61, s61, 36864\n"
    "ds_read_b64_tr_b16 a[242:243], v224 offset:4608\n"
    "v_mfma_f32_16x16x32_bf16 v[84:87], a[220:223], v[172:175], v[84:87]\n"
    "s_add_u32 s61, s61, s69\n"
    "v_mfma_f32_16x16x32_bf16 v[88:91], a[216:219], v[184:187], v[88:91]\n"
    "s_add_u32 m0, s63, 36864\n"
    "ds_read_b64_tr_b16 a[244:245], v224 offset:9728\n"
    "v_mfma_f32_16x16x32_bf16 v[88:91], a[220:223], v[188:191], v[88:91]\n"
    "buffer_load_dwordx4 v220, s[44:47], s61 offen offset:0 lds\n"
    "v_mfma_f32_16x16x32_bf16 v[92:95], a[216:219], v[200:203], v[92:95]\n"
    "ds_read_b64_tr_b16 a[246:247], v224 offset:13824\n"
    "v_mfma_f32_16x16x32_bf16 v[92:95], a[220:223], v[204:207], v[92:95]\n"
    "s_waitcnt lgkmcnt(8)\n"
    "v_mfma_f32_16x16x32_bf16 v[96:99], a[224:227], v[152:155], v[96:99]\n"
    "ds_read_b64_tr_b16 a[248:249], v225 offset:512\n"
    "v_mfma_f32_16x16x32_bf16 v[96:99], a[228:231], v[156:159], v[96:99]\n"
    "buffer_load_dwordx4 v221, s[44:47], s61 offen offset:1024 lds\n"
    "v_mfma_f32_16x16x32_bf16 v[100:103], a[224:227], v[168:171], v[100:103]\n"
    "ds_read_b64_tr_b16 a[250:251], v225 offset:4608\n"
    "v_mfma_f32_16x16x32_bf16 v[100:103], a[228:231], v[172:175], v[100:103]\n"
    "v_mfma_f32_16x16x32_bf16 v[104:107], a[224:227], v[184:187], v[104:107]\n"
    "ds_read_b64_tr_b16 a[252:253], v225 offset:9728\n"
    "v_mfma_f32_16x16x32_bf16 v[104:107], a[228:231], v[188:191], v[104:107]\n"
    "buffer_load_dwordx4 v222, s[44:47], s61 offen offset:2048 lds\n"
    "v_mfma_f32_16x16x32_bf16 v[108:111], a[224:227], v[200:203], v[108:111]\n"
    "ds_read_b64_tr_b16 a[254:255], v225 offset:13824\n"
    "v_mfma_f32_16x16x32_bf16 v[108:111], a[228:231], v[204:207], v[108:111]\n"
    "s_waitcnt lgkmcnt(8)\n"
    "v_mfma_f32_16x16x32_bf16 v[112:115], a[232:235], v[152:155], v[112:115]\n"
    "ds_read_b64_tr_b16 a[216:217], v224 offset:768\n"
    "v_mfma_f32_16x16x32_bf16 v[112:115], a[236:239], v[156:159], v[112:115]\n"
    "buffer_load_dwordx4 v223, s[44:47], s61 offen offset:3072 lds\n"
    "v_mfma_f32_16x16x32_bf16 v[116:119], a[232:235], v[168:171], v[116:119]\n"
    "s_add_u32 m0, s65, 36864\n"
    "ds_read_b64_tr_b16 a[218:219], v224 offset:4864\n"
    "v_mfma_f32_16x16x32_bf16 v[116:119], a[236:239], v[172:175], v[116:119]\n"
    "v_mfma_f32_16x16x32_bf16 v[120:123], a[232:235], v[184:187], v[120:123]\n"
    "ds_read_b64_tr_b16 a[220:221], v224 offset:9984\n"
    "v_mfma_f32_16x16x32_bf16 v[120:123], a[236:239], v[188:191], v[120:123]\n"
    "s_mov_b64 exec, s[66:67]\n"
    "buffer_load_dwordx4 v250, s[44:47], s61 offen lds\n"
    "s_mov_b64 exec, -1\n"
    "v_mfma_f32_16x16x32_bf16 v[124:127], a[232:235], v[200:203], v[124:127]\n"
    "s_add_u32 s61, %22, 14\n"
    "ds_read_b64_tr_b16 a[222:223], v224 offset:14080\n"
    "s_waitcnt vmcnt(7)\n"
    "v_cmp_eq_u32 vcc, %27, v246\n"
    "s_cmp_eq_u64 vcc, -1\n"
    "s_cbranch_scc1 PDA1\n"
    "s_mov_b32 s78, 0x40000\n"
    "PLA1:\n"
    "global_load_dword v246, v244, %13 offset:512 sc1\n"
    "s_waitcnt vmcnt(0)\n"
    "v_cmp_eq_u32 vcc, %27, v246\n"
    "s_cmp_eq_u64 vcc, -1\n"
    "s_cbranch_scc1 PDA1\n"
    "s_sleep 1\n"
    "s_sub_u32 s78, s78, 1\n"
    "s_cmp_lg_u32 s78, 0\n"
    "s_cbranch_scc1 PLA1\n"
    "PDA1:\n"
    "v_mfma_f32_16x16x32_bf16 v[124:127], a[236:239], v[204:207], v[124:127]\n"
    "s_waitcnt lgkmcnt(8)\n"
    "s_add_u32 s60, %22, 4\n"
    "v_add_u32 v224, 18432, v234\n"
    "v_mfma_f32_16x16x32_bf16 v[128:131], a[240:243], v[152:155], v[128:131]\n"
    "s_and_b32 s60, s60, 15\n"
    "ds_read_b64_tr_b16 a[224:225], v225 offset:768\n"
    "v_mfma_f32_16x16x32_bf16 v[128:131], a[244:247], v[156:159], v[128:131]\n"
    "s_lshl_b32 s60, s60, 10\n"
    "buffer_load_dwordx4 a[0:3], v230, s[40:43], s60 offen offset:0 sc1\n"
    "v_mfma_f32_16x16x32_bf16 v[132:135], a[240:243], v[168:171], v[132:135]\n"
    "s_and_b32 s61, s61, 15\n"
    "ds_read_b64_tr_b16 a[226:227], v225 offset:4864\n"
    "v_mfma_f32_16x16x32_bf16 v[132:135], a[244:247], v[172:175], v[132:135]\n"
    "s_mul_i32 s61, s61, 36864\n"
    "v_mfma_f32_16x16x32_bf16 v[136:139], a[240:243], v[184:187], v[136:139]\n"
    "s_add_u32 s61, s61, s69\n"
    "ds_read_b64_tr_b16 a[228:229], v225 offset:9984\n"
    "v_mfma_f32_16x16x32_bf16 v[136:139], a[244:247], v[188:191], v[136:139]\n"
    "s_add_u32 m0, s63, 55296\n"
    "buffer_load_dwordx4 a[8:11], v231, s[40:43], s60 offen offset:0 sc1\n"
    "v_mfma_f32_16x16x32_bf16 v[140:143], a[240:243], v[200:203], v[140:143]\n"
    "ds_read_b64_tr_b16 a[230:231], v225 offset:14080\n"
    "v_mfma_f32_16x16x32_bf16 v[140:143], a[244:247], v[204:207], v[140:143]\n"
    "s_waitcnt lgkmcnt(8)\n"
    "v_add_u32 v225, 18432, v235\n"
    "v_mfma_f32_16x16x32_bf16 v[0:3], a[248:251], v[152:155], v[0:3]\n"
    "ds_read_b64_tr_b16 a[232:233], v226 offset:8192\n"
    "v_mfma_f32_16x16x32_bf16 v[0:3], a[252:255], v[156:159], v[0:3]\n"
    "buffer_load_dwordx4 a[16:19], v232, s[40:43], s60 offen offset:0 sc1\n"
    "v_mfma_f32_16x16x32_bf16 v[4:7], a[248:251], v[168:171], v[4:7]\n"
    "ds_read_b64_tr_b16 a[234:235], v226 offset:8704\n"
    "v_mfma_f32_16x16x32_bf16 v[4:7], a[252:255], v[172:175], v[4:7]\n"
    "v_mfma_f32_16x16x32_bf16 v[8:11], a[248:251], v[184:187], v[8:11]\n"
    "ds_read_b64_tr_b16 a[236:237], v226 offset:17408\n"
    "v_mfma_f32_16x16x32_bf16 v[8:11], a[252:255], v[188:191], v[8:11]\n"
    "buffer_load_dwordx4 a[24:27], v233, s[40:43], s60 offen offset:0 sc1\n"
    "v_mfma_f32_16x16x32_bf16 v[12:15], a[248:251], v[200:203], v[12:15]\n"
    "s_waitcnt vmcnt(43)\n"
    "ds_read_b64_tr_b16 a[238:239], v226 offset:17920\n"
    "v_mfma_f32_16x16x32_bf16 v[12:15], a[252:255], v[204:207], v[12:15]\n"
    "s_waitcnt lgkmcnt(8)\n"
    "s_barrier\n"
    "v_add_u32 v226, 18432, v242\n"
    "v_mfma_f32_16x16x32_bf16 v[16:19], a[216:219], v[152:155], v[16:19]\n"
    "s_add_u32 s62, %22, 4\n"
    "ds_read_b64_tr_b16 a[240:241], v224 offset:0\n"
    "v_mfma_f32_16x16x32_bf16 v[16:19], a[220:223], v[156:159], v[16:19]\n"
    "s_and_b32 s62, s62, 15\n"
    "v_mfma_f32_16x16x32_bf16 v[20:23], a[216:219], v[168:171], v[20:23]\n"
    "s_lshl_b32 s62, s62, 10\n"
    "ds_read_b64_tr_b16 a[242:243], v224 offset:4096\n"
    "v_mfma_f32_16x16x32_bf16 v[20:23], a[220:223], v[172:175], v[20:23]\n"
    "buffer_load_dwordx4 a[4:7], v230, s[40:43], s62 offen offset:1024 sc1\n"
    "v_mfma_f32_16x16x32_bf16 v[24:27], a[216:219], v[184:187], v[24:27]\n"
    "ds_read_b64_tr_b16 a[244:245], v224 offset:9216\n"
    "v_mfma_f32_16x16x32_bf16 v[24:27], a[220:223], v[188:191], v[24:27]\n"
    "v_mfma_f32_16x16x32_bf16 v[28:31], a[216:219], v[200:203], v[28:31]\n"
    "ds_read_b64_tr_b16 a[246:247], v224 offset:13312\n"
    "v_mfma_f32_16x16x32_bf16 v[28:31], a[220:223], v[204:207], v[28:31]\n"
    "s_waitcnt lgkmcnt(8)\n"
    "buffer_load_dwordx4 a[12:15], v231, s[40:43], s62 offen offset:1024 sc1\n"
    "v_mfma_f32_16x16x32_bf16 v[32:35], a[224:227], v[152:155], v[32:35]\n"
    "ds_read_b64_tr_b16 a[248:249], v225 offset:0\n"
    "v_mfma_f32_16x16x32_bf16 v[32:35], a[228:231], v[156:159], v[32:35]\n"
    "v_mfma_f32_16x16x32_bf16 v[36:39], a[224:227], v[168:171], v[36:39]\n"
    "ds_read_b64_tr_b16 a[250:251], v225 offset:4096\n"
    "v_mfma_f32_16x16x32_bf16 v[36:39], a[228:231], v[172:175], v[36:39]\n"
    "buffer_load_dwordx4 a[20:23], v232, s[40:43], s62 offen offset:1024 sc1\n"
    "v_mfma_f32_16x16x32_bf16 v[40:43], a[224:227], v[184:187], v[40:43]\n"
    "ds_read_b64_tr_b16 a[252:253], v225 offset:9216\n"
    "v_mfma_f32_16x16x32_bf16 v[40:43], a[228:231], v[188:191], v[40:43]\n"
    "v_mfma_f32_16x16x32_bf16 v[44:47], a[224:227], v[200:203], v[44:47]\n"
    "ds_read_b64_tr_b16 a[254:255], v225 offset:13312\n"
    "v_mfma_f32_16x16x32_bf16 v[44:47], a[228:231], v[204:207], v[44:47]\n"
    "s_waitcnt lgkmcnt(8)\n"
    "buffer_load_dwordx4 a[28:31], v233, s[40:43], s62 offen offset:1024 sc1\n"
    "v_mfma_f32_16x16x32_bf16 v[48:51], a[232:235], v[152:155], v[48:51]\n"
    "s_add_u32 s60, %22, 8\n"
    "ds_read_b64_tr_b16 a[216:217], v224 offset:256\n"
    "v_mfma_f32_16x16x32_bf16 v[48:51], a[236:239], v[156:159], v[48:51]\n"
    "s_and_b32 s60, s60, 15\n"
    "v_mfma_f32_16x16x32_bf16 v[52:55], a[232:235], v[168:171], v[52:55]\n"
    "s_lshl_b32 s60, s60, 10\n"
    "ds_read_b64_tr_b16 a[218:219], v224 offset:4352\n"
    "v_mfma_f32_16x16x32_bf16 v[52:55], a[236:239], v[172:175], v[52:55]\n"
    "buffer_load_dwordx4 a[32:35], v230, s[40:43], s60 offen offset:0 sc1\n"
    "v_mfma_f32_16x16x32_bf16 v[56:59], a[232:235], v[184:187], v[56:59]\n"
    "ds_read_b64_tr_b16 a[220:221], v224 offset:9472\n"
    "v_mfma_f32_16x16x32_bf16 v[56:59], a[236:239], v[188:191], v[56:59]\n"
    "v_mfma_f32_16x16x32_bf16 v[60:63], a[232:235], v[200:203], v[60:63]\n"
    "ds_read_b64_tr_b16 a[222:223], v224 offset:13568\n"
    "v_mfma_f32_16x16x32_bf16 v[60:63], a[236:239], v[204:207], v[60:63]\n"
    "s_waitcnt lgkmcnt(8)\n"
    "s_waitcnt vmcnt(1)\n"
    "buffer_load_dwordx4 a[40:43], v231, s[40:43], s60 offen offset:0 sc1\n"
    "v_mfma_f32_16x16x32_bf16 v[64:67], a[240:243], a[0:3], v[64:67]\n"
    "ds_read_b64_tr_b16 a[224:225], v225 offset:256\n"
    "v_mfma_f32_16x16x32_bf16 v[64:67], a[244:247], a[4:7], v[64:67]\n"
    "v_add_u32 v227, 36864, v234\n"
    "v_mfma_f32_16x16x32_bf16 v[68:71], a[240:243], a[8:11], v[68:71]\n"
    "ds_read_b64_tr_b16 a[226:227], v225 offset:4352\n"
    "v_mfma_f32_16x16x32_bf16 v[68:71], a[244:247], a[12:15], v[68:71]\n"
    "buffer_load_dwordx4 a[48:51], v232, s[40:43], s60 offen offset:0 sc1\n"
    "v_add_u32 v228, 36864, v235\n"
    "v_mfma_f32_16x16x32_bf16 v[72:75], a[240:243], a[16:19], v[72:75]\n"
    "ds_read_b64_tr_b16 a[228:229], v225 offset:9472\n"
    "v_mfma_f32_16x16x32_bf16 v[72:75], a[244:247], a[20:23], v[72:75]\n"
    "v_add_u32 v229, 36864, v242\n"
    "v_mfma_f32_16x16x32_bf16 v[76:79], a[240:243], a[24:27], v[76:79]\n"
    "ds_read_b64_tr_b16 a[230:231], v225 offset:13568\n"
    "v_mfma_f32_16x16x32_bf16 v[76:79], a[244:247], a[28:31], v[76:79]\n"
    "s_waitcnt lgkmcnt(8)\n"
    "buffer_load_dwordx4 a[56:59], v233, s[40:43], s60 offen offset:0 sc1\n"
    "v_mfma_f32_16x16x32_bf16 v[80:83], a[248:251], a[0:3], v[80:83]\n"
    "s_add_u32 s62, %22, 8\n"
    "ds_read_b64_tr_b16 a[232:233], v224 offset:512\n"
    "v_mfma_f32_16x16x32_bf16 v[80:83], a[252:255], a[4:7], v[80:83]\n"
    "s_and_b32 s62, s62, 15\n"
    "v_mfma_f32_16x16x32_bf16 v[84:87], a[248:251], a[8:11], v[84:87]\n"
    "s_lshl_b32 s62, s62, 10\n"
    "ds_read_b64_tr_b16 a[234:235], v224 offset:4608\n"
    "v_mfma_f32_16x16x32_bf16 v[84:87], a[252:255], a[12:15], v[84:87]\n"
    "buffer_load_dwordx4 a[36:39], v230, s[40:43], s62 offen offset:1024 sc1\n"
    "v_mfma_f32_16x16x32_bf16 v[88:91], a[248:251], a[16:19], v[88:91]\n"
    "ds_read_b64_tr_b16 a[236:237], v224 offset:9728\n"
    "v_mfma_f32_16x16x32_bf16 v[88:91], a[252:255], a[20:23], v[88:91]\n"
    "v_mfma_f32_16x16x32_bf16 v[92:95], a[248:251], a[24:27], v[92:95]\n"
    "ds_read_b64_tr_b16 a[238:239], v224 offset:13824\n"
    "v_mfma_f32_16x16x32_bf16 v[92:95], a[252:255], a[28:31], v[92:95]\n"
    "s_waitcnt lgkmcnt(8)\n"
    "buffer_load_dwordx4 a[44:47], v231, s[40:43], s62 offen offset:1024 sc1\n"
    "v_mfma_f32_16x16x32_bf16 v[96:99], a[216:219], a[0:3], v[96:99]\n"
    "ds_read_b64_tr_b16 a[240:241], v225 offset:512\n"
    "v_mfma_f32_16x16x32_bf16 v[96:99], a[220:223], a[4:7], v[96:99]\n"
    "v_mfma_f32_16x16x32_bf16 v[100:103], a[216:219], a[8:11], v[100:103]\n"
    "ds_read_b64_tr_b16 a[242:243], v225 offset:4608\n"
    "v_mfma_f32_16x16x32_bf16 v[100:103], a[220:223], a[12:15], v[100:103]\n"
    "buffer_load_dwordx4 a[52:55], v232, s[40:43], s62 offen offset:1024 sc1\n"
    "v_mfma_f32_16x16x32_bf16 v[104:107], a[216:219], a[16:19], v[104:107]\n"
    "ds_read_b64_tr_b16 a[244:245], v225 offset:9728\n"
    "v_mfma_f32_16x16x32_bf16 v[104:107], a[220:223], a[20:23], v[104:107]\n"
    "v_mfma_f32_16x16x32_bf16 v[108:111], a[216:219], a[24:27], v[108:111]\n"
    "ds_read_b64_tr_b16 a[246:247], v225 offset:13824\n"
    "v_mfma_f32_16x16x32_bf16 v[108:111], a[220:223], a[28:31], v[108:111]\n"
    "s_waitcnt lgkmcnt(8)\n"
    "buffer_load_dwordx4 a[60:63], v233, s[40:43], s62 offen offset:1024 sc1\n"
    "v_mfma_f32_16x16x32_bf16 v[112:115], a[224:227], a[0:3], v[112:115]\n"
    "s_add_u32 s60, %22, 12\n"
    "ds_read_b64_tr_b16 a[248:249], v224 offset:768\n"
    "v_mfma_f32_16x16x32_bf16 v[112:115], a[228:231], a[4:7], v[112:115]\n"
    "s_and_b32 s60, s60, 15\n"
    "v_mfma_f32_16x16x32_bf16 v[116:119], a[224:227], a[8:11], v[116:119]\n"
    "s_lshl_b32 s60, s60, 10\n"
    "ds_read_b64_tr_b16 a[250:251], v224 offset:4864\n"
    "v_mfma_f32_16x16x32_bf16 v[116:119], a[228:231], a[12:15], v[116:119]\n"
    "buffer_load_dwordx4 a[64:67], v230, s[40:43], s60 offen offset:0 sc1\n"
    "v_mfma_f32_16x16x32_bf16 v[120:123], a[224:227], a[16:19], v[120:123]\n"
    "ds_read_b64_tr_b16 a[252:253], v224 offset:9984\n"
    "v_mfma_f32_16x16x32_bf16 v[120:123], a[228:231], a[20:23], v[120:123]\n"
    "v_mfma_f32_16x16x32_bf16 v[124:127], a[224:227], a[24:27], v[124:127]\n"
    "ds_read_b64_tr_b16 a[254:255], v224 offset:14080\n"
    "v_mfma_f32_16x16x32_bf16 v[124:127], a[228:231], a[28:31], v[124:127]\n"
    "s_waitcnt lgkmcnt(8)\n"
    "buffer_load_dwordx4 a[72:75], v231, s[40:43], s60 offen offset:0 sc1\n"
    "v_add_u32 v224, 73728, v234\n"
    "v_mfma_f32_16x16x32_bf16 v[128:131], a[232:235], a[0:3], v[128:131]\n"
    "ds_read_b64_tr_b16 a[216:217], v225 offset:768\n"
    "v_mfma_f32_16x16x32_bf16 v[128:131], a[236:239], a[4:7], v[128:131]\n"
    "v_mfma_f32_16x16x32_bf16 v[132:135], a[232:235], a[8:11], v[132:135]\n"
    "ds_read_b64_tr_b16 a[218:219], v225 offset:4864\n"
    "v_mfma_f32_16x16x32_bf16 v[132:135], a[236:239], a[12:15], v[132:135]\n"
    "buffer_load_dwordx4 a[80:83], v232, s[40:43], s60 offen offset:0 sc1\n"
    "v_mfma_f32_16x16x32_bf16 v[136:139], a[232:235], a[16:19], v[136:139]\n"
    "ds_read_b64_tr_b16 a[220:221], v225 offset:9984\n"
    "v_mfma_f32_16x16x32_bf16 v[136:139], a[236:239], a[20:23], v[136:139]\n"
    "v_mfma_f32_16x16x32_bf16 v[140:143], a[232:235], a[24:27], v[140:143]\n"
    "ds_read_b64_tr_b16 a[222:223], v225 offset:14080\n"
    "v_mfma_f32_16x16x32_bf16 v[140:143], a[236:239], a[28:31], v[140:143]\n"
    "s_waitcnt lgkmcnt(8)\n"
    "buffer_load_dwordx4 a[88:91], v233, s[40:43], s60 offen offset:0 sc1\n"
    "v_add_u32 v225, 73728, v235\n"
    "v_mfma_f32_16x16x32_bf16 v[0:3], a[240:243], a[0:3], v[0:3]\n"
    "s_add_u32 s62, %22, 12\n"
    "ds_read_b64_tr_b16 a[224:225], v226 offset:8192\n"
    "v_mfma_f32_16x16x32_bf16 v[0:3], a[244:247], a[4:7], v[0:3]\n"
    "s_and_b32 s62, s62, 15\n"
    "v_mfma_f32_16x16x32_bf16 v[4:7], a[240:243], a[8:11], v[4:7]\n"
    "s_lshl_b32 s62, s62, 10\n"
    "ds_read_b64_tr_b16 a[226:227], v226 offset:8704\n"
    "v_mfma_f32_16x16x32_bf16 v[4:7], a[244:247], a[12:15], v[4:7]\n"
    "buffer_load_dwordx4 a[68:71], v230, s[40:43], s62 offen offset:1024 sc1\n"
    "v_mfma_f32_16x16x32_bf16 v[8:11], a[240:243], a[16:19], v[8:11]\n"
    "ds_read_b64_tr_b16 a[228:229], v226 offset:17408\n"
    "v_mfma_f32_16x16x32_bf16 v[8:11], a[244:247], a[20:23], v[8:11]\n"
    "v_mfma_f32_16x16x32_bf16 v[12:15], a[240:243], a[24:27], v[12:15]\n"
    "s_waitcnt vmcnt(26)\n"
    "ds_read_b64_tr_b16 a[230:231], v226 offset:17920\n"
    "v_mfma_f32_16x16x32_bf16 v[12:15], a[244:247], a[28:31], v[12:15]\n"
    "s_waitcnt lgkmcnt(8)\n"
    "s_barrier\n"
    "buffer_load_dwordx4 a[76:79], v231, s[40:43], s62 offen offset:1024 sc1\n"
    "v_add_u32 v226, 73728, v242\n"
    "v_mfma_f32_16x16x32_bf16 v[16:19], a[248:251], a[0:3], v[16:19]\n"
    "ds_read_b64_tr_b16 a[232:233], v226 offset:8192\n"
    "v_mfma_f32_16x16x32_bf16 v[16:19], a[252:255], a[4:7], v[16:19]\n"
    "v_mfma_f32_16x16x32_bf16 v[20:23], a[248:251], a[8:11], v[20:23]\n"
    "ds_read_b64_tr_b16 a[234:235], v226 offset:8704\n"
    "v_mfma_f32_16x16x32_bf16 v[20:23], a[252:255], a[12:15], v[20:23]\n"
    "buffer_load_dwordx4 a[84:87], v232, s[40:43], s62 offen offset:1024 sc1\n"
    "v_mfma_f32_16x16x32_bf16 v[24:27], a[248:251], a[16:19], v[24:27]\n"
    "ds_read_b64_tr_b16 a[236:237], v226 offset:17408\n"
    "v_mfma_f32_16x16x32_bf16 v[24:27], a[252:255], a[20:23], v[24:27]\n"
    "v_mfma_f32_16x16x32_bf16 v[28:31], a[248:251], a[24:27], v[28:31]\n"
    "ds_read_b64_tr_b16 a[238:239], v226 offset:17920\n"
    "v_mfma_f32_16x16x32_bf16 v[28:31], a[252:255], a[28:31], v[28:31]\n"
    "s_waitcnt lgkmcnt(8)\n"
    "buffer_load_dwordx4 a[92:95], v233, s[40:43], s62 offen offset:1024 sc1\n"
    "v_mfma_f32_16x16x32_bf16 v[32:35], a[216:219], a[0:3], v[32:35]\n"
    "ds_read_b64_tr_b16 a[240:241], v224 offset:0\n"
    "v_mfma_f32_16x16x32_bf16 v[32:35], a[220:223], a[4:7], v[32:35]\n"
    "v_mfma_f32_16x16x32_bf16 v[36:39], a[216:219], a[8:11], v[36:39]\n"
    "ds_read_b64_tr_b16 a[242:243], v224 offset:4096\n"
    "v_mfma_f32_16x16x32_bf16 v[36:39], a[220:223], a[12:15], v[36:39]\n"
    "global_load_dword v246, v244, %13 offset:-512 sc1\n"
    "s_waitcnt vmcnt(0)\n"
    "v_cmp_eq_u32 vcc, %27, v246\n"
    "s_cmp_eq_u64 vcc, -1\n"
    "s_cbranch_scc1 PDA2\n"
    "s_mov_b32 s78, 0x40000\n"
    "PLA2:\n"
    "global_load_dword v246, v244, %13 offset:-512 sc1\n"
    "s_waitcnt vmcnt(0)\n"
    "v_cmp_eq_u32 vcc, %27, v246\n"
    "s_cmp_eq_u64 vcc, -1\n"
    "s_cbranch_scc1 PDA2\n"
    "s_sleep 1\n"
    "s_sub_u32 s78, s78, 1\n"
    "s_cmp_lg_u32 s78, 0\n"
    "s_cbranch_scc1 PLA2\n"
    "PDA2:\n"
    "v_mfma_f32_16x16x32_bf16 v[40:43], a[216:219], a[16:19], v[40:43]\n"
    "s_add_u32 s60, %22, 6\n"
    "ds_read_b64_tr_b16 a[244:245], v224 offset:9216\n"
    "v_mfma_f32_16x16x32_bf16 v[40:43], a[220:223], a[20:23], v[40:43]\n"
    "s_and_b32 s60, s60, 15\n"
    "v_mfma_f32_16x16x32_bf16 v[44:47], a[216:219], a[24:27], v[44:47]\n"
    "s_lshl_b32 s60, s60, 10\n"
    "ds_read_b64_tr_b16 a[246:247], v224 offset:13312\n"
    "v_mfma_f32_16x16x32_bf16 v[44:47], a[220:223], a[28:31], v[44:47]\n"
    "s_waitcnt lgkmcnt(8)\n"
    "buffer_load_dwordx4 a[96:99], v230, s[40:43], s60 offen offset:0 sc1\n"
    "v_mfma_f32_16x16x32_bf16 v[48:51], a[224:227], a[0:3], v[48:51]\n"
    "ds_read_b64_tr_b16 a[248:249], v225 offset:0\n"
    "v_mfma_f32_16x16x32_bf16 v[48:51], a[228:231], a[4:7], v[48:51]\n"
    "v_mfma_f32_16x16x32_bf16 v[52:55], a[224:227], a[8:11], v[52:55]\n"
    "ds_read_b64_tr_b16 a[250:251], v225 offset:4096\n"
    "v_mfma_f32_16x16x32_bf16 v[52:55], a[228:231], a[12:15], v[52:55]\n"
    "buffer_load_dwordx4 a[104:107], v231, s[40:43], s60 offen offset:0 sc1\n"
    "v_mfma_f32_16x16x32_bf16 v[56:59], a[224:227], a[16:19], v[56:59]\n"
    "ds_read_b64_tr_b16 a[252:253], v225 offset:9216\n"
    "v_mfma_f32_16x16x32_bf16 v[56:59], a[228:231], a[20:23], v[56:59]\n"
    "v_mfma_f32_16x16x32_bf16 v[60:63], a[224:227], a[24:27], v[60:63]\n"
    "ds_read_b64_tr_b16 a[254:255], v225 offset:13312\n"
    "v_mfma_f32_16x16x32_bf16 v[60:63], a[228:231], a[28:31], v[60:63]\n"
    "s_waitcnt lgkmcnt(8)\n"
    "s_waitcnt vmcnt(11)\n"
    "buffer_load_dwordx4 a[112:115], v232, s[40:43], s60 offen offset:0 sc1\n"
    "v_mfma_f32_16x16x32_bf16 v[48:51], a[232:235], a[32:35], v[48:51]\n"
    "ds_read_b64_tr_b16 a[216:217], v226 offset:26624\n"
    "v_mfma_f32_16x16x32_bf16 v[48:51], a[236:239], a[36:39], v[48:51]\n"
    "v_mfma_f32_16x16x32_bf16 v[52:55], a[232:235], a[40:43], v[52:55]\n"
    "ds_read_b64_tr_b16 a[218:219], v226 offset:27136\n"
    "v_mfma_f32_16x16x32_bf16 v[52:55], a[236:239], a[44:47], v[52:55]\n"
    "buffer_load_dwordx4 a[120:123], v233, s[40:43], s60 offen offset:0 sc1\n"
    "v_mfma_f32_16x16x32_bf16 v[56:59], a[232:235], a[48:51], v[56:59]\n"
    "s_add_u32 s62, %22, 6\n"
    "ds_read_b64_tr_b16 a[220:221], v226 offset:35840\n"
    "v_mfma_f32_16x16x32_bf16 v[56:59], a[236:239], a[52:55], v[56:59]\n"
    "s_and_b32 s62, s62, 15\n"
    "v_mfma_f32_16x16x32_bf16 v[60:63], a[232:235], a[56:59], v[60:63]\n"
    "s_lshl_b32 s62, s62, 10\n"
    "ds_read_b64_tr_b16 a[222:223], v226 offset:36352\n"
    "v_mfma_f32_16x16x32_bf16 v[60:63], a[236:239], a[60:63], v[60:63]\n"
    "s_waitcnt lgkmcnt(8)\n"
    "buffer_load_dwordx4 a[100:103], v230, s[40:43], s62 offen offset:1024 sc1\n"
    "v_mfma_f32_16x16x32_bf16 v[64:67], a[240:243], a[32:35], v[64:67]\n"
    "ds_read_b64_tr_b16 a[224:225], v224 offset:18432\n"
    "buffer_load_dwordx4 v220, s[44:47], s61 offen offset:0 lds\n"
    "buffer_load_dwordx4 v221, s[44:47], s61 offen offset:1024 lds\n"
    "buffer_load_dwordx4 v222, s[44:47], s61 offen offset:2048 lds\n"
    "buffer_load_dwordx4 v223, s[44:47], s61 offen offset:3072 lds\n"
    "s_add_u32 m0, s65, 55296\n"
    "s_mov_b64 exec, s[66:67]\n"
    "buffer_load_dwordx4 v250, s[44:47], s61 offen lds\n"
    "s_mov_b64 exec, -1\n"
    "v_mfma_f32_16x16x32_bf16 v[64:67], a[244:247], a[36:39], v[64:67]\n"
    "v_mfma_f32_16x16x32_bf16 v[68:71], a[240:243], a[40:43], v[68:71]\n"
    "ds_read_b64_tr_b16 a[226:227], v224 offset:22528\n"
    "v_mfma_f32_16x16x32_bf16 v[68:71], a[244:247], a[44:47], v[68:71]\n"
    "buffer_load_dwordx4 a[108:111], v231, s[40:43], s62 offen offset:1024 sc1\n"
    "v_mfma_f32_16x16x32_bf16 v[72:75], a[240:243], a[48:51], v[72:75]\n"
    "ds_read_b64_tr_b16 a[228:229], v224 offset:27648\n"
    "v_mfma_f32_16x16x32_bf16 v[72:75], a[244:247], a[52:55], v[72:75]\n"
    "v_mfma_f32_16x16x32_bf16 v[76:79], a[240:243], a[56:59], v[76:79]\n"
    "ds_read_b64_tr_b16 a[230:231], v224 offset:31744\n"
    "v_mfma_f32_16x16x32_bf16 v[76:79], a[244:247], a[60:63], v[76:79]\n"
    "s_waitcnt lgkmcnt(8)\n"
    "buffer_load_dwordx4 a[116:119], v232, s[40:43], s62 offen offset:1024 sc1\n"
    "v_mfma_f32_16x16x32_bf16 v[80:83], a[248:251], a[32:35], v[80:83]\n"
    "ds_read_b64_tr_b16 a[232:233], v225 offset:18432\n"
    "v_mfma_f32_16x16x32_bf16 v[80:83], a[252:255], a[36:39], v[80:83]\n"
    "v_mfma_f32_16x16x32_bf16 v[84:87], a[248:251], a[40:43], v[84:87]\n"
    "ds_read_b64_tr_b16 a[234:235], v225 offset:22528\n"
    "v_mfma_f32_16x16x32_bf16 v[84:87], a[252:255], a[44:47], v[84:87]\n"
    "buffer_load_dwordx4 a[124:127], v233, s[40:43], s62 offen offset:1024 sc1\n"
    "v_mfma_f32_16x16x32_bf16 v[88:91], a[248:251], a[48:51], v[88:91]\n"
    "s_add_u32 s60, %22, 10\n"
    "ds_read_b64_tr_b16 a[236:237], v225 offset:27648\n"
    "v_mfma_f32_16x16x32_bf16 v[88:91], a[252:255], a[52:55], v[88:91]\n"
    "s_and_b32 s60, s60, 15\n"
    "v_mfma_f32_16x16x32_bf16 v[92:95], a[248:251], a[56:59], v[92:95]\n"
    "s_lshl_b32 s60, s60, 10\n"
    "ds_read_b64_tr_b16 a[238:239], v225 offset:31744\n"
    "v_mfma_f32_16x16x32_bf16 v[92:95], a[252:255], a[60:63], v[92:95]\n"
    "s_waitcnt lgkmcnt(8)\n"
    "s_waitcnt vmcnt(14)\n"
    "buffer_load_dwordx4 a[128:131], v230, s[40:43], s60 offen offset:0 sc1\n"
    "v_mfma_f32_16x16x32_bf16 v[48:51], a[216:219], a[64:67], v[48:51]\n"
    "ds_read_b64_tr_b16 a[240:241], v226 offset:45056\n"
    "v_mfma_f32_16x16x32_bf16 v[48:51], a[220:223], a[68:71], v[48:51]\n"
    "v_mfma_f32_16x16x32_bf16 v[52:55], a[216:219], a[72:75], v[52:55]\n"
    "ds_read_b64_tr_b16 a[242:243], v226 offset:45568\n"
    "v_mfma_f32_16x16x32_bf16 v[52:55], a[220:223], a[76:79], v[52:55]\n"
    "buffer_load_dwordx4 a[136:139], v231, s[40:43], s60 offen offset:0 sc1\n"
    "v_mfma_f32_16x16x32_bf16 v[56:59], a[216:219], a[80:83], v[56:59]\n"
    "ds_read_b64_tr_b16 a[244:245], v226 offset:54272\n"
    "v_mfma_f32_16x16x32_bf16 v[56:59], a[220:223], a[84:87], v[56:59]\n"
    "v_mfma_f32_16x16x32_bf16 v[60:63], a[216:219], a[88:91], v[60:63]\n"
    "ds_read_b64_tr_b16 a[246:247], v226 offset:54784\n"
    "v_mfma_f32_16x16x32_bf16 v[60:63], a[220:223], a[92:95], v[60:63]\n"
    "s_waitcnt lgkmcnt(8)\n"
    "buffer_load_dwordx4 a[144:147], v232, s[40:43], s60 offen offset:0 sc1\n"
    "v_mfma_f32_16x16x32_bf16 v[64:67], a[224:227], a[64:67], v[64:67]\n"
    "ds_read_b64_tr_b16 a[248:249], v224 offset:36864\n"
    "v_mfma_f32_16x16x32_bf16 v[64:67], a[228:231], a[68:71], v[64:67]\n"
    "v_mfma_f32_16x16x32_bf16 v[68:71], a[224:227], a[72:75], v[68:71]\n"
    "ds_read_b64_tr_b16 a[250:251], v224 offset:40960\n"
    "v_mfma_f32_16x16x32_bf16 v[68:71], a[228:231], a[76:79], v[68:71]\n"
    "buffer_load_dwordx4 a[152:155], v233, s[40:43], s60 offen offset:0 sc1\n"
    "v_mfma_f32_16x16x32_bf16 v[72:75], a[224:227], a[80:83], v[72:75]\n"
    "s_add_u32 s62, %22, 10\n"
    "ds_read_b64_tr_b16 a[252:253], v224 offset:46080\n"
    "v_mfma_f32_16x16x32_bf16 v[72:75], a[228:231], a[84:87], v[72:75]\n"
    "s_and_b32 s62, s62, 15\n"
    "v_mfma_f32_16x16x32_bf16 v[76:79], a[224:227], a[88:91], v[76:79]\n"
    "s_lshl_b32 s62, s62, 10\n"
    "ds_read_b64_tr_b16 a[254:255], v224 offset:50176\n"
    "v_mfma_f32_16x16x32_bf16 v[76:79], a[228:231], a[92:95], v[76:79]\n"
    "s_waitcnt lgkmcnt(8)\n"
    "buffer_load_dwordx4 a[132:135], v230, s[40:43], s62 offen offset:1024 sc1\n"
    "v_mfma_f32_16x16x32_bf16 v[80:83], a[232:235], a[64:67], v[80:83]\n"
    "ds_read_b64_tr_b16 a[216:217], v225 offset:36864\n"
    "v_mfma_f32_16x16x32_bf16 v[80:83], a[236:239], a[68:71], v[80:83]\n"
    "v_mfma_f32_16x16x32_bf16 v[84:87], a[232:235], a[72:75], v[84:87]\n"
    "ds_read_b64_tr_b16 a[218:219], v225 offset:40960\n"
    "v_mfma_f32_16x16x32_bf16 v[84:87], a[236:239], a[76:79], v[84:87]\n"
    "buffer_load_dwordx4 a[140:143], v231, s[40:43], s62 offen offset:1024 sc1\n"
    "v_mfma_f32_16x16x32_bf16 v[88:91], a[232:235], a[80:83], v[88:91]\n"
    "ds_read_b64_tr_b16 a[220:221], v225 offset:46080\n"
    "v_mfma_f32_16x16x32_bf16 v[88:91], a[236:239], a[84:87], v[88:91]\n"
    "v_mfma_f32_16x16x32_bf16 v[92:95], a[232:235], a[88:91], v[92:95]\n"
    "s_waitcnt vmcnt(9)\n"
    "ds_read_b64_tr_b16 a[222:223], v225 offset:50176\n"
    "v_mfma_f32_16x16x32_bf16 v[92:95], a[236:239], a[92:95], v[92:95]\n"
    "s_waitcnt lgkmcnt(8)\n"
    "s_waitcnt vmcnt(6)\n"
    "s_barrier\n"
    "buffer_load_dwordx4 a[148:151], v232, s[40:43], s62 offen offset:1024 sc1\n"
    "v_mfma_f32_16x16x32_bf16 v[48:51], a[240:243], a[96:99], v[48:51]\n"
    "ds_read_b64_tr_b16 a[224:225], v229 offset:8192\n"
    "v_mfma_f32_16x16x32_bf16 v[48:51], a[244:247], a[100:103], v[48:51]\n"
    "v_mfma_f32_16x16x32_bf16 v[52:55], a[240:243], a[104:107], v[52:55]\n"
    "ds_read_b64_tr_b16 a[226:227], v229 offset:8704\n"
    "v_mfma_f32_16x16x32_bf16 v[52:55], a[244:247], a[108:111], v[52:55]\n"
    "buffer_load_dwordx4 a[156:159], v233, s[40:43], s62 offen offset:1024 sc1\n"
    "v_mfma_f32_16x16x32_bf16 v[56:59], a[240:243], a[112:115], v[56:59]\n"
    "s_add_u32 s60, %22, 14\n"
    "ds_read_b64_tr_b16 a[228:229], v229 offset:17408\n"
    "v_mfma_f32_16x16x32_bf16 v[56:59], a[244:247], a[116:119], v[56:59]\n"
    "s_and_b32 s60, s60, 15\n"
    "v_mfma_f32_16x16x32_bf16 v[60:63], a[240:243], a[120:123], v[60:63]\n"
    "s_lshl_b32 s60, s60, 10\n"
    "ds_read_b64_tr_b16 a[230:231], v229 offset:17920\n"
    "v_mfma_f32_16x16x32_bf16 v[60:63], a[244:247], a[124:127], v[60:63]\n"
    "s_waitcnt lgkmcnt(8)\n"
    "buffer_load_dwordx4 a[160:163], v230, s[40:43], s60 offen offset:0 sc1\n"
    "v_mfma_f32_16x16x32_bf16 v[64:67], a[248:251], a[96:99], v[64:67]\n"
    "ds_read_b64_tr_b16 a[232:233], v227 offset:0\n"
    "v_mfma_f32_16x16x32_bf16 v[64:67], a[252:255], a[100:103], v[64:67]\n"
    "v_mfma_f32_16x16x32_bf16 v[68:71], a[248:251], a[104:107], v[68:71]\n"
    "ds_read_b64_tr_b16 a[234:235], v227 offset:4096\n"
    "v_mfma_f32_16x16x32_bf16 v[68:71], a[252:255], a[108:111], v[68:71]\n"
    "buffer_load_dwordx4 a[168:171], v231, s[40:43], s60 offen offset:0 sc1\n"
    "v_mfma_f32_16x16x32_bf16 v[72:75], a[248:251], a[112:115], v[72:75]\n"
    "ds_read_b64_tr_b16 a[236:237], v227 offset:9216\n"
    "v_mfma_f32_16x16x32_bf16 v[72:75], a[252:255], a[116:119], v[72:75]\n"
    "v_mfma_f32_16x16x32_bf16 v[76:79], a[248:251], a[120:123], v[76:79]\n"
    "ds_read_b64_tr_b16 a[238:239], v227 offset:13312\n"
    "v_mfma_f32_16x16x32_bf16 v[76:79], a[252:255], a[124:127], v[76:79]\n"
    "s_waitcnt lgkmcnt(8)\n"
    "buffer_load_dwordx4 a[176:179], v232, s[40:43], s60 offen offset:0 sc1\n"
    "v_mfma_f32_16x16x32_bf16 v[80:83], a[216:219], a[96:99], v[80:83]\n"
    "ds_read_b64_tr_b16 a[240:241], v228 offset:0\n"
    "v_mfma_f32_16x16x32_bf16 v[80:83], a[220:223], a[100:103], v[80:83]\n"
    "v_mfma_f32_16x16x32_bf16 v[84:87], a[216:219], a[104:107], v[84:87]\n"
    "ds_read_b64_tr_b16 a[242:243], v228 offset:4096\n"
    "v_mfma_f32_16x16x32_bf16 v[84:87], a[220:223], a[108:111], v[84:87]\n"
    "buffer_load_dwordx4 a[184:187], v233, s[40:43], s60 offen offset:0 sc1\n"
    "v_mfma_f32_16x16x32_bf16 v[88:91], a[216:219], a[112:115], v[88:91]\n"
    "s_add_u32 s62, %22, 14\n"
    "ds_read_b64_tr_b16 a[244:245], v228 offset:9216\n"
    "v_mfma_f32_16x16x32_bf16 v[88:91], a[220:223], a[116:119], v[88:91]\n"
    "s_and_b32 s62, s62, 15\n"
    "v_mfma_f32_16x16x32_bf16 v[92:95], a[216:219], a[120:123], v[92:95]\n"
    "s_lshl_b32 s62, s62, 10\n"
    "ds_read_b64_tr_b16 a[246:247], v228 offset:13312\n"
    "v_mfma_f32_16x16x32_bf16 v[92:95], a[220:223], a[124:127], v[92:95]\n"
    "s_waitcnt lgkmcnt(8)\n"
    "s_waitcnt vmcnt(4)\n"
    "buffer_load_dwordx4 a[164:167], v230, s[40:43], s62 offen offset:1024 sc1\n"
    "v_mfma_f32_16x16x32_bf16 v[48:51], a[224:227], a[128:131], v[48:51]\n"
    "s_mov_b64 s[56:57], %23\n"
    "ds_read_b64_tr_b16 a[248:249], v229 offset:26624\n"
    "v_mfma_f32_16x16x32_bf16 v[48:51], a[228:231], a[132:135], v[48:51]\n"
    "s_and_b32 s57, s57, 0xffff\n"
    "v_mfma_f32_16x16x32_bf16 v[52:55], a[224:227], a[136:139], v[52:55]\n"
    "s_mov_b32 s58, 5308416\n"
    "ds_read_b64_tr_b16 a[250:251], v229 offset:27136\n"
    "v_mfma_f32_16x16x32_bf16 v[52:55], a[228:231], a[140:143], v[52:55]\n"
    "s_mov_b32 s59, 0x20000\n"
    "buffer_load_dwordx4 a[172:175], v231, s[40:43], s62 offen offset:1024 sc1\n"
    "v_mfma_f32_16x16x32_bf16 v[56:59], a[224:227], a[144:147], v[56:59]\n"
    "s_lshr_b32 s74, %6, 1\n"
    "ds_read_b64_tr_b16 a[252:253], v229 offset:35840\n"
    "v_mfma_f32_16x16x32_bf16 v[56:59], a[228:231], a[148:151], v[56:59]\n"
    "s_mul_i32 s75, s74, 294912\n"
    "v_mfma_f32_16x16x32_bf16 v[60:63], a[224:227], a[152:155], v[60:63]\n"
    "s_lshl_b32 s76, s74, 11\n"
    "ds_read_b64_tr_b16 a[254:255], v229 offset:36352\n"
    "v_mfma_f32_16x16x32_bf16 v[60:63], a[228:231], a[156:159], v[60:63]\n"
    "s_waitcnt lgkmcnt(8)\n"
    "s_and_b32 s74, %6, 1\n"
    "buffer_load_dwordx4 a[180:183], v232, s[40:43], s62 offen offset:1024 sc1\n"
    "v_mfma_f32_16x16x32_bf16 v[64:67], a[232:235], a[128:131], v[64:67]\n"
    "s_lshl_b32 s74, s74, 12\n"
    "ds_read_b64_tr_b16 a[216:217], v227 offset:18432\n"
    "v_mfma_f32_16x16x32_bf16 v[64:67], a[236:239], a[132:135], v[64:67]\n"
    "s_add_u32 s76, s76, s74\n"
    "v_mfma_f32_16x16x32_bf16 v[68:71], a[232:235], a[136:139], v[68:71]\n"
    "s_lshr_b32 s77, s76, 1\n"
    "ds_read_b64_tr_b16 a[218:219], v227 offset:22528\n"
    "v_mfma_f32_16x16x32_bf16 v[68:71], a[236:239], a[140:143], v[68:71]\n"
    "s_add_u32 s76, s76, %7\n"
    "buffer_load_dwordx4 a[188:191], v233, s[40:43], s62 offen offset:1024 sc1\n"
    "v_mfma_f32_16x16x32_bf16 v[72:75], a[232:235], a[144:147], v[72:75]\n"
    "s_add_u32 s77, s77, %7\n"
    "ds_read_b64_tr_b16 a[220:221], v227 offset:27648\n"
    "v_lshrrev_b32 v146, 2, v237\n"
    "v_mfma_f32_16x16x32_bf16 v[72:75], a[236:239], a[148:151], v[72:75]\n"
    "v_mul_u32_u24 v146, 9216, v146\n"
    "v_add_u32 v146, %24, v146\n"
    "v_mfma_f32_16x16x32_bf16 v[76:79], a[232:235], a[152:155], v[76:79]\n"
    "ds_read_b64_tr_b16 a[222:223], v227 offset:31744\n"
    "v_bfe_u32 v147, v237, 4, 1\n"
    "v_mfma_f32_16x16x32_bf16 v[76:79], a[236:239], a[156:159], v[76:79]\n"
    "s_waitcnt lgkmcnt(8)\n"
    "v_lshlrev_b32 v147, 1, v147\n"
    "v_xor_b32 v147, v147, v237\n"
    "v_mfma_f32_16x16x32_bf16 v[80:83], a[240:243], a[128:131], v[80:83]\n"
    "ds_read_b64_tr_b16 a[224:225], v228 offset:18432\n"
    "v_and_b32 v147, 3, v147\n"
    "v_mfma_f32_16x16x32_bf16 v[80:83], a[244:247], a[132:135], v[80:83]\n"
    "v_lshl_add_u32 v144, v147, 4, v146\n"
    "v_add_u32 v145, 146432, v144\n"
    "v_mfma_f32_16x16x32_bf16 v[84:87], a[240:243], a[136:139], v[84:87]\n"
    "s_add_u32 m0, s76, 0\n"
    "ds_read_b64_tr_b16 a[226:227], v228 offset:22528\n"
    "v_mfma_f32_16x16x32_bf16 v[84:87], a[244:247], a[140:143], v[84:87]\n"
    "s_add_u32 s74, s75, 0\n"
    "buffer_load_dwordx4 v144, s[56:59], s74 offen lds\n"
    "v_mfma_f32_16x16x32_bf16 v[88:91], a[240:243], a[144:147], v[88:91]\n"
    "ds_read_b64_tr_b16 a[228:229], v228 offset:27648\n"
    "v_mfma_f32_16x16x32_bf16 v[88:91], a[244:247], a[148:151], v[88:91]\n"
    "v_mfma_f32_16x16x32_bf16 v[92:95], a[240:243], a[152:155], v[92:95]\n"
    "ds_read_b64_tr_b16 a[230:231], v228 offset:31744\n"
    "v_mfma_f32_16x16x32_bf16 v[92:95], a[244:247], a[156:159], v[92:95]\n"
    "s_waitcnt lgkmcnt(8)\n"
    "s_waitcnt vmcnt(1)\n"
    "buffer_load_dwordx4 v145, s[56:59], s74 offen offset:1024 lds\n"
    "v_mfma_f32_16x16x32_bf16 v[48:51], a[248:251], a[160:163], v[48:51]\n"
    "s_add_u32 m0, s76, 8192\n"
    "ds_read_b64_tr_b16 a[232:233], v224 offset:256\n"
    "v_mfma_f32_16x16x32_bf16 v[48:51], a[252:255], a[164:167], v[48:51]\n"
    "s_add_u32 s74, s75, 589824\n"
    "v_mfma_f32_16x16x32_bf16 v[52:55], a[248:251], a[168:171], v[52:55]\n"
    "ds_read_b64_tr_b16 a[234:235], v224 offset:4352\n"
    "v_mfma_f32_16x16x32_bf16 v[52:55], a[252:255], a[172:175], v[52:55]\n"
    "v_cvt_pk_bf16_f32 v48, v48, v49\n"
    "v_cvt_pk_bf16_f32 v49, v50, v51\n"
    "v_mfma_f32_16x16x32_bf16 v[56:59], a[248:251], a[176:179], v[56:59]\n"
    "ds_read_b64_tr_b16 a[236:237], v224 offset:9472\n"
    "v_mfma_f32_16x16x32_bf16 v[56:59], a[252:255], a[180:183], v[56:59]\n"
    "buffer_store_dwordx2 v[48:49], v216, s[52:55], s71 offen sc1\n"
    "v_cvt_pk_bf16_f32 v52, v52, v53\n"
    "v_cvt_pk_bf16_f32 v53, v54, v55\n"
    "v_mfma_f32_16x16x32_bf16 v[60:63], a[248:251], a[184:187], v[60:63]\n"
    "ds_read_b64_tr_b16 a[238:239], v224 offset:13568\n"
    "v_mfma_f32_16x16x32_bf16 v[60:63], a[252:255], a[188:191], v[60:63]\n"
    "s_waitcnt lgkmcnt(8)\n"
    "v_cvt_pk_bf16_f32 v56, v56, v57\n"
    "v_cvt_pk_bf16_f32 v57, v58, v59\n"
    "v_mfma_f32_16x16x32_bf16 v[64:67], a[216:219], a[160:163], v[64:67]\n"
    "ds_read_b64_tr_b16 a[240:241], v224 offset:18688\n"
    "v_mfma_f32_16x16x32_bf16 v[64:67], a[220:223], a[164:167], v[64:67]\n"
    "v_cvt_pk_bf16_f32 v60, v60, v61\n"
    "v_cvt_pk_bf16_f32 v61, v62, v63\n"
    "v_mfma_f32_16x16x32_bf16 v[68:71], a[216:219], a[168:171], v[68:71]\n"
    "ds_read_b64_tr_b16 a[242:243], v224 offset:22784\n"
    "v_mfma_f32_16x16x32_bf16 v[68:71], a[220:223], a[172:175], v[68:71]\n"
    "v_mfma_f32_16x16x32_bf16 v[72:75], a[216:219], a[176:179], v[72:75]\n"
    "ds_read_b64_tr_b16 a[244:245], v224 offset:27904\n"
    "v_mfma_f32_16x16x32_bf16 v[72:75], a[220:223], a[180:183], v[72:75]\n"
    "v_mfma_f32_16x16x32_bf16 v[76:79], a[216:219], a[184:187], v[76:79]\n"
    "ds_read_b64_tr_b16 a[246:247], v224 offset:32000\n"
    "v_mfma_f32_16x16x32_bf16 v[76:79], a[220:223], a[188:191], v[76:79]\n"
    "s_waitcnt lgkmcnt(8)\n"
    "v_mfma_f32_16x16x32_bf16 v[80:83], a[224:227], a[160:163], v[80:83]\n"
    "ds_read_b64_tr_b16 a[248:249], v224 offset:37120\n"
    "v_mfma_f32_16x16x32_bf16 v[80:83], a[228:231], a[164:167], v[80:83]\n"
    "buffer_store_dwordx2 v[52:53], v217, s[52:55], s71 offen sc1\n"
    "v_mfma_f32_16x16x32_bf16 v[84:87], a[224:227], a[168:171], v[84:87]\n"
    "ds_read_b64_tr_b16 a[250:251], v224 offset:41216\n"
    "v_mfma_f32_16x16x32_bf16 v[84:87], a[228:231], a[172:175], v[84:87]\n"
    "v_cvt_pk_bf16_f32 v64, v64, v65\n"
    "v_cvt_pk_bf16_f32 v65, v66, v67\n"
    "v_mfma_f32_16x16x32_bf16 v[88:91], a[224:227], a[176:179], v[88:91]\n"
    "ds_read_b64_tr_b16 a[252:253], v224 offset:46336\n"
    "v_cvt_pk_bf16_f32 v66, v80, v81\n"
    "v_mfma_f32_16x16x32_bf16 v[88:91], a[228:231], a[180:183], v[88:91]\n"
    "v_cvt_pk_bf16_f32 v67, v82, v83\n"
    "v_cvt_pk_bf16_f32 v68, v68, v69\n"
    "v_mfma_f32_16x16x32_bf16 v[92:95], a[224:227], a[184:187], v[92:95]\n"
    "ds_read_b64_tr_b16 a[254:255], v224 offset:50432\n"
    "v_cvt_pk_bf16_f32 v69, v70, v71\n"
    "v_mfma_f32_16x16x32_bf16 v[92:95], a[228:231], a[188:191], v[92:95]\n"
    "s_waitcnt lgkmcnt(8)\n"
    "v_cvt_pk_bf16_f32 v70, v84, v85\n"
    "v_cvt_pk_bf16_f32 v71, v86, v87\n"
    "v_mfma_f32_16x16x32_bf16 v[96:99], a[232:235], a[32:35], v[96:99]\n"
    "ds_read_b64_tr_b16 a[216:217], v227 offset:256\n"
    "v_cvt_pk_bf16_f32 v72, v72, v73\n"
    "v_mfma_f32_16x16x32_bf16 v[96:99], a[236:239], a[36:39], v[96:99]\n"
    "v_cvt_pk_bf16_f32 v73, v74, v75\n"
    "v_cvt_pk_bf16_f32 v74, v88, v89\n"
    "v_mfma_f32_16x16x32_bf16 v[100:103], a[232:235], a[40:43], v[100:103]\n"
    "ds_read_b64_tr_b16 a[218:219], v227 offset:4352\n"
    "v_cvt_pk_bf16_f32 v75, v90, v91\n"
    "v_mfma_f32_16x16x32_bf16 v[100:103], a[236:239], a[44:47], v[100:103]\n"
    "v_cvt_pk_bf16_f32 v76, v76, v77\n"
    "v_cvt_pk_bf16_f32 v77, v78, v79\n"
    "v_mfma_f32_16x16x32_bf16 v[104:107], a[232:235], a[48:51], v[104:107]\n"
    "ds_read_b64_tr_b16 a[220:221], v227 offset:9472\n"
    "v_cvt_pk_bf16_f32 v78, v92, v93\n"
    "v_mfma_f32_16x16x32_bf16 v[104:107], a[236:239], a[52:55], v[104:107]\n"
    "buffer_store_dwordx2 v[56:57], v218, s[52:55], s71 offen sc1\n"
    "v_cvt_pk_bf16_f32 v79, v94, v95\n"
    "v_mfma_f32_16x16x32_bf16 v[108:111], a[232:235], a[56:59], v[108:111]\n"
    "ds_read_b64_tr_b16 a[222:223], v227 offset:13568\n"
    "v_mfma_f32_16x16x32_bf16 v[108:111], a[236:239], a[60:63], v[108:111]\n"
    "s_waitcnt lgkmcnt(8)\n"
    "v_mfma_f32_16x16x32_bf16 v[96:99], a[240:243], a[64:67], v[96:99]\n"
    "ds_read_b64_tr_b16 a[224:225], v227 offset:18688\n"
    "v_mfma_f32_16x16x32_bf16 v[96:99], a[244:247], a[68:71], v[96:99]\n"
    "v_mfma_f32_16x16x32_bf16 v[100:103], a[240:243], a[72:75], v[100:103]\n"
    "ds_read_b64_tr_b16 a[226:227], v227 offset:22784\n"
    "v_mfma_f32_16x16x32_bf16 v[100:103], a[244:247], a[76:79], v[100:103]\n"
    "v_mfma_f32_16x16x32_bf16 v[104:107], a[240:243], a[80:83], v[104:107]\n"
    "ds_read_b64_tr_b16 a[228:229], v227 offset:27904\n"
    "v_mfma_f32_16x16x32_bf16 v[104:107], a[244:247], a[84:87], v[104:107]\n"
    "v_mfma_f32_16x16x32_bf16 v[108:111], a[240:243], a[88:91], v[108:111]\n"
    "ds_read_b64_tr_b16 a[230:231], v227 offset:32000\n"
    "v_mfma_f32_16x16x32_bf16 v[108:111], a[244:247], a[92:95], v[108:111]\n"
    "s_waitcnt lgkmcnt(8)\n"
    "v_mfma_f32_16x16x32_bf16 v[96:99], a[248:251], a[96:99], v[96:99]\n"
    "ds_read_b64_tr_b16 a[232:233], v225 offset:256\n"
    "v_mfma_f32_16x16x32_bf16 v[96:99], a[252:255], a[100:103], v[96:99]\n"
    "buffer_store_dwordx2 v[60:61], v219, s[52:55], s71 offen sc1\n"
    "v_mfma_f32_16x16x32_bf16 v[100:103], a[248:251], a[104:107], v[100:103]\n"
    "ds_read_b64_tr_b16 a[234:235], v225 offset:4352\n"
    "v_mfma_f32_16x16x32_bf16 v[100:103], a[252:255], a[108:111], v[100:103]\n"
    "v_mfma_f32_16x16x32_bf16 v[104:107], a[248:251], a[112:115], v[104:107]\n"
    "ds_read_b64_tr_b16 a[236:237], v225 offset:9472\n"
    "v_mfma_f32_16x16x32_bf16 v[104:107], a[252:255], a[116:119], v[104:107]\n"
    "v_mfma_f32_16x16x32_bf16 v[108:111], a[248:251], a[120:123], v[108:111]\n"
    "ds_read_b64_tr_b16 a[238:239], v225 offset:13568\n"
    "v_mfma_f32_16x16x32_bf16 v[108:111], a[252:255], a[124:127], v[108:111]\n"
    "s_waitcnt lgkmcnt(8)\n"
    "v_mfma_f32_16x16x32_bf16 v[96:99], a[216:219], a[128:131], v[96:99]\n"
    "ds_read_b64_tr_b16 a[240:241], v225 offset:18688\n"
    "v_mfma_f32_16x16x32_bf16 v[96:99], a[220:223], a[132:135], v[96:99]\n"
    "v_mfma_f32_16x16x32_bf16 v[100:103], a[216:219], a[136:139], v[100:103]\n"
    "ds_read_b64_tr_b16 a[242:243], v225 offset:22784\n"
    "v_mfma_f32_16x16x32_bf16 v[100:103], a[220:223], a[140:143], v[100:103]\n"
    "v_mfma_f32_16x16x32_bf16 v[104:107], a[216:219], a[144:147], v[104:107]\n"
    "ds_read_b64_tr_b16 a[244:245], v225 offset:27904\n"
    "v_mfma_f32_16x16x32_bf16 v[104:107], a[220:223], a[148:151], v[104:107]\n"
    "buffer_store_dwordx4 v[64:67], v216, s[52:55], s70 offen offset:0 sc1\n"
    "v_mfma_f32_16x16x32_bf16 v[108:111], a[216:219], a[152:155], v[108:111]\n"
    "ds_read_b64_tr_b16 a[246:247], v225 offset:32000\n"
    "v_mfma_f32_16x16x32_bf16 v[108:111], a[220:223], a[156:159], v[108:111]\n"
    "s_waitcnt lgkmcnt(8)\n"
    "v_mfma_f32_16x16x32_bf16 v[96:99], a[224:227], a[160:163], v[96:99]\n"
    "ds_read_b64_tr_b16 a[248:249], v225 offset:37120\n"
    "v_mfma_f32_16x16x32_bf16 v[96:99], a[228:231], a[164:167], v[96:99]\n"
    "v_mfma_f32_16x16x32_bf16 v[100:103], a[224:227], a[168:171], v[100:103]\n"
    "ds_read_b64_tr_b16 a[250:251], v225 offset:41216\n"
    "v_mfma_f32_16x16x32_bf16 v[100:103], a[228:231], a[172:175], v[100:103]\n"
    "v_mfma_f32_16x16x32_bf16 v[104:107], a[224:227], a[176:179], v[104:107]\n"
    "ds_read_b64_tr_b16 a[252:253], v225 offset:46336\n"
    "v_mfma_f32_16x16x32_bf16 v[104:107], a[228:231], a[180:183], v[104:107]\n"
    "buffer_store_dwordx4 v[68:71], v217, s[52:55], s70 offen offset:0 sc1\n"
    "v_mfma_f32_16x16x32_bf16 v[108:111], a[224:227], a[184:187], v[108:111]\n"
    "ds_read_b64_tr_b16 a[254:255], v225 offset:50432\n"
    "v_mfma_f32_16x16x32_bf16 v[108:111], a[228:231], a[188:191], v[108:111]\n"
    "s_waitcnt lgkmcnt(8)\n"
    "v_mfma_f32_16x16x32_bf16 v[112:115], a[232:235], a[32:35], v[112:115]\n"
    "ds_read_b64_tr_b16 a[216:217], v228 offset:256\n"
    "v_mfma_f32_16x16x32_bf16 v[112:115], a[236:239], a[36:39], v[112:115]\n"
    "v_mfma_f32_16x16x32_bf16 v[116:119], a[232:235], a[40:43], v[116:119]\n"
    "ds_read_b64_tr_b16 a[218:219], v228 offset:4352\n"
    "v_mfma_f32_16x16x32_bf16 v[116:119], a[236:239], a[44:47], v[116:119]\n"
    "v_mfma_f32_16x16x32_bf16 v[120:123], a[232:235], a[48:51], v[120:123]\n"
    "ds_read_b64_tr_b16 a[220:221], v228 offset:9472\n"
    "v_mfma_f32_16x16x32_bf16 v[120:123], a[236:239], a[52:55], v[120:123]\n"
    "buffer_store_dwordx4 v[72:75], v218, s[52:55], s70 offen offset:0 sc1\n"
    "v_mfma_f32_16x16x32_bf16 v[124:127], a[232:235], a[56:59], v[124:127]\n"
    "ds_read_b64_tr_b16 a[222:223], v228 offset:13568\n"
    "v_mfma_f32_16x16x32_bf16 v[124:127], a[236:239], a[60:63], v[124:127]\n"
    "s_waitcnt lgkmcnt(8)\n"
    "v_mfma_f32_16x16x32_bf16 v[112:115], a[240:243], a[64:67], v[112:115]\n"
    "ds_read_b64_tr_b16 a[224:225], v228 offset:18688\n"
    "v_mfma_f32_16x16x32_bf16 v[112:115], a[244:247], a[68:71], v[112:115]\n"
    "v_mfma_f32_16x16x32_bf16 v[116:119], a[240:243], a[72:75], v[116:119]\n"
    "ds_read_b64_tr_b16 a[226:227], v228 offset:22784\n"
    "v_mfma_f32_16x16x32_bf16 v[116:119], a[244:247], a[76:79], v[116:119]\n"
    "v_mfma_f32_16x16x32_bf16 v[120:123], a[240:243], a[80:83], v[120:123]\n"
    "ds_read_b64_tr_b16 a[228:229], v228 offset:27904\n"
    "v_mfma_f32_16x16x32_bf16 v[120:123], a[244:247], a[84:87], v[120:123]\n"
    "buffer_store_dwordx4 v[76:79], v219, s[52:55], s70 offen offset:0 sc1\n"
    "v_mfma_f32_16x16x32_bf16 v[124:127], a[240:243], a[88:91], v[124:127]\n"
    "ds_read_b64_tr_b16 a[230:231], v228 offset:32000\n"
    "v_mfma_f32_16x16x32_bf16 v[124:127], a[244:247], a[92:95], v[124:127]\n"
    "s_waitcnt lgkmcnt(8)\n"
    "v_mfma_f32_16x16x32_bf16 v[112:115], a[248:251], a[96:99], v[112:115]\n"
    "ds_read_b64_tr_b16 a[232:233], v224 offset:512\n"
    "v_mfma_f32_16x16x32_bf16 v[112:115], a[252:255], a[100:103], v[112:115]\n"
    "v_mfma_f32_16x16x32_bf16 v[116:119], a[248:251], a[104:107], v[116:119]\n"
    "ds_read_b64_tr_b16 a[234:235], v224 offset:4608\n"
    "v_mfma_f32_16x16x32_bf16 v[116:119], a[252:255], a[108:111], v[116:119]\n"
    "v_mfma_f32_16x16x32_bf16 v[120:123], a[248:251], a[112:115], v[120:123]\n"
    "ds_read_b64_tr_b16 a[236:237], v224 offset:9728\n"
    "v_mfma_f32_16x16x32_bf16 v[120:123], a[252:255], a[116:119], v[120:123]\n"
    "buffer_load_dwordx4 v144, s[56:59], s74 offen lds\n"
    "v_mfma_f32_16x16x32_bf16 v[124:127], a[248:251], a[120:123], v[124:127]\n"
    "ds_read_b64_tr_b16 a[238:239], v224 offset:13824\n"
    "v_mfma_f32_16x16x32_bf16 v[124:127], a[252:255], a[124:127], v[124:127]\n"
    "s_waitcnt lgkmcnt(8)\n"
    "v_mfma_f32_16x16x32_bf16 v[112:115], a[216:219], a[128:131], v[112:115]\n"
    "ds_read_b64_tr_b16 a[240:241], v224 offset:18944\n"
    "v_mfma_f32_16x16x32_bf16 v[112:115], a[220:223], a[132:135], v[112:115]\n"
    "buffer_load_dwordx4 v145, s[56:59], s74 offen offset:1024 lds\n"
    "v_mfma_f32_16x16x32_bf16 v[116:119], a[216:219], a[136:139], v[116:119]\n"
    "s_add_u32 m0, s76, 16384\n"
    "ds_read_b64_tr_b16 a[242:243], v224 offset:23040\n"
    "v_mfma_f32_16x16x32_bf16 v[116:119], a[220:223], a[140:143], v[116:119]\n"
    "s_add_u32 s74, s75, 1179648\n"
    "v_mfma_f32_16x16x32_bf16 v[120:123], a[216:219], a[144:147], v[120:123]\n"
    "ds_read_b64_tr_b16 a[244:245], v224 offset:28160\n"
    "v_mfma_f32_16x16x32_bf16 v[120:123], a[220:223], a[148:151], v[120:123]\n"
    "buffer_load_dwordx4 v144, s[56:59], s74 offen lds\n"
    "v_mfma_f32_16x16x32_bf16 v[124:127], a[216:219], a[152:155], v[124:127]\n"
    "ds_read_b64_tr_b16 a[246:247], v224 offset:32256\n"
    "v_mfma_f32_16x16x32_bf16 v[124:127], a[220:223], a[156:159], v[124:127]\n"
    "s_waitcnt lgkmcnt(8)\n"
    "v_mfma_f32_16x16x32_bf16 v[112:115], a[224:227], a[160:163], v[112:115]\n"
    "ds_read_b64_tr_b16 a[248:249], v224 offset:37376\n"
    "v_mfma_f32_16x16x32_bf16 v[112:115], a[228:231], a[164:167], v[112:115]\n"
    "buffer_load_dwordx4 v145, s[56:59], s74 offen offset:1024 lds\n"
    "v_mfma_f32_16x16x32_bf16 v[116:119], a[224:227], a[168:171], v[116:119]\n"
    "s_add_u32 m0, s76, 24576\n"
    "ds_read_b64_tr_b16 a[250:251], v224 offset:41472\n"
    "v_mfma_f32_16x16x32_bf16 v[116:119], a[228:231], a[172:175], v[116:119]\n"
    "s_add_u32 s74, s75, 1769472\n"
    "v_cvt_pk_bf16_f32 v96, v96, v97\n"
    "v_cvt_pk_bf16_f32 v97, v98, v99\n"
    "v_mfma_f32_16x16x32_bf16 v[120:123], a[224:227], a[176:179], v[120:123]\n"
    "ds_read_b64_tr_b16 a[252:253], v224 offset:46592\n"
    "v_cvt_pk_bf16_f32 v98, v112, v113\n"
    "v_mfma_f32_16x16x32_bf16 v[120:123], a[228:231], a[180:183], v[120:123]\n"
    "v_cvt_pk_bf16_f32 v99, v114, v115\n"
    "v_cvt_pk_bf16_f32 v100, v100, v101\n"
    "v_mfma_f32_16x16x32_bf16 v[124:127], a[224:227], a[184:187], v[124:127]\n"
    "ds_read_b64_tr_b16 a[254:255], v224 offset:50688\n"
    "v_cvt_pk_bf16_f32 v101, v102, v103\n"
    "v_mfma_f32_16x16x32_bf16 v[124:127], a[228:231], a[188:191], v[124:127]\n"
    "s_waitcnt lgkmcnt(8)\n"
    "buffer_store_dwordx4 v[96:99], v216, s[52:55], s70 offen offset:1024 sc1\n"
    "v_cvt_pk_bf16_f32 v102, v116, v117\n"
    "v_cvt_pk_bf16_f32 v103, v118, v119\n"
    "v_mfma_f32_16x16x32_bf16 v[128:131], a[232:235], a[32:35], v[128:131]\n"
    "ds_read_b64_tr_b16 a[216:217], v227 offset:512\n"
    "v_cvt_pk_bf16_f32 v104, v104, v105\n"
    "v_mfma_f32_16x16x32_bf16 v[128:131], a[236:239], a[36:39], v[128:131]\n"
    "v_cvt_pk_bf16_f32 v105, v106, v107\n"
    "v_cvt_pk_bf16_f32 v106, v120, v121\n"
    "v_mfma_f32_16x16x32_bf16 v[132:135], a[232:235], a[40:43], v[132:135]\n"
    "ds_read_b64_tr_b16 a[218:219], v227 offset:4608\n"
    "v_cvt_pk_bf16_f32 v107, v122, v123\n"
    "v_mfma_f32_16x16x32_bf16 v[132:135], a[236:239], a[44:47], v[132:135]\n"
    "v_cvt_pk_bf16_f32 v108, v108, v109\n"
    "v_cvt_pk_bf16_f32 v109, v110, v111\n"
    "v_mfma_f32_16x16x32_bf16 v[136:139], a[232:235], a[48:51], v[136:139]\n"
    "ds_read_b64_tr_b16 a[220:221], v227 offset:9728\n"
    "v_cvt_pk_bf16_f32 v110, v124, v125\n"
    "v_mfma_f32_16x16x32_bf16 v[136:139], a[236:239], a[52:55], v[136:139]\n"
    "v_cvt_pk_bf16_f32 v111, v126, v127\n"
    "v_mfma_f32_16x16x32_bf16 v[140:143], a[232:235], a[56:59], v[140:143]\n"
    "ds_read_b64_tr_b16 a[222:223], v227 offset:13824\n"
    "v_mfma_f32_16x16x32_bf16 v[140:143], a[236:239], a[60:63], v[140:143]\n"
    "s_waitcnt lgkmcnt(8)\n"
    "buffer_store_dwordx4 v[100:103], v217, s[52:55], s70 offen offset:1024 sc1\n"
    "v_mfma_f32_16x16x32_bf16 v[128:131], a[240:243], a[64:67], v[128:131]\n"
    "ds_read_b64_tr_b16 a[224:225], v227 offset:18944\n"
    "v_mfma_f32_16x16x32_bf16 v[128:131], a[244:247], a[68:71], v[128:131]\n"
    "v_mfma_f32_16x16x32_bf16 v[132:135], a[240:243], a[72:75], v[132:135]\n"
    "ds_read_b64_tr_b16 a[226:227], v227 offset:23040\n"
    "v_mfma_f32_16x16x32_bf16 v[132:135], a[244:247], a[76:79], v[132:135]\n"
    "v_mfma_f32_16x16x32_bf16 v[136:139], a[240:243], a[80:83], v[136:139]\n"
    "ds_read_b64_tr_b16 a[228:229], v227 offset:28160\n"
    "v_mfma_f32_16x16x32_bf16 v[136:139], a[244:247], a[84:87], v[136:139]\n"
    "v_mfma_f32_16x16x32_bf16 v[140:143], a[240:243], a[88:91], v[140:143]\n"
    "ds_read_b64_tr_b16 a[230:231], v227 offset:32256\n"
    "v_mfma_f32_16x16x32_bf16 v[140:143], a[244:247], a[92:95], v[140:143]\n"
    "s_waitcnt lgkmcnt(8)\n"
    "buffer_store_dwordx4 v[104:107], v218, s[52:55], s70 offen offset:1024 sc1\n"
    "v_mfma_f32_16x16x32_bf16 v[128:131], a[248:251], a[96:99], v[128:131]\n"
    "ds_read_b64_tr_b16 a[232:233], v225 offset:512\n"
    "v_mfma_f32_16x16x32_bf16 v[128:131], a[252:255], a[100:103], v[128:131]\n"
    "v_mfma_f32_16x16x32_bf16 v[132:135], a[248:251], a[104:107], v[132:135]\n"
    "ds_read_b64_tr_b16 a[234:235], v225 offset:4608\n"
    "v_mfma_f32_16x16x32_bf16 v[132:135], a[252:255], a[108:111], v[132:135]\n"
    "v_mfma_f32_16x16x32_bf16 v[136:139], a[248:251], a[112:115], v[136:139]\n"
    "ds_read_b64_tr_b16 a[236:237], v225 offset:9728\n"
    "v_mfma_f32_16x16x32_bf16 v[136:139], a[252:255], a[116:119], v[136:139]\n"
    "v_mfma_f32_16x16x32_bf16 v[140:143], a[248:251], a[120:123], v[140:143]\n"
    "ds_read_b64_tr_b16 a[238:239], v225 offset:13824\n"
    "v_mfma_f32_16x16x32_bf16 v[140:143], a[252:255], a[124:127], v[140:143]\n"
    "s_waitcnt lgkmcnt(8)\n"
    "buffer_store_dwordx4 v[108:111], v219, s[52:55], s70 offen offset:1024 sc1\n"
    "v_mfma_f32_16x16x32_bf16 v[128:131], a[216:219], a[128:131], v[128:131]\n"
    "ds_read_b64_tr_b16 a[240:241], v225 offset:18944\n"
    "v_mfma_f32_16x16x32_bf16 v[128:131], a[220:223], a[132:135], v[128:131]\n"
    "v_mfma_f32_16x16x32_bf16 v[132:135], a[216:219], a[136:139], v[132:135]\n"
    "ds_read_b64_tr_b16 a[242:243], v225 offset:23040\n"
    "v_mfma_f32_16x16x32_bf16 v[132:135], a[220:223], a[140:143], v[132:135]\n"
    "v_mfma_f32_16x16x32_bf16 v[136:139], a[216:219], a[144:147], v[136:139]\n"
    "ds_read_b64_tr_b16 a[244:245], v225 offset:28160\n"
    "v_mfma_f32_16x16x32_bf16 v[136:139], a[220:223], a[148:151], v[136:139]\n"
    "v_mfma_f32_16x16x32_bf16 v[140:143], a[216:219], a[152:155], v[140:143]\n"
    "ds_read_b64_tr_b16 a[246:247], v225 offset:32256\n"
    "v_mfma_f32_16x16x32_bf16 v[140:143], a[220:223], a[156:159], v[140:143]\n"
    "s_waitcnt lgkmcnt(8)\n"
    "buffer_load_dwordx4 v144, s[56:59], s74 offen lds\n"
    "v_mfma_f32_16x16x32_bf16 v[128:131], a[224:227], a[160:163], v[128:131]\n"
    "ds_read_b64_tr_b16 a[248:249], v225 offset:37376\n"
    "v_mfma_f32_16x16x32_bf16 v[128:131], a[228:231], a[164:167], v[128:131]\n"
    "v_mfma_f32_16x16x32_bf16 v[132:135], a[224:227], a[168:171], v[132:135]\n"
    "ds_read_b64_tr_b16 a[250:251], v225 offset:41472\n"
    "v_mfma_f32_16x16x32_bf16 v[132:135], a[228:231], a[172:175], v[132:135]\n"
    "buffer_load_dwordx4 v145, s[56:59], s74 offen offset:1024 lds\n"
    "v_mfma_f32_16x16x32_bf16 v[136:139], a[224:227], a[176:179], v[136:139]\n"
    "ds_read_b64_tr_b16 a[252:253], v225 offset:46592\n"
    "v_mfma_f32_16x16x32_bf16 v[136:139], a[228:231], a[180:183], v[136:139]\n"
    "v_mfma_f32_16x16x32_bf16 v[140:143], a[224:227], a[184:187], v[140:143]\n"
    "ds_read_b64_tr_b16 a[254:255], v225 offset:50688\n"
    "v_mfma_f32_16x16x32_bf16 v[140:143], a[228:231], a[188:191], v[140:143]\n"
    "s_waitcnt lgkmcnt(8)\n"
    "v_mfma_f32_16x16x32_bf16 v[0:3], a[232:235], a[32:35], v[0:3]\n"
    "ds_read_b64_tr_b16 a[216:217], v228 offset:512\n"
    "v_mfma_f32_16x16x32_bf16 v[0:3], a[236:239], a[36:39], v[0:3]\n"
    "v_mfma_f32_16x16x32_bf16 v[4:7], a[232:235], a[40:43], v[4:7]\n"
    "ds_read_b64_tr_b16 a[218:219], v228 offset:4608\n"
    "v_mfma_f32_16x16x32_bf16 v[4:7], a[236:239], a[44:47], v[4:7]\n"
    "v_mfma_f32_16x16x32_bf16 v[8:11], a[232:235], a[48:51], v[8:11]\n"
    "ds_read_b64_tr_b16 a[220:221], v228 offset:9728\n"
    "v_mfma_f32_16x16x32_bf16 v[8:11], a[236:239], a[52:55], v[8:11]\n"
    "v_mfma_f32_16x16x32_bf16 v[12:15], a[232:235], a[56:59], v[12:15]\n"
    "ds_read_b64_tr_b16 a[222:223], v228 offset:13824\n"
    "v_mfma_f32_16x16x32_bf16 v[12:15], a[236:239], a[60:63], v[12:15]\n"
    "s_waitcnt lgkmcnt(8)\n"
    "v_mfma_f32_16x16x32_bf16 v[0:3], a[240:243], a[64:67], v[0:3]\n"
    "ds_read_b64_tr_b16 a[224:225], v228 offset:18944\n"
    "v_mfma_f32_16x16x32_bf16 v[0:3], a[244:247], a[68:71], v[0:3]\n"
    "v_mfma_f32_16x16x32_bf16 v[4:7], a[240:243], a[72:75], v[4:7]\n"
    "ds_read_b64_tr_b16 a[226:227], v228 offset:23040\n"
    "v_mfma_f32_16x16x32_bf16 v[4:7], a[244:247], a[76:79], v[4:7]\n"
    "v_mfma_f32_16x16x32_bf16 v[8:11], a[240:243], a[80:83], v[8:11]\n"
    "ds_read_b64_tr_b16 a[228:229], v228 offset:28160\n"
    "v_mfma_f32_16x16x32_bf16 v[8:11], a[244:247], a[84:87], v[8:11]\n"
    "v_mfma_f32_16x16x32_bf16 v[12:15], a[240:243], a[88:91], v[12:15]\n"
    "ds_read_b64_tr_b16 a[230:231], v228 offset:32256\n"
    "v_mfma_f32_16x16x32_bf16 v[12:15], a[244:247], a[92:95], v[12:15]\n"
    "s_waitcnt lgkmcnt(8)\n"
    "v_mfma_f32_16x16x32_bf16 v[0:3], a[248:251], a[96:99], v[0:3]\n"
    "ds_read_b64_tr_b16 a[232:233], v224 offset:768\n"
    "v_mfma_f32_16x16x32_bf16 v[0:3], a[252:255], a[100:103], v[0:3]\n"
    "v_mfma_f32_16x16x32_bf16 v[4:7], a[248:251], a[104:107], v[4:7]\n"
    "ds_read_b64_tr_b16 a[234:235], v224 offset:4864\n"
    "v_mfma_f32_16x16x32_bf16 v[4:7], a[252:255], a[108:111], v[4:7]\n"
    "v_mfma_f32_16x16x32_bf16 v[8:11], a[248:251], a[112:115], v[8:11]\n"
    "ds_read_b64_tr_b16 a[236:237], v224 offset:9984\n"
    "v_mfma_f32_16x16x32_bf16 v[8:11], a[252:255], a[116:119], v[8:11]\n"
    "v_mfma_f32_16x16x32_bf16 v[12:15], a[248:251], a[120:123], v[12:15]\n"
    "ds_read_b64_tr_b16 a[238:239], v224 offset:14080\n"
    "v_mfma_f32_16x16x32_bf16 v[12:15], a[252:255], a[124:127], v[12:15]\n"
    "s_waitcnt lgkmcnt(8)\n"
    "v_mfma_f32_16x16x32_bf16 v[0:3], a[216:219], a[128:131], v[0:3]\n"
    "ds_read_b64_tr_b16 a[240:241], v224 offset:19200\n"
    "v_mfma_f32_16x16x32_bf16 v[0:3], a[220:223], a[132:135], v[0:3]\n"
    "v_mfma_f32_16x16x32_bf16 v[4:7], a[216:219], a[136:139], v[4:7]\n"
    "ds_read_b64_tr_b16 a[242:243], v224 offset:23296\n"
    "v_mfma_f32_16x16x32_bf16 v[4:7], a[220:223], a[140:143], v[4:7]\n"
    "v_mfma_f32_16x16x32_bf16 v[8:11], a[216:219], a[144:147], v[8:11]\n"
    "ds_read_b64_tr_b16 a[244:245], v224 offset:28416\n"
    "v_mfma_f32_16x16x32_bf16 v[8:11], a[220:223], a[148:151], v[8:11]\n"
    "v_mfma_f32_16x16x32_bf16 v[12:15], a[216:219], a[152:155], v[12:15]\n"
    "ds_read_b64_tr_b16 a[246:247], v224 offset:32512\n"
    "v_mfma_f32_16x16x32_bf16 v[12:15], a[220:223], a[156:159], v[12:15]\n"
    "s_waitcnt lgkmcnt(8)\n"
    "v_mfma_f32_16x16x32_bf16 v[0:3], a[224:227], a[160:163], v[0:3]\n"
    "ds_read_b64_tr_b16 a[248:249], v224 offset:37632\n"
    "v_mfma_f32_16x16x32_bf16 v[0:3], a[228:231], a[164:167], v[0:3]\n"
    "v_mfma_f32_16x16x32_bf16 v[4:7], a[224:227], a[168:171], v[4:7]\n"
    "ds_read_b64_tr_b16 a[250:251], v224 offset:41728\n"
    "v_mfma_f32_16x16x32_bf16 v[4:7], a[228:231], a[172:175], v[4:7]\n"
    "v_cvt_pk_bf16_f32 v128, v128, v129\n"
    "v_cvt_pk_bf16_f32 v129, v130, v131\n"
    "v_mfma_f32_16x16x32_bf16 v[8:11], a[224:227], a[176:179], v[8:11]\n"
    "ds_read_b64_tr_b16 a[252:253], v224 offset:46848\n"
    "v_cvt_pk_bf16_f32 v130, v0, v1\n"
    "v_mfma_f32_16x16x32_bf16 v[8:11], a[228:231], a[180:183], v[8:11]\n"
    "v_cvt_pk_bf16_f32 v131, v2, v3\n"
    "v_cvt_pk_bf16_f32 v132, v132, v133\n"
    "v_mfma_f32_16x16x32_bf16 v[12:15], a[224:227], a[184:187], v[12:15]\n"
    "ds_read_b64_tr_b16 a[254:255], v224 offset:50944\n"
    "v_cvt_pk_bf16_f32 v133, v134, v135\n"
    "v_mfma_f32_16x16x32_bf16 v[12:15], a[228:231], a[188:191], v[12:15]\n"
    "s_waitcnt lgkmcnt(8)\n"
    "buffer_store_dwordx4 v[128:131], v216, s[52:55], s70 offen offset:2048 sc1\n"
    "v_cvt_pk_bf16_f32 v134, v4, v5\n"
    "v_cvt_pk_bf16_f32 v135, v6, v7\n"
    "v_mfma_f32_16x16x32_bf16 v[16:19], a[232:235], a[32:35], v[16:19]\n"
    "ds_read_b64_tr_b16 a[216:217], v227 offset:768\n"
    "v_cvt_pk_bf16_f32 v136, v136, v137\n"
    "v_mfma_f32_16x16x32_bf16 v[16:19], a[236:239], a[36:39], v[16:19]\n"
    "v_cvt_pk_bf16_f32 v137, v138, v139\n"
    "v_cvt_pk_bf16_f32 v138, v8, v9\n"
    "v_mfma_f32_16x16x32_bf16 v[20:23], a[232:235], a[40:43], v[20:23]\n"
    "ds_read_b64_tr_b16 a[218:219], v227 offset:4864\n"
    "v_cvt_pk_bf16_f32 v139, v10, v11\n"
    "v_mfma_f32_16x16x32_bf16 v[20:23], a[236:239], a[44:47], v[20:23]\n"
    "v_cvt_pk_bf16_f32 v140, v140, v141\n"
    "v_cvt_pk_bf16_f32 v141, v142, v143\n"
    "v_mfma_f32_16x16x32_bf16 v[24:27], a[232:235], a[48:51], v[24:27]\n"
    "ds_read_b64_tr_b16 a[220:221], v227 offset:9984\n"
    "v_cvt_pk_bf16_f32 v142, v12, v13\n"
    "v_mfma_f32_16x16x32_bf16 v[24:27], a[236:239], a[52:55], v[24:27]\n"
    "v_cvt_pk_bf16_f32 v143, v14, v15\n"
    "v_mfma_f32_16x16x32_bf16 v[28:31], a[232:235], a[56:59], v[28:31]\n"
    "ds_read_b64_tr_b16 a[222:223], v227 offset:14080\n"
    "v_mfma_f32_16x16x32_bf16 v[28:31], a[236:239], a[60:63], v[28:31]\n"
    "s_waitcnt lgkmcnt(8)\n"
    "buffer_store_dwordx4 v[132:135], v217, s[52:55], s70 offen offset:2048 sc1\n"
    "v_mfma_f32_16x16x32_bf16 v[16:19], a[240:243], a[64:67], v[16:19]\n"
    "ds_read_b64_tr_b16 a[224:225], v227 offset:19200\n"
    "v_mfma_f32_16x16x32_bf16 v[16:19], a[244:247], a[68:71], v[16:19]\n"
    "v_mfma_f32_16x16x32_bf16 v[20:23], a[240:243], a[72:75], v[20:23]\n"
    "ds_read_b64_tr_b16 a[226:227], v227 offset:23296\n"
    "v_mfma_f32_16x16x32_bf16 v[20:23], a[244:247], a[76:79], v[20:23]\n"
    "v_mfma_f32_16x16x32_bf16 v[24:27], a[240:243], a[80:83], v[24:27]\n"
    "ds_read_b64_tr_b16 a[228:229], v227 offset:28416\n"
    "v_mfma_f32_16x16x32_bf16 v[24:27], a[244:247], a[84:87], v[24:27]\n"
    "v_mfma_f32_16x16x32_bf16 v[28:31], a[240:243], a[88:91], v[28:31]\n"
    "ds_read_b64_tr_b16 a[230:231], v227 offset:32512\n"
    "v_mfma_f32_16x16x32_bf16 v[28:31], a[244:247], a[92:95], v[28:31]\n"
    "s_waitcnt lgkmcnt(8)\n"
    "buffer_store_dwordx4 v[136:139], v218, s[52:55], s70 offen offset:2048 sc1\n"
    "v_mfma_f32_16x16x32_bf16 v[16:19], a[248:251], a[96:99], v[16:19]\n"
    "ds_read_b64_tr_b16 a[232:233], v225 offset:768\n"
    "v_mfma_f32_16x16x32_bf16 v[16:19], a[252:255], a[100:103], v[16:19]\n"
    "v_mfma_f32_16x16x32_bf16 v[20:23], a[248:251], a[104:107], v[20:23]\n"
    "ds_read_b64_tr_b16 a[234:235], v225 offset:4864\n"
    "v_mfma_f32_16x16x32_bf16 v[20:23], a[252:255], a[108:111], v[20:23]\n"
    "v_mfma_f32_16x16x32_bf16 v[24:27], a[248:251], a[112:115], v[24:27]\n"
    "ds_read_b64_tr_b16 a[236:237], v225 offset:9984\n"
    "v_mfma_f32_16x16x32_bf16 v[24:27], a[252:255], a[116:119], v[24:27]\n"
    "v_mfma_f32_16x16x32_bf16 v[28:31], a[248:251], a[120:123], v[28:31]\n"
    "ds_read_b64_tr_b16 a[238:239], v225 offset:14080\n"
    "v_mfma_f32_16x16x32_bf16 v[28:31], a[252:255], a[124:127], v[28:31]\n"
    "s_waitcnt lgkmcnt(8)\n"
    "buffer_store_dwordx4 v[140:143], v219, s[52:55], s70 offen offset:2048 sc1\n"
    "v_mfma_f32_16x16x32_bf16 v[16:19], a[216:219], a[128:131], v[16:19]\n"
    "ds_read_b64_tr_b16 a[240:241], v225 offset:19200\n"
    "v_mfma_f32_16x16x32_bf16 v[16:19], a[220:223], a[132:135], v[16:19]\n"
    "v_mfma_f32_16x16x32_bf16 v[20:23], a[216:219], a[136:139], v[20:23]\n"
    "ds_read_b64_tr_b16 a[242:243], v225 offset:23296\n"
    "v_mfma_f32_16x16x32_bf16 v[20:23], a[220:223], a[140:143], v[20:23]\n"
    "v_mfma_f32_16x16x32_bf16 v[24:27], a[216:219], a[144:147], v[24:27]\n"
    "ds_read_b64_tr_b16 a[244:245], v225 offset:28416\n"
    "v_mfma_f32_16x16x32_bf16 v[24:27], a[220:223], a[148:151], v[24:27]\n"
    "v_mfma_f32_16x16x32_bf16 v[28:31], a[216:219], a[152:155], v[28:31]\n"
    "ds_read_b64_tr_b16 a[246:247], v225 offset:32512\n"
    "v_mfma_f32_16x16x32_bf16 v[28:31], a[220:223], a[156:159], v[28:31]\n"
    "s_waitcnt lgkmcnt(8)\n"
    "v_mfma_f32_16x16x32_bf16 v[16:19], a[224:227], a[160:163], v[16:19]\n"
    "ds_read_b64_tr_b16 a[248:249], v225 offset:37632\n"
    "v_mfma_f32_16x16x32_bf16 v[16:19], a[228:231], a[164:167], v[16:19]\n"
    "v_mfma_f32_16x16x32_bf16 v[20:23], a[224:227], a[168:171], v[20:23]\n"
    "ds_read_b64_tr_b16 a[250:251], v225 offset:41728\n"
    "v_mfma_f32_16x16x32_bf16 v[20:23], a[228:231], a[172:175], v[20:23]\n"
    "v_mfma_f32_16x16x32_bf16 v[24:27], a[224:227], a[176:179], v[24:27]\n"
    "ds_read_b64_tr_b16 a[252:253], v225 offset:46848\n"
    "v_mfma_f32_16x16x32_bf16 v[24:27], a[228:231], a[180:183], v[24:27]\n"
    "v_mfma_f32_16x16x32_bf16 v[28:31], a[224:227], a[184:187], v[28:31]\n"
    "ds_read_b64_tr_b16 a[254:255], v225 offset:50944\n"
    "v_mfma_f32_16x16x32_bf16 v[28:31], a[228:231], a[188:191], v[28:31]\n"
    "s_waitcnt lgkmcnt(8)\n"
    "v_mfma_f32_16x16x32_bf16 v[32:35], a[232:235], a[32:35], v[32:35]\n"
    "ds_read_b64_tr_b16 a[216:217], v228 offset:768\n"
    "v_mfma_f32_16x16x32_bf16 v[32:35], a[236:239], a[36:39], v[32:35]\n"
    "v_mfma_f32_16x16x32_bf16 v[36:39], a[232:235], a[40:43], v[36:39]\n"
    "ds_read_b64_tr_b16 a[218:219], v228 offset:4864\n"
    "v_mfma_f32_16x16x32_bf16 v[36:39], a[236:239], a[44:47], v[36:39]\n"
    "v_mfma_f32_16x16x32_bf16 v[40:43], a[232:235], a[48:51], v[40:43]\n"
    "ds_read_b64_tr_b16 a[220:221], v228 offset:9984\n"
    "v_mfma_f32_16x16x32_bf16 v[40:43], a[236:239], a[52:55], v[40:43]\n"
    "v_mfma_f32_16x16x32_bf16 v[44:47], a[232:235], a[56:59], v[44:47]\n"
    "ds_read_b64_tr_b16 a[222:223], v228 offset:14080\n"
    "v_mfma_f32_16x16x32_bf16 v[44:47], a[236:239], a[60:63], v[44:47]\n"
    "s_waitcnt lgkmcnt(8)\n"
    "v_mfma_f32_16x16x32_bf16 v[32:35], a[240:243], a[64:67], v[32:35]\n"
    "ds_read_b64_tr_b16 a[224:225], v228 offset:19200\n"
    "v_mfma_f32_16x16x32_bf16 v[32:35], a[244:247], a[68:71], v[32:35]\n"
    "v_mfma_f32_16x16x32_bf16 v[36:39], a[240:243], a[72:75], v[36:39]\n"
    "ds_read_b64_tr_b16 a[226:227], v228 offset:23296\n"
    "v_mfma_f32_16x16x32_bf16 v[36:39], a[244:247], a[76:79], v[36:39]\n"
    "v_mfma_f32_16x16x32_bf16 v[40:43], a[240:243], a[80:83], v[40:43]\n"
    "ds_read_b64_tr_b16 a[228:229], v228 offset:28416\n"
    "v_mfma_f32_16x16x32_bf16 v[40:43], a[244:247], a[84:87], v[40:43]\n"
    "v_mfma_f32_16x16x32_bf16 v[44:47], a[240:243], a[88:91], v[44:47]\n"
    "ds_read_b64_tr_b16 a[230:231], v228 offset:32512\n"
    "v_mfma_f32_16x16x32_bf16 v[44:47], a[244:247], a[92:95], v[44:47]\n"
    "s_waitcnt lgkmcnt(8)\n"
    "v_mfma_f32_16x16x32_bf16 v[32:35], a[248:251], a[96:99], v[32:35]\n"
    "v_mfma_f32_16x16x32_bf16 v[32:35], a[252:255], a[100:103], v[32:35]\n"
    "v_mfma_f32_16x16x32_bf16 v[36:39], a[248:251], a[104:107], v[36:39]\n"
    "v_mfma_f32_16x16x32_bf16 v[36:39], a[252:255], a[108:111], v[36:39]\n"
    "v_mfma_f32_16x16x32_bf16 v[40:43], a[248:251], a[112:115], v[40:43]\n"
    "v_mfma_f32_16x16x32_bf16 v[40:43], a[252:255], a[116:119], v[40:43]\n"
    "v_mfma_f32_16x16x32_bf16 v[44:47], a[248:251], a[120:123], v[44:47]\n"
    "v_mfma_f32_16x16x32_bf16 v[44:47], a[252:255], a[124:127], v[44:47]\n"
    "s_waitcnt lgkmcnt(4)\n"
    "v_mfma_f32_16x16x32_bf16 v[32:35], a[216:219], a[128:131], v[32:35]\n"
    "v_mfma_f32_16x16x32_bf16 v[32:35], a[220:223], a[132:135], v[32:35]\n"
    "v_mfma_f32_16x16x32_bf16 v[36:39], a[216:219], a[136:139], v[36:39]\n"
    "v_mfma_f32_16x16x32_bf16 v[36:39], a[220:223], a[140:143], v[36:39]\n"
    "v_mfma_f32_16x16x32_bf16 v[40:43], a[216:219], a[144:147], v[40:43]\n"
    "v_mfma_f32_16x16x32_bf16 v[40:43], a[220:223], a[148:151], v[40:43]\n"
    "v_mfma_f32_16x16x32_bf16 v[44:47], a[216:219], a[152:155], v[44:47]\n"
    "v_mfma_f32_16x16x32_bf16 v[44:47], a[220:223], a[156:159], v[44:47]\n"
    "s_waitcnt lgkmcnt(0)\n"
    "v_mfma_f32_16x16x32_bf16 v[32:35], a[224:227], a[160:163], v[32:35]\n"
    "v_mfma_f32_16x16x32_bf16 v[32:35], a[228:231], a[164:167], v[32:35]\n"
    "v_mfma_f32_16x16x32_bf16 v[36:39], a[224:227], a[168:171], v[36:39]\n"
    "v_mfma_f32_16x16x32_bf16 v[36:39], a[228:231], a[172:175], v[36:39]\n"
    "v_cvt_pk_bf16_f32 v16, v16, v17\n"
    "v_cvt_pk_bf16_f32 v17, v18, v19\n"
    "v_mfma_f32_16x16x32_bf16 v[40:43], a[224:227], a[176:179], v[40:43]\n"
    "v_cvt_pk_bf16_f32 v18, v32, v33\n"
    "v_cvt_pk_bf16_f32 v19, v34, v35\n"
    "v_mfma_f32_16x16x32_bf16 v[40:43], a[228:231], a[180:183], v[40:43]\n"
    "buffer_store_dwordx4 v[16:19], v216, s[52:55], s70 offen offset:3072 sc1\n"
    "v_cvt_pk_bf16_f32 v20, v20, v21\n"
    "v_cvt_pk_bf16_f32 v21, v22, v23\n"
    "v_mfma_f32_16x16x32_bf16 v[44:47], a[224:227], a[184:187], v[44:47]\n"
    "v_cvt_pk_bf16_f32 v22, v36, v37\n"
    "v_cvt_pk_bf16_f32 v23, v38, v39\n"
    "v_mfma_f32_16x16x32_bf16 v[44:47], a[228:231], a[188:191], v[44:47]\n"
    "v_cvt_pk_bf16_f32 v24, v24, v25\n"
    "v_cvt_pk_bf16_f32 v25, v26, v27\n"
    "v_cvt_pk_bf16_f32 v26, v40, v41\n"
    "v_cvt_pk_bf16_f32 v27, v42, v43\n"
    "v_cvt_pk_bf16_f32 v28, v28, v29\n"
    "v_cvt_pk_bf16_f32 v29, v30, v31\n"
    "s_nop 1\n"
    "v_cvt_pk_bf16_f32 v30, v44, v45\n"
    "v_cvt_pk_bf16_f32 v31, v46, v47\n"
    "buffer_store_dwordx4 v[20:23], v217, s[52:55], s70 offen offset:3072 sc1\n"
    "buffer_store_dwordx4 v[24:27], v218, s[52:55], s70 offen offset:3072 sc1\n"
    "buffer_store_dwordx4 v[28:31], v219, s[52:55], s70 offen offset:3072 sc1\n"
    "s_waitcnt vmcnt(0)\n"
    "v_mov_b32 v245, %27\n"
    "s_mov_b64 exec, 1\n"
    "global_store_dword v243, v245, %13 offset:1024 sc1\n"
    "s_mov_b64 exec, -1\n"
    "s_waitcnt lgkmcnt(0)\n"
    "s_branch LA_end\n"
    "SA_0_0_X0:\n"
    "s_mov_b32 s75, 0\n"
    "s_getpc_b64 s[76:77]\n"
    "s_branch WA_0_0\n"
    "s_branch RA_0_0_X0\n"
    "SA_0_1_X0:\n"
    "s_mov_b32 s75, 0\n"
    "s_getpc_b64 s[76:77]\n"
    "s_branch WA_1_0\n"
    "s_branch RA_0_1_X0\n"
    "SA_0_2_L:\n"
    "s_mov_b32 s75, 0\n"
    "s_getpc_b64 s[76:77]\n"
    "s_branch WA_2_0\n"
    "s_branch RA_0_2_L\n"
    "SA_0_3_L:\n"
    "s_mov_b32 s75, 0\n"
    "s_getpc_b64 s[76:77]\n"
    "s_branch WA_3_0\n"
    "s_branch RA_0_3_L\n"
    "SA_1_0_L:\n"
    "s_mov_b32 s75, 16384\n"
    "s_getpc_b64 s[76:77]\n"
    "s_branch WA_0_1\n"
    "s_branch RA_1_0_L\n"
    "SA_1_1_L:\n"
    "s_mov_b32 s75, 16384\n"
    "s_getpc_b64 s[76:77]\n"
    "s_branch WA_1_1\n"
    "s_branch RA_1_1_L\n"
    "SA_1_2_L:\n"
    "s_mov_b32 s75, 16384\n"
    "s_getpc_b64 s[76:77]\n"
    "s_branch WA_2_1\n"
    "s_branch RA_1_2_L\n"
    "SA_1_3_L:\n"
    "s_mov_b32 s75, 16384\n"
    "s_getpc_b64 s[76:77]\n"
    "s_branch WA_3_1\n"
    "s_branch RA_1_3_L\n"
    "SA_2_0_L:\n"
    "s_mov_b32 s75, 32768\n"
    "s_getpc_b64 s[76:77]\n"
    "s_branch WA_0_0\n"
    "s_branch RA_2_0_L\n"
    "SA_2_1_L:\n"
    "s_mov_b32 s75, 32768\n"
    "s_getpc_b64 s[76:77]\n"
    "s_branch WA_1_0\n"
    "s_branch RA_2_1_L\n"
    "SA_2_2_L:\n"
    "s_mov_b32 s75, 32768\n"
    "s_getpc_b64 s[76:77]\n"
    "s_branch WA_2_0\n"
    "s_branch RA_2_2_L\n"
    "SA_2_3_L:\n"
    "s_mov_b32 s75, 32768\n"
    "s_getpc_b64 s[76:77]\n"
    "s_branch WA_3_0\n"
    "s_branch RA_2_3_L\n"
    "SA_3_0_L:\n"
    "s_mov_b32 s75, 49152\n"
    "s_getpc_b64 s[76:77]\n"
    "s_branch WA_0_1\n"
    "s_branch RA_3_0_L\n"
    "SA_3_1_L:\n"
    "s_mov_b32 s75, 49152\n"
    "s_getpc_b64 s[76:77]\n"
    "s_branch WA_1_1\n"
    "s_branch RA_3_1_L\n"
    "SA_3_2_L:\n"
    "s_mov_b32 s75, 49152\n"
    "s_getpc_b64 s[76:77]\n"
    "s_branch WA_2_1\n"
    "s_branch RA_3_2_L\n"
    "SA_3_3_L:\n"
    "s_mov_b32 s75, 49152\n"
    "s_getpc_b64 s[76:77]\n"
    "s_branch WA_3_1\n"
    "s_branch RA_3_3_L\n"
    "SA_0_0_L:\n"
    "s_mov_b32 s75, 0\n"
    "s_getpc_b64 s[76:77]\n"
    "s_branch WA_0_0\n"
    "s_branch RA_0_0_L\n"
    "SA_0_1_L:\n"
    "s_mov_b32 s75, 0\n"
    "s_getpc_b64 s[76:77]\n"
    "s_branch WA_1_0\n"
    "s_branch RA_0_1_L\n"
    "SA_2_2_X31:\n"
    "s_mov_b32 s75, 32768\n"
    "s_getpc_b64 s[76:77]\n"
    "s_branch WA_2_0\n"
    "s_branch RA_2_2_X31\n"
    "SA_2_3_X31:\n"
    "s_mov_b32 s75, 32768\n"
    "s_getpc_b64 s[76:77]\n"
    "s_branch WA_3_0\n"
    "s_branch RA_2_3_X31\n"
    "SA_3_0_X31:\n"
    "s_mov_b32 s75, 49152\n"
    "s_getpc_b64 s[76:77]\n"
    "s_branch WA_0_1\n"
    "s_branch RA_3_0_X31\n"
    "SA_3_1_X31:\n"
    "s_mov_b32 s75, 49152\n"
    "s_getpc_b64 s[76:77]\n"
    "s_branch WA_1_1\n"
    "s_branch RA_3_1_X31\n"
    "SA_3_2_X32:\n"
    "s_mov_b32 s75, 49152\n"
    "s_getpc_b64 s[76:77]\n"
    "s_branch WA_2_1\n"
    "s_branch RA_3_2_X32\n"
    "SA_3_3_X32:\n"
    "s_mov_b32 s75, 49152\n"
    "s_getpc_b64 s[76:77]\n"
    "s_branch WA_3_1\n"
    "s_branch RA_3_3_X32\n"
    "WA_0_0:\n"
    "s_waitcnt lgkmcnt(0)\n"
    "s_nop 7\n"
    "s_nop 7\n"
    "v_add_u32 v208, s75, v224\n"
    "v_add_u32 v209, s75, v225\n"
    "v_add_u32 v210, s75, v226\n"
    "v_add_u32 v211, s75, v227\n"
    "ds_read_b128 a[196:199], v208 offset:0\n"
    "ds_read_b128 a[200:203], v209 offset:0\n"
    "ds_read_b128 a[204:207], v210 offset:0\n"
    "ds_read_b128 a[208:211], v211 offset:0\n"
    "s_waitcnt lgkmcnt(0)\n"
    "v_mfma_f32_16x16x32_bf16 v[188:191], a[196:199], a[0:3], 0\n"
    "v_mfma_f32_16x16x32_bf16 v[188:191], a[200:203], a[4:7], v[188:191]\n"
    "v_mfma_f32_16x16x32_bf16 v[188:191], a[204:207], a[8:11], v[188:191]\n"
    "v_mfma_f32_16x16x32_bf16 v[188:191], a[208:211], a[12:15], v[188:191]\n"
    "s_nop 7\n"
    "ds_read_b128 a[196:199], v208 offset:4096\n"
    "ds_read_b128 a[200:203], v209 offset:4096\n"
    "ds_read_b128 a[204:207], v210 offset:4096\n"
    "ds_read_b128 a[208:211], v211 offset:4096\n"
    "s_waitcnt lgkmcnt(0)\n"
    "v_mfma_f32_16x16x32_bf16 v[192:195], a[196:199], a[0:3], 0\n"
    "v_mfma_f32_16x16x32_bf16 v[192:195], a[200:203], a[4:7], v[192:195]\n"
    "v_mfma_f32_16x16x32_bf16 v[192:195], a[204:207], a[8:11], v[192:195]\n"
    "v_mfma_f32_16x16x32_bf16 v[192:195], a[208:211], a[12:15], v[192:195]\n"
    "s_nop 7\n"
    "ds_read_b128 a[196:199], v208 offset:8192\n"
    "ds_read_b128 a[200:203], v209 offset:8192\n"
    "ds_read_b128 a[204:207], v210 offset:8192\n"
    "ds_read_b128 a[208:211], v211 offset:8192\n"
    "s_waitcnt lgkmcnt(0)\n"
    "v_mfma_f32_16x16x32_bf16 v[196:199], a[196:199], a[0:3], 0\n"
    "v_mfma_f32_16x16x32_bf16 v[196:199], a[200:203], a[4:7], v[196:199]\n"
    "v_mfma_f32_16x16x32_bf16 v[196:199], a[204:207], a[8:11], v[196:199]\n"
    "v_mfma_f32_16x16x32_bf16 v[196:199], a[208:211], a[12:15], v[196:199]\n"
    "s_nop 7\n"
    "ds_read_b128 a[196:199], v208 offset:12288\n"
    "ds_read_b128 a[200:203], v209 offset:12288\n"
    "ds_read_b128 a[204:207], v210 offset:12288\n"
    "ds_read_b128 a[208:211], v211 offset:12288\n"
    "s_waitcnt lgkmcnt(0)\n"
    "v_mfma_f32_16x16x32_bf16 v[200:203], a[196:199], a[0:3], 0\n"
    "v_mfma_f32_16x16x32_bf16 v[200:203], a[200:203], a[4:7], v[200:203]\n"
    "v_mfma_f32_16x16x32_bf16 v[200:203], a[204:207], a[8:11], v[200:203]\n"
    "v_mfma_f32_16x16x32_bf16 v[200:203], a[208:211], a[12:15], v[200:203]\n"
    "s_nop 7\n"
    "s_nop 7\n"
    "v_max3_f32 v244, v188, v189, v190\n"
    "v_max3_f32 v244, v244, v191, v192\n"
    "v_max3_f32 v244, v244, v193, v194\n"
    "v_max3_f32 v244, v244, v195, v196\n"
    "v_max3_f32 v244, v244, v197, v198\n"
    "v_max3_f32 v244, v244, v199, v200\n"
    "v_max3_f32 v244, v244, v201, v202\n"
    "v_max_f32 v244, v244, v203\n"
    "v_mov_b32 v245, v244\n"
    "s_nop 1\n"
    "v_permlane32_swap_b32 v244, v245\n"
    "v_max_f32 v244, v244, v245\n"
    "v_mov_b32 v245, v244\n"
    "s_nop 1\n"
    "v_permlane16_swap_b32 v244, v245\n"
    "v_max_f32 v244, v244, v245\n"
    "v_max_f32 v244, v244, -v160\n"
    "v_add_f32 v245, v244, v160\n"
    "v_exp_f32 v245, -v245\n"
    "v_sub_f32 v160, 0, v244\n"
    "v_sub_f32 v161, 0, v244\n"
    "v_sub_f32 v162, 0, v244\n"
    "v_sub_f32 v163, 0, v244\n"
    "v_accvgpr_read_b32 v246, a128\n"
    "v_mul_f32 v246, v245, v246\n"
    "v_accvgpr_write_b32 a128, v246\n"
    "v_accvgpr_read_b32 v246, a129\n"
    "v_mul_f32 v246, v245, v246\n"
    "v_accvgpr_write_b32 a129, v246\n"
    "v_accvgpr_read_b32 v246, a130\n"
    "v_mul_f32 v246, v245, v246\n"
    "v_accvgpr_write_b32 a130, v246\n"
    "v_accvgpr_read_b32 v246, a131\n"
    "v_mul_f32 v246, v245, v246\n"
    "v_accvgpr_write_b32 a131, v246\n"
    "v_accvgpr_read_b32 v246, a144\n"
    "v_mul_f32 v246, v245, v246\n"
    "v_accvgpr_write_b32 a144, v246\n"
    "v_accvgpr_read_b32 v246, a145\n"
    "v_mul_f32 v246, v245, v246\n"
    "v_accvgpr_write_b32 a145, v246\n"
    "v_accvgpr_read_b32 v246, a146\n"
    "v_mul_f32 v246, v245, v246\n"
    "v_accvgpr_write_b32 a146, v246\n"
    "v_accvgpr_read_b32 v246, a147\n"
    "v_mul_f32 v246, v245, v246\n"
    "v_accvgpr_write_b32 a147, v246\n"
    "v_accvgpr_read_b32 v246, a160\n"
    "v_mul_f32 v246, v245, v246\n"
    "v_accvgpr_write_b32 a160, v246\n"
    "v_accvgpr_read_b32 v246, a161\n"
    "v_mul_f32 v246, v245, v246\n"
    "v_accvgpr_write_b32 a161, v246\n"
    "v_accvgpr_read_b32 v246, a162\n"
    "v_mul_f32 v246, v245, v246\n"
    "v_accvgpr_write_b32 a162, v246\n"
    "v_accvgpr_read_b32 v246, a163\n"
    "v_mul_f32 v246, v245, v246\n"
    "v_accvgpr_write_b32 a163, v246\n"
    "v_accvgpr_read_b32 v246, a176\n"
    "v_mul_f32 v246, v245, v246\n"
    "v_accvgpr_write_b32 a176, v246\n"
    "v_accvgpr_read_b32 v246, a177\n"
    "v_mul_f32 v246, v245, v246\n"
    "v_accvgpr_write_b32 a177, v246\n"
    "v_accvgpr_read_b32 v246, a178\n"
    "v_mul_f32 v246, v245, v246\n"
    "v_accvgpr_write_b32 a178, v246\n"
    "v_accvgpr_read_b32 v246, a179\n"
    "v_mul_f32 v246, v245, v246\n"
    "v_accvgpr_write_b32 a179, v246\n"
    "v_mul_f32 v0, v245, v0\n"
    "v_mul_f32 v1, v245, v1\n"
    "v_mul_f32 v2, v245, v2\n"
    "v_mul_f32 v3, v245, v3\n"
    "v_mul_f32 v16, v245, v16\n"
    "v_mul_f32 v17, v245, v17\n"
    "v_mul_f32 v18, v245, v18\n"
    "v_mul_f32 v19, v245, v19\n"
    "v_mul_f32 v32, v245, v32\n"
    "v_mul_f32 v33, v245, v33\n"
    "v_mul_f32 v34, v245, v34\n"
    "v_mul_f32 v35, v245, v35\n"
    "v_mul_f32 v48, v245, v48\n"
    "v_mul_f32 v49, v245, v49\n"
    "v_mul_f32 v50, v245, v50\n"
    "v_mul_f32 v51, v245, v51\n"
    "v_mul_f32 v238, v245, v238\n"
    "v_sub_f32 v188, v188, v244\n"
    "v_sub_f32 v189, v189, v244\n"
    "v_sub_f32 v190, v190, v244\n"
    "v_sub_f32 v191, v191, v244\n"
    "v_sub_f32 v192, v192, v244\n"
    "v_sub_f32 v193, v193, v244\n"
    "v_sub_f32 v194, v194, v244\n"
    "v_sub_f32 v195, v195, v244\n"
    "v_sub_f32 v196, v196, v244\n"
    "v_sub_f32 v197, v197, v244\n"
    "v_sub_f32 v198, v198, v244\n"
    "v_sub_f32 v199, v199, v244\n"
    "v_sub_f32 v200, v200, v244\n"
    "v_sub_f32 v201, v201, v244\n"
    "v_sub_f32 v202, v202, v244\n"
    "v_sub_f32 v203, v203, v244\n"
    "v_exp_f32 v188, v188\n"
    "v_exp_f32 v189, v189\n"
    "v_exp_f32 v190, v190\n"
    "v_exp_f32 v191, v191\n"
    "v_exp_f32 v192, v192\n"
    "v_exp_f32 v193, v193\n"
    "v_exp_f32 v194, v194\n"
    "v_exp_f32 v195, v195\n"
    "v_exp_f32 v196, v196\n"
    "v_exp_f32 v197, v197\n"
    "v_exp_f32 v198, v198\n"
    "v_exp_f32 v199, v199\n"
    "v_exp_f32 v200, v200\n"
    "v_exp_f32 v201, v201\n"
    "v_exp_f32 v202, v202\n"
    "v_exp_f32 v203, v203\n"
    "s_nop 1\n"
    "v_cvt_pk_bf16_f32 v64, v188, v189\n"
    "v_cvt_pk_bf16_f32 v65, v190, v191\n"
    "v_cvt_pk_bf16_f32 v66, v192, v193\n"
    "v_cvt_pk_bf16_f32 v67, v194, v195\n"
    "v_cvt_pk_bf16_f32 v68, v196, v197\n"
    "v_cvt_pk_bf16_f32 v69, v198, v199\n"
    "v_cvt_pk_bf16_f32 v70, v200, v201\n"
    "v_cvt_pk_bf16_f32 v71, v202, v203\n"
    "s_nop 3\n"
    "v_mfma_f32_16x16x32_bf16 v[176:179], a[192:195], v[64:67], 0\n"
    "v_mfma_f32_16x16x32_bf16 v[176:179], a[192:195], v[68:71], v[176:179]\n"
    "s_nop 7\n"
    "s_nop 7\n"
    "s_add_u32 s76, s76, 4\n"
    "s_addc_u32 s77, s77, 0\n"
    "s_setpc_b64 s[76:77]\n"
    "WA_0_1:\n"
    "s_waitcnt lgkmcnt(0)\n"
    "s_nop 7\n"
    "s_nop 7\n"
    "v_add_u32 v208, s75, v224\n"
    "v_add_u32 v209, s75, v225\n"
    "v_add_u32 v210, s75, v226\n"
    "v_add_u32 v211, s75, v227\n"
    "ds_read_b128 a[196:199], v208 offset:0\n"
    "ds_read_b128 a[200:203], v209 offset:0\n"
    "ds_read_b128 a[204:207], v210 offset:0\n"
    "ds_read_b128 a[208:211], v211 offset:0\n"
    "s_waitcnt lgkmcnt(0)\n"
    "v_mfma_f32_16x16x32_bf16 v[188:191], a[196:199], a[0:3], 0\n"
    "v_mfma_f32_16x16x32_bf16 v[188:191], a[200:203], a[4:7], v[188:191]\n"
    "v_mfma_f32_16x16x32_bf16 v[188:191], a[204:207], a[8:11], v[188:191]\n"
    "v_mfma_f32_16x16x32_bf16 v[188:191], a[208:211], a[12:15], v[188:191]\n"
    "s_nop 7\n"
    "ds_read_b128 a[196:199], v208 offset:4096\n"
    "ds_read_b128 a[200:203], v209 offset:4096\n"
    "ds_read_b128 a[204:207], v210 offset:4096\n"
    "ds_read_b128 a[208:211], v211 offset:4096\n"
    "s_waitcnt lgkmcnt(0)\n"
    "v_mfma_f32_16x16x32_bf16 v[192:195], a[196:199], a[0:3], 0\n"
    "v_mfma_f32_16x16x32_bf16 v[192:195], a[200:203], a[4:7], v[192:195]\n"
    "v_mfma_f32_16x16x32_bf16 v[192:195], a[204:207], a[8:11], v[192:195]\n"
    "v_mfma_f32_16x16x32_bf16 v[192:195], a[208:211], a[12:15], v[192:195]\n"
    "s_nop 7\n"
    "ds_read_b128 a[196:199], v208 offset:8192\n"
    "ds_read_b128 a[200:203], v209 offset:8192\n"
    "ds_read_b128 a[204:207], v210 offset:8192\n"
    "ds_read_b128 a[208:211], v211 offset:8192\n"
    "s_waitcnt lgkmcnt(0)\n"
    "v_mfma_f32_16x16x32_bf16 v[196:199], a[196:199], a[0:3], 0\n"
    "v_mfma_f32_16x16x32_bf16 v[196:199], a[200:203], a[4:7], v[196:199]\n"
    "v_mfma_f32_16x16x32_bf16 v[196:199], a[204:207], a[8:11], v[196:199]\n"
    "v_mfma_f32_16x16x32_bf16 v[196:199], a[208:211], a[12:15], v[196:199]\n"
    "s_nop 7\n"
    "ds_read_b128 a[196:199], v208 offset:12288\n"
    "ds_read_b128 a[200:203], v209 offset:12288\n"
    "ds_read_b128 a[204:207], v210 offset:12288\n"
    "ds_read_b128 a[208:211], v211 offset:12288\n"
    "s_waitcnt lgkmcnt(0)\n"
    "v_mfma_f32_16x16x32_bf16 v[200:203], a[196:199], a[0:3], 0\n"
    "v_mfma_f32_16x16x32_bf16 v[200:203], a[200:203], a[4:7], v[200:203]\n"
    "v_mfma_f32_16x16x32_bf16 v[200:203], a[204:207], a[8:11], v[200:203]\n"
    "v_mfma_f32_16x16x32_bf16 v[200:203], a[208:211], a[12:15], v[200:203]\n"
    "s_nop 7\n"
    "s_nop 7\n"
    "v_max3_f32 v244, v188, v189, v190\n"
    "v_max3_f32 v244, v244, v191, v192\n"
    "v_max3_f32 v244, v244, v193, v194\n"
    "v_max3_f32 v244, v244, v195, v196\n"
    "v_max3_f32 v244, v244, v197, v198\n"
    "v_max3_f32 v244, v244, v199, v200\n"
    "v_max3_f32 v244, v244, v201, v202\n"
    "v_max_f32 v244, v244, v203\n"
    "v_mov_b32 v245, v244\n"
    "s_nop 1\n"
    "v_permlane32_swap_b32 v244, v245\n"
    "v_max_f32 v244, v244, v245\n"
    "v_mov_b32 v245, v244\n"
    "s_nop 1\n"
    "v_permlane16_swap_b32 v244, v245\n"
    "v_max_f32 v244, v244, v245\n"
    "v_max_f32 v244, v244, -v160\n"
    "v_add_f32 v245, v244, v160\n"
    "v_exp_f32 v245, -v245\n"
    "v_sub_f32 v160, 0, v244\n"
    "v_sub_f32 v161, 0, v244\n"
    "v_sub_f32 v162, 0, v244\n"
    "v_sub_f32 v163, 0, v244\n"
    "v_accvgpr_read_b32 v246, a128\n"
    "v_mul_f32 v246, v245, v246\n"
    "v_accvgpr_write_b32 a128, v246\n"
    "v_accvgpr_read_b32 v246, a129\n"
    "v_mul_f32 v246, v245, v246\n"
    "v_accvgpr_write_b32 a129, v246\n"
    "v_accvgpr_read_b32 v246, a130\n"
    "v_mul_f32 v246, v245, v246\n"
    "v_accvgpr_write_b32 a130, v246\n"
    "v_accvgpr_read_b32 v246, a131\n"
    "v_mul_f32 v246, v245, v246\n"
    "v_accvgpr_write_b32 a131, v246\n"
    "v_accvgpr_read_b32 v246, a144\n"
    "v_mul_f32 v246, v245, v246\n"
    "v_accvgpr_write_b32 a144, v246\n"
    "v_accvgpr_read_b32 v246, a145\n"
    "v_mul_f32 v246, v245, v246\n"
    "v_accvgpr_write_b32 a145, v246\n"
    "v_accvgpr_read_b32 v246, a146\n"
    "v_mul_f32 v246, v245, v246\n"
    "v_accvgpr_write_b32 a146, v246\n"
    "v_accvgpr_read_b32 v246, a147\n"
    "v_mul_f32 v246, v245, v246\n"
    "v_accvgpr_write_b32 a147, v246\n"
    "v_accvgpr_read_b32 v246, a160\n"
    "v_mul_f32 v246, v245, v246\n"
    "v_accvgpr_write_b32 a160, v246\n"
    "v_accvgpr_read_b32 v246, a161\n"
    "v_mul_f32 v246, v245, v246\n"
    "v_accvgpr_write_b32 a161, v246\n"
    "v_accvgpr_read_b32 v246, a162\n"
    "v_mul_f32 v246, v245, v246\n"
    "v_accvgpr_write_b32 a162, v246\n"
    "v_accvgpr_read_b32 v246, a163\n"
    "v_mul_f32 v246, v245, v246\n"
    "v_accvgpr_write_b32 a163, v246\n"
    "v_accvgpr_read_b32 v246, a176\n"
    "v_mul_f32 v246, v245, v246\n"
    "v_accvgpr_write_b32 a176, v246\n"
    "v_accvgpr_read_b32 v246, a177\n"
    "v_mul_f32 v246, v245, v246\n"
    "v_accvgpr_write_b32 a177, v246\n"
    "v_accvgpr_read_b32 v246, a178\n"
    "v_mul_f32 v246, v245, v246\n"
    "v_accvgpr_write_b32 a178, v246\n"
    "v_accvgpr_read_b32 v246, a179\n"
    "v_mul_f32 v246, v245, v246\n"
    "v_accvgpr_write_b32 a179, v246\n"
    "v_mul_f32 v0, v245, v0\n"
    "v_mul_f32 v1, v245, v1\n"
    "v_mul_f32 v2, v245, v2\n"
    "v_mul_f32 v3, v245, v3\n"
    "v_mul_f32 v16, v245, v16\n"
    "v_mul_f32 v17, v245, v17\n"
    "v_mul_f32 v18, v245, v18\n"
    "v_mul_f32 v19, v245, v19\n"
    "v_mul_f32 v32, v245, v32\n"
    "v_mul_f32 v33, v245, v33\n"
    "v_mul_f32 v34, v245, v34\n"
    "v_mul_f32 v35, v245, v35\n"
    "v_mul_f32 v48, v245, v48\n"
    "v_mul_f32 v49, v245, v49\n"
    "v_mul_f32 v50, v245, v50\n"
    "v_mul_f32 v51, v245, v51\n"
    "v_mul_f32 v238, v245, v238\n"
    "v_sub_f32 v188, v188, v244\n"
    "v_sub_f32 v189, v189, v244\n"
    "v_sub_f32 v190, v190, v244\n"
    "v_sub_f32 v191, v191, v244\n"
    "v_sub_f32 v192, v192, v244\n"
    "v_sub_f32 v193, v193, v244\n"
    "v_sub_f32 v194, v194, v244\n"
    "v_sub_f32 v195, v195, v244\n"
    "v_sub_f32 v196, v196, v244\n"
    "v_sub_f32 v197, v197, v244\n"
    "v_sub_f32 v198, v198, v244\n"
    "v_sub_f32 v199, v199, v244\n"
    "v_sub_f32 v200, v200, v244\n"
    "v_sub_f32 v201, v201, v244\n"
    "v_sub_f32 v202, v202, v244\n"
    "v_sub_f32 v203, v203, v244\n"
    "v_exp_f32 v188, v188\n"
    "v_exp_f32 v189, v189\n"
    "v_exp_f32 v190, v190\n"
    "v_exp_f32 v191, v191\n"
    "v_exp_f32 v192, v192\n"
    "v_exp_f32 v193, v193\n"
    "v_exp_f32 v194, v194\n"
    "v_exp_f32 v195, v195\n"
    "v_exp_f32 v196, v196\n"
    "v_exp_f32 v197, v197\n"
    "v_exp_f32 v198, v198\n"
    "v_exp_f32 v199, v199\n"
    "v_exp_f32 v200, v200\n"
    "v_exp_f32 v201, v201\n"
    "v_exp_f32 v202, v202\n"
    "v_exp_f32 v203, v203\n"
    "s_nop 1\n"
    "v_cvt_pk_bf16_f32 v96, v188, v189\n"
    "v_cvt_pk_bf16_f32 v97, v190, v191\n"
    "v_cvt_pk_bf16_f32 v98, v192, v193\n"
    "v_cvt_pk_bf16_f32 v99, v194, v195\n"
    "v_cvt_pk_bf16_f32 v100, v196, v197\n"
    "v_cvt_pk_bf16_f32 v101, v198, v199\n"
    "v_cvt_pk_bf16_f32 v102, v200, v201\n"
    "v_cvt_pk_bf16_f32 v103, v202, v203\n"
    "s_nop 3\n"
    "v_mfma_f32_16x16x32_bf16 v[176:179], a[192:195], v[96:99], 0\n"
    "v_mfma_f32_16x16x32_bf16 v[176:179], a[192:195], v[100:103], v[176:179]\n"
    "s_nop 7\n"
    "s_nop 7\n"
    "s_add_u32 s76, s76, 4\n"
    "s_addc_u32 s77, s77, 0\n"
    "s_setpc_b64 s[76:77]\n"
    "WA_1_0:\n"
    "s_waitcnt lgkmcnt(0)\n"
    "s_nop 7\n"
    "s_nop 7\n"
    "v_add_u32 v208, s75, v224\n"
    "v_add_u32 v209, s75, v225\n"
    "v_add_u32 v210, s75, v226\n"
    "v_add_u32 v211, s75, v227\n"
    "ds_read_b128 a[196:199], v208 offset:0\n"
    "ds_read_b128 a[200:203], v209 offset:0\n"
    "ds_read_b128 a[204:207], v210 offset:0\n"
    "ds_read_b128 a[208:211], v211 offset:0\n"
    "s_waitcnt lgkmcnt(0)\n"
    "v_mfma_f32_16x16x32_bf16 v[188:191], a[196:199], a[16:19], 0\n"
    "v_mfma_f32_16x16x32_bf16 v[188:191], a[200:203], a[20:23], v[188:191]\n"
    "v_mfma_f32_16x16x32_bf16 v[188:191], a[204:207], a[24:27], v[188:191]\n"
    "v_mfma_f32_16x16x32_bf16 v[188:191], a[208:211], a[28:31], v[188:191]\n"
    "s_nop 7\n"
    "ds_read_b128 a[196:199], v208 offset:4096\n"
    "ds_read_b128 a[200:203], v209 offset:4096\n"
    "ds_read_b128 a[204:207], v210 offset:4096\n"
    "ds_read_b128 a[208:211], v211 offset:4096\n"
    "s_waitcnt lgkmcnt(0)\n"
    "v_mfma_f32_16x16x32_bf16 v[192:195], a[196:199], a[16:19], 0\n"
    "v_mfma_f32_16x16x32_bf16 v[192:195], a[200:203], a[20:23], v[192:195]\n"
    "v_mfma_f32_16x16x32_bf16 v[192:195], a[204:207], a[24:27], v[192:195]\n"
    "v_mfma_f32_16x16x32_bf16 v[192:195], a[208:211], a[28:31], v[192:195]\n"
    "s_nop 7\n"
    "ds_read_b128 a[196:199], v208 offset:8192\n"
    "ds_read_b128 a[200:203], v209 offset:8192\n"
    "ds_read_b128 a[204:207], v210 offset:8192\n"
    "ds_read_b128 a[208:211], v211 offset:8192\n"
    "s_waitcnt lgkmcnt(0)\n"
    "v_mfma_f32_16x16x32_bf16 v[196:199], a[196:199], a[16:19], 0\n"
    "v_mfma_f32_16x16x32_bf16 v[196:199], a[200:203], a[20:23], v[196:199]\n"
    "v_mfma_f32_16x16x32_bf16 v[196:199], a[204:207], a[24:27], v[196:199]\n"
    "v_mfma_f32_16x16x32_bf16 v[196:199], a[208:211], a[28:31], v[196:199]\n"
    "s_nop 7\n"
    "ds_read_b128 a[196:199], v208 offset:12288\n"
    "ds_read_b128 a[200:203], v209 offset:12288\n"
    "ds_read_b128 a[204:207], v210 offset:12288\n"
    "ds_read_b128 a[208:211], v211 offset:12288\n"
    "s_waitcnt lgkmcnt(0)\n"
    "v_mfma_f32_16x16x32_bf16 v[200:203], a[196:199], a[16:19], 0\n"
    "v_mfma_f32_16x16x32_bf16 v[200:203], a[200:203], a[20:23], v[200:203]\n"
    "v_mfma_f32_16x16x32_bf16 v[200:203], a[204:207], a[24:27], v[200:203]\n"
    "v_mfma_f32_16x16x32_bf16 v[200:203], a[208:211], a[28:31], v[200:203]\n"
    "s_nop 7\n"
    "s_nop 7\n"
    "v_max3_f32 v244, v188, v189, v190\n"
    "v_max3_f32 v244, v244, v191, v192\n"
    "v_max3_f32 v244, v244, v193, v194\n"
    "v_max3_f32 v244, v244, v195, v196\n"
    "v_max3_f32 v244, v244, v197, v198\n"
    "v_max3_f32 v244, v244, v199, v200\n"
    "v_max3_f32 v244, v244, v201, v202\n"
    "v_max_f32 v244, v244, v203\n"
    "v_mov_b32 v245, v244\n"
    "s_nop 1\n"
    "v_permlane32_swap_b32 v244, v245\n"
    "v_max_f32 v244, v244, v245\n"
    "v_mov_b32 v245, v244\n"
    "s_nop 1\n"
    "v_permlane16_swap_b32 v244, v245\n"
    "v_max_f32 v244, v244, v245\n"
    "v_max_f32 v244, v244, -v164\n"
    "v_add_f32 v245, v244, v164\n"
    "v_exp_f32 v245, -v245\n"
    "v_sub_f32 v164, 0, v244\n"
    "v_sub_f32 v165, 0, v244\n"
    "v_sub_f32 v166, 0, v244\n"
    "v_sub_f32 v167, 0, v244\n"
    "v_accvgpr_read_b32 v246, a132\n"
    "v_mul_f32 v246, v245, v246\n"
    "v_accvgpr_write_b32 a132, v246\n"
    "v_accvgpr_read_b32 v246, a133\n"
    "v_mul_f32 v246, v245, v246\n"
    "v_accvgpr_write_b32 a133, v246\n"
    "v_accvgpr_read_b32 v246, a134\n"
    "v_mul_f32 v246, v245, v246\n"
    "v_accvgpr_write_b32 a134, v246\n"
    "v_accvgpr_read_b32 v246, a135\n"
    "v_mul_f32 v246, v245, v246\n"
    "v_accvgpr_write_b32 a135, v246\n"
    "v_accvgpr_read_b32 v246, a148\n"
    "v_mul_f32 v246, v245, v246\n"
    "v_accvgpr_write_b32 a148, v246\n"
    "v_accvgpr_read_b32 v246, a149\n"
    "v_mul_f32 v246, v245, v246\n"
    "v_accvgpr_write_b32 a149, v246\n"
    "v_accvgpr_read_b32 v246, a150\n"
    "v_mul_f32 v246, v245, v246\n"
    "v_accvgpr_write_b32 a150, v246\n"
    "v_accvgpr_read_b32 v246, a151\n"
    "v_mul_f32 v246, v245, v246\n"
    "v_accvgpr_write_b32 a151, v246\n"
    "v_accvgpr_read_b32 v246, a164\n"
    "v_mul_f32 v246, v245, v246\n"
    "v_accvgpr_write_b32 a164, v246\n"
    "v_accvgpr_read_b32 v246, a165\n"
    "v_mul_f32 v246, v245, v246\n"
    "v_accvgpr_write_b32 a165, v246\n"
    "v_accvgpr_read_b32 v246, a166\n"
    "v_mul_f32 v246, v245, v246\n"
    "v_accvgpr_write_b32 a166, v246\n"
    "v_accvgpr_read_b32 v246, a167\n"
    "v_mul_f32 v246, v245, v246\n"
    "v_accvgpr_write_b32 a167, v246\n"
    "v_accvgpr_read_b32 v246, a180\n"
    "v_mul_f32 v246, v245, v246\n"
    "v_accvgpr_write_b32 a180, v246\n"
    "v_accvgpr_read_b32 v246, a181\n"
    "v_mul_f32 v246, v245, v246\n"
    "v_accvgpr_write_b32 a181, v246\n"
    "v_accvgpr_read_b32 v246, a182\n"
    "v_mul_f32 v246, v245, v246\n"
    "v_accvgpr_write_b32 a182, v246\n"
    "v_accvgpr_read_b32 v246, a183\n"
    "v_mul_f32 v246, v245, v246\n"
    "v_accvgpr_write_b32 a183, v246\n"
    "v_mul_f32 v4, v245, v4\n"
    "v_mul_f32 v5, v245, v5\n"
    "v_mul_f32 v6, v245, v6\n"
    "v_mul_f32 v7, v245, v7\n"
    "v_mul_f32 v20, v245, v20\n"
    "v_mul_f32 v21, v245, v21\n"
    "v_mul_f32 v22, v245, v22\n"
    "v_mul_f32 v23, v245, v23\n"
    "v_mul_f32 v36, v245, v36\n"
    "v_mul_f32 v37, v245, v37\n"
    "v_mul_f32 v38, v245, v38\n"
    "v_mul_f32 v39, v245, v39\n"
    "v_mul_f32 v52, v245, v52\n"
    "v_mul_f32 v53, v245, v53\n"
    "v_mul_f32 v54, v245, v54\n"
    "v_mul_f32 v55, v245, v55\n"
    "v_mul_f32 v239, v245, v239\n"
    "v_sub_f32 v188, v188, v244\n"
    "v_sub_f32 v189, v189, v244\n"
    "v_sub_f32 v190, v190, v244\n"
    "v_sub_f32 v191, v191, v244\n"
    "v_sub_f32 v192, v192, v244\n"
    "v_sub_f32 v193, v193, v244\n"
    "v_sub_f32 v194, v194, v244\n"
    "v_sub_f32 v195, v195, v244\n"
    "v_sub_f32 v196, v196, v244\n"
    "v_sub_f32 v197, v197, v244\n"
    "v_sub_f32 v198, v198, v244\n"
    "v_sub_f32 v199, v199, v244\n"
    "v_sub_f32 v200, v200, v244\n"
    "v_sub_f32 v201, v201, v244\n"
    "v_sub_f32 v202, v202, v244\n"
    "v_sub_f32 v203, v203, v244\n"
    "v_exp_f32 v188, v188\n"
    "v_exp_f32 v189, v189\n"
    "v_exp_f32 v190, v190\n"
    "v_exp_f32 v191, v191\n"
    "v_exp_f32 v192, v192\n"
    "v_exp_f32 v193, v193\n"
    "v_exp_f32 v194, v194\n"
    "v_exp_f32 v195, v195\n"
    "v_exp_f32 v196, v196\n"
    "v_exp_f32 v197, v197\n"
    "v_exp_f32 v198, v198\n"
    "v_exp_f32 v199, v199\n"
    "v_exp_f32 v200, v200\n"
    "v_exp_f32 v201, v201\n"
    "v_exp_f32 v202, v202\n"
    "v_exp_f32 v203, v203\n"
    "s_nop 1\n"
    "v_cvt_pk_bf16_f32 v72, v188, v189\n"
    "v_cvt_pk_bf16_f32 v73, v190, v191\n"
    "v_cvt_pk_bf16_f32 v74, v192, v193\n"
    "v_cvt_pk_bf16_f32 v75, v194, v195\n"
    "v_cvt_pk_bf16_f32 v76, v196, v197\n"
    "v_cvt_pk_bf16_f32 v77, v198, v199\n"
    "v_cvt_pk_bf16_f32 v78, v200, v201\n"
    "v_cvt_pk_bf16_f32 v79, v202, v203\n"
    "s_nop 3\n"
    "v_mfma_f32_16x16x32_bf16 v[180:183], a[192:195], v[72:75], 0\n"
    "v_mfma_f32_16x16x32_bf16 v[180:183], a[192:195], v[76:79], v[180:183]\n"
    "s_nop 7\n"
    "s_nop 7\n"
    "s_add_u32 s76, s76, 4\n"
    "s_addc_u32 s77, s77, 0\n"
    "s_setpc_b64 s[76:77]\n"
    "WA_1_1:\n"
    "s_waitcnt lgkmcnt(0)\n"
    "s_nop 7\n"
    "s_nop 7\n"
    "v_add_u32 v208, s75, v224\n"
    "v_add_u32 v209, s75, v225\n"
    "v_add_u32 v210, s75, v226\n"
    "v_add_u32 v211, s75, v227\n"
    "ds_read_b128 a[196:199], v208 offset:0\n"
    "ds_read_b128 a[200:203], v209 offset:0\n"
    "ds_read_b128 a[204:207], v210 offset:0\n"
    "ds_read_b128 a[208:211], v211 offset:0\n"
    "s_waitcnt lgkmcnt(0)\n"
    "v_mfma_f32_16x16x32_bf16 v[188:191], a[196:199], a[16:19], 0\n"
    "v_mfma_f32_16x16x32_bf16 v[188:191], a[200:203], a[20:23], v[188:191]\n"
    "v_mfma_f32_16x16x32_bf16 v[188:191], a[204:207], a[24:27], v[188:191]\n"
    "v_mfma_f32_16x16x32_bf16 v[188:191], a[208:211], a[28:31], v[188:191]\n"
    "s_nop 7\n"
    "ds_read_b128 a[196:199], v208 offset:4096\n"
    "ds_read_b128 a[200:203], v209 offset:4096\n"
    "ds_read_b128 a[204:207], v210 offset:4096\n"
    "ds_read_b128 a[208:211], v211 offset:4096\n"
    "s_waitcnt lgkmcnt(0)\n"
    "v_mfma_f32_16x16x32_bf16 v[192:195], a[196:199], a[16:19], 0\n"
    "v_mfma_f32_16x16x32_bf16 v[192:195], a[200:203], a[20:23], v[192:195]\n"
    "v_mfma_f32_16x16x32_bf16 v[192:195], a[204:207], a[24:27], v[192:195]\n"
    "v_mfma_f32_16x16x32_bf16 v[192:195], a[208:211], a[28:31], v[192:195]\n"
    "s_nop 7\n"
    "ds_read_b128 a[196:199], v208 offset:8192\n"
    "ds_read_b128 a[200:203], v209 offset:8192\n"
    "ds_read_b128 a[204:207], v210 offset:8192\n"
    "ds_read_b128 a[208:211], v211 offset:8192\n"
    "s_waitcnt lgkmcnt(0)\n"
    "v_mfma_f32_16x16x32_bf16 v[196:199], a[196:199], a[16:19], 0\n"
    "v_mfma_f32_16x16x32_bf16 v[196:199], a[200:203], a[20:23], v[196:199]\n"
    "v_mfma_f32_16x16x32_bf16 v[196:199], a[204:207], a[24:27], v[196:199]\n"
    "v_mfma_f32_16x16x32_bf16 v[196:199], a[208:211], a[28:31], v[196:199]\n"
    "s_nop 7\n"
    "ds_read_b128 a[196:199], v208 offset:12288\n"
    "ds_read_b128 a[200:203], v209 offset:12288\n"
    "ds_read_b128 a[204:207], v210 offset:12288\n"
    "ds_read_b128 a[208:211], v211 offset:12288\n"
    "s_waitcnt lgkmcnt(0)\n"
    "v_mfma_f32_16x16x32_bf16 v[200:203], a[196:199], a[16:19], 0\n"
    "v_mfma_f32_16x16x32_bf16 v[200:203], a[200:203], a[20:23], v[200:203]\n"
    "v_mfma_f32_16x16x32_bf16 v[200:203], a[204:207], a[24:27], v[200:203]\n"
    "v_mfma_f32_16x16x32_bf16 v[200:203], a[208:211], a[28:31], v[200:203]\n"
    "s_nop 7\n"
    "s_nop 7\n"
    "v_max3_f32 v244, v188, v189, v190\n"
    "v_max3_f32 v244, v244, v191, v192\n"
    "v_max3_f32 v244, v244, v193, v194\n"
    "v_max3_f32 v244, v244, v195, v196\n"
    "v_max3_f32 v244, v244, v197, v198\n"
    "v_max3_f32 v244, v244, v199, v200\n"
    "v_max3_f32 v244, v244, v201, v202\n"
    "v_max_f32 v244, v244, v203\n"
    "v_mov_b32 v245, v244\n"
    "s_nop 1\n"
    "v_permlane32_swap_b32 v244, v245\n"
    "v_max_f32 v244, v244, v245\n"
    "v_mov_b32 v245, v244\n"
    "s_nop 1\n"
    "v_permlane16_swap_b32 v244, v245\n"
    "v_max_f32 v244, v244, v245\n"
    "v_max_f32 v244, v244, -v164\n"
    "v_add_f32 v245, v244, v164\n"
    "v_exp_f32 v245, -v245\n"
    "v_sub_f32 v164, 0, v244\n"
    "v_sub_f32 v165, 0, v244\n"
    "v_sub_f32 v166, 0, v244\n"
    "v_sub_f32 v167, 0, v244\n"
    "v_accvgpr_read_b32 v246, a132\n"
    "v_mul_f32 v246, v245, v246\n"
    "v_accvgpr_write_b32 a132, v246\n"
    "v_accvgpr_read_b32 v246, a133\n"
    "v_mul_f32 v246, v245, v246\n"
    "v_accvgpr_write_b32 a133, v246\n"
    "v_accvgpr_read_b32 v246, a134\n"
    "v_mul_f32 v246, v245, v246\n"
    "v_accvgpr_write_b32 a134, v246\n"
    "v_accvgpr_read_b32 v246, a135\n"
    "v_mul_f32 v246, v245, v246\n"
    "v_accvgpr_write_b32 a135, v246\n"
    "v_accvgpr_read_b32 v246, a148\n"
    "v_mul_f32 v246, v245, v246\n"
    "v_accvgpr_write_b32 a148, v246\n"
    "v_accvgpr_read_b32 v246, a149\n"
    "v_mul_f32 v246, v245, v246\n"
    "v_accvgpr_write_b32 a149, v246\n"
    "v_accvgpr_read_b32 v246, a150\n"
    "v_mul_f32 v246, v245, v246\n"
    "v_accvgpr_write_b32 a150, v246\n"
    "v_accvgpr_read_b32 v246, a151\n"
    "v_mul_f32 v246, v245, v246\n"
    "v_accvgpr_write_b32 a151, v246\n"
    "v_accvgpr_read_b32 v246, a164\n"
    "v_mul_f32 v246, v245, v246\n"
    "v_accvgpr_write_b32 a164, v246\n"
    "v_accvgpr_read_b32 v246, a165\n"
    "v_mul_f32 v246, v245, v246\n"
    "v_accvgpr_write_b32 a165, v246\n"
    "v_accvgpr_read_b32 v246, a166\n"
    "v_mul_f32 v246, v245, v246\n"
    "v_accvgpr_write_b32 a166, v246\n"
    "v_accvgpr_read_b32 v246, a167\n"
    "v_mul_f32 v246, v245, v246\n"
    "v_accvgpr_write_b32 a167, v246\n"
    "v_accvgpr_read_b32 v246, a180\n"
    "v_mul_f32 v246, v245, v246\n"
    "v_accvgpr_write_b32 a180, v246\n"
    "v_accvgpr_read_b32 v246, a181\n"
    "v_mul_f32 v246, v245, v246\n"
    "v_accvgpr_write_b32 a181, v246\n"
    "v_accvgpr_read_b32 v246, a182\n"
    "v_mul_f32 v246, v245, v246\n"
    "v_accvgpr_write_b32 a182, v246\n"
    "v_accvgpr_read_b32 v246, a183\n"
    "v_mul_f32 v246, v245, v246\n"
    "v_accvgpr_write_b32 a183, v246\n"
    "v_mul_f32 v4, v245, v4\n"
    "v_mul_f32 v5, v245, v5\n"
    "v_mul_f32 v6, v245, v6\n"
    "v_mul_f32 v7, v245, v7\n"
    "v_mul_f32 v20, v245, v20\n"
    "v_mul_f32 v21, v245, v21\n"
    "v_mul_f32 v22, v245, v22\n"
    "v_mul_f32 v23, v245, v23\n"
    "v_mul_f32 v36, v245, v36\n"
    "v_mul_f32 v37, v245, v37\n"
    "v_mul_f32 v38, v245, v38\n"
    "v_mul_f32 v39, v245, v39\n"
    "v_mul_f32 v52, v245, v52\n"
    "v_mul_f32 v53, v245, v53\n"
    "v_mul_f32 v54, v245, v54\n"
    "v_mul_f32 v55, v245, v55\n"
    "v_mul_f32 v239, v245, v239\n"
    "v_sub_f32 v188, v188, v244\n"
    "v_sub_f32 v189, v189, v244\n"
    "v_sub_f32 v190, v190, v244\n"
    "v_sub_f32 v191, v191, v244\n"
    "v_sub_f32 v192, v192, v244\n"
    "v_sub_f32 v193, v193, v244\n"
    "v_sub_f32 v194, v194, v244\n"
    "v_sub_f32 v195, v195, v244\n"
    "v_sub_f32 v196, v196, v244\n"
    "v_sub_f32 v197, v197, v244\n"
    "v_sub_f32 v198, v198, v244\n"
    "v_sub_f32 v199, v199, v244\n"
    "v_sub_f32 v200, v200, v244\n"
    "v_sub_f32 v201, v201, v244\n"
    "v_sub_f32 v202, v202, v244\n"
    "v_sub_f32 v203, v203, v244\n"
    "v_exp_f32 v188, v188\n"
    "v_exp_f32 v189, v189\n"
    "v_exp_f32 v190, v190\n"
    "v_exp_f32 v191, v191\n"
    "v_exp_f32 v192, v192\n"
    "v_exp_f32 v193, v193\n"
    "v_exp_f32 v194, v194\n"
    "v_exp_f32 v195, v195\n"
    "v_exp_f32 v196, v196\n"
    "v_exp_f32 v197, v197\n"
    "v_exp_f32 v198, v198\n"
    "v_exp_f32 v199, v199\n"
    "v_exp_f32 v200, v200\n"
    "v_exp_f32 v201, v201\n"
    "v_exp_f32 v202, v202\n"
    "v_exp_f32 v203, v203\n"
    "s_nop 1\n"
    "v_cvt_pk_bf16_f32 v104, v188, v189\n"
    "v_cvt_pk_bf16_f32 v105, v190, v191\n"
    "v_cvt_pk_bf16_f32 v106, v192, v193\n"
    "v_cvt_pk_bf16_f32 v107, v194, v195\n"
    "v_cvt_pk_bf16_f32 v108, v196, v197\n"
    "v_cvt_pk_bf16_f32 v109, v198, v199\n"
    "v_cvt_pk_bf16_f32 v110, v200, v201\n"
    "v_cvt_pk_bf16_f32 v111, v202, v203\n"
    "s_nop 3\n"
    "v_mfma_f32_16x16x32_bf16 v[180:183], a[192:195], v[104:107], 0\n"
    "v_mfma_f32_16x16x32_bf16 v[180:183], a[192:195], v[108:111], v[180:183]\n"
    "s_nop 7\n"
    "s_nop 7\n"
    "s_add_u32 s76, s76, 4\n"
    "s_addc_u32 s77, s77, 0\n"
    "s_setpc_b64 s[76:77]\n"
    "WA_2_0:\n"
    "s_waitcnt lgkmcnt(0)\n"
    "s_nop 7\n"
    "s_nop 7\n"
    "v_add_u32 v208, s75, v224\n"
    "v_add_u32 v209, s75, v225\n"
    "v_add_u32 v210, s75, v226\n"
    "v_add_u32 v211, s75, v227\n"
    "ds_read_b128 a[196:199], v208 offset:0\n"
    "ds_read_b128 a[200:203], v209 offset:0\n"
    "ds_read_b128 a[204:207], v210 offset:0\n"
    "ds_read_b128 a[208:211], v211 offset:0\n"
    "s_waitcnt lgkmcnt(0)\n"
    "v_mfma_f32_16x16x32_bf16 v[188:191], a[196:199], a[32:35], 0\n"
    "v_mfma_f32_16x16x32_bf16 v[188:191], a[200:203], a[36:39], v[188:191]\n"
    "v_mfma_f32_16x16x32_bf16 v[188:191], a[204:207], a[40:43], v[188:191]\n"
    "v_mfma_f32_16x16x32_bf16 v[188:191], a[208:211], a[44:47], v[188:191]\n"
    "s_nop 7\n"
    "ds_read_b128 a[196:199], v208 offset:4096\n"
    "ds_read_b128 a[200:203], v209 offset:4096\n"
    "ds_read_b128 a[204:207], v210 offset:4096\n"
    "ds_read_b128 a[208:211], v211 offset:4096\n"
    "s_waitcnt lgkmcnt(0)\n"
    "v_mfma_f32_16x16x32_bf16 v[192:195], a[196:199], a[32:35], 0\n"
    "v_mfma_f32_16x16x32_bf16 v[192:195], a[200:203], a[36:39], v[192:195]\n"
    "v_mfma_f32_16x16x32_bf16 v[192:195], a[204:207], a[40:43], v[192:195]\n"
    "v_mfma_f32_16x16x32_bf16 v[192:195], a[208:211], a[44:47], v[192:195]\n"
    "s_nop 7\n"
    "ds_read_b128 a[196:199], v208 offset:8192\n"
    "ds_read_b128 a[200:203], v209 offset:8192\n"
    "ds_read_b128 a[204:207], v210 offset:8192\n"
    "ds_read_b128 a[208:211], v211 offset:8192\n"
    "s_waitcnt lgkmcnt(0)\n"
    "v_mfma_f32_16x16x32_bf16 v[196:199], a[196:199], a[32:35], 0\n"
    "v_mfma_f32_16x16x32_bf16 v[196:199], a[200:203], a[36:39], v[196:199]\n"
    "v_mfma_f32_16x16x32_bf16 v[196:199], a[204:207], a[40:43], v[196:199]\n"
    "v_mfma_f32_16x16x32_bf16 v[196:199], a[208:211], a[44:47], v[196:199]\n"
    "s_nop 7\n"
    "ds_read_b128 a[196:199], v208 offset:12288\n"
    "ds_read_b128 a[200:203], v209 offset:12288\n"
    "ds_read_b128 a[204:207], v210 offset:12288\n"
    "ds_read_b128 a[208:211], v211 offset:12288\n"
    "s_waitcnt lgkmcnt(0)\n"
    "v_mfma_f32_16x16x32_bf16 v[200:203], a[196:199], a[32:35], 0\n"
    "v_mfma_f32_16x16x32_bf16 v[200:203], a[200:203], a[36:39], v[200:203]\n"
    "v_mfma_f32_16x16x32_bf16 v[200:203], a[204:207], a[40:43], v[200:203]\n"
    "v_mfma_f32_16x16x32_bf16 v[200:203], a[208:211], a[44:47], v[200:203]\n"
    "s_nop 7\n"
    "s_nop 7\n"
    "v_max3_f32 v244, v188, v189, v190\n"
    "v_max3_f32 v244, v244, v191, v192\n"
    "v_max3_f32 v244, v244, v193, v194\n"
    "v_max3_f32 v244, v244, v195, v196\n"
    "v_max3_f32 v244, v244, v197, v198\n"
    "v_max3_f32 v244, v244, v199, v200\n"
    "v_max3_f32 v244, v244, v201, v202\n"
    "v_max_f32 v244, v244, v203\n"
    "v_mov_b32 v245, v244\n"
    "s_nop 1\n"
    "v_permlane32_swap_b32 v244, v245\n"
    "v_max_f32 v244, v244, v245\n"
    "v_mov_b32 v245, v244\n"
    "s_nop 1\n"
    "v_permlane16_swap_b32 v244, v245\n"
    "v_max_f32 v244, v244, v245\n"
    "v_max_f32 v244, v244, -v168\n"
    "v_add_f32 v245, v244, v168\n"
    "v_exp_f32 v245, -v245\n"
    "v_sub_f32 v168, 0, v244\n"
    "v_sub_f32 v169, 0, v244\n"
    "v_sub_f32 v170, 0, v244\n"
    "v_sub_f32 v171, 0, v244\n"
    "v_accvgpr_read_b32 v246, a136\n"
    "v_mul_f32 v246, v245, v246\n"
    "v_accvgpr_write_b32 a136, v246\n"
    "v_accvgpr_read_b32 v246, a137\n"
    "v_mul_f32 v246, v245, v246\n"
    "v_accvgpr_write_b32 a137, v246\n"
    "v_accvgpr_read_b32 v246, a138\n"
    "v_mul_f32 v246, v245, v246\n"
    "v_accvgpr_write_b32 a138, v246\n"
    "v_accvgpr_read_b32 v246, a139\n"
    "v_mul_f32 v246, v245, v246\n"
    "v_accvgpr_write_b32 a139, v246\n"
    "v_accvgpr_read_b32 v246, a152\n"
    "v_mul_f32 v246, v245, v246\n"
    "v_accvgpr_write_b32 a152, v246\n"
    "v_accvgpr_read_b32 v246, a153\n"
    "v_mul_f32 v246, v245, v246\n"
    "v_accvgpr_write_b32 a153, v246\n"
    "v_accvgpr_read_b32 v246, a154\n"
    "v_mul_f32 v246, v245, v246\n"
    "v_accvgpr_write_b32 a154, v246\n"
    "v_accvgpr_read_b32 v246, a155\n"
    "v_mul_f32 v246, v245, v246\n"
    "v_accvgpr_write_b32 a155, v246\n"
    "v_accvgpr_read_b32 v246, a168\n"
    "v_mul_f32 v246, v245, v246\n"
    "v_accvgpr_write_b32 a168, v246\n"
    "v_accvgpr_read_b32 v246, a169\n"
    "v_mul_f32 v246, v245, v246\n"
    "v_accvgpr_write_b32 a169, v246\n"
    "v_accvgpr_read_b32 v246, a170\n"
    "v_mul_f32 v246, v245, v246\n"
    "v_accvgpr_write_b32 a170, v246\n"
    "v_accvgpr_read_b32 v246, a171\n"
    "v_mul_f32 v246, v245, v246\n"
    "v_accvgpr_write_b32 a171, v246\n"
    "v_accvgpr_read_b32 v246, a184\n"
    "v_mul_f32 v246, v245, v246\n"
    "v_accvgpr_write_b32 a184, v246\n"
    "v_accvgpr_read_b32 v246, a185\n"
    "v_mul_f32 v246, v245, v246\n"
    "v_accvgpr_write_b32 a185, v246\n"
    "v_accvgpr_read_b32 v246, a186\n"
    "v_mul_f32 v246, v245, v246\n"
    "v_accvgpr_write_b32 a186, v246\n"
    "v_accvgpr_read_b32 v246, a187\n"
    "v_mul_f32 v246, v245, v246\n"
    "v_accvgpr_write_b32 a187, v246\n"
    "v_mul_f32 v8, v245, v8\n"
    "v_mul_f32 v9, v245, v9\n"
    "v_mul_f32 v10, v245, v10\n"
    "v_mul_f32 v11, v245, v11\n"
    "v_mul_f32 v24, v245, v24\n"
    "v_mul_f32 v25, v245, v25\n"
    "v_mul_f32 v26, v245, v26\n"
    "v_mul_f32 v27, v245, v27\n"
    "v_mul_f32 v40, v245, v40\n"
    "v_mul_f32 v41, v245, v41\n"
    "v_mul_f32 v42, v245, v42\n"
    "v_mul_f32 v43, v245, v43\n"
    "v_mul_f32 v56, v245, v56\n"
    "v_mul_f32 v57, v245, v57\n"
    "v_mul_f32 v58, v245, v58\n"
    "v_mul_f32 v59, v245, v59\n"
    "v_mul_f32 v240, v245, v240\n"
    "v_sub_f32 v188, v188, v244\n"
    "v_sub_f32 v189, v189, v244\n"
    "v_sub_f32 v190, v190, v244\n"
    "v_sub_f32 v191, v191, v244\n"
    "v_sub_f32 v192, v192, v244\n"
    "v_sub_f32 v193, v193, v244\n"
    "v_sub_f32 v194, v194, v244\n"
    "v_sub_f32 v195, v195, v244\n"
    "v_sub_f32 v196, v196, v244\n"
    "v_sub_f32 v197, v197, v244\n"
    "v_sub_f32 v198, v198, v244\n"
    "v_sub_f32 v199, v199, v244\n"
    "v_sub_f32 v200, v200, v244\n"
    "v_sub_f32 v201, v201, v244\n"
    "v_sub_f32 v202, v202, v244\n"
    "v_sub_f32 v203, v203, v244\n"
    "v_exp_f32 v188, v188\n"
    "v_exp_f32 v189, v189\n"
    "v_exp_f32 v190, v190\n"
    "v_exp_f32 v191, v191\n"
    "v_exp_f32 v192, v192\n"
    "v_exp_f32 v193, v193\n"
    "v_exp_f32 v194, v194\n"
    "v_exp_f32 v195, v195\n"
    "v_exp_f32 v196, v196\n"
    "v_exp_f32 v197, v197\n"
    "v_exp_f32 v198, v198\n"
    "v_exp_f32 v199, v199\n"
    "v_exp_f32 v200, v200\n"
    "v_exp_f32 v201, v201\n"
    "v_exp_f32 v202, v202\n"
    "v_exp_f32 v203, v203\n"
    "s_nop 1\n"
    "v_cvt_pk_bf16_f32 v80, v188, v189\n"
    "v_cvt_pk_bf16_f32 v81, v190, v191\n"
    "v_cvt_pk_bf16_f32 v82, v192, v193\n"
    "v_cvt_pk_bf16_f32 v83, v194, v195\n"
    "v_cvt_pk_bf16_f32 v84, v196, v197\n"
    "v_cvt_pk_bf16_f32 v85, v198, v199\n"
    "v_cvt_pk_bf16_f32 v86, v200, v201\n"
    "v_cvt_pk_bf16_f32 v87, v202, v203\n"
    "s_nop 3\n"
    "v_mfma_f32_16x16x32_bf16 v[176:179], a[192:195], v[80:83], 0\n"
    "v_mfma_f32_16x16x32_bf16 v[176:179], a[192:195], v[84:87], v[176:179]\n"
    "s_nop 7\n"
    "s_nop 7\n"
    "s_add_u32 s76, s76, 4\n"
    "s_addc_u32 s77, s77, 0\n"
    "s_setpc_b64 s[76:77]\n"
    "WA_2_1:\n"
    "s_waitcnt lgkmcnt(0)\n"
    "s_nop 7\n"
    "s_nop 7\n"
    "v_add_u32 v208, s75, v224\n"
    "v_add_u32 v209, s75, v225\n"
    "v_add_u32 v210, s75, v226\n"
    "v_add_u32 v211, s75, v227\n"
    "ds_read_b128 a[196:199], v208 offset:0\n"
    "ds_read_b128 a[200:203], v209 offset:0\n"
    "ds_read_b128 a[204:207], v210 offset:0\n"
    "ds_read_b128 a[208:211], v211 offset:0\n"
    "s_waitcnt lgkmcnt(0)\n"
    "v_mfma_f32_16x16x32_bf16 v[188:191], a[196:199], a[32:35], 0\n"
    "v_mfma_f32_16x16x32_bf16 v[188:191], a[200:203], a[36:39], v[188:191]\n"
    "v_mfma_f32_16x16x32_bf16 v[188:191], a[204:207], a[40:43], v[188:191]\n"
    "v_mfma_f32_16x16x32_bf16 v[188:191], a[208:211], a[44:47], v[188:191]\n"
    "s_nop 7\n"
    "ds_read_b128 a[196:199], v208 offset:4096\n"
    "ds_read_b128 a[200:203], v209 offset:4096\n"
    "ds_read_b128 a[204:207], v210 offset:4096\n"
    "ds_read_b128 a[208:211], v211 offset:4096\n"
    "s_waitcnt lgkmcnt(0)\n"
    "v_mfma_f32_16x16x32_bf16 v[192:195], a[196:199], a[32:35], 0\n"
    "v_mfma_f32_16x16x32_bf16 v[192:195], a[200:203], a[36:39], v[192:195]\n"
    "v_mfma_f32_16x16x32_bf16 v[192:195], a[204:207], a[40:43], v[192:195]\n"
    "v_mfma_f32_16x16x32_bf16 v[192:195], a[208:211], a[44:47], v[192:195]\n"
    "s_nop 7\n"
    "ds_read_b128 a[196:199], v208 offset:8192\n"
    "ds_read_b128 a[200:203], v209 offset:8192\n"
    "ds_read_b128 a[204:207], v210 offset:8192\n"
    "ds_read_b128 a[208:211], v211 offset:8192\n"
    "s_waitcnt lgkmcnt(0)\n"
    "v_mfma_f32_16x16x32_bf16 v[196:199], a[196:199], a[32:35], 0\n"
    "v_mfma_f32_16x16x32_bf16 v[196:199], a[200:203], a[36:39], v[196:199]\n"
    "v_mfma_f32_16x16x32_bf16 v[196:199], a[204:207], a[40:43], v[196:199]\n"
    "v_mfma_f32_16x16x32_bf16 v[196:199], a[208:211], a[44:47], v[196:199]\n"
    "s_nop 7\n"
    "ds_read_b128 a[196:199], v208 offset:12288\n"
    "ds_read_b128 a[200:203], v209 offset:12288\n"
    "ds_read_b128 a[204:207], v210 offset:12288\n"
    "ds_read_b128 a[208:211], v211 offset:12288\n"
    "s_waitcnt lgkmcnt(0)\n"
    "v_mfma_f32_16x16x32_bf16 v[200:203], a[196:199], a[32:35], 0\n"
    "v_mfma_f32_16x16x32_bf16 v[200:203], a[200:203], a[36:39], v[200:203]\n"
    "v_mfma_f32_16x16x32_bf16 v[200:203], a[204:207], a[40:43], v[200:203]\n"
    "v_mfma_f32_16x16x32_bf16 v[200:203], a[208:211], a[44:47], v[200:203]\n"
    "s_nop 7\n"
    "s_nop 7\n"
    "v_max3_f32 v244, v188, v189, v190\n"
    "v_max3_f32 v244, v244, v191, v192\n"
    "v_max3_f32 v244, v244, v193, v194\n"
    "v_max3_f32 v244, v244, v195, v196\n"
    "v_max3_f32 v244, v244, v197, v198\n"
    "v_max3_f32 v244, v244, v199, v200\n"
    "v_max3_f32 v244, v244, v201, v202\n"
    "v_max_f32 v244, v244, v203\n"
    "v_mov_b32 v245, v244\n"
    "s_nop 1\n"
    "v_permlane32_swap_b32 v244, v245\n"
    "v_max_f32 v244, v244, v245\n"
    "v_mov_b32 v245, v244\n"
    "s_nop 1\n"
    "v_permlane16_swap_b32 v244, v245\n"
    "v_max_f32 v244, v244, v245\n"
    "v_max_f32 v244, v244, -v168\n"
    "v_add_f32 v245, v244, v168\n"
    "v_exp_f32 v245, -v245\n"
    "v_sub_f32 v168, 0, v244\n"
    "v_sub_f32 v169, 0, v244\n"
    "v_sub_f32 v170, 0, v244\n"
    "v_sub_f32 v171, 0, v244\n"
    "v_accvgpr_read_b32 v246, a136\n"
    "v_mul_f32 v246, v245, v246\n"
    "v_accvgpr_write_b32 a136, v246\n"
    "v_accvgpr_read_b32 v246, a137\n"
    "v_mul_f32 v246, v245, v246\n"
    "v_accvgpr_write_b32 a137, v246\n"
    "v_accvgpr_read_b32 v246, a138\n"
    "v_mul_f32 v246, v245, v246\n"
    "v_accvgpr_write_b32 a138, v246\n"
    "v_accvgpr_read_b32 v246, a139\n"
    "v_mul_f32 v246, v245, v246\n"
    "v_accvgpr_write_b32 a139, v246\n"
    "v_accvgpr_read_b32 v246, a152\n"
    "v_mul_f32 v246, v245, v246\n"
    "v_accvgpr_write_b32 a152, v246\n"
    "v_accvgpr_read_b32 v246, a153\n"
    "v_mul_f32 v246, v245, v246\n"
    "v_accvgpr_write_b32 a153, v246\n"
    "v_accvgpr_read_b32 v246, a154\n"
    "v_mul_f32 v246, v245, v246\n"
    "v_accvgpr_write_b32 a154, v246\n"
    "v_accvgpr_read_b32 v246, a155\n"
    "v_mul_f32 v246, v245, v246\n"
    "v_accvgpr_write_b32 a155, v246\n"
    "v_accvgpr_read_b32 v246, a168\n"
    "v_mul_f32 v246, v245, v246\n"
    "v_accvgpr_write_b32 a168, v246\n"
    "v_accvgpr_read_b32 v246, a169\n"
    "v_mul_f32 v246, v245, v246\n"
    "v_accvgpr_write_b32 a169, v246\n"
    "v_accvgpr_read_b32 v246, a170\n"
    "v_mul_f32 v246, v245, v246\n"
    "v_accvgpr_write_b32 a170, v246\n"
    "v_accvgpr_read_b32 v246, a171\n"
    "v_mul_f32 v246, v245, v246\n"
    "v_accvgpr_write_b32 a171, v246\n"
    "v_accvgpr_read_b32 v246, a184\n"
    "v_mul_f32 v246, v245, v246\n"
    "v_accvgpr_write_b32 a184, v246\n"
    "v_accvgpr_read_b32 v246, a185\n"
    "v_mul_f32 v246, v245, v246\n"
    "v_accvgpr_write_b32 a185, v246\n"
    "v_accvgpr_read_b32 v246, a186\n"
    "v_mul_f32 v246, v245, v246\n"
    "v_accvgpr_write_b32 a186, v246\n"
    "v_accvgpr_read_b32 v246, a187\n"
    "v_mul_f32 v246, v245, v246\n"
    "v_accvgpr_write_b32 a187, v246\n"
    "v_mul_f32 v8, v245, v8\n"
    "v_mul_f32 v9, v245, v9\n"
    "v_mul_f32 v10, v245, v10\n"
    "v_mul_f32 v11, v245, v11\n"
    "v_mul_f32 v24, v245, v24\n"
    "v_mul_f32 v25, v245, v25\n"
    "v_mul_f32 v26, v245, v26\n"
    "v_mul_f32 v27, v245, v27\n"
    "v_mul_f32 v40, v245, v40\n"
    "v_mul_f32 v41, v245, v41\n"
    "v_mul_f32 v42, v245, v42\n"
    "v_mul_f32 v43, v245, v43\n"
    "v_mul_f32 v56, v245, v56\n"
    "v_mul_f32 v57, v245, v57\n"
    "v_mul_f32 v58, v245, v58\n"
    "v_mul_f32 v59, v245, v59\n"
    "v_mul_f32 v240, v245, v240\n"
    "v_sub_f32 v188, v188, v244\n"
    "v_sub_f32 v189, v189, v244\n"
    "v_sub_f32 v190, v190, v244\n"
    "v_sub_f32 v191, v191, v244\n"
    "v_sub_f32 v192, v192, v244\n"
    "v_sub_f32 v193, v193, v244\n"
    "v_sub_f32 v194, v194, v244\n"
    "v_sub_f32 v195, v195, v244\n"
    "v_sub_f32 v196, v196, v244\n"
    "v_sub_f32 v197, v197, v244\n"
    "v_sub_f32 v198, v198, v244\n"
    "v_sub_f32 v199, v199, v244\n"
    "v_sub_f32 v200, v200, v244\n"
    "v_sub_f32 v201, v201, v244\n"
    "v_sub_f32 v202, v202, v244\n"
    "v_sub_f32 v203, v203, v244\n"
    "v_exp_f32 v188, v188\n"
    "v_exp_f32 v189, v189\n"
    "v_exp_f32 v190, v190\n"
    "v_exp_f32 v191, v191\n"
    "v_exp_f32 v192, v192\n"
    "v_exp_f32 v193, v193\n"
    "v_exp_f32 v194, v194\n"
    "v_exp_f32 v195, v195\n"
    "v_exp_f32 v196, v196\n"
    "v_exp_f32 v197, v197\n"
    "v_exp_f32 v198, v198\n"
    "v_exp_f32 v199, v199\n"
    "v_exp_f32 v200, v200\n"
    "v_exp_f32 v201, v201\n"
    "v_exp_f32 v202, v202\n"
    "v_exp_f32 v203, v203\n"
    "s_nop 1\n"
    "v_cvt_pk_bf16_f32 v112, v188, v189\n"
    "v_cvt_pk_bf16_f32 v113, v190, v191\n"
    "v_cvt_pk_bf16_f32 v114, v192, v193\n"
    "v_cvt_pk_bf16_f32 v115, v194, v195\n"
    "v_cvt_pk_bf16_f32 v116, v196, v197\n"
    "v_cvt_pk_bf16_f32 v117, v198, v199\n"
    "v_cvt_pk_bf16_f32 v118, v200, v201\n"
    "v_cvt_pk_bf16_f32 v119, v202, v203\n"
    "s_nop 3\n"
    "v_mfma_f32_16x16x32_bf16 v[176:179], a[192:195], v[112:115], 0\n"
    "v_mfma_f32_16x16x32_bf16 v[176:179], a[192:195], v[116:119], v[176:179]\n"
    "s_nop 7\n"
    "s_nop 7\n"
    "s_add_u32 s76, s76, 4\n"
    "s_addc_u32 s77, s77, 0\n"
    "s_setpc_b64 s[76:77]\n"
    "WA_3_0:\n"
    "s_waitcnt lgkmcnt(0)\n"
    "s_nop 7\n"
    "s_nop 7\n"
    "v_add_u32 v208, s75, v224\n"
    "v_add_u32 v209, s75, v225\n"
    "v_add_u32 v210, s75, v226\n"
    "v_add_u32 v211, s75, v227\n"
    "ds_read_b128 a[196:199], v208 offset:0\n"
    "ds_read_b128 a[200:203], v209 offset:0\n"
    "ds_read_b128 a[204:207], v210 offset:0\n"
    "ds_read_b128 a[208:211], v211 offset:0\n"
    "s_waitcnt lgkmcnt(0)\n"
    "v_mfma_f32_16x16x32_bf16 v[188:191], a[196:199], a[48:51], 0\n"
    "v_mfma_f32_16x16x32_bf16 v[188:191], a[200:203], a[52:55], v[188:191]\n"
    "v_mfma_f32_16x16x32_bf16 v[188:191], a[204:207], a[56:59], v[188:191]\n"
    "v_mfma_f32_16x16x32_bf16 v[188:191], a[208:211], a[60:63], v[188:191]\n"
    "s_nop 7\n"
    "ds_read_b128 a[196:199], v208 offset:4096\n"
    "ds_read_b128 a[200:203], v209 offset:4096\n"
    "ds_read_b128 a[204:207], v210 offset:4096\n"
    "ds_read_b128 a[208:211], v211 offset:4096\n"
    "s_waitcnt lgkmcnt(0)\n"
    "v_mfma_f32_16x16x32_bf16 v[192:195], a[196:199], a[48:51], 0\n"
    "v_mfma_f32_16x16x32_bf16 v[192:195], a[200:203], a[52:55], v[192:195]\n"
    "v_mfma_f32_16x16x32_bf16 v[192:195], a[204:207], a[56:59], v[192:195]\n"
    "v_mfma_f32_16x16x32_bf16 v[192:195], a[208:211], a[60:63], v[192:195]\n"
    "s_nop 7\n"
    "ds_read_b128 a[196:199], v208 offset:8192\n"
    "ds_read_b128 a[200:203], v209 offset:8192\n"
    "ds_read_b128 a[204:207], v210 offset:8192\n"
    "ds_read_b128 a[208:211], v211 offset:8192\n"
    "s_waitcnt lgkmcnt(0)\n"
    "v_mfma_f32_16x16x32_bf16 v[196:199], a[196:199], a[48:51], 0\n"
    "v_mfma_f32_16x16x32_bf16 v[196:199], a[200:203], a[52:55], v[196:199]\n"
    "v_mfma_f32_16x16x32_bf16 v[196:199], a[204:207], a[56:59], v[196:199]\n"
    "v_mfma_f32_16x16x32_bf16 v[196:199], a[208:211], a[60:63], v[196:199]\n"
    "s_nop 7\n"
    "ds_read_b128 a[196:199], v208 offset:12288\n"
    "ds_read_b128 a[200:203], v209 offset:12288\n"
    "ds_read_b128 a[204:207], v210 offset:12288\n"
    "ds_read_b128 a[208:211], v211 offset:12288\n"
    "s_waitcnt lgkmcnt(0)\n"
    "v_mfma_f32_16x16x32_bf16 v[200:203], a[196:199], a[48:51], 0\n"
    "v_mfma_f32_16x16x32_bf16 v[200:203], a[200:203], a[52:55], v[200:203]\n"
    "v_mfma_f32_16x16x32_bf16 v[200:203], a[204:207], a[56:59], v[200:203]\n"
    "v_mfma_f32_16x16x32_bf16 v[200:203], a[208:211], a[60:63], v[200:203]\n"
    "s_nop 7\n"
    "s_nop 7\n"
    "v_max3_f32 v244, v188, v189, v190\n"
    "v_max3_f32 v244, v244, v191, v192\n"
    "v_max3_f32 v244, v244, v193, v194\n"
    "v_max3_f32 v244, v244, v195, v196\n"
    "v_max3_f32 v244, v244, v197, v198\n"
    "v_max3_f32 v244, v244, v199, v200\n"
    "v_max3_f32 v244, v244, v201, v202\n"
    "v_max_f32 v244, v244, v203\n"
    "v_mov_b32 v245, v244\n"
    "s_nop 1\n"
    "v_permlane32_swap_b32 v244, v245\n"
    "v_max_f32 v244, v244, v245\n"
    "v_mov_b32 v245, v244\n"
    "s_nop 1\n"
    "v_permlane16_swap_b32 v244, v245\n"
    "v_max_f32 v244, v244, v245\n"
    "v_max_f32 v244, v244, -v172\n"
    "v_add_f32 v245, v244, v172\n"
    "v_exp_f32 v245, -v245\n"
    "v_sub_f32 v172, 0, v244\n"
    "v_sub_f32 v173, 0, v244\n"
    "v_sub_f32 v174, 0, v244\n"
    "v_sub_f32 v175, 0, v244\n"
    "v_accvgpr_read_b32 v246, a140\n"
    "v_mul_f32 v246, v245, v246\n"
    "v_accvgpr_write_b32 a140, v246\n"
    "v_accvgpr_read_b32 v246, a141\n"
    "v_mul_f32 v246, v245, v246\n"
    "v_accvgpr_write_b32 a141, v246\n"
    "v_accvgpr_read_b32 v246, a142\n"
    "v_mul_f32 v246, v245, v246\n"
    "v_accvgpr_write_b32 a142, v246\n"
    "v_accvgpr_read_b32 v246, a143\n"
    "v_mul_f32 v246, v245, v246\n"
    "v_accvgpr_write_b32 a143, v246\n"
    "v_accvgpr_read_b32 v246, a156\n"
    "v_mul_f32 v246, v245, v246\n"
    "v_accvgpr_write_b32 a156, v246\n"
    "v_accvgpr_read_b32 v246, a157\n"
    "v_mul_f32 v246, v245, v246\n"
    "v_accvgpr_write_b32 a157, v246\n"
    "v_accvgpr_read_b32 v246, a158\n"
    "v_mul_f32 v246, v245, v246\n"
    "v_accvgpr_write_b32 a158, v246\n"
    "v_accvgpr_read_b32 v246, a159\n"
    "v_mul_f32 v246, v245, v246\n"
    "v_accvgpr_write_b32 a159, v246\n"
    "v_accvgpr_read_b32 v246, a172\n"
    "v_mul_f32 v246, v245, v246\n"
    "v_accvgpr_write_b32 a172, v246\n"
    "v_accvgpr_read_b32 v246, a173\n"
    "v_mul_f32 v246, v245, v246\n"
    "v_accvgpr_write_b32 a173, v246\n"
    "v_accvgpr_read_b32 v246, a174\n"
    "v_mul_f32 v246, v245, v246\n"
    "v_accvgpr_write_b32 a174, v246\n"
    "v_accvgpr_read_b32 v246, a175\n"
    "v_mul_f32 v246, v245, v246\n"
    "v_accvgpr_write_b32 a175, v246\n"
    "v_accvgpr_read_b32 v246, a188\n"
    "v_mul_f32 v246, v245, v246\n"
    "v_accvgpr_write_b32 a188, v246\n"
    "v_accvgpr_read_b32 v246, a189\n"
    "v_mul_f32 v246, v245, v246\n"
    "v_accvgpr_write_b32 a189, v246\n"
    "v_accvgpr_read_b32 v246, a190\n"
    "v_mul_f32 v246, v245, v246\n"
    "v_accvgpr_write_b32 a190, v246\n"
    "v_accvgpr_read_b32 v246, a191\n"
    "v_mul_f32 v246, v245, v246\n"
    "v_accvgpr_write_b32 a191, v246\n"
    "v_mul_f32 v12, v245, v12\n"
    "v_mul_f32 v13, v245, v13\n"
    "v_mul_f32 v14, v245, v14\n"
    "v_mul_f32 v15, v245, v15\n"
    "v_mul_f32 v28, v245, v28\n"
    "v_mul_f32 v29, v245, v29\n"
    "v_mul_f32 v30, v245, v30\n"
    "v_mul_f32 v31, v245, v31\n"
    "v_mul_f32 v44, v245, v44\n"
    "v_mul_f32 v45, v245, v45\n"
    "v_mul_f32 v46, v245, v46\n"
    "v_mul_f32 v47, v245, v47\n"
    "v_mul_f32 v60, v245, v60\n"
    "v_mul_f32 v61, v245, v61\n"
    "v_mul_f32 v62, v245, v62\n"
    "v_mul_f32 v63, v245, v63\n"
    "v_mul_f32 v241, v245, v241\n"
    "v_sub_f32 v188, v188, v244\n"
    "v_sub_f32 v189, v189, v244\n"
    "v_sub_f32 v190, v190, v244\n"
    "v_sub_f32 v191, v191, v244\n"
    "v_sub_f32 v192, v192, v244\n"
    "v_sub_f32 v193, v193, v244\n"
    "v_sub_f32 v194, v194, v244\n"
    "v_sub_f32 v195, v195, v244\n"
    "v_sub_f32 v196, v196, v244\n"
    "v_sub_f32 v197, v197, v244\n"
    "v_sub_f32 v198, v198, v244\n"
    "v_sub_f32 v199, v199, v244\n"
    "v_sub_f32 v200, v200, v244\n"
    "v_sub_f32 v201, v201, v244\n"
    "v_sub_f32 v202, v202, v244\n"
    "v_sub_f32 v203, v203, v244\n"
    "v_exp_f32 v188, v188\n"
    "v_exp_f32 v189, v189\n"
    "v_exp_f32 v190, v190\n"
    "v_exp_f32 v191, v191\n"
    "v_exp_f32 v192, v192\n"
    "v_exp_f32 v193, v193\n"
    "v_exp_f32 v194, v194\n"
    "v_exp_f32 v195, v195\n"
    "v_exp_f32 v196, v196\n"
    "v_exp_f32 v197, v197\n"
    "v_exp_f32 v198, v198\n"
    "v_exp_f32 v199, v199\n"
    "v_exp_f32 v200, v200\n"
    "v_exp_f32 v201, v201\n"
    "v_exp_f32 v202, v202\n"
    "v_exp_f32 v203, v203\n"
    "s_nop 1\n"
    "v_cvt_pk_bf16_f32 v88, v188, v189\n"
    "v_cvt_pk_bf16_f32 v89, v190, v191\n"
    "v_cvt_pk_bf16_f32 v90, v192, v193\n"
    "v_cvt_pk_bf16_f32 v91, v194, v195\n"
    "v_cvt_pk_bf16_f32 v92, v196, v197\n"
    "v_cvt_pk_bf16_f32 v93, v198, v199\n"
    "v_cvt_pk_bf16_f32 v94, v200, v201\n"
    "v_cvt_pk_bf16_f32 v95, v202, v203\n"
    "s_nop 3\n"
    "v_mfma_f32_16x16x32_bf16 v[180:183], a[192:195], v[88:91], 0\n"
    "v_mfma_f32_16x16x32_bf16 v[180:183], a[192:195], v[92:95], v[180:183]\n"
    "s_nop 7\n"
    "s_nop 7\n"
    "s_add_u32 s76, s76, 4\n"
    "s_addc_u32 s77, s77, 0\n"
    "s_setpc_b64 s[76:77]\n"
    "WA_3_1:\n"
    "s_waitcnt lgkmcnt(0)\n"
    "s_nop 7\n"
    "s_nop 7\n"
    "v_add_u32 v208, s75, v224\n"
    "v_add_u32 v209, s75, v225\n"
    "v_add_u32 v210, s75, v226\n"
    "v_add_u32 v211, s75, v227\n"
    "ds_read_b128 a[196:199], v208 offset:0\n"
    "ds_read_b128 a[200:203], v209 offset:0\n"
    "ds_read_b128 a[204:207], v210 offset:0\n"
    "ds_read_b128 a[208:211], v211 offset:0\n"
    "s_waitcnt lgkmcnt(0)\n"
    "v_mfma_f32_16x16x32_bf16 v[188:191], a[196:199], a[48:51], 0\n"
    "v_mfma_f32_16x16x32_bf16 v[188:191], a[200:203], a[52:55], v[188:191]\n"
    "v_mfma_f32_16x16x32_bf16 v[188:191], a[204:207], a[56:59], v[188:191]\n"
    "v_mfma_f32_16x16x32_bf16 v[188:191], a[208:211], a[60:63], v[188:191]\n"
    "s_nop 7\n"
    "ds_read_b128 a[196:199], v208 offset:4096\n"
    "ds_read_b128 a[200:203], v209 offset:4096\n"
    "ds_read_b128 a[204:207], v210 offset:4096\n"
    "ds_read_b128 a[208:211], v211 offset:4096\n"
    "s_waitcnt lgkmcnt(0)\n"
    "v_mfma_f32_16x16x32_bf16 v[192:195], a[196:199], a[48:51], 0\n"
    "v_mfma_f32_16x16x32_bf16 v[192:195], a[200:203], a[52:55], v[192:195]\n"
    "v_mfma_f32_16x16x32_bf16 v[192:195], a[204:207], a[56:59], v[192:195]\n"
    "v_mfma_f32_16x16x32_bf16 v[192:195], a[208:211], a[60:63], v[192:195]\n"
    "s_nop 7\n"
    "ds_read_b128 a[196:199], v208 offset:8192\n"
    "ds_read_b128 a[200:203], v209 offset:8192\n"
    "ds_read_b128 a[204:207], v210 offset:8192\n"
    "ds_read_b128 a[208:211], v211 offset:8192\n"
    "s_waitcnt lgkmcnt(0)\n"
    "v_mfma_f32_16x16x32_bf16 v[196:199], a[196:199], a[48:51], 0\n"
    "v_mfma_f32_16x16x32_bf16 v[196:199], a[200:203], a[52:55], v[196:199]\n"
    "v_mfma_f32_16x16x32_bf16 v[196:199], a[204:207], a[56:59], v[196:199]\n"
    "v_mfma_f32_16x16x32_bf16 v[196:199], a[208:211], a[60:63], v[196:199]\n"
    "s_nop 7\n"
    "ds_read_b128 a[196:199], v208 offset:12288\n"
    "ds_read_b128 a[200:203], v209 offset:12288\n"
    "ds_read_b128 a[204:207], v210 offset:12288\n"
    "ds_read_b128 a[208:211], v211 offset:12288\n"
    "s_waitcnt lgkmcnt(0)\n"
    "v_mfma_f32_16x16x32_bf16 v[200:203], a[196:199], a[48:51], 0\n"
    "v_mfma_f32_16x16x32_bf16 v[200:203], a[200:203], a[52:55], v[200:203]\n"
    "v_mfma_f32_16x16x32_bf16 v[200:203], a[204:207], a[56:59], v[200:203]\n"
    "v_mfma_f32_16x16x32_bf16 v[200:203], a[208:211], a[60:63], v[200:203]\n"
    "s_nop 7\n"
    "s_nop 7\n"
    "v_max3_f32 v244, v188, v189, v190\n"
    "v_max3_f32 v244, v244, v191, v192\n"
    "v_max3_f32 v244, v244, v193, v194\n"
    "v_max3_f32 v244, v244, v195, v196\n"
    "v_max3_f32 v244, v244, v197, v198\n"
    "v_max3_f32 v244, v244, v199, v200\n"
    "v_max3_f32 v244, v244, v201, v202\n"
    "v_max_f32 v244, v244, v203\n"
    "v_mov_b32 v245, v244\n"
    "s_nop 1\n"
    "v_permlane32_swap_b32 v244, v245\n"
    "v_max_f32 v244, v244, v245\n"
    "v_mov_b32 v245, v244\n"
    "s_nop 1\n"
    "v_permlane16_swap_b32 v244, v245\n"
    "v_max_f32 v244, v244, v245\n"
    "v_max_f32 v244, v244, -v172\n"
    "v_add_f32 v245, v244, v172\n"
    "v_exp_f32 v245, -v245\n"
    "v_sub_f32 v172, 0, v244\n"
    "v_sub_f32 v173, 0, v244\n"
    "v_sub_f32 v174, 0, v244\n"
    "v_sub_f32 v175, 0, v244\n"
    "v_accvgpr_read_b32 v246, a140\n"
    "v_mul_f32 v246, v245, v246\n"
    "v_accvgpr_write_b32 a140, v246\n"
    "v_accvgpr_read_b32 v246, a141\n"
    "v_mul_f32 v246, v245, v246\n"
    "v_accvgpr_write_b32 a141, v246\n"
    "v_accvgpr_read_b32 v246, a142\n"
    "v_mul_f32 v246, v245, v246\n"
    "v_accvgpr_write_b32 a142, v246\n"
    "v_accvgpr_read_b32 v246, a143\n"
    "v_mul_f32 v246, v245, v246\n"
    "v_accvgpr_write_b32 a143, v246\n"
    "v_accvgpr_read_b32 v246, a156\n"
    "v_mul_f32 v246, v245, v246\n"
    "v_accvgpr_write_b32 a156, v246\n"
    "v_accvgpr_read_b32 v246, a157\n"
    "v_mul_f32 v246, v245, v246\n"
    "v_accvgpr_write_b32 a157, v246\n"
    "v_accvgpr_read_b32 v246, a158\n"
    "v_mul_f32 v246, v245, v246\n"
    "v_accvgpr_write_b32 a158, v246\n"
    "v_accvgpr_read_b32 v246, a159\n"
    "v_mul_f32 v246, v245, v246\n"
    "v_accvgpr_write_b32 a159, v246\n"
    "v_accvgpr_read_b32 v246, a172\n"
    "v_mul_f32 v246, v245, v246\n"
    "v_accvgpr_write_b32 a172, v246\n"
    "v_accvgpr_read_b32 v246, a173\n"
    "v_mul_f32 v246, v245, v246\n"
    "v_accvgpr_write_b32 a173, v246\n"
    "v_accvgpr_read_b32 v246, a174\n"
    "v_mul_f32 v246, v245, v246\n"
    "v_accvgpr_write_b32 a174, v246\n"
    "v_accvgpr_read_b32 v246, a175\n"
    "v_mul_f32 v246, v245, v246\n"
    "v_accvgpr_write_b32 a175, v246\n"
    "v_accvgpr_read_b32 v246, a188\n"
    "v_mul_f32 v246, v245, v246\n"
    "v_accvgpr_write_b32 a188, v246\n"
    "v_accvgpr_read_b32 v246, a189\n"
    "v_mul_f32 v246, v245, v246\n"
    "v_accvgpr_write_b32 a189, v246\n"
    "v_accvgpr_read_b32 v246, a190\n"
    "v_mul_f32 v246, v245, v246\n"
    "v_accvgpr_write_b32 a190, v246\n"
    "v_accvgpr_read_b32 v246, a191\n"
    "v_mul_f32 v246, v245, v246\n"
    "v_accvgpr_write_b32 a191, v246\n"
    "v_mul_f32 v12, v245, v12\n"
    "v_mul_f32 v13, v245, v13\n"
    "v_mul_f32 v14, v245, v14\n"
    "v_mul_f32 v15, v245, v15\n"
    "v_mul_f32 v28, v245, v28\n"
    "v_mul_f32 v29, v245, v29\n"
    "v_mul_f32 v30, v245, v30\n"
    "v_mul_f32 v31, v245, v31\n"
    "v_mul_f32 v44, v245, v44\n"
    "v_mul_f32 v45, v245, v45\n"
    "v_mul_f32 v46, v245, v46\n"
    "v_mul_f32 v47, v245, v47\n"
    "v_mul_f32 v60, v245, v60\n"
    "v_mul_f32 v61, v245, v61\n"
    "v_mul_f32 v62, v245, v62\n"
    "v_mul_f32 v63, v245, v63\n"
    "v_mul_f32 v241, v245, v241\n"
    "v_sub_f32 v188, v188, v244\n"
    "v_sub_f32 v189, v189, v244\n"
    "v_sub_f32 v190, v190, v244\n"
    "v_sub_f32 v191, v191, v244\n"
    "v_sub_f32 v192, v192, v244\n"
    "v_sub_f32 v193, v193, v244\n"
    "v_sub_f32 v194, v194, v244\n"
    "v_sub_f32 v195, v195, v244\n"
    "v_sub_f32 v196, v196, v244\n"
    "v_sub_f32 v197, v197, v244\n"
    "v_sub_f32 v198, v198, v244\n"
    "v_sub_f32 v199, v199, v244\n"
    "v_sub_f32 v200, v200, v244\n"
    "v_sub_f32 v201, v201, v244\n"
    "v_sub_f32 v202, v202, v244\n"
    "v_sub_f32 v203, v203, v244\n"
    "v_exp_f32 v188, v188\n"
    "v_exp_f32 v189, v189\n"
    "v_exp_f32 v190, v190\n"
    "v_exp_f32 v191, v191\n"
    "v_exp_f32 v192, v192\n"
    "v_exp_f32 v193, v193\n"
    "v_exp_f32 v194, v194\n"
    "v_exp_f32 v195, v195\n"
    "v_exp_f32 v196, v196\n"
    "v_exp_f32 v197, v197\n"
    "v_exp_f32 v198, v198\n"
    "v_exp_f32 v199, v199\n"
    "v_exp_f32 v200, v200\n"
    "v_exp_f32 v201, v201\n"
    "v_exp_f32 v202, v202\n"
    "v_exp_f32 v203, v203\n"
    "s_nop 1\n"
    "v_cvt_pk_bf16_f32 v120, v188, v189\n"
    "v_cvt_pk_bf16_f32 v121, v190, v191\n"
    "v_cvt_pk_bf16_f32 v122, v192, v193\n"
    "v_cvt_pk_bf16_f32 v123, v194, v195\n"
    "v_cvt_pk_bf16_f32 v124, v196, v197\n"
    "v_cvt_pk_bf16_f32 v125, v198, v199\n"
    "v_cvt_pk_bf16_f32 v126, v200, v201\n"
    "v_cvt_pk_bf16_f32 v127, v202, v203\n"
    "s_nop 3\n"
    "v_mfma_f32_16x16x32_bf16 v[180:183], a[192:195], v[120:123], 0\n"
    "v_mfma_f32_16x16x32_bf16 v[180:183], a[192:195], v[124:127], v[180:183]\n"
    "s_nop 7\n"
    "s_nop 7\n"
    "s_add_u32 s76, s76, 4\n"
    "s_addc_u32 s77, s77, 0\n"
    "s_setpc_b64 s[76:77]\n"
    "LA_end:\n"
    :: "s"((const char*)h1t + tb0 * 18432u), "s"(2359296u - tb0 * 18432u), "s"((const char*)wq + mi * 256u), "s"((const char*)kc + (size_t)mi * 524288u), "s"((const char*)vc + (size_t)mi * 524288u), "s"((char*)ot + (size_t)tb0 * 131072u + mi * 4096u), "s"(wv), "s"(ldsb), "s"((const char*)wo + (size_t)(xx * 512u + min(ww * 4u, 504u)) * 1152u), "s"((const char*)wg + (size_t)grow * 9216u + xx * 1152u), "s"((const char*)wu + (size_t)grow * 9216u + xx * 1152u), "s"((const char*)wd + (size_t)(xx * 576u + grow) * 1152u), "s"(0), "s"(fl), "s"(mi), "s"((const char*)ot + (size_t)tb0 * 131072u + xx * 16384u), "s"((const char*)wo + (size_t)xx * 589824u), "s"((144u * (ii) + 16u * ((ii) & 1u)) * 2u), "s"((144u * (ii) + (((ii) & 1u) ? 0u : 128u)) * 2u), "s"((char*)part + (size_t)((rt * 8u + xx) * 16u + wv * 4u) * 18432u), "s"(((144u * (ii) + 16u * ((ii) & 1u)) >> 5) * 1024u), "s"(((144u * (ii) + (((ii) & 1u) ? 0u : 128u)) >> 5) * 1024u + 8u * (((144u * (ii) + (((ii) & 1u) ? 0u : 128u)) >> 4) & 1u)), "s"(ii * 4u), "s"((const char*)((wv & 1u) ? wu : wg)), "s"((144u * (mi) + 16u * ((mi) & 1u)) * 2u), "s"((144u * (mi) + (((mi) & 1u) ? 0u : 128u)) * 2u), "s"(rt), "s"(tag)
    : "v0","v1","v2","v3","v4","v5","v6","v7","v8","v9","v10","v11","v12","v13","v14","v15","v16","v17","v18","v19","v20","v21","v22","v23","v24","v25","v26","v27","v28","v29","v30","v31","v32","v33","v34","v35","v36","v37","v38","v39","v40","v41","v42","v43","v44","v45","v46","v47","v48","v49","v50","v51","v52","v53","v54","v55","v56","v57","v58","v59","v60","v61","v62","v63","v64","v65","v66","v67","v68","v69","v70","v71","v72","v73","v74","v75","v76","v77","v78","v79","v80","v81","v82","v83","v84","v85","v86","v87","v88","v89","v90","v91","v92","v93","v94","v95","v96","v97","v98","v99","v100","v101","v102","v103","v104","v105","v106","v107","v108","v109","v110","v111","v112","v113","v114","v115","v116","v117","v118","v119","v120","v121","v122","v123","v124","v125","v126","v127","v128","v129","v130","v131","v132","v133","v134","v135","v136","v137","v138","v139","v140","v141","v142","v143","v144","v145","v146","v147","v148","v149","v150","v151","v152","v153","v154","v155","v156","v157","v158","v159","v160","v161","v162","v163","v164","v165","v166","v167","v168","v169","v170","v171","v172","v173","v174","v175","v176","v177","v178","v179","v180","v181","v182","v183","v184","v185","v186","v187","v188","v189","v190","v191","v192","v193","v194","v195","v196","v197","v198","v199","v200","v201","v202","v203","v204","v205","v206","v207","v208","v209","v210","v211","v212","v213","v214","v215","v216","v217","v218","v219","v220","v221","v222","v223","v224","v225","v226","v227","v228","v229","v230","v231","v232","v233","v234","v235","v236","v237","v238","v239","v240","v241","v242","v243","v244","v245","v246","v247","v248","v249","v250","v251","a0","a1","a2","a3","a4","a5","a6","a7","a8","a9","a10","a11","a12","a13","a14","a15","a16","a17","a18","a19","a20","a21","a22","a23","a24","a25","a26","a27","a28","a29","a30","a31","a32","a33","a34","a35","a36","a37","a38","a39","a40","a41","a42","a43","a44","a45","a46","a47","a48","a49","a50","a51","a52","a53","a54","a55","a56","a57","a58","a59","a60","a61","a62","a63","a64","a65","a66","a67","a68","a69","a70","a71","a72","a73","a74","a75","a76","a77","a78","a79","a80","a81","a82","a83","a84","a85","a86","a87","a88","a89","a90","a91","a92","a93","a94","a95","a96","a97","a98","a99","a100","a101","a102","a103","a104","a105","a106","a107","a108","a109","a110","a111","a112","a113","a114","a115","a116","a117","a118","a119","a120","a121","a122","a123","a124","a125","a126","a127","a128","a129","a130","a131","a132","a133","a134","a135","a136","a137","a138","a139","a140","a141","a142","a143","a144","a145","a146","a147","a148","a149","a150","a151","a152","a153","a154","a155","a156","a157","a158","a159","a160","a161","a162","a163","a164","a165","a166","a167","a168","a169","a170","a171","a172","a173","a174","a175","a176","a177","a178","a179","a180","a181","a182","a183","a184","a185","a186","a187","a188","a189","a190","a191","a192","a193","a194","a195","a196","a197","a198","a199","a200","a201","a202","a203","a204","a205","a206","a207","a208","a209","a210","a211","a212","a213","a214","a215","a216","a217","a218","a219","a220","a221","a222","a223","a224","a225","a226","a227","a228","a229","a230","a231","a232","a233","a234","a235","a236","a237","a238","a239","a240","a241","a242","a243","a244","a245","a246","a247","a248","a249","a250","a251","a252","a253","a254","a255","s40","s41","s42","s43","s44","s45","s46","s47","s48","s49","s50","s51","s52","s53","s54","s55","s56","s57","s58","s59","s60","s61","s62","s63","s64","s65","s66","s67","s68","s69","s70","s71","s72","s73","s74","s75","s76","s77","s78","s79","s80","s81","s82","s83","s84","s85","s86","s87","vcc","scc","memory");

  __syncthreads();
  sweep_mod<864>(gran + 2 * D * 8, modl, tag, wv);
  wait_flags(fl + 2 * 128 + (mi >> 3) * 32u, 32, tag, wv);
  row_phase<1>([](int) {}, nullptr, x, part, modl + D, modl + 2 * D, modl, x1s, red, h2t, row0, wv);
  publish_flag(fl + 3 * 128 + mi, tag, wv);
    asm volatile(
    "s_mov_b64 s[44:45], %0\n"
    "s_and_b32 s45, s45, 0xffff\n"
    "s_mov_b32 s46, 5308416\n"
    "s_mov_b32 s47, 0x20000\n"
    "s_lshr_b32 s63, %2, 1\n"
    "s_mul_i32 s62, s63, 294912\n"
    "s_lshl_b32 s60, s63, 11\n"
    "s_and_b32 s63, %2, 1\n"
    "s_lshl_b32 s63, s63, 12\n"
    "s_add_u32 s60, s60, s63\n"
    "s_lshr_b32 s61, s60, 1\n"
    "s_add_u32 s60, s60, %3\n"
    "s_add_u32 s61, s61, %3\n"
    "v_mbcnt_lo_u32_b32 v246, -1, 0\n"
    "v_mbcnt_hi_u32_b32 v246, -1, v246\n"
    "v_lshrrev_b32 v247, 2, v246\n"
    "v_mul_u32_u24 v247, 9216, v247\n"
    "v_add_u32 v247, %1, v247\n"
    "v_bfe_u32 v248, v246, 4, 1\n"
    "v_lshlrev_b32 v248, 1, v248\n"
    "v_xor_b32 v248, v248, v246\n"
    "v_and_b32 v248, 3, v248\n"
    "v_lshl_add_u32 v244, v248, 4, v247\n"
    "v_add_u32 v245, 146432, v244\n"
    "s_add_u32 m0, s60, 32768\n"
    "s_add_u32 s63, s62, 2359296\n"
    "buffer_load_dwordx4 v244, s[44:47], s63 offen lds\n"
    "buffer_load_dwordx4 v245, s[44:47], s63 offen offset:1024 lds\n"
    "s_add_u32 m0, s60, 40960\n"
    "s_add_u32 s63, s62, 2949120\n"
    "buffer_load_dwordx4 v244, s[44:47], s63 offen lds\n"
    "buffer_load_dwordx4 v245, s[44:47], s63 offen offset:1024 lds\n"
    "s_add_u32 m0, s60, 49152\n"
    "s_add_u32 s63, s62, 3538944\n"
    "buffer_load_dwordx4 v244, s[44:47], s63 offen lds\n"
    "buffer_load_dwordx4 v245, s[44:47], s63 offen offset:1024 lds\n"
    "s_add_u32 m0, s60, 57344\n"
    "s_add_u32 s63, s62, 4128768\n"
    "buffer_load_dwordx4 v244, s[44:47], s63 offen lds\n"
    "buffer_load_dwordx4 v245, s[44:47], s63 offen offset:1024 lds\n"
    "s_add_u32 m0, s60, 65536\n"
    "s_add_u32 s63, s62, 4718592\n"
    "buffer_load_dwordx4 v244, s[44:47], s63 offen lds\n"
    "buffer_load_dwordx4 v245, s[44:47], s63 offen offset:1024 lds\n"
    "s_add_u32 m0, s60, 73728\n"
    "s_add_u32 s63, s62, 64\n"
    "buffer_load_dwordx4 v244, s[44:47], s63 offen lds\n"
    "buffer_load_dwordx4 v245, s[44:47], s63 offen offset:1024 lds\n"
    "s_add_u32 m0, s60, 81920\n"
    "s_add_u32 s63, s62, 589888\n"
    "buffer_load_dwordx4 v244, s[44:47], s63 offen lds\n"
    "buffer_load_dwordx4 v245, s[44:47], s63 offen offset:1024 lds\n"
    "s_add_u32 m0, s60, 90112\n"
    "s_add_u32 s63, s62, 1179712\n"
    "buffer_load_dwordx4 v244, s[44:47], s63 offen lds\n"
    "buffer_load_dwordx4 v245, s[44:47], s63 offen offset:1024 lds\n"
    "s_add_u32 m0, s60, 98304\n"
    "s_add_u32 s63, s62, 1769536\n"
    "buffer_load_dwordx4 v244, s[44:47], s63 offen lds\n"
    "buffer_load_dwordx4 v245, s[44:47], s63 offen offset:1024 lds\n"
    "s_add_u32 m0, s60, 106496\n"
    "s_add_u32 s63, s62, 2359360\n"
    "buffer_load_dwordx4 v244, s[44:47], s63 offen lds\n"
    "buffer_load_dwordx4 v245, s[44:47], s63 offen offset:1024 lds\n"
    "s_add_u32 m0, s60, 114688\n"
    "s_add_u32 s63, s62, 2949184\n"
    "buffer_load_dwordx4 v244, s[44:47], s63 offen lds\n"
    "buffer_load_dwordx4 v245, s[44:47], s63 offen offset:1024 lds\n"
    "s_add_u32 m0, s60, 122880\n"
    "s_add_u32 s63, s62, 3539008\n"
    "buffer_load_dwordx4 v244, s[44:47], s63 offen lds\n"
    "buffer_load_dwordx4 v245, s[44:47], s63 offen offset:1024 lds\n"
    "s_add_u32 m0, s60, 131072\n"
    "s_add_u32 s63, s62, 4128832\n"
    "buffer_load_dwordx4 v244, s[44:47], s63 offen lds\n"
    "buffer_load_dwordx4 v245, s[44:47], s63 offen offset:1024 lds\n"
    "s_add_u32 m0, s60, 139264\n"
    "s_add_u32 s63, s62, 4718656\n"
    "buffer_load_dwordx4 v244, s[44:47], s63 offen lds\n"
    "buffer_load_dwordx4 v245, s[44:47], s63 offen offset:1024 lds\n"
    :: "s"((const char*)((wv & 1u) ? wu : wg)), "s"((144u * (mi) + 16u * ((mi) & 1u)) * 2u), "s"(wv), "s"(ldsb)
    : "v244","v245","v246","v247","v248","s44","s45","s46","s47","s48","s49","s50","s51","s52","s53","s54","s55","s56","s57","s58","s59","s60","s61","s62","s63","scc","memory");
  asm volatile(
    "s_mov_b64 s[40:41], %0\n"
    "s_and_b32 s41, s41, 0xffff\n"
    "s_mov_b32 s42, %1\n"
    "s_mov_b32 s43, 0x20000\n"
    "v_mbcnt_lo_u32_b32 v234, -1, 0\n"
    "v_mbcnt_hi_u32_b32 v234, -1, v234\n"
    "v_lshlrev_b32 v238, 4, v234\n"
    "v_add_u32 v213, 0, v238\n"
    "v_add_u32 v214, 18432, v238\n"
    "v_add_u32 v215, 36864, v238\n"
    "v_add_u32 v216, 55296, v238\n"
    "s_lshl_b32 s66, %8, 5\n"
    "v_and_b32 v236, 7, v234\n"
    "v_lshlrev_b32 v236, 2, v236\n"
    "v_add_u32 v236, s66, v236\n"
    "s_mov_b32 s67, 0x40000\n"
    "PLB0:\n"
    "global_load_dword v237, v236, %11 offset:1536 sc1\n"
    "s_waitcnt vmcnt(0)\n"
    "v_cmp_eq_u32 vcc, %21, v237\n"
    "s_cmp_eq_u64 vcc, -1\n"
    "s_cbranch_scc1 PDB0\n"
    "s_sleep 1\n"
    "s_sub_u32 s67, s67, 1\n"
    "s_cmp_lg_u32 s67, 0\n"
    "s_cbranch_scc1 PLB0\n"
    "PDB0:\n"
    "s_mov_b32 s68, 0\n"
    "buffer_load_dwordx4 a[0:3], v213, s[40:43], s68 offen offset:0 sc1\n"
    "buffer_load_dwordx4 a[4:7], v213, s[40:43], s68 offen offset:1024 sc1\n"
    "buffer_load_dwordx4 a[8:11], v214, s[40:43], s68 offen offset:0 sc1\n"
    "buffer_load_dwordx4 a[12:15], v214, s[40:43], s68 offen offset:1024 sc1\n"
    "buffer_load_dwordx4 a[16:19], v215, s[40:43], s68 offen offset:0 sc1\n"
    "buffer_load_dwordx4 a[20:23], v215, s[40:43], s68 offen offset:1024 sc1\n"
    "buffer_load_dwordx4 a[24:27], v216, s[40:43], s68 offen offset:0 sc1\n"
    "buffer_load_dwordx4 a[28:31], v216, s[40:43], s68 offen offset:1024 sc1\n"
    "s_mov_b32 s68, 2048\n"
    "buffer_load_dwordx4 a[32:35], v213, s[40:43], s68 offen offset:0 sc1\n"
    "buffer_load_dwordx4 a[36:39], v213, s[40:43], s68 offen offset:1024 sc1\n"
    "buffer_load_dwordx4 a[40:43], v214, s[40:43], s68 offen offset:0 sc1\n"
    "buffer_load_dwordx4 a[44:47], v214, s[40:43], s68 offen offset:1024 sc1\n"
    "buffer_load_dwordx4 a[48:51], v215, s[40:43], s68 offen offset:0 sc1\n"
    "buffer_load_dwordx4 a[52:55], v215, s[40:43], s68 offen offset:1024 sc1\n"
    "buffer_load_dwordx4 a[56:59], v216, s[40:43], s68 offen offset:0 sc1\n"
    "buffer_load_dwordx4 a[60:63], v216, s[40:43], s68 offen offset:1024 sc1\n"
    "s_mov_b32 s68, 4096\n"
    "buffer_load_dwordx4 a[64:67], v213, s[40:43], s68 offen offset:0 sc1\n"
    "buffer_load_dwordx4 a[68:71], v213, s[40:43], s68 offen offset:1024 sc1\n"
    "buffer_load_dwordx4 a[72:75], v214, s[40:43], s68 offen offset:0 sc1\n"
    "buffer_load_dwordx4 a[76:79], v214, s[40:43], s68 offen offset:1024 sc1\n"
    "buffer_load_dwordx4 a[80:83], v215, s[40:43], s68 offen offset:0 sc1\n"
    "buffer_load_dwordx4 a[84:87], v215, s[40:43], s68 offen offset:1024 sc1\n"
    "buffer_load_dwordx4 a[88:91], v216, s[40:43], s68 offen offset:0 sc1\n"
    "buffer_load_dwordx4 a[92:95], v216, s[40:43], s68 offen offset:1024 sc1\n"
    "v_add_u32 v217, 0, v238\n"
    "v_add_u32 v218, 147456, v238\n"
    "v_add_u32 v219, 294912, v238\n"
    "v_add_u32 v220, 442368, v238\n"
    "s_mov_b64 s[44:45], %2\n"
    "s_and_b32 s45, s45, 0xffff\n"
    "s_mov_b32 s46, 5308416\n"
    "s_mov_b32 s47, 0x20000\n"
    "s_lshr_b32 s63, %8, 1\n"
    "s_mul_i32 s62, s63, 294912\n"
    "s_lshl_b32 s60, s63, 11\n"
    "s_and_b32 s63, %8, 1\n"
    "s_lshl_b32 s63, s63, 12\n"
    "s_add_u32 s60, s60, s63\n"
    "s_lshr_b32 s61, s60, 1\n"
    "s_add_u32 s60, s60, %9\n"
    "s_add_u32 s61, s61, %9\n"
    "v_lshrrev_b32 v236, 2, v234\n"
    "v_mul_u32_u24 v236, 9216, v236\n"
    "v_add_u32 v236, %3, v236\n"
    "v_bfe_u32 v237, v234, 4, 1\n"
    "v_lshlrev_b32 v237, 1, v237\n"
    "v_xor_b32 v237, v237, v234\n"
    "v_and_b32 v237, 3, v237\n"
    "v_lshl_add_u32 v208, v237, 4, v236\n"
    "v_add_u32 v209, 146432, v208\n"
    "v_lshrrev_b32 v236, 1, v234\n"
    "v_mul_u32_u24 v236, 9216, v236\n"
    "v_and_b32 v237, 1, v234\n"
    "v_lshl_add_u32 v236, v237, 4, v236\n"
    "v_add_u32 v212, %4, v236\n"
    "s_mov_b64 s[48:49], %5\n"
    "s_and_b32 s49, s49, 0xffff\n"
    "s_mov_b32 s50, 589824\n"
    "s_mov_b32 s51, 0x20000\n"
    "s_mov_b32 s64, %6\n"
    "s_mov_b32 s65, %7\n"
    "v_lshrrev_b32 v236, 4, v234\n"
    "v_bfe_u32 v237, v234, 2, 2\n"
    "v_and_b32 v238, 3, v234\n"
    "v_lshlrev_b32 v238, 3, v238\n"
    "v_add_u32 v238, %9, v238\n"
    "v_lshl_add_u32 v223, v236, 7, v238\n"
    "v_lshl_add_u32 v223, v237, 5, v223\n"
    "v_lshl_add_u32 v221, v236, 10, v238\n"
    "v_lshl_add_u32 v221, v237, 6, v221\n"
    "v_and_b32 v236, 1, v236\n"
    "v_lshl_add_u32 v221, v236, 5, v221\n"
    "v_xor_b32 v222, 32, v221\n"
    "v_lshrrev_b32 v236, 4, v234\n"
    "v_mul_u32_u24 v237, 768, v236\n"
    "v_sub_u32 v227, v221, v237\n"
    "v_xor_b32 v230, 32, v227\n"
    "v_add_u32 v228, 49152, v227\n"
    "v_add_u32 v231, 49152, v230\n"
    "v_add_u32 v229, 98304, v227\n"
    "v_add_u32 v232, 98304, v230\n"
    "s_bfe_u32 s80, %20, 524288\n"
    "s_bfe_u32 s81, %20, 524296\n"
    "s_bfe_u32 s82, %20, 524304\n"
    "s_bfe_u32 s83, %20, 524312\n"
    "s_add_u32 s67, s66, %12\n"
    "s_lshl_b32 s67, s67, 2\n"
    "v_mov_b32 v235, s67\n"
    "v_and_b32 v236, 3, v234\n"
    "v_and_b32 v237, -16, v235\n"
    "v_lshl_add_u32 v239, v236, 2, v237\n"
    "s_barrier\n"
    "ds_read_b64_tr_b16 v[160:161], v227 offset:0\n"
    "ds_read_b64_tr_b16 v[162:163], v227 offset:1024\n"
    "ds_read_b64_tr_b16 v[164:165], v227 offset:2048\n"
    "ds_read_b64_tr_b16 v[166:167], v227 offset:3072\n"
    "ds_read_b64_tr_b16 v[168:169], v227 offset:4096\n"
    "ds_read_b64_tr_b16 v[170:171], v227 offset:5120\n"
    "ds_read_b64_tr_b16 v[172:173], v227 offset:6144\n"
    "ds_read_b64_tr_b16 v[174:175], v227 offset:7168\n"
    "ds_read_b64_tr_b16 v[176:177], v230 offset:0\n"
    "ds_read_b64_tr_b16 v[178:179], v230 offset:1024\n"
    "ds_read_b64_tr_b16 v[180:181], v230 offset:2048\n"
    "ds_read_b64_tr_b16 v[182:183], v230 offset:3072\n"
    "s_waitcnt lgkmcnt(8)\n"
    "s_waitcnt vmcnt(16)\n"
    "v_mfma_f32_16x16x32_bf16 v[32:35], v[160:163], a[0:3], 0\n"
    "s_mov_b32 s68, 6144\n"
    "ds_read_b64_tr_b16 v[184:185], v230 offset:4096\n"
    "v_mfma_f32_16x16x32_bf16 v[32:35], v[164:167], a[4:7], v[32:35]\n"
    "buffer_load_dwordx4 a[96:99], v213, s[40:43], s68 offen offset:0 sc1\n"
    "v_mfma_f32_16x16x32_bf16 v[36:39], v[160:163], a[8:11], 0\n"
    "ds_read_b64_tr_b16 v[186:187], v230 offset:5120\n"
    "v_mfma_f32_16x16x32_bf16 v[36:39], v[164:167], a[12:15], v[36:39]\n"
    "v_mfma_f32_16x16x32_bf16 v[40:43], v[160:163], a[16:19], 0\n"
    "ds_read_b64_tr_b16 v[188:189], v230 offset:6144\n"
    "v_mfma_f32_16x16x32_bf16 v[40:43], v[164:167], a[20:23], v[40:43]\n"
    "buffer_load_dwordx4 a[100:103], v213, s[40:43], s68 offen offset:1024 sc1\n"
    "v_mfma_f32_16x16x32_bf16 v[44:47], v[160:163], a[24:27], 0\n"
    "ds_read_b64_tr_b16 v[190:191], v230 offset:7168\n"
    "v_mfma_f32_16x16x32_bf16 v[44:47], v[164:167], a[28:31], v[44:47]\n"
    "s_waitcnt lgkmcnt(8)\n"
    "v_mfma_f32_16x16x32_bf16 v[48:51], v[168:171], a[0:3], 0\n"
    "ds_read_b64_tr_b16 v[192:193], v227 offset:8192\n"
    "v_mfma_f32_16x16x32_bf16 v[48:51], v[172:175], a[4:7], v[48:51]\n"
    "buffer_load_dwordx4 a[104:107], v214, s[40:43], s68 offen offset:0 sc1\n"
    "v_mfma_f32_16x16x32_bf16 v[52:55], v[168:171], a[8:11], 0\n"
    "ds_read_b64_tr_b16 v[194:195], v227 offset:9216\n"
    "v_mfma_f32_16x16x32_bf16 v[52:55], v[172:175], a[12:15], v[52:55]\n"
    "v_mfma_f32_16x16x32_bf16 v[56:59], v[168:171], a[16:19], 0\n"
    "ds_read_b64_tr_b16 v[196:197], v227 offset:10240\n"
    "v_mfma_f32_16x16x32_bf16 v[56:59], v[172:175], a[20:23], v[56:59]\n"
    "buffer_load_dwordx4 a[108:111], v214, s[40:43], s68 offen offset:1024 sc1\n"
    "v_mfma_f32_16x16x32_bf16 v[60:63], v[168:171], a[24:27], 0\n"
    "ds_read_b64_tr_b16 v[198:199], v227 offset:11264\n"
    "v_mfma_f32_16x16x32_bf16 v[60:63], v[172:175], a[28:31], v[60:63]\n"
    "s_waitcnt lgkmcnt(8)\n"
    "v_mfma_f32_16x16x32_bf16 v[64:67], v[176:179], a[0:3], 0\n"
    "ds_read_b64_tr_b16 v[200:201], v227 offset:12288\n"
    "v_mfma_f32_16x16x32_bf16 v[64:67], v[180:183], a[4:7], v[64:67]\n"
    "buffer_load_dwordx4 a[112:115], v215, s[40:43], s68 offen offset:0 sc1\n"
    "v_mfma_f32_16x16x32_bf16 v[68:71], v[176:179], a[8:11], 0\n"
    "ds_read_b64_tr_b16 v[202:203], v227 offset:13312\n"
    "v_mfma_f32_16x16x32_bf16 v[68:71], v[180:183], a[12:15], v[68:71]\n"
    "v_mfma_f32_16x16x32_bf16 v[72:75], v[176:179], a[16:19], 0\n"
    "ds_read_b64_tr_b16 v[204:205], v227 offset:14336\n"
    "v_mfma_f32_16x16x32_bf16 v[72:75], v[180:183], a[20:23], v[72:75]\n"
    "buffer_load_dwordx4 a[116:119], v215, s[40:43], s68 offen offset:1024 sc1\n"
    "v_mfma_f32_16x16x32_bf16 v[76:79], v[176:179], a[24:27], 0\n"
    "ds_read_b64_tr_b16 v[206:207], v227 offset:15360\n"
    "v_mfma_f32_16x16x32_bf16 v[76:79], v[180:183], a[28:31], v[76:79]\n"
    "s_waitcnt lgkmcnt(8)\n"
    "v_mfma_f32_16x16x32_bf16 v[80:83], v[184:187], a[0:3], 0\n"
    "ds_read_b64_tr_b16 v[160:161], v230 offset:8192\n"
    "v_mfma_f32_16x16x32_bf16 v[80:83], v[188:191], a[4:7], v[80:83]\n"
    "buffer_load_dwordx4 a[120:123], v216, s[40:43], s68 offen offset:0 sc1\n"
    "v_mfma_f32_16x16x32_bf16 v[84:87], v[184:187], a[8:11], 0\n"
    "ds_read_b64_tr_b16 v[162:163], v230 offset:9216\n"
    "v_mfma_f32_16x16x32_bf16 v[84:87], v[188:191], a[12:15], v[84:87]\n"
    "v_mfma_f32_16x16x32_bf16 v[88:91], v[184:187], a[16:19], 0\n"
    "ds_read_b64_tr_b16 v[164:165], v230 offset:10240\n"
    "v_mfma_f32_16x16x32_bf16 v[88:91], v[188:191], a[20:23], v[88:91]\n"
    "buffer_load_dwordx4 a[124:127], v216, s[40:43], s68 offen offset:1024 sc1\n"
    "v_mfma_f32_16x16x32_bf16 v[92:95], v[184:187], a[24:27], 0\n"
    "s_mov_b32 s68, 8192\n"
    "ds_read_b64_tr_b16 v[166:167], v230 offset:11264\n"
    "v_mfma_f32_16x16x32_bf16 v[92:95], v[188:191], a[28:31], v[92:95]\n"
    "s_waitcnt lgkmcnt(8)\n"
    "s_waitcnt vmcnt(16)\n"
    "v_mfma_f32_16x16x32_bf16 v[32:35], v[192:195], a[32:35], v[32:35]\n"
    "ds_read_b64_tr_b16 v[168:169], v230 offset:12288\n"
    "v_mfma_f32_16x16x32_bf16 v[32:35], v[196:199], a[36:39], v[32:35]\n"
    "buffer_load_dwordx4 a[128:131], v213, s[40:43], s68 offen offset:0 sc1\n"
    "v_mfma_f32_16x16x32_bf16 v[36:39], v[192:195], a[40:43], v[36:39]\n"
    "ds_read_b64_tr_b16 v[170:171], v230 offset:13312\n"
    "v_mfma_f32_16x16x32_bf16 v[36:39], v[196:199], a[44:47], v[36:39]\n"
    "v_mfma_f32_16x16x32_bf16 v[40:43], v[192:195], a[48:51], v[40:43]\n"
    "ds_read_b64_tr_b16 v[172:173], v230 offset:14336\n"
    "v_mfma_f32_16x16x32_bf16 v[40:43], v[196:199], a[52:55], v[40:43]\n"
    "buffer_load_dwordx4 a[132:135], v213, s[40:43], s68 offen offset:1024 sc1\n"
    "v_mfma_f32_16x16x32_bf16 v[44:47], v[192:195], a[56:59], v[44:47]\n"
    "ds_read_b64_tr_b16 v[174:175], v230 offset:15360\n"
    "v_mfma_f32_16x16x32_bf16 v[44:47], v[196:199], a[60:63], v[44:47]\n"
    "s_waitcnt lgkmcnt(8)\n"
    "v_mfma_f32_16x16x32_bf16 v[48:51], v[200:203], a[32:35], v[48:51]\n"
    "ds_read_b64_tr_b16 v[176:177], v227 offset:16384\n"
    "v_mfma_f32_16x16x32_bf16 v[48:51], v[204:207], a[36:39], v[48:51]\n"
    "buffer_load_dwordx4 a[136:139], v214, s[40:43], s68 offen offset:0 sc1\n"
    "v_mfma_f32_16x16x32_bf16 v[52:55], v[200:203], a[40:43], v[52:55]\n"
    "ds_read_b64_tr_b16 v[178:179], v227 offset:17408\n"
    "v_mfma_f32_16x16x32_bf16 v[52:55], v[204:207], a[44:47], v[52:55]\n"
    "v_mfma_f32_16x16x32_bf16 v[56:59], v[200:203], a[48:51], v[56:59]\n"
    "ds_read_b64_tr_b16 v[180:181], v227 offset:18432\n"
    "v_mfma_f32_16x16x32_bf16 v[56:59], v[204:207], a[52:55], v[56:59]\n"
    "buffer_load_dwordx4 a[140:143], v214, s[40:43], s68 offen offset:1024 sc1\n"
    "v_mfma_f32_16x16x32_bf16 v[60:63], v[200:203], a[56:59], v[60:63]\n"
    "ds_read_b64_tr_b16 v[182:183], v227 offset:19456\n"
    "v_mfma_f32_16x16x32_bf16 v[60:63], v[204:207], a[60:63], v[60:63]\n"
    "s_waitcnt lgkmcnt(8)\n"
    "v_mfma_f32_16x16x32_bf16 v[64:67], v[160:163], a[32:35], v[64:67]\n"
    "ds_read_b64_tr_b16 v[184:185], v227 offset:20480\n"
    "v_mfma_f32_16x16x32_bf16 v[64:67], v[164:167], a[36:39], v[64:67]\n"
    "buffer_load_dwordx4 a[144:147], v215, s[40:43], s68 offen offset:0 sc1\n"
    "v_mfma_f32_16x16x32_bf16 v[68:71], v[160:163], a[40:43], v[68:71]\n"
    "ds_read_b64_tr_b16 v[186:187], v227 offset:21504\n"
    "v_mfma_f32_16x16x32_bf16 v[68:71], v[164:167], a[44:47], v[68:71]\n"
    "v_mfma_f32_16x16x32_bf16 v[72:75], v[160:163], a[48:51], v[72:75]\n"
    "ds_read_b64_tr_b16 v[188:189], v227 offset:22528\n"
    "v_mfma_f32_16x16x32_bf16 v[72:75], v[164:167], a[52:55], v[72:75]\n"
    "buffer_load_dwordx4 a[148:151], v215, s[40:43], s68 offen offset:1024 sc1\n"
    "v_mfma_f32_16x16x32_bf16 v[76:79], v[160:163], a[56:59], v[76:79]\n"
    "ds_read_b64_tr_b16 v[190:191], v227 offset:23552\n"
    "v_mfma_f32_16x16x32_bf16 v[76:79], v[164:167], a[60:63], v[76:79]\n"
    "s_waitcnt lgkmcnt(8)\n"
    "v_mfma_f32_16x16x32_bf16 v[80:83], v[168:171], a[32:35], v[80:83]\n"
    "ds_read_b64_tr_b16 v[192:193], v230 offset:16384\n"
    "v_mfma_f32_16x16x32_bf16 v[80:83], v[172:175], a[36:39], v[80:83]\n"
    "buffer_load_dwordx4 a[152:155], v216, s[40:43], s68 offen offset:0 sc1\n"
    "v_mfma_f32_16x16x32_bf16 v[84:87], v[168:171], a[40:43], v[84:87]\n"
    "ds_read_b64_tr_b16 v[194:195], v230 offset:17408\n"
    "v_mfma_f32_16x16x32_bf16 v[84:87], v[172:175], a[44:47], v[84:87]\n"
    "v_mfma_f32_16x16x32_bf16 v[88:91], v[168:171], a[48:51], v[88:91]\n"
    "ds_read_b64_tr_b16 v[196:197], v230 offset:18432\n"
    "v_mfma_f32_16x16x32_bf16 v[88:91], v[172:175], a[52:55], v[88:91]\n"
    "buffer_load_dwordx4 a[156:159], v216, s[40:43], s68 offen offset:1024 sc1\n"
    "v_mfma_f32_16x16x32_bf16 v[92:95], v[168:171], a[56:59], v[92:95]\n"
    "s_mov_b32 s68, 10240\n"
    "ds_read_b64_tr_b16 v[198:199], v230 offset:19456\n"
    "v_mfma_f32_16x16x32_bf16 v[92:95], v[172:175], a[60:63], v[92:95]\n"
    "s_waitcnt lgkmcnt(8)\n"
    "s_waitcnt vmcnt(16)\n"
    "v_mfma_f32_16x16x32_bf16 v[32:35], v[176:179], a[64:67], v[32:35]\n"
    "ds_read_b64_tr_b16 v[200:201], v230 offset:20480\n"
    "v_mfma_f32_16x16x32_bf16 v[32:35], v[180:183], a[68:71], v[32:35]\n"
    "buffer_load_dwordx4 a[160:163], v213, s[40:43], s68 offen offset:0 sc1\n"
    "v_mfma_f32_16x16x32_bf16 v[36:39], v[176:179], a[72:75], v[36:39]\n"
    "ds_read_b64_tr_b16 v[202:203], v230 offset:21504\n"
    "v_mfma_f32_16x16x32_bf16 v[36:39], v[180:183], a[76:79], v[36:39]\n"
    "v_mfma_f32_16x16x32_bf16 v[40:43], v[176:179], a[80:83], v[40:43]\n"
    "ds_read_b64_tr_b16 v[204:205], v230 offset:22528\n"
    "v_mfma_f32_16x16x32_bf16 v[40:43], v[180:183], a[84:87], v[40:43]\n"
    "buffer_load_dwordx4 a[164:167], v213, s[40:43], s68 offen offset:1024 sc1\n"
    "v_mfma_f32_16x16x32_bf16 v[44:47], v[176:179], a[88:91], v[44:47]\n"
    "ds_read_b64_tr_b16 v[206:207], v230 offset:23552\n"
    "v_mfma_f32_16x16x32_bf16 v[44:47], v[180:183], a[92:95], v[44:47]\n"
    "s_waitcnt lgkmcnt(8)\n"
    "v_mfma_f32_16x16x32_bf16 v[48:51], v[184:187], a[64:67], v[48:51]\n"
    "ds_read_b64_tr_b16 v[160:161], v227 offset:24576\n"
    "v_mfma_f32_16x16x32_bf16 v[48:51], v[188:191], a[68:71], v[48:51]\n"
    "buffer_load_dwordx4 a[168:171], v214, s[40:43], s68 offen offset:0 sc1\n"
    "v_mfma_f32_16x16x32_bf16 v[52:55], v[184:187], a[72:75], v[52:55]\n"
    "ds_read_b64_tr_b16 v[162:163], v227 offset:25600\n"
    "v_mfma_f32_16x16x32_bf16 v[52:55], v[188:191], a[76:79], v[52:55]\n"
    "v_mfma_f32_16x16x32_bf16 v[56:59], v[184:187], a[80:83], v[56:59]\n"
    "ds_read_b64_tr_b16 v[164:165], v227 offset:26624\n"
    "v_mfma_f32_16x16x32_bf16 v[56:59], v[188:191], a[84:87], v[56:59]\n"
    "buffer_load_dwordx4 a[172:175], v214, s[40:43], s68 offen offset:1024 sc1\n"
    "v_mfma_f32_16x16x32_bf16 v[60:63], v[184:187], a[88:91], v[60:63]\n"
    "ds_read_b64_tr_b16 v[166:167], v227 offset:27648\n"
    "v_mfma_f32_16x16x32_bf16 v[60:63], v[188:191], a[92:95], v[60:63]\n"
    "s_waitcnt lgkmcnt(8)\n"
    "v_mfma_f32_16x16x32_bf16 v[64:67], v[192:195], a[64:67], v[64:67]\n"
    "ds_read_b64_tr_b16 v[168:169], v227 offset:28672\n"
    "v_mfma_f32_16x16x32_bf16 v[64:67], v[196:199], a[68:71], v[64:67]\n"
    "buffer_load_dwordx4 a[176:179], v215, s[40:43], s68 offen offset:0 sc1\n"
    "v_mfma_f32_16x16x32_bf16 v[68:71], v[192:195], a[72:75], v[68:71]\n"
    "ds_read_b64_tr_b16 v[170:171], v227 offset:29696\n"
    "v_mfma_f32_16x16x32_bf16 v[68:71], v[196:199], a[76:79], v[68:71]\n"
    "v_mfma_f32_16x16x32_bf16 v[72:75], v[192:195], a[80:83], v[72:75]\n"
    "ds_read_b64_tr_b16 v[172:173], v227 offset:30720\n"
    "v_mfma_f32_16x16x32_bf16 v[72:75], v[196:199], a[84:87], v[72:75]\n"
    "buffer_load_dwordx4 a[180:183], v215, s[40:43], s68 offen offset:1024 sc1\n"
    "v_mfma_f32_16x16x32_bf16 v[76:79], v[192:195], a[88:91], v[76:79]\n"
    "ds_read_b64_tr_b16 v[174:175], v227 offset:31744\n"
    "v_mfma_f32_16x16x32_bf16 v[76:79], v[196:199], a[92:95], v[76:79]\n"
    "s_waitcnt lgkmcnt(8)\n"
    "v_mfma_f32_16x16x32_bf16 v[80:83], v[200:203], a[64:67], v[80:83]\n"
    "ds_read_b64_tr_b16 v[176:177], v230 offset:24576\n"
    "v_mfma_f32_16x16x32_bf16 v[80:83], v[204:207], a[68:71], v[80:83]\n"
    "buffer_load_dwordx4 a[184:187], v216, s[40:43], s68 offen offset:0 sc1\n"
    "v_mfma_f32_16x16x32_bf16 v[84:87], v[200:203], a[72:75], v[84:87]\n"
    "ds_read_b64_tr_b16 v[178:179], v230 offset:25600\n"
    "v_mfma_f32_16x16x32_bf16 v[84:87], v[204:207], a[76:79], v[84:87]\n"
    "v_mfma_f32_16x16x32_bf16 v[88:91], v[200:203], a[80:83], v[88:91]\n"
    "ds_read_b64_tr_b16 v[180:181], v230 offset:26624\n"
    "v_mfma_f32_16x16x32_bf16 v[88:91], v[204:207], a[84:87], v[88:91]\n"
    "buffer_load_dwordx4 a[188:191], v216, s[40:43], s68 offen offset:1024 sc1\n"
    "v_mfma_f32_16x16x32_bf16 v[92:95], v[200:203], a[88:91], v[92:95]\n"
    "s_mov_b32 s68, 12288\n"
    "ds_read_b64_tr_b16 v[182:183], v230 offset:27648\n"
    "v_mfma_f32_16x16x32_bf16 v[92:95], v[204:207], a[92:95], v[92:95]\n"
    "s_waitcnt lgkmcnt(8)\n"
    "s_waitcnt vmcnt(16)\n"
    "v_mfma_f32_16x16x32_bf16 v[32:35], v[160:163], a[96:99], v[32:35]\n"
    "ds_read_b64_tr_b16 v[184:185], v230 offset:28672\n"
    "v_mfma_f32_16x16x32_bf16 v[32:35], v[164:167], a[100:103], v[32:35]\n"
    "buffer_load_dwordx4 a[192:195], v213, s[40:43], s68 offen offset:0 sc1\n"
    "v_mfma_f32_16x16x32_bf16 v[36:39], v[160:163], a[104:107], v[36:39]\n"
    "ds_read_b64_tr_b16 v[186:187], v230 offset:29696\n"
    "v_mfma_f32_16x16x32_bf16 v[36:39], v[164:167], a[108:111], v[36:39]\n"
    "v_mfma_f32_16x16x32_bf16 v[40:43], v[160:163], a[112:115], v[40:43]\n"
    "ds_read_b64_tr_b16 v[188:189], v230 offset:30720\n"
    "v_mfma_f32_16x16x32_bf16 v[40:43], v[164:167], a[116:119], v[40:43]\n"
    "buffer_load_dwordx4 a[196:199], v213, s[40:43], s68 offen offset:1024 sc1\n"
    "v_mfma_f32_16x16x32_bf16 v[44:47], v[160:163], a[120:123], v[44:47]\n"
    "ds_read_b64_tr_b16 v[190:191], v230 offset:31744\n"
    "v_mfma_f32_16x16x32_bf16 v[44:47], v[164:167], a[124:127], v[44:47]\n"
    "s_waitcnt lgkmcnt(8)\n"
    "v_mfma_f32_16x16x32_bf16 v[48:51], v[168:171], a[96:99], v[48:51]\n"
    "ds_read_b64_tr_b16 v[192:193], v227 offset:32768\n"
    "v_mfma_f32_16x16x32_bf16 v[48:51], v[172:175], a[100:103], v[48:51]\n"
    "buffer_load_dwordx4 a[200:203], v214, s[40:43], s68 offen offset:0 sc1\n"
    "v_mfma_f32_16x16x32_bf16 v[52:55], v[168:171], a[104:107], v[52:55]\n"
    "ds_read_b64_tr_b16 v[194:195], v227 offset:33792\n"
    "v_mfma_f32_16x16x32_bf16 v[52:55], v[172:175], a[108:111], v[52:55]\n"
    "v_mfma_f32_16x16x32_bf16 v[56:59], v[168:171], a[112:115], v[56:59]\n"
    "ds_read_b64_tr_b16 v[196:197], v227 offset:34816\n"
    "v_mfma_f32_16x16x32_bf16 v[56:59], v[172:175], a[116:119], v[56:59]\n"
    "buffer_load_dwordx4 a[204:207], v214, s[40:43], s68 offen offset:1024 sc1\n"
    "v_mfma_f32_16x16x32_bf16 v[60:63], v[168:171], a[120:123], v[60:63]\n"
    "ds_read_b64_tr_b16 v[198:199], v227 offset:35840\n"
    "v_mfma_f32_16x16x32_bf16 v[60:63], v[172:175], a[124:127], v[60:63]\n"
    "s_waitcnt lgkmcnt(8)\n"
    "v_mfma_f32_16x16x32_bf16 v[64:67], v[176:179], a[96:99], v[64:67]\n"
    "ds_read_b64_tr_b16 v[200:201], v227 offset:36864\n"
    "v_mfma_f32_16x16x32_bf16 v[64:67], v[180:183], a[100:103], v[64:67]\n"
    "buffer_load_dwordx4 a[208:211], v215, s[40:43], s68 offen offset:0 sc1\n"
    "v_mfma_f32_16x16x32_bf16 v[68:71], v[176:179], a[104:107], v[68:71]\n"
    "ds_read_b64_tr_b16 v[202:203], v227 offset:37888\n"
    "v_mfma_f32_16x16x32_bf16 v[68:71], v[180:183], a[108:111], v[68:71]\n"
    "v_mfma_f32_16x16x32_bf16 v[72:75], v[176:179], a[112:115], v[72:75]\n"
    "ds_read_b64_tr_b16 v[204:205], v227 offset:38912\n"
    "v_mfma_f32_16x16x32_bf16 v[72:75], v[180:183], a[116:119], v[72:75]\n"
    "buffer_load_dwordx4 a[212:215], v215, s[40:43], s68 offen offset:1024 sc1\n"
    "v_mfma_f32_16x16x32_bf16 v[76:79], v[176:179], a[120:123], v[76:79]\n"
    "ds_read_b64_tr_b16 v[206:207], v227 offset:39936\n"
    "v_mfma_f32_16x16x32_bf16 v[76:79], v[180:183], a[124:127], v[76:79]\n"
    "s_waitcnt lgkmcnt(8)\n"
    "v_mfma_f32_16x16x32_bf16 v[80:83], v[184:187], a[96:99], v[80:83]\n"
    "ds_read_b64_tr_b16 v[160:161], v230 offset:32768\n"
    "v_mfma_f32_16x16x32_bf16 v[80:83], v[188:191], a[100:103], v[80:83]\n"
    "buffer_load_dwordx4 a[216:219], v216, s[40:43], s68 offen offset:0 sc1\n"
    "v_mfma_f32_16x16x32_bf16 v[84:87], v[184:187], a[104:107], v[84:87]\n"
    "ds_read_b64_tr_b16 v[162:163], v230 offset:33792\n"
    "v_mfma_f32_16x16x32_bf16 v[84:87], v[188:191], a[108:111], v[84:87]\n"
    "v_mfma_f32_16x16x32_bf16 v[88:91], v[184:187], a[112:115], v[88:91]\n"
    "ds_read_b64_tr_b16 v[164:165], v230 offset:34816\n"
    "v_mfma_f32_16x16x32_bf16 v[88:91], v[188:191], a[116:119], v[88:91]\n"
    "buffer_load_dwordx4 a[220:223], v216, s[40:43], s68 offen offset:1024 sc1\n"
    "v_mfma_f32_16x16x32_bf16 v[92:95], v[184:187], a[120:123], v[92:95]\n"
    "s_mov_b32 s68, 14336\n"
    "ds_read_b64_tr_b16 v[166:167], v230 offset:35840\n"
    "v_mfma_f32_16x16x32_bf16 v[92:95], v[188:191], a[124:127], v[92:95]\n"
    "s_waitcnt lgkmcnt(8)\n"
    "s_waitcnt vmcnt(16)\n"
    "v_mfma_f32_16x16x32_bf16 v[32:35], v[192:195], a[128:131], v[32:35]\n"
    "ds_read_b64_tr_b16 v[168:169], v230 offset:36864\n"
    "v_mfma_f32_16x16x32_bf16 v[32:35], v[196:199], a[132:135], v[32:35]\n"
    "buffer_load_dwordx4 a[224:227], v213, s[40:43], s68 offen offset:0 sc1\n"
    "v_mfma_f32_16x16x32_bf16 v[36:39], v[192:195], a[136:139], v[36:39]\n"
    "ds_read_b64_tr_b16 v[170:171], v230 offset:37888\n"
    "v_mfma_f32_16x16x32_bf16 v[36:39], v[196:199], a[140:143], v[36:39]\n"
    "v_mfma_f32_16x16x32_bf16 v[40:43], v[192:195], a[144:147], v[40:43]\n"
    "ds_read_b64_tr_b16 v[172:173], v230 offset:38912\n"
    "v_mfma_f32_16x16x32_bf16 v[40:43], v[196:199], a[148:151], v[40:43]\n"
    "buffer_load_dwordx4 a[228:231], v213, s[40:43], s68 offen offset:1024 sc1\n"
    "v_mfma_f32_16x16x32_bf16 v[44:47], v[192:195], a[152:155], v[44:47]\n"
    "ds_read_b64_tr_b16 v[174:175], v230 offset:39936\n"
    "v_mfma_f32_16x16x32_bf16 v[44:47], v[196:199], a[156:159], v[44:47]\n"
    "s_waitcnt lgkmcnt(8)\n"
    "v_mfma_f32_16x16x32_bf16 v[48:51], v[200:203], a[128:131], v[48:51]\n"
    "ds_read_b64_tr_b16 v[176:177], v227 offset:40960\n"
    "v_mfma_f32_16x16x32_bf16 v[48:51], v[204:207], a[132:135], v[48:51]\n"
    "buffer_load_dwordx4 a[232:235], v214, s[40:43], s68 offen offset:0 sc1\n"
    "v_mfma_f32_16x16x32_bf16 v[52:55], v[200:203], a[136:139], v[52:55]\n"
    "ds_read_b64_tr_b16 v[178:179], v227 offset:41984\n"
    "v_mfma_f32_16x16x32_bf16 v[52:55], v[204:207], a[140:143], v[52:55]\n"
    "v_mfma_f32_16x16x32_bf16 v[56:59], v[200:203], a[144:147], v[56:59]\n"
    "ds_read_b64_tr_b16 v[180:181], v227 offset:43008\n"
    "v_mfma_f32_16x16x32_bf16 v[56:59], v[204:207], a[148:151], v[56:59]\n"
    "buffer_load_dwordx4 a[236:239], v214, s[40:43], s68 offen offset:1024 sc1\n"
    "v_mfma_f32_16x16x32_bf16 v[60:63], v[200:203], a[152:155], v[60:63]\n"
    "ds_read_b64_tr_b16 v[182:183], v227 offset:44032\n"
    "v_mfma_f32_16x16x32_bf16 v[60:63], v[204:207], a[156:159], v[60:63]\n"
    "s_waitcnt lgkmcnt(8)\n"
    "v_mfma_f32_16x16x32_bf16 v[64:67], v[160:163], a[128:131], v[64:67]\n"
    "ds_read_b64_tr_b16 v[184:185], v227 offset:45056\n"
    "v_mfma_f32_16x16x32_bf16 v[64:67], v[164:167], a[132:135], v[64:67]\n"
    "buffer_load_dwordx4 a[240:243], v215, s[40:43], s68 offen offset:0 sc1\n"
    "v_mfma_f32_16x16x32_bf16 v[68:71], v[160:163], a[136:139], v[68:71]\n"
    "ds_read_b64_tr_b16 v[186:187], v227 offset:46080\n"
    "v_mfma_f32_16x16x32_bf16 v[68:71], v[164:167], a[140:143], v[68:71]\n"
    "v_mfma_f32_16x16x32_bf16 v[72:75], v[160:163], a[144:147], v[72:75]\n"
    "ds_read_b64_tr_b16 v[188:189], v227 offset:47104\n"
    "v_mfma_f32_16x16x32_bf16 v[72:75], v[164:167], a[148:151], v[72:75]\n"
    "buffer_load_dwordx4 a[244:247], v215, s[40:43], s68 offen offset:1024 sc1\n"
    "v_mfma_f32_16x16x32_bf16 v[76:79], v[160:163], a[152:155], v[76:79]\n"
    "ds_read_b64_tr_b16 v[190:191], v227 offset:48128\n"
    "v_mfma_f32_16x16x32_bf16 v[76:79], v[164:167], a[156:159], v[76:79]\n"
    "s_waitcnt lgkmcnt(8)\n"
    "v_mfma_f32_16x16x32_bf16 v[80:83], v[168:171], a[128:131], v[80:83]\n"
    "ds_read_b64_tr_b16 v[192:193], v230 offset:40960\n"
    "v_mfma_f32_16x16x32_bf16 v[80:83], v[172:175], a[132:135], v[80:83]\n"
    "buffer_load_dwordx4 a[248:251], v216, s[40:43], s68 offen offset:0 sc1\n"
    "v_mfma_f32_16x16x32_bf16 v[84:87], v[168:171], a[136:139], v[84:87]\n"
    "ds_read_b64_tr_b16 v[194:195], v230 offset:41984\n"
    "v_mfma_f32_16x16x32_bf16 v[84:87], v[172:175], a[140:143], v[84:87]\n"
    "v_mfma_f32_16x16x32_bf16 v[88:91], v[168:171], a[144:147], v[88:91]\n"
    "ds_read_b64_tr_b16 v[196:197], v230 offset:43008\n"
    "v_mfma_f32_16x16x32_bf16 v[88:91], v[172:175], a[148:151], v[88:91]\n"
    "buffer_load_dwordx4 a[252:255], v216, s[40:43], s68 offen offset:1024 sc1\n"
    "v_mfma_f32_16x16x32_bf16 v[92:95], v[168:171], a[152:155], v[92:95]\n"
    "s_mov_b32 s68, 16384\n"
    "ds_read_b64_tr_b16 v[198:199], v230 offset:44032\n"
    "v_mfma_f32_16x16x32_bf16 v[92:95], v[172:175], a[156:159], v[92:95]\n"
    "s_waitcnt lgkmcnt(8)\n"
    "s_waitcnt vmcnt(16)\n"
    "v_mfma_f32_16x16x32_bf16 v[32:35], v[176:179], a[160:163], v[32:35]\n"
    "ds_read_b64_tr_b16 v[200:201], v230 offset:45056\n"
    "v_mfma_f32_16x16x32_bf16 v[32:35], v[180:183], a[164:167], v[32:35]\n"
    "buffer_load_dwordx4 v[0:3], v213, s[40:43], s68 offen offset:0 sc1\n"
    "v_mfma_f32_16x16x32_bf16 v[36:39], v[176:179], a[168:171], v[36:39]\n"
    "ds_read_b64_tr_b16 v[202:203], v230 offset:46080\n"
    "v_mfma_f32_16x16x32_bf16 v[36:39], v[180:183], a[172:175], v[36:39]\n"
    "v_mfma_f32_16x16x32_bf16 v[40:43], v[176:179], a[176:179], v[40:43]\n"
    "ds_read_b64_tr_b16 v[204:205], v230 offset:47104\n"
    "v_mfma_f32_16x16x32_bf16 v[40:43], v[180:183], a[180:183], v[40:43]\n"
    "buffer_load_dwordx4 v[4:7], v213, s[40:43], s68 offen offset:1024 sc1\n"
    "v_mfma_f32_16x16x32_bf16 v[44:47], v[176:179], a[184:187], v[44:47]\n"
    "ds_read_b64_tr_b16 v[206:207], v230 offset:48128\n"
    "v_mfma_f32_16x16x32_bf16 v[44:47], v[180:183], a[188:191], v[44:47]\n"
    "s_waitcnt lgkmcnt(8)\n"
    "v_mfma_f32_16x16x32_bf16 v[48:51], v[184:187], a[160:163], v[48:51]\n"
    "ds_read_b64_tr_b16 v[160:161], v228 offset:0\n"
    "v_mfma_f32_16x16x32_bf16 v[48:51], v[188:191], a[164:167], v[48:51]\n"
    "buffer_load_dwordx4 v[8:11], v214, s[40:43], s68 offen offset:0 sc1\n"
    "v_mfma_f32_16x16x32_bf16 v[52:55], v[184:187], a[168:171], v[52:55]\n"
    "ds_read_b64_tr_b16 v[162:163], v228 offset:1024\n"
    "v_mfma_f32_16x16x32_bf16 v[52:55], v[188:191], a[172:175], v[52:55]\n"
    "v_mfma_f32_16x16x32_bf16 v[56:59], v[184:187], a[176:179], v[56:59]\n"
    "ds_read_b64_tr_b16 v[164:165], v228 offset:2048\n"
    "v_mfma_f32_16x16x32_bf16 v[56:59], v[188:191], a[180:183], v[56:59]\n"
    "buffer_load_dwordx4 v[12:15], v214, s[40:43], s68 offen offset:1024 sc1\n"
    "v_mfma_f32_16x16x32_bf16 v[60:63], v[184:187], a[184:187], v[60:63]\n"
    "ds_read_b64_tr_b16 v[166:167], v228 offset:3072\n"
    "v_mfma_f32_16x16x32_bf16 v[60:63], v[188:191], a[188:191], v[60:63]\n"
    "s_waitcnt lgkmcnt(8)\n"
    "v_mfma_f32_16x16x32_bf16 v[64:67], v[192:195], a[160:163], v[64:67]\n"
    "ds_read_b64_tr_b16 v[168:169], v228 offset:4096\n"
    "v_mfma_f32_16x16x32_bf16 v[64:67], v[196:199], a[164:167], v[64:67]\n"
    "buffer_load_dwordx4 v[16:19], v215, s[40:43], s68 offen offset:0 sc1\n"
    "v_mfma_f32_16x16x32_bf16 v[68:71], v[192:195], a[168:171], v[68:71]\n"
    "ds_read_b64_tr_b16 v[170:171], v228 offset:5120\n"
    "v_mfma_f32_16x16x32_bf16 v[68:71], v[196:199], a[172:175], v[68:71]\n"
    "v_mfma_f32_16x16x32_bf16 v[72:75], v[192:195], a[176:179], v[72:75]\n"
    "ds_read_b64_tr_b16 v[172:173], v228 offset:6144\n"
    "v_mfma_f32_16x16x32_bf16 v[72:75], v[196:199], a[180:183], v[72:75]\n"
    "buffer_load_dwordx4 v[20:23], v215, s[40:43], s68 offen offset:1024 sc1\n"
    "v_mfma_f32_16x16x32_bf16 v[76:79], v[192:195], a[184:187], v[76:79]\n"
    "ds_read_b64_tr_b16 v[174:175], v228 offset:7168\n"
    "v_mfma_f32_16x16x32_bf16 v[76:79], v[196:199], a[188:191], v[76:79]\n"
    "s_waitcnt lgkmcnt(8)\n"
    "v_mfma_f32_16x16x32_bf16 v[80:83], v[200:203], a[160:163], v[80:83]\n"
    "ds_read_b64_tr_b16 v[176:177], v231 offset:0\n"
    "v_mfma_f32_16x16x32_bf16 v[80:83], v[204:207], a[164:167], v[80:83]\n"
    "buffer_load_dwordx4 v[24:27], v216, s[40:43], s68 offen offset:0 sc1\n"
    "v_mfma_f32_16x16x32_bf16 v[84:87], v[200:203], a[168:171], v[84:87]\n"
    "ds_read_b64_tr_b16 v[178:179], v231 offset:1024\n"
    "v_mfma_f32_16x16x32_bf16 v[84:87], v[204:207], a[172:175], v[84:87]\n"
    "v_mfma_f32_16x16x32_bf16 v[88:91], v[200:203], a[176:179], v[88:91]\n"
    "ds_read_b64_tr_b16 v[180:181], v231 offset:2048\n"
    "v_mfma_f32_16x16x32_bf16 v[88:91], v[204:207], a[180:183], v[88:91]\n"
    "buffer_load_dwordx4 v[28:31], v216, s[40:43], s68 offen offset:1024 sc1\n"
    "v_mfma_f32_16x16x32_bf16 v[92:95], v[200:203], a[184:187], v[92:95]\n"
    "ds_read_b64_tr_b16 v[182:183], v231 offset:3072\n"
    "v_mfma_f32_16x16x32_bf16 v[92:95], v[204:207], a[188:191], v[92:95]\n"
    "s_waitcnt lgkmcnt(8)\n"
    "s_waitcnt vmcnt(16)\n"
    "v_mfma_f32_16x16x32_bf16 v[32:35], v[160:163], a[192:195], v[32:35]\n"
    "ds_read_b64_tr_b16 v[184:185], v231 offset:4096\n"
    "v_mfma_f32_16x16x32_bf16 v[32:35], v[164:167], a[196:199], v[32:35]\n"
    "v_mfma_f32_16x16x32_bf16 v[36:39], v[160:163], a[200:203], v[36:39]\n"
    "ds_read_b64_tr_b16 v[186:187], v231 offset:5120\n"
    "v_mfma_f32_16x16x32_bf16 v[36:39], v[164:167], a[204:207], v[36:39]\n"
    "v_mfma_f32_16x16x32_bf16 v[40:43], v[160:163], a[208:211], v[40:43]\n"
    "ds_read_b64_tr_b16 v[188:189], v231 offset:6144\n"
    "v_mfma_f32_16x16x32_bf16 v[40:43], v[164:167], a[212:215], v[40:43]\n"
    "v_mfma_f32_16x16x32_bf16 v[44:47], v[160:163], a[216:219], v[44:47]\n"
    "ds_read_b64_tr_b16 v[190:191], v231 offset:7168\n"
    "v_mfma_f32_16x16x32_bf16 v[44:47], v[164:167], a[220:223], v[44:47]\n"
    "s_waitcnt lgkmcnt(8)\n"
    "v_mfma_f32_16x16x32_bf16 v[48:51], v[168:171], a[192:195], v[48:51]\n"
    "ds_read_b64_tr_b16 v[192:193], v228 offset:8192\n"
    "v_mfma_f32_16x16x32_bf16 v[48:51], v[172:175], a[196:199], v[48:51]\n"
    "v_mfma_f32_16x16x32_bf16 v[52:55], v[168:171], a[200:203], v[52:55]\n"
    "ds_read_b64_tr_b16 v[194:195], v228 offset:9216\n"
    "v_mfma_f32_16x16x32_bf16 v[52:55], v[172:175], a[204:207], v[52:55]\n"
    "v_mfma_f32_16x16x32_bf16 v[56:59], v[168:171], a[208:211], v[56:59]\n"
    "ds_read_b64_tr_b16 v[196:197], v228 offset:10240\n"
    "v_mfma_f32_16x16x32_bf16 v[56:59], v[172:175], a[212:215], v[56:59]\n"
    "v_mfma_f32_16x16x32_bf16 v[60:63], v[168:171], a[216:219], v[60:63]\n"
    "ds_read_b64_tr_b16 v[198:199], v228 offset:11264\n"
    "v_mfma_f32_16x16x32_bf16 v[60:63], v[172:175], a[220:223], v[60:63]\n"
    "s_waitcnt lgkmcnt(8)\n"
    "v_mfma_f32_16x16x32_bf16 v[64:67], v[176:179], a[192:195], v[64:67]\n"
    "ds_read_b64_tr_b16 v[200:201], v228 offset:12288\n"
    "v_mfma_f32_16x16x32_bf16 v[64:67], v[180:183], a[196:199], v[64:67]\n"
    "v_mfma_f32_16x16x32_bf16 v[68:71], v[176:179], a[200:203], v[68:71]\n"
    "ds_read_b64_tr_b16 v[202:203], v228 offset:13312\n"
    "v_mfma_f32_16x16x32_bf16 v[68:71], v[180:183], a[204:207], v[68:71]\n"
    "v_mfma_f32_16x16x32_bf16 v[72:75], v[176:179], a[208:211], v[72:75]\n"
    "ds_read_b64_tr_b16 v[204:205], v228 offset:14336\n"
    "v_mfma_f32_16x16x32_bf16 v[72:75], v[180:183], a[212:215], v[72:75]\n"
    "v_mfma_f32_16x16x32_bf16 v[76:79], v[176:179], a[216:219], v[76:79]\n"
    "ds_read_b64_tr_b16 v[206:207], v228 offset:15360\n"
    "v_mfma_f32_16x16x32_bf16 v[76:79], v[180:183], a[220:223], v[76:79]\n"
    "s_waitcnt lgkmcnt(8)\n"
    "v_mfma_f32_16x16x32_bf16 v[80:83], v[184:187], a[192:195], v[80:83]\n"
    "ds_read_b64_tr_b16 v[160:161], v231 offset:8192\n"
    "v_mfma_f32_16x16x32_bf16 v[80:83], v[188:191], a[196:199], v[80:83]\n"
    "v_mfma_f32_16x16x32_bf16 v[84:87], v[184:187], a[200:203], v[84:87]\n"
    "ds_read_b64_tr_b16 v[162:163], v231 offset:9216\n"
    "v_mfma_f32_16x16x32_bf16 v[84:87], v[188:191], a[204:207], v[84:87]\n"
    "v_mfma_f32_16x16x32_bf16 v[88:91], v[184:187], a[208:211], v[88:91]\n"
    "ds_read_b64_tr_b16 v[164:165], v231 offset:10240\n"
    "v_mfma_f32_16x16x32_bf16 v[88:91], v[188:191], a[212:215], v[88:91]\n"
    "v_mfma_f32_16x16x32_bf16 v[92:95], v[184:187], a[216:219], v[92:95]\n"
    "ds_read_b64_tr_b16 v[166:167], v231 offset:11264\n"
    "v_mfma_f32_16x16x32_bf16 v[92:95], v[188:191], a[220:223], v[92:95]\n"
    "s_waitcnt lgkmcnt(8)\n"
    "s_waitcnt vmcnt(8)\n"
    "v_mfma_f32_16x16x32_bf16 v[32:35], v[192:195], a[224:227], v[32:35]\n"
    "ds_read_b64_tr_b16 v[168:169], v231 offset:12288\n"
    "v_mfma_f32_16x16x32_bf16 v[32:35], v[196:199], a[228:231], v[32:35]\n"
    "v_mfma_f32_16x16x32_bf16 v[36:39], v[192:195], a[232:235], v[36:39]\n"
    "ds_read_b64_tr_b16 v[170:171], v231 offset:13312\n"
    "v_mfma_f32_16x16x32_bf16 v[36:39], v[196:199], a[236:239], v[36:39]\n"
    "v_mfma_f32_16x16x32_bf16 v[40:43], v[192:195], a[240:243], v[40:43]\n"
    "ds_read_b64_tr_b16 v[172:173], v231 offset:14336\n"
    "v_mfma_f32_16x16x32_bf16 v[40:43], v[196:199], a[244:247], v[40:43]\n"
    "v_mfma_f32_16x16x32_bf16 v[44:47], v[192:195], a[248:251], v[44:47]\n"
    "ds_read_b64_tr_b16 v[174:175], v231 offset:15360\n"
    "v_mfma_f32_16x16x32_bf16 v[44:47], v[196:199], a[252:255], v[44:47]\n"
    "s_waitcnt lgkmcnt(8)\n"
    "v_mfma_f32_16x16x32_bf16 v[48:51], v[200:203], a[224:227], v[48:51]\n"
    "ds_read_b64_tr_b16 v[176:177], v228 offset:16384\n"
    "v_mfma_f32_16x16x32_bf16 v[48:51], v[204:207], a[228:231], v[48:51]\n"
    "v_mfma_f32_16x16x32_bf16 v[52:55], v[200:203], a[232:235], v[52:55]\n"
    "ds_read_b64_tr_b16 v[178:179], v228 offset:17408\n"
    "v_mfma_f32_16x16x32_bf16 v[52:55], v[204:207], a[236:239], v[52:55]\n"
    "v_mfma_f32_16x16x32_bf16 v[56:59], v[200:203], a[240:243], v[56:59]\n"
    "ds_read_b64_tr_b16 v[180:181], v228 offset:18432\n"
    "v_mfma_f32_16x16x32_bf16 v[56:59], v[204:207], a[244:247], v[56:59]\n"
    "v_mfma_f32_16x16x32_bf16 v[60:63], v[200:203], a[248:251], v[60:63]\n"
    "ds_read_b64_tr_b16 v[182:183], v228 offset:19456\n"
    "v_mfma_f32_16x16x32_bf16 v[60:63], v[204:207], a[252:255], v[60:63]\n"
    "s_waitcnt lgkmcnt(8)\n"
    "v_mfma_f32_16x16x32_bf16 v[64:67], v[160:163], a[224:227], v[64:67]\n"
    "ds_read_b64_tr_b16 v[184:185], v228 offset:20480\n"
    "v_mfma_f32_16x16x32_bf16 v[64:67], v[164:167], a[228:231], v[64:67]\n"
    "v_mfma_f32_16x16x32_bf16 v[68:71], v[160:163], a[232:235], v[68:71]\n"
    "ds_read_b64_tr_b16 v[186:187], v228 offset:21504\n"
    "v_mfma_f32_16x16x32_bf16 v[68:71], v[164:167], a[236:239], v[68:71]\n"
    "v_mfma_f32_16x16x32_bf16 v[72:75], v[160:163], a[240:243], v[72:75]\n"
    "ds_read_b64_tr_b16 v[188:189], v228 offset:22528\n"
    "v_mfma_f32_16x16x32_bf16 v[72:75], v[164:167], a[244:247], v[72:75]\n"
    "v_mfma_f32_16x16x32_bf16 v[76:79], v[160:163], a[248:251], v[76:79]\n"
    "ds_read_b64_tr_b16 v[190:191], v228 offset:23552\n"
    "v_mfma_f32_16x16x32_bf16 v[76:79], v[164:167], a[252:255], v[76:79]\n"
    "s_waitcnt lgkmcnt(8)\n"
    "v_mfma_f32_16x16x32_bf16 v[80:83], v[168:171], a[224:227], v[80:83]\n"
    "ds_read_b64_tr_b16 v[192:193], v231 offset:16384\n"
    "v_mfma_f32_16x16x32_bf16 v[80:83], v[172:175], a[228:231], v[80:83]\n"
    "v_mfma_f32_16x16x32_bf16 v[84:87], v[168:171], a[232:235], v[84:87]\n"
    "ds_read_b64_tr_b16 v[194:195], v231 offset:17408\n"
    "v_mfma_f32_16x16x32_bf16 v[84:87], v[172:175], a[236:239], v[84:87]\n"
    "v_mfma_f32_16x16x32_bf16 v[88:91], v[168:171], a[240:243], v[88:91]\n"
    "ds_read_b64_tr_b16 v[196:197], v231 offset:18432\n"
    "v_mfma_f32_16x16x32_bf16 v[88:91], v[172:175], a[244:247], v[88:91]\n"
    "v_mfma_f32_16x16x32_bf16 v[92:95], v[168:171], a[248:251], v[92:95]\n"
    "ds_read_b64_tr_b16 v[198:199], v231 offset:19456\n"
    "v_mfma_f32_16x16x32_bf16 v[92:95], v[172:175], a[252:255], v[92:95]\n"
    "s_waitcnt lgkmcnt(8)\n"
    "s_waitcnt vmcnt(0)\n"
    "v_mfma_f32_16x16x32_bf16 v[32:35], v[176:179], v[0:3], v[32:35]\n"
    "ds_read_b64_tr_b16 v[200:201], v231 offset:20480\n"
    "v_mfma_f32_16x16x32_bf16 v[32:35], v[180:183], v[4:7], v[32:35]\n"
    "v_mfma_f32_16x16x32_bf16 v[36:39], v[176:179], v[8:11], v[36:39]\n"
    "ds_read_b64_tr_b16 v[202:203], v231 offset:21504\n"
    "v_mfma_f32_16x16x32_bf16 v[36:39], v[180:183], v[12:15], v[36:39]\n"
    "v_mfma_f32_16x16x32_bf16 v[40:43], v[176:179], v[16:19], v[40:43]\n"
    "ds_read_b64_tr_b16 v[204:205], v231 offset:22528\n"
    "v_mfma_f32_16x16x32_bf16 v[40:43], v[180:183], v[20:23], v[40:43]\n"
    "v_mfma_f32_16x16x32_bf16 v[44:47], v[176:179], v[24:27], v[44:47]\n"
    "s_waitcnt vmcnt(63)\n"
    "ds_read_b64_tr_b16 v[206:207], v231 offset:23552\n"
    "v_mfma_f32_16x16x32_bf16 v[44:47], v[180:183], v[28:31], v[44:47]\n"
    "s_waitcnt lgkmcnt(8)\n"
    "s_barrier\n"
    "v_mfma_f32_16x16x32_bf16 v[48:51], v[184:187], v[0:3], v[48:51]\n"
    "ds_read_b64_tr_b16 v[160:161], v228 offset:24576\n"
    "v_mfma_f32_16x16x32_bf16 v[48:51], v[188:191], v[4:7], v[48:51]\n"
    "v_mfma_f32_16x16x32_bf16 v[52:55], v[184:187], v[8:11], v[52:55]\n"
    "ds_read_b64_tr_b16 v[162:163], v228 offset:25600\n"
    "v_mfma_f32_16x16x32_bf16 v[52:55], v[188:191], v[12:15], v[52:55]\n"
    "v_max_f32 v32, 0, v32\n"
    "v_max_f32 v33, 0, v33\n"
    "v_mfma_f32_16x16x32_bf16 v[56:59], v[184:187], v[16:19], v[56:59]\n"
    "ds_read_b64_tr_b16 v[164:165], v228 offset:26624\n"
    "v_max_f32 v34, 0, v34\n"
    "v_mfma_f32_16x16x32_bf16 v[56:59], v[188:191], v[20:23], v[56:59]\n"
    "v_max_f32 v35, 0, v35\n"
    "v_mul_f32 v32, v32, v48\n"
    "v_mfma_f32_16x16x32_bf16 v[60:63], v[184:187], v[24:27], v[60:63]\n"
    "ds_read_b64_tr_b16 v[166:167], v228 offset:27648\n"
    "v_mul_f32 v33, v33, v49\n"
    "v_mfma_f32_16x16x32_bf16 v[60:63], v[188:191], v[28:31], v[60:63]\n"
    "s_waitcnt lgkmcnt(8)\n"
    "v_mul_f32 v34, v34, v50\n"
    "v_mul_f32 v35, v35, v51\n"
    "v_mfma_f32_16x16x32_bf16 v[64:67], v[192:195], v[0:3], v[64:67]\n"
    "ds_read_b64_tr_b16 v[168:169], v228 offset:28672\n"
    "v_cvt_pk_bf16_f32 v32, v32, v33\n"
    "v_mfma_f32_16x16x32_bf16 v[64:67], v[196:199], v[4:7], v[64:67]\n"
    "v_cvt_pk_bf16_f32 v33, v34, v35\n"
    "v_max_f32 v36, 0, v36\n"
    "v_mfma_f32_16x16x32_bf16 v[68:71], v[192:195], v[8:11], v[68:71]\n"
    "ds_read_b64_tr_b16 v[170:171], v228 offset:29696\n"
    "v_max_f32 v37, 0, v37\n"
    "v_mfma_f32_16x16x32_bf16 v[68:71], v[196:199], v[12:15], v[68:71]\n"
    "v_max_f32 v38, 0, v38\n"
    "v_max_f32 v39, 0, v39\n"
    "v_mfma_f32_16x16x32_bf16 v[72:75], v[192:195], v[16:19], v[72:75]\n"
    "ds_read_b64_tr_b16 v[172:173], v228 offset:30720\n"
    "v_mul_f32 v36, v36, v52\n"
    "v_mfma_f32_16x16x32_bf16 v[72:75], v[196:199], v[20:23], v[72:75]\n"
    "v_mul_f32 v37, v37, v53\n"
    "v_mul_f32 v38, v38, v54\n"
    "v_mfma_f32_16x16x32_bf16 v[76:79], v[192:195], v[24:27], v[76:79]\n"
    "ds_read_b64_tr_b16 v[174:175], v228 offset:31744\n"
    "v_mul_f32 v39, v39, v55\n"
    "v_mfma_f32_16x16x32_bf16 v[76:79], v[196:199], v[28:31], v[76:79]\n"
    "s_waitcnt lgkmcnt(8)\n"
    "v_cvt_pk_bf16_f32 v36, v36, v37\n"
    "v_cvt_pk_bf16_f32 v37, v38, v39\n"
    "v_mfma_f32_16x16x32_bf16 v[80:83], v[200:203], v[0:3], v[80:83]\n"
    "ds_read_b64_tr_b16 v[176:177], v231 offset:24576\n"
    "v_max_f32 v40, 0, v40\n"
    "v_mfma_f32_16x16x32_bf16 v[80:83], v[204:207], v[4:7], v[80:83]\n"
    "v_max_f32 v41, 0, v41\n"
    "v_max_f32 v42, 0, v42\n"
    "v_mfma_f32_16x16x32_bf16 v[84:87], v[200:203], v[8:11], v[84:87]\n"
    "ds_read_b64_tr_b16 v[178:179], v231 offset:25600\n"
    "v_max_f32 v43, 0, v43\n"
    "v_mfma_f32_16x16x32_bf16 v[84:87], v[204:207], v[12:15], v[84:87]\n"
    "v_mul_f32 v40, v40, v56\n"
    "v_mul_f32 v41, v41, v57\n"
    "v_mfma_f32_16x16x32_bf16 v[88:91], v[200:203], v[16:19], v[88:91]\n"
    "ds_read_b64_tr_b16 v[180:181], v231 offset:26624\n"
    "v_mul_f32 v42, v42, v58\n"
    "v_mfma_f32_16x16x32_bf16 v[88:91], v[204:207], v[20:23], v[88:91]\n"
    "v_mul_f32 v43, v43, v59\n"
    "v_cvt_pk_bf16_f32 v40, v40, v41\n"
    "v_mfma_f32_16x16x32_bf16 v[92:95], v[200:203], v[24:27], v[92:95]\n"
    "ds_read_b64_tr_b16 v[182:183], v231 offset:27648\n"
    "v_cvt_pk_bf16_f32 v41, v42, v43\n"
    "v_mfma_f32_16x16x32_bf16 v[92:95], v[204:207], v[28:31], v[92:95]\n"
    "s_waitcnt lgkmcnt(8)\n"
    "v_max_f32 v44, 0, v44\n"
    "v_max_f32 v45, 0, v45\n"
    "v_mfma_f32_16x16x32_bf16 v[96:99], v[160:163], a[0:3], 0\n"
    "ds_read_b64_tr_b16 v[184:185], v231 offset:28672\n"
    "v_max_f32 v46, 0, v46\n"
    "v_mfma_f32_16x16x32_bf16 v[96:99], v[164:167], a[4:7], v[96:99]\n"
    "v_max_f32 v47, 0, v47\n"
    "v_mul_f32 v44, v44, v60\n"
    "v_mfma_f32_16x16x32_bf16 v[100:103], v[160:163], a[8:11], 0\n"
    "ds_read_b64_tr_b16 v[186:187], v231 offset:29696\n"
    "v_mul_f32 v45, v45, v61\n"
    "v_mfma_f32_16x16x32_bf16 v[100:103], v[164:167], a[12:15], v[100:103]\n"
    "v_mul_f32 v46, v46, v62\n"
    "v_mul_f32 v47, v47, v63\n"
    "v_mfma_f32_16x16x32_bf16 v[104:107], v[160:163], a[16:19], 0\n"
    "ds_read_b64_tr_b16 v[188:189], v231 offset:30720\n"
    "v_cvt_pk_bf16_f32 v44, v44, v45\n"
    "v_mfma_f32_16x16x32_bf16 v[104:107], v[164:167], a[20:23], v[104:107]\n"
    "v_cvt_pk_bf16_f32 v45, v46, v47\n"
    "v_max_f32 v64, 0, v64\n"
    "v_mfma_f32_16x16x32_bf16 v[108:111], v[160:163], a[24:27], 0\n"
    "ds_read_b64_tr_b16 v[190:191], v231 offset:31744\n"
    "v_max_f32 v65, 0, v65\n"
    "v_mfma_f32_16x16x32_bf16 v[108:111], v[164:167], a[28:31], v[108:111]\n"
    "s_waitcnt lgkmcnt(8)\n"
    "v_max_f32 v66, 0, v66\n"
    "v_max_f32 v67, 0, v67\n"
    "v_mfma_f32_16x16x32_bf16 v[112:115], v[168:171], a[0:3], 0\n"
    "ds_read_b64_tr_b16 v[192:193], v228 offset:32768\n"
    "v_mul_f32 v64, v64, v80\n"
    "v_mfma_f32_16x16x32_bf16 v[112:115], v[172:175], a[4:7], v[112:115]\n"
    "s_barrier\n"
    "v_mul_f32 v65, v65, v81\n"
    "v_mul_f32 v66, v66, v82\n"
    "v_mfma_f32_16x16x32_bf16 v[116:119], v[168:171], a[8:11], 0\n"
    "s_add_u32 m0, s61, 0\n"
    "ds_read_b64_tr_b16 v[194:195], v228 offset:33792\n"
    "v_mul_f32 v67, v67, v83\n"
    "v_mfma_f32_16x16x32_bf16 v[116:119], v[172:175], a[12:15], v[116:119]\n"
    "s_add_u32 s63, s62, 0\n"
    "v_cvt_pk_bf16_f32 v34, v64, v65\n"
    "v_cvt_pk_bf16_f32 v35, v66, v67\n"
    "v_mfma_f32_16x16x32_bf16 v[120:123], v[168:171], a[16:19], 0\n"
    "ds_read_b64_tr_b16 v[196:197], v228 offset:34816\n"
    "v_max_f32 v68, 0, v68\n"
    "v_mfma_f32_16x16x32_bf16 v[120:123], v[172:175], a[20:23], v[120:123]\n"
    "buffer_store_dwordx4 v[32:35], v217, s[48:51], s64 offen offset:0 sc1\n"
    "v_max_f32 v69, 0, v69\n"
    "v_max_f32 v70, 0, v70\n"
    "v_mfma_f32_16x16x32_bf16 v[124:127], v[168:171], a[24:27], 0\n"
    "ds_read_b64_tr_b16 v[198:199], v228 offset:35840\n"
    "v_max_f32 v71, 0, v71\n"
    "v_mfma_f32_16x16x32_bf16 v[124:127], v[172:175], a[28:31], v[124:127]\n"
    "s_waitcnt lgkmcnt(8)\n"
    "v_mul_f32 v68, v68, v84\n"
    "v_mul_f32 v69, v69, v85\n"
    "v_mfma_f32_16x16x32_bf16 v[128:131], v[176:179], a[0:3], 0\n"
    "ds_read_b64_tr_b16 v[200:201], v228 offset:36864\n"
    "v_mul_f32 v70, v70, v86\n"
    "v_mfma_f32_16x16x32_bf16 v[128:131], v[180:183], a[4:7], v[128:131]\n"
    "v_mul_f32 v71, v71, v87\n"
    "v_cvt_pk_bf16_f32 v38, v68, v69\n"
    "v_mfma_f32_16x16x32_bf16 v[132:135], v[176:179], a[8:11], 0\n"
    "ds_read_b64_tr_b16 v[202:203], v228 offset:37888\n"
    "v_cvt_pk_bf16_f32 v39, v70, v71\n"
    "v_mfma_f32_16x16x32_bf16 v[132:135], v[180:183], a[12:15], v[132:135]\n"
    "v_max_f32 v72, 0, v72\n"
    "v_max_f32 v73, 0, v73\n"
    "v_mfma_f32_16x16x32_bf16 v[136:139], v[176:179], a[16:19], 0\n"
    "ds_read_b64_tr_b16 v[204:205], v228 offset:38912\n"
    "v_max_f32 v74, 0, v74\n"
    "v_mfma_f32_16x16x32_bf16 v[136:139], v[180:183], a[20:23], v[136:139]\n"
    "buffer_store_dwordx4 v[36:39], v218, s[48:51], s64 offen offset:0 sc1\n"
    "v_max_f32 v75, 0, v75\n"
    "v_mul_f32 v72, v72, v88\n"
    "v_mfma_f32_16x16x32_bf16 v[140:143], v[176:179], a[24:27], 0\n"
    "ds_read_b64_tr_b16 v[206:207], v228 offset:39936\n"
    "v_mul_f32 v73, v73, v89\n"
    "v_mfma_f32_16x16x32_bf16 v[140:143], v[180:183], a[28:31], v[140:143]\n"
    "s_waitcnt lgkmcnt(8)\n"
    "v_mul_f32 v74, v74, v90\n"
    "v_mul_f32 v75, v75, v91\n"
    "v_mfma_f32_16x16x32_bf16 v[144:147], v[184:187], a[0:3], 0\n"
    "ds_read_b64_tr_b16 v[160:161], v231 offset:32768\n"
    "v_cvt_pk_bf16_f32 v42, v72, v73\n"
    "v_mfma_f32_16x16x32_bf16 v[144:147], v[188:191], a[4:7], v[144:147]\n"
    "v_cvt_pk_bf16_f32 v43, v74, v75\n"
    "v_max_f32 v76, 0, v76\n"
    "v_mfma_f32_16x16x32_bf16 v[148:151], v[184:187], a[8:11], 0\n"
    "ds_read_b64_tr_b16 v[162:163], v231 offset:33792\n"
    "v_max_f32 v77, 0, v77\n"
    "v_mfma_f32_16x16x32_bf16 v[148:151], v[188:191], a[12:15], v[148:151]\n"
    "v_max_f32 v78, 0, v78\n"
    "v_max_f32 v79, 0, v79\n"
    "v_mfma_f32_16x16x32_bf16 v[152:155], v[184:187], a[16:19], 0\n"
    "ds_read_b64_tr_b16 v[164:165], v231 offset:34816\n"
    "v_mul_f32 v76, v76, v92\n"
    "v_mfma_f32_16x16x32_bf16 v[152:155], v[188:191], a[20:23], v[152:155]\n"
    "buffer_store_dwordx4 v[40:43], v219, s[48:51], s64 offen offset:0 sc1\n"
    "v_mul_f32 v77, v77, v93\n"
    "v_mul_f32 v78, v78, v94\n"
    "v_mfma_f32_16x16x32_bf16 v[156:159], v[184:187], a[24:27], 0\n"
    "ds_read_b64_tr_b16 v[166:167], v231 offset:35840\n"
    "v_mul_f32 v79, v79, v95\n"
    "v_mfma_f32_16x16x32_bf16 v[156:159], v[188:191], a[28:31], v[156:159]\n"
    "s_waitcnt lgkmcnt(8)\n"
    "v_cvt_pk_bf16_f32 v46, v76, v77\n"
    "v_cvt_pk_bf16_f32 v47, v78, v79\n"
    "v_mfma_f32_16x16x32_bf16 v[96:99], v[192:195], a[32:35], v[96:99]\n"
    "ds_read_b64_tr_b16 v[168:169], v231 offset:36864\n"
    "v_mfma_f32_16x16x32_bf16 v[96:99], v[196:199], a[36:39], v[96:99]\n"
    "v_mfma_f32_16x16x32_bf16 v[100:103], v[192:195], a[40:43], v[100:103]\n"
    "ds_read_b64_tr_b16 v[170:171], v231 offset:37888\n"
    "v_mfma_f32_16x16x32_bf16 v[100:103], v[196:199], a[44:47], v[100:103]\n"
    "v_mfma_f32_16x16x32_bf16 v[104:107], v[192:195], a[48:51], v[104:107]\n"
    "ds_read_b64_tr_b16 v[172:173], v231 offset:38912\n"
    "v_mfma_f32_16x16x32_bf16 v[104:107], v[196:199], a[52:55], v[104:107]\n"
    "buffer_store_dwordx4 v[44:47], v220, s[48:51], s64 offen offset:0 sc1\n"
    "v_mfma_f32_16x16x32_bf16 v[108:111], v[192:195], a[56:59], v[108:111]\n"
    "ds_read_b64_tr_b16 v[174:175], v231 offset:39936\n"
    "v_mfma_f32_16x16x32_bf16 v[108:111], v[196:199], a[60:63], v[108:111]\n"
    "s_waitcnt lgkmcnt(8)\n"
    "v_mfma_f32_16x16x32_bf16 v[112:115], v[200:203], a[32:35], v[112:115]\n"
    "ds_read_b64_tr_b16 v[176:177], v228 offset:40960\n"
    "v_mfma_f32_16x16x32_bf16 v[112:115], v[204:207], a[36:39], v[112:115]\n"
    "v_mfma_f32_16x16x32_bf16 v[116:119], v[200:203], a[40:43], v[116:119]\n"
    "ds_read_b64_tr_b16 v[178:179], v228 offset:41984\n"
    "v_mfma_f32_16x16x32_bf16 v[116:119], v[204:207], a[44:47], v[116:119]\n"
    "v_mfma_f32_16x16x32_bf16 v[120:123], v[200:203], a[48:51], v[120:123]\n"
    "ds_read_b64_tr_b16 v[180:181], v228 offset:43008\n"
    "v_mfma_f32_16x16x32_bf16 v[120:123], v[204:207], a[52:55], v[120:123]\n"
    "buffer_load_dwordx4 v212, s[44:47], s63 offen lds\n"
    "v_mfma_f32_16x16x32_bf16 v[124:127], v[200:203], a[56:59], v[124:127]\n"
    "s_add_u32 m0, s61, 4096\n"
    "ds_read_b64_tr_b16 v[182:183], v228 offset:44032\n"
    "v_mfma_f32_16x16x32_bf16 v[124:127], v[204:207], a[60:63], v[124:127]\n"
    "s_waitcnt lgkmcnt(8)\n"
    "s_add_u32 s63, s62, 589824\n"
    "v_mfma_f32_16x16x32_bf16 v[128:131], v[160:163], a[32:35], v[128:131]\n"
    "ds_read_b64_tr_b16 v[184:185], v228 offset:45056\n"
    "v_mfma_f32_16x16x32_bf16 v[128:131], v[164:167], a[36:39], v[128:131]\n"
    "v_mfma_f32_16x16x32_bf16 v[132:135], v[160:163], a[40:43], v[132:135]\n"
    "ds_read_b64_tr_b16 v[186:187], v228 offset:46080\n"
    "v_mfma_f32_16x16x32_bf16 v[132:135], v[164:167], a[44:47], v[132:135]\n"
    "v_mfma_f32_16x16x32_bf16 v[136:139], v[160:163], a[48:51], v[136:139]\n"
    "ds_read_b64_tr_b16 v[188:189], v228 offset:47104\n"
    "v_mfma_f32_16x16x32_bf16 v[136:139], v[164:167], a[52:55], v[136:139]\n"
    "v_mfma_f32_16x16x32_bf16 v[140:143], v[160:163], a[56:59], v[140:143]\n"
    "ds_read_b64_tr_b16 v[190:191], v228 offset:48128\n"
    "v_mfma_f32_16x16x32_bf16 v[140:143], v[164:167], a[60:63], v[140:143]\n"
    "s_waitcnt lgkmcnt(8)\n"
    "buffer_load_dwordx4 v212, s[44:47], s63 offen lds\n"
    "v_mfma_f32_16x16x32_bf16 v[144:147], v[168:171], a[32:35], v[144:147]\n"
    "s_add_u32 m0, s61, 8192\n"
    "ds_read_b64_tr_b16 v[192:193], v231 offset:40960\n"
    "v_mfma_f32_16x16x32_bf16 v[144:147], v[172:175], a[36:39], v[144:147]\n"
    "s_add_u32 s63, s62, 1179648\n"
    "v_mfma_f32_16x16x32_bf16 v[148:151], v[168:171], a[40:43], v[148:151]\n"
    "ds_read_b64_tr_b16 v[194:195], v231 offset:41984\n"
    "v_mfma_f32_16x16x32_bf16 v[148:151], v[172:175], a[44:47], v[148:151]\n"
    "v_mfma_f32_16x16x32_bf16 v[152:155], v[168:171], a[48:51], v[152:155]\n"
    "ds_read_b64_tr_b16 v[196:197], v231 offset:43008\n"
    "v_mfma_f32_16x16x32_bf16 v[152:155], v[172:175], a[52:55], v[152:155]\n"
    "v_mfma_f32_16x16x32_bf16 v[156:159], v[168:171], a[56:59], v[156:159]\n"
    "ds_read_b64_tr_b16 v[198:199], v231 offset:44032\n"
    "v_mfma_f32_16x16x32_bf16 v[156:159], v[172:175], a[60:63], v[156:159]\n"
    "s_waitcnt lgkmcnt(8)\n"
    "v_mfma_f32_16x16x32_bf16 v[96:99], v[176:179], a[64:67], v[96:99]\n"
    "ds_read_b64_tr_b16 v[200:201], v231 offset:45056\n"
    "v_mfma_f32_16x16x32_bf16 v[96:99], v[180:183], a[68:71], v[96:99]\n"
    "buffer_load_dwordx4 v212, s[44:47], s63 offen lds\n"
    "v_mfma_f32_16x16x32_bf16 v[100:103], v[176:179], a[72:75], v[100:103]\n"
    "s_add_u32 m0, s61, 12288\n"
    "ds_read_b64_tr_b16 v[202:203], v231 offset:46080\n"
    "v_mfma_f32_16x16x32_bf16 v[100:103], v[180:183], a[76:79], v[100:103]\n"
    "s_add_u32 s63, s62, 1769472\n"
    "v_mfma_f32_16x16x32_bf16 v[104:107], v[176:179], a[80:83], v[104:107]\n"
    "ds_read_b64_tr_b16 v[204:205], v231 offset:47104\n"
    "v_mfma_f32_16x16x32_bf16 v[104:107], v[180:183], a[84:87], v[104:107]\n"
    "v_mfma_f32_16x16x32_bf16 v[108:111], v[176:179], a[88:91], v[108:111]\n"
    "ds_read_b64_tr_b16 v[206:207], v231 offset:48128\n"
    "v_mfma_f32_16x16x32_bf16 v[108:111], v[180:183], a[92:95], v[108:111]\n"
    "s_waitcnt lgkmcnt(8)\n"
    "v_mfma_f32_16x16x32_bf16 v[112:115], v[184:187], a[64:67], v[112:115]\n"
    "ds_read_b64_tr_b16 v[160:161], v229 offset:0\n"
    "v_mfma_f32_16x16x32_bf16 v[112:115], v[188:191], a[68:71], v[112:115]\n"
    "v_mfma_f32_16x16x32_bf16 v[116:119], v[184:187], a[72:75], v[116:119]\n"
    "ds_read_b64_tr_b16 v[162:163], v229 offset:1024\n"
    "v_mfma_f32_16x16x32_bf16 v[116:119], v[188:191], a[76:79], v[116:119]\n"
    "buffer_load_dwordx4 v212, s[44:47], s63 offen lds\n"
    "v_mfma_f32_16x16x32_bf16 v[120:123], v[184:187], a[80:83], v[120:123]\n"
    "s_add_u32 m0, s61, 16384\n"
    "ds_read_b64_tr_b16 v[164:165], v229 offset:2048\n"
    "v_mfma_f32_16x16x32_bf16 v[120:123], v[188:191], a[84:87], v[120:123]\n"
    "s_add_u32 s63, s62, 2359296\n"
    "v_mfma_f32_16x16x32_bf16 v[124:127], v[184:187], a[88:91], v[124:127]\n"
    "ds_read_b64_tr_b16 v[166:167], v229 offset:3072\n"
    "v_mfma_f32_16x16x32_bf16 v[124:127], v[188:191], a[92:95], v[124:127]\n"
    "s_waitcnt lgkmcnt(8)\n"
    "v_mfma_f32_16x16x32_bf16 v[128:131], v[192:195], a[64:67], v[128:131]\n"
    "ds_read_b64_tr_b16 v[168:169], v229 offset:4096\n"
    "v_mfma_f32_16x16x32_bf16 v[128:131], v[196:199], a[68:71], v[128:131]\n"
    "v_mfma_f32_16x16x32_bf16 v[132:135], v[192:195], a[72:75], v[132:135]\n"
    "ds_read_b64_tr_b16 v[170:171], v229 offset:5120\n"
    "v_mfma_f32_16x16x32_bf16 v[132:135], v[196:199], a[76:79], v[132:135]\n"
    "v_mfma_f32_16x16x32_bf16 v[136:139], v[192:195], a[80:83], v[136:139]\n"
    "ds_read_b64_tr_b16 v[172:173], v229 offset:6144\n"
    "v_mfma_f32_16x16x32_bf16 v[136:139], v[196:199], a[84:87], v[136:139]\n"
    "buffer_load_dwordx4 v212, s[44:47], s63 offen lds\n"
    "v_mfma_f32_16x16x32_bf16 v[140:143], v[192:195], a[88:91], v[140:143]\n"
    "s_add_u32 m0, s61, 20480\n"
    "ds_read_b64_tr_b16 v[174:175], v229 offset:7168\n"
    "v_mfma_f32_16x16x32_bf16 v[140:143], v[196:199], a[92:95], v[140:143]\n"
    "s_waitcnt lgkmcnt(8)\n"
    "s_add_u32 s63, s62, 2949120\n"
    "v_mfma_f32_16x16x32_bf16 v[144:147], v[200:203], a[64:67], v[144:147]\n"
    "ds_read_b64_tr_b16 v[176:177], v232 offset:0\n"
    "v_mfma_f32_16x16x32_bf16 v[144:147], v[204:207], a[68:71], v[144:147]\n"
    "v_mfma_f32_16x16x32_bf16 v[148:151], v[200:203], a[72:75], v[148:151]\n"
    "ds_read_b64_tr_b16 v[178:179], v232 offset:1024\n"
    "v_mfma_f32_16x16x32_bf16 v[148:151], v[204:207], a[76:79], v[148:151]\n"
    "v_mfma_f32_16x16x32_bf16 v[152:155], v[200:203], a[80:83], v[152:155]\n"
    "ds_read_b64_tr_b16 v[180:181], v232 offset:2048\n"
    "v_mfma_f32_16x16x32_bf16 v[152:155], v[204:207], a[84:87], v[152:155]\n"
    "v_mfma_f32_16x16x32_bf16 v[156:159], v[200:203], a[88:91], v[156:159]\n"
    "ds_read_b64_tr_b16 v[182:183], v232 offset:3072\n"
    "v_mfma_f32_16x16x32_bf16 v[156:159], v[204:207], a[92:95], v[156:159]\n"
    "s_waitcnt lgkmcnt(8)\n"
    "buffer_load_dwordx4 v212, s[44:47], s63 offen lds\n"
    "v_mfma_f32_16x16x32_bf16 v[96:99], v[160:163], a[96:99], v[96:99]\n"
    "s_add_u32 m0, s61, 24576\n"
    "ds_read_b64_tr_b16 v[184:185], v232 offset:4096\n"
    "v_mfma_f32_16x16x32_bf16 v[96:99], v[164:167], a[100:103], v[96:99]\n"
    "s_add_u32 s63, s62, 3538944\n"
    "v_mfma_f32_16x16x32_bf16 v[100:103], v[160:163], a[104:107], v[100:103]\n"
    "ds_read_b64_tr_b16 v[186:187], v232 offset:5120\n"
    "v_mfma_f32_16x16x32_bf16 v[100:103], v[164:167], a[108:111], v[100:103]\n"
    "v_mfma_f32_16x16x32_bf16 v[104:107], v[160:163], a[112:115], v[104:107]\n"
    "ds_read_b64_tr_b16 v[188:189], v232 offset:6144\n"
    "v_mfma_f32_16x16x32_bf16 v[104:107], v[164:167], a[116:119], v[104:107]\n"
    "v_mfma_f32_16x16x32_bf16 v[108:111], v[160:163], a[120:123], v[108:111]\n"
    "ds_read_b64_tr_b16 v[190:191], v232 offset:7168\n"
    "v_mfma_f32_16x16x32_bf16 v[108:111], v[164:167], a[124:127], v[108:111]\n"
    "s_waitcnt lgkmcnt(8)\n"
    "v_mfma_f32_16x16x32_bf16 v[112:115], v[168:171], a[96:99], v[112:115]\n"
    "ds_read_b64_tr_b16 v[192:193], v229 offset:8192\n"
    "v_mfma_f32_16x16x32_bf16 v[112:115], v[172:175], a[100:103], v[112:115]\n"
    "buffer_load_dwordx4 v212, s[44:47], s63 offen lds\n"
    "v_mfma_f32_16x16x32_bf16 v[116:119], v[168:171], a[104:107], v[116:119]\n"
    "s_add_u32 m0, s61, 28672\n"
    "ds_read_b64_tr_b16 v[194:195], v229 offset:9216\n"
    "v_mfma_f32_16x16x32_bf16 v[116:119], v[172:175], a[108:111], v[116:119]\n"
    "s_add_u32 s63, s62, 4128768\n"
    "v_mfma_f32_16x16x32_bf16 v[120:123], v[168:171], a[112:115], v[120:123]\n"
    "ds_read_b64_tr_b16 v[196:197], v229 offset:10240\n"
    "v_mfma_f32_16x16x32_bf16 v[120:123], v[172:175], a[116:119], v[120:123]\n"
    "v_mfma_f32_16x16x32_bf16 v[124:127], v[168:171], a[120:123], v[124:127]\n"
    "ds_read_b64_tr_b16 v[198:199], v229 offset:11264\n"
    "v_mfma_f32_16x16x32_bf16 v[124:127], v[172:175], a[124:127], v[124:127]\n"
    "s_waitcnt lgkmcnt(8)\n"
    "v_mfma_f32_16x16x32_bf16 v[128:131], v[176:179], a[96:99], v[128:131]\n"
    "ds_read_b64_tr_b16 v[200:201], v229 offset:12288\n"
    "v_mfma_f32_16x16x32_bf16 v[128:131], v[180:183], a[100:103], v[128:131]\n"
    "v_mfma_f32_16x16x32_bf16 v[132:135], v[176:179], a[104:107], v[132:135]\n"
    "ds_read_b64_tr_b16 v[202:203], v229 offset:13312\n"
    "v_mfma_f32_16x16x32_bf16 v[132:135], v[180:183], a[108:111], v[132:135]\n"
    "buffer_load_dwordx4 v212, s[44:47], s63 offen lds\n"
    "v_mfma_f32_16x16x32_bf16 v[136:139], v[176:179], a[112:115], v[136:139]\n"
    "s_add_u32 m0, s61, 32768\n"
    "ds_read_b64_tr_b16 v[204:205], v229 offset:14336\n"
    "v_mfma_f32_16x16x32_bf16 v[136:139], v[180:183], a[116:119], v[136:139]\n"
    "s_add_u32 s63, s62, 4718592\n"
    "v_mfma_f32_16x16x32_bf16 v[140:143], v[176:179], a[120:123], v[140:143]\n"
    "ds_read_b64_tr_b16 v[206:207], v229 offset:15360\n"
    "v_mfma_f32_16x16x32_bf16 v[140:143], v[180:183], a[124:127], v[140:143]\n"
    "s_waitcnt lgkmcnt(8)\n"
    "v_mfma_f32_16x16x32_bf16 v[144:147], v[184:187], a[96:99], v[144:147]\n"
    "ds_read_b64_tr_b16 v[160:161], v232 offset:8192\n"
    "v_mfma_f32_16x16x32_bf16 v[144:147], v[188:191], a[100:103], v[144:147]\n"
    "v_mfma_f32_16x16x32_bf16 v[148:151], v[184:187], a[104:107], v[148:151]\n"
    "ds_read_b64_tr_b16 v[162:163], v232 offset:9216\n"
    "v_mfma_f32_16x16x32_bf16 v[148:151], v[188:191], a[108:111], v[148:151]\n"
    "v_mfma_f32_16x16x32_bf16 v[152:155], v[184:187], a[112:115], v[152:155]\n"
    "ds_read_b64_tr_b16 v[164:165], v232 offset:10240\n"
    "v_mfma_f32_16x16x32_bf16 v[152:155], v[188:191], a[116:119], v[152:155]\n"
    "buffer_load_dwordx4 v212, s[44:47], s63 offen lds\n"
    "v_mfma_f32_16x16x32_bf16 v[156:159], v[184:187], a[120:123], v[156:159]\n"
    "s_add_u32 m0, s60, 36864\n"
    "ds_read_b64_tr_b16 v[166:167], v232 offset:11264\n"
    "v_mfma_f32_16x16x32_bf16 v[156:159], v[188:191], a[124:127], v[156:159]\n"
    "s_waitcnt lgkmcnt(8)\n"
    "s_add_u32 s63, s62, 128\n"
    "v_mfma_f32_16x16x32_bf16 v[96:99], v[192:195], a[128:131], v[96:99]\n"
    "ds_read_b64_tr_b16 v[168:169], v232 offset:12288\n"
    "v_mfma_f32_16x16x32_bf16 v[96:99], v[196:199], a[132:135], v[96:99]\n"
    "v_mfma_f32_16x16x32_bf16 v[100:103], v[192:195], a[136:139], v[100:103]\n"
    "ds_read_b64_tr_b16 v[170:171], v232 offset:13312\n"
    "v_mfma_f32_16x16x32_bf16 v[100:103], v[196:199], a[140:143], v[100:103]\n"
    "v_mfma_f32_16x16x32_bf16 v[104:107], v[192:195], a[144:147], v[104:107]\n"
    "ds_read_b64_tr_b16 v[172:173], v232 offset:14336\n"
    "v_mfma_f32_16x16x32_bf16 v[104:107], v[196:199], a[148:151], v[104:107]\n"
    "v_mfma_f32_16x16x32_bf16 v[108:111], v[192:195], a[152:155], v[108:111]\n"
    "ds_read_b64_tr_b16 v[174:175], v232 offset:15360\n"
    "v_mfma_f32_16x16x32_bf16 v[108:111], v[196:199], a[156:159], v[108:111]\n"
    "s_waitcnt lgkmcnt(8)\n"
    "buffer_load_dwordx4 v208, s[44:47], s63 offen lds\n"
    "v_mfma_f32_16x16x32_bf16 v[112:115], v[200:203], a[128:131], v[112:115]\n"
    "ds_read_b64_tr_b16 v[176:177], v229 offset:16384\n"
    "v_mfma_f32_16x16x32_bf16 v[112:115], v[204:207], a[132:135], v[112:115]\n"
    "v_mfma_f32_16x16x32_bf16 v[116:119], v[200:203], a[136:139], v[116:119]\n"
    "ds_read_b64_tr_b16 v[178:179], v229 offset:17408\n"
    "v_mfma_f32_16x16x32_bf16 v[116:119], v[204:207], a[140:143], v[116:119]\n"
    "v_mfma_f32_16x16x32_bf16 v[120:123], v[200:203], a[144:147], v[120:123]\n"
    "ds_read_b64_tr_b16 v[180:181], v229 offset:18432\n"
    "v_mfma_f32_16x16x32_bf16 v[120:123], v[204:207], a[148:151], v[120:123]\n"
    "v_mfma_f32_16x16x32_bf16 v[124:127], v[200:203], a[152:155], v[124:127]\n"
    "ds_read_b64_tr_b16 v[182:183], v229 offset:19456\n"
    "v_mfma_f32_16x16x32_bf16 v[124:127], v[204:207], a[156:159], v[124:127]\n"
    "s_waitcnt lgkmcnt(8)\n"
    "v_mfma_f32_16x16x32_bf16 v[128:131], v[160:163], a[128:131], v[128:131]\n"
    "ds_read_b64_tr_b16 v[184:185], v229 offset:20480\n"
    "v_mfma_f32_16x16x32_bf16 v[128:131], v[164:167], a[132:135], v[128:131]\n"
    "buffer_load_dwordx4 v209, s[44:47], s63 offen offset:1024 lds\n"
    "v_mfma_f32_16x16x32_bf16 v[132:135], v[160:163], a[136:139], v[132:135]\n"
    "s_add_u32 m0, s60, 45056\n"
    "ds_read_b64_tr_b16 v[186:187], v229 offset:21504\n"
    "v_mfma_f32_16x16x32_bf16 v[132:135], v[164:167], a[140:143], v[132:135]\n"
    "s_add_u32 s63, s62, 589952\n"
    "v_mfma_f32_16x16x32_bf16 v[136:139], v[160:163], a[144:147], v[136:139]\n"
    "ds_read_b64_tr_b16 v[188:189], v229 offset:22528\n"
    "v_mfma_f32_16x16x32_bf16 v[136:139], v[164:167], a[148:151], v[136:139]\n"
    "v_mfma_f32_16x16x32_bf16 v[140:143], v[160:163], a[152:155], v[140:143]\n"
    "ds_read_b64_tr_b16 v[190:191], v229 offset:23552\n"
    "v_mfma_f32_16x16x32_bf16 v[140:143], v[164:167], a[156:159], v[140:143]\n"
    "s_waitcnt lgkmcnt(8)\n"
    "v_mfma_f32_16x16x32_bf16 v[144:147], v[168:171], a[128:131], v[144:147]\n"
    "ds_read_b64_tr_b16 v[192:193], v232 offset:16384\n"
    "v_mfma_f32_16x16x32_bf16 v[144:147], v[172:175], a[132:135], v[144:147]\n"
    "v_mfma_f32_16x16x32_bf16 v[148:151], v[168:171], a[136:139], v[148:151]\n"
    "ds_read_b64_tr_b16 v[194:195], v232 offset:17408\n"
    "v_mfma_f32_16x16x32_bf16 v[148:151], v[172:175], a[140:143], v[148:151]\n"
    "buffer_load_dwordx4 v208, s[44:47], s63 offen lds\n"
    "v_mfma_f32_16x16x32_bf16 v[152:155], v[168:171], a[144:147], v[152:155]\n"
    "ds_read_b64_tr_b16 v[196:197], v232 offset:18432\n"
    "v_mfma_f32_16x16x32_bf16 v[152:155], v[172:175], a[148:151], v[152:155]\n"
    "v_mfma_f32_16x16x32_bf16 v[156:159], v[168:171], a[152:155], v[156:159]\n"
    "ds_read_b64_tr_b16 v[198:199], v232 offset:19456\n"
    "v_mfma_f32_16x16x32_bf16 v[156:159], v[172:175], a[156:159], v[156:159]\n"
    "s_waitcnt lgkmcnt(8)\n"
    "v_mfma_f32_16x16x32_bf16 v[96:99], v[176:179], a[160:163], v[96:99]\n"
    "ds_read_b64_tr_b16 v[200:201], v232 offset:20480\n"
    "v_mfma_f32_16x16x32_bf16 v[96:99], v[180:183], a[164:167], v[96:99]\n"
    "v_mfma_f32_16x16x32_bf16 v[100:103], v[176:179], a[168:171], v[100:103]\n"
    "ds_read_b64_tr_b16 v[202:203], v232 offset:21504\n"
    "v_mfma_f32_16x16x32_bf16 v[100:103], v[180:183], a[172:175], v[100:103]\n"
    "v_mfma_f32_16x16x32_bf16 v[104:107], v[176:179], a[176:179], v[104:107]\n"
    "ds_read_b64_tr_b16 v[204:205], v232 offset:22528\n"
    "v_mfma_f32_16x16x32_bf16 v[104:107], v[180:183], a[180:183], v[104:107]\n"
    "buffer_load_dwordx4 v209, s[44:47], s63 offen offset:1024 lds\n"
    "v_mfma_f32_16x16x32_bf16 v[108:111], v[176:179], a[184:187], v[108:111]\n"
    "s_add_u32 m0, s60, 53248\n"
    "ds_read_b64_tr_b16 v[206:207], v232 offset:23552\n"
    "v_mfma_f32_16x16x32_bf16 v[108:111], v[180:183], a[188:191], v[108:111]\n"
    "s_waitcnt lgkmcnt(8)\n"
    "s_add_u32 s63, s62, 1179776\n"
    "v_mfma_f32_16x16x32_bf16 v[112:115], v[184:187], a[160:163], v[112:115]\n"
    "ds_read_b64_tr_b16 v[160:161], v229 offset:24576\n"
    "v_mfma_f32_16x16x32_bf16 v[112:115], v[188:191], a[164:167], v[112:115]\n"
    "v_mfma_f32_16x16x32_bf16 v[116:119], v[184:187], a[168:171], v[116:119]\n"
    "ds_read_b64_tr_b16 v[162:163], v229 offset:25600\n"
    "v_mfma_f32_16x16x32_bf16 v[116:119], v[188:191], a[172:175], v[116:119]\n"
    "v_mfma_f32_16x16x32_bf16 v[120:123], v[184:187], a[176:179], v[120:123]\n"
    "ds_read_b64_tr_b16 v[164:165], v229 offset:26624\n"
    "v_mfma_f32_16x16x32_bf16 v[120:123], v[188:191], a[180:183], v[120:123]\n"
    "v_mfma_f32_16x16x32_bf16 v[124:127], v[184:187], a[184:187], v[124:127]\n"
    "ds_read_b64_tr_b16 v[166:167], v229 offset:27648\n"
    "v_mfma_f32_16x16x32_bf16 v[124:127], v[188:191], a[188:191], v[124:127]\n"
    "s_waitcnt lgkmcnt(8)\n"
    "buffer_load_dwordx4 v208, s[44:47], s63 offen lds\n"
    "v_mfma_f32_16x16x32_bf16 v[128:131], v[192:195], a[160:163], v[128:131]\n"
    "ds_read_b64_tr_b16 v[168:169], v229 offset:28672\n"
    "v_mfma_f32_16x16x32_bf16 v[128:131], v[196:199], a[164:167], v[128:131]\n"
    "v_mfma_f32_16x16x32_bf16 v[132:135], v[192:195], a[168:171], v[132:135]\n"
    "ds_read_b64_tr_b16 v[170:171], v229 offset:29696\n"
    "v_mfma_f32_16x16x32_bf16 v[132:135], v[196:199], a[172:175], v[132:135]\n"
    "v_mfma_f32_16x16x32_bf16 v[136:139], v[192:195], a[176:179], v[136:139]\n"
    "ds_read_b64_tr_b16 v[172:173], v229 offset:30720\n"
    "v_mfma_f32_16x16x32_bf16 v[136:139], v[196:199], a[180:183], v[136:139]\n"
    "v_mfma_f32_16x16x32_bf16 v[140:143], v[192:195], a[184:187], v[140:143]\n"
    "ds_read_b64_tr_b16 v[174:175], v229 offset:31744\n"
    "v_mfma_f32_16x16x32_bf16 v[140:143], v[196:199], a[188:191], v[140:143]\n"
    "s_waitcnt lgkmcnt(8)\n"
    "v_mfma_f32_16x16x32_bf16 v[144:147], v[200:203], a[160:163], v[144:147]\n"
    "ds_read_b64_tr_b16 v[176:177], v232 offset:24576\n"
    "v_mfma_f32_16x16x32_bf16 v[144:147], v[204:207], a[164:167], v[144:147]\n"
    "buffer_load_dwordx4 v209, s[44:47], s63 offen offset:1024 lds\n"
    "v_mfma_f32_16x16x32_bf16 v[148:151], v[200:203], a[168:171], v[148:151]\n"
    "s_add_u32 m0, s60, 61440\n"
    "ds_read_b64_tr_b16 v[178:179], v232 offset:25600\n"
    "v_mfma_f32_16x16x32_bf16 v[148:151], v[204:207], a[172:175], v[148:151]\n"
    "s_add_u32 s63, s62, 1769600\n"
    "v_mfma_f32_16x16x32_bf16 v[152:155], v[200:203], a[176:179], v[152:155]\n"
    "ds_read_b64_tr_b16 v[180:181], v232 offset:26624\n"
    "v_mfma_f32_16x16x32_bf16 v[152:155], v[204:207], a[180:183], v[152:155]\n"
    "v_mfma_f32_16x16x32_bf16 v[156:159], v[200:203], a[184:187], v[156:159]\n"
    "ds_read_b64_tr_b16 v[182:183], v232 offset:27648\n"
    "v_mfma_f32_16x16x32_bf16 v[156:159], v[204:207], a[188:191], v[156:159]\n"
    "s_waitcnt lgkmcnt(8)\n"
    "v_mfma_f32_16x16x32_bf16 v[96:99], v[160:163], a[192:195], v[96:99]\n"
    "ds_read_b64_tr_b16 v[184:185], v232 offset:28672\n"
    "v_mfma_f32_16x16x32_bf16 v[96:99], v[164:167], a[196:199], v[96:99]\n"
    "v_mfma_f32_16x16x32_bf16 v[100:103], v[160:163], a[200:203], v[100:103]\n"
    "ds_read_b64_tr_b16 v[186:187], v232 offset:29696\n"
    "v_mfma_f32_16x16x32_bf16 v[100:103], v[164:167], a[204:207], v[100:103]\n"
    "buffer_load_dwordx4 v208, s[44:47], s63 offen lds\n"
    "v_mfma_f32_16x16x32_bf16 v[104:107], v[160:163], a[208:211], v[104:107]\n"
    "ds_read_b64_tr_b16 v[188:189], v232 offset:30720\n"
    "v_mfma_f32_16x16x32_bf16 v[104:107], v[164:167], a[212:215], v[104:107]\n"
    "v_mfma_f32_16x16x32_bf16 v[108:111], v[160:163], a[216:219], v[108:111]\n"
    "ds_read_b64_tr_b16 v[190:191], v232 offset:31744\n"
    "v_mfma_f32_16x16x32_bf16 v[108:111], v[164:167], a[220:223], v[108:111]\n"
    "s_waitcnt lgkmcnt(8)\n"
    "v_mfma_f32_16x16x32_bf16 v[112:115], v[168:171], a[192:195], v[112:115]\n"
    "ds_read_b64_tr_b16 v[192:193], v229 offset:32768\n"
    "v_mfma_f32_16x16x32_bf16 v[112:115], v[172:175], a[196:199], v[112:115]\n"
    "v_mfma_f32_16x16x32_bf16 v[116:119], v[168:171], a[200:203], v[116:119]\n"
    "ds_read_b64_tr_b16 v[194:195], v229 offset:33792\n"
    "v_mfma_f32_16x16x32_bf16 v[116:119], v[172:175], a[204:207], v[116:119]\n"
    "v_mfma_f32_16x16x32_bf16 v[120:123], v[168:171], a[208:211], v[120:123]\n"
    "ds_read_b64_tr_b16 v[196:197], v229 offset:34816\n"
    "v_mfma_f32_16x16x32_bf16 v[120:123], v[172:175], a[212:215], v[120:123]\n"
    "buffer_load_dwordx4 v209, s[44:47], s63 offen offset:1024 lds\n"
    "v_mfma_f32_16x16x32_bf16 v[124:127], v[168:171], a[216:219], v[124:127]\n"
    "ds_read_b64_tr_b16 v[198:199], v229 offset:35840\n"
    "v_mfma_f32_16x16x32_bf16 v[124:127], v[172:175], a[220:223], v[124:127]\n"
    "s_waitcnt lgkmcnt(8)\n"
    "v_mfma_f32_16x16x32_bf16 v[128:131], v[176:179], a[192:195], v[128:131]\n"
    "ds_read_b64_tr_b16 v[200:201], v229 offset:36864\n"
    "v_mfma_f32_16x16x32_bf16 v[128:131], v[180:183], a[196:199], v[128:131]\n"
    "v_mfma_f32_16x16x32_bf16 v[132:135], v[176:179], a[200:203], v[132:135]\n"
    "ds_read_b64_tr_b16 v[202:203], v229 offset:37888\n"
    "v_mfma_f32_16x16x32_bf16 v[132:135], v[180:183], a[204:207], v[132:135]\n"
    "v_mfma_f32_16x16x32_bf16 v[136:139], v[176:179], a[208:211], v[136:139]\n"
    "ds_read_b64_tr_b16 v[204:205], v229 offset:38912\n"
    "v_mfma_f32_16x16x32_bf16 v[136:139], v[180:183], a[212:215], v[136:139]\n"
    "v_mfma_f32_16x16x32_bf16 v[140:143], v[176:179], a[216:219], v[140:143]\n"
    "ds_read_b64_tr_b16 v[206:207], v229 offset:39936\n"
    "v_mfma_f32_16x16x32_bf16 v[140:143], v[180:183], a[220:223], v[140:143]\n"
    "s_waitcnt lgkmcnt(8)\n"
    "v_mfma_f32_16x16x32_bf16 v[144:147], v[184:187], a[192:195], v[144:147]\n"
    "ds_read_b64_tr_b16 v[160:161], v232 offset:32768\n"
    "v_mfma_f32_16x16x32_bf16 v[144:147], v[188:191], a[196:199], v[144:147]\n"
    "v_mfma_f32_16x16x32_bf16 v[148:151], v[184:187], a[200:203], v[148:151]\n"
    "ds_read_b64_tr_b16 v[162:163], v232 offset:33792\n"
    "v_mfma_f32_16x16x32_bf16 v[148:151], v[188:191], a[204:207], v[148:151]\n"
    "v_mfma_f32_16x16x32_bf16 v[152:155], v[184:187], a[208:211], v[152:155]\n"
    "ds_read_b64_tr_b16 v[164:165], v232 offset:34816\n"
    "v_mfma_f32_16x16x32_bf16 v[152:155], v[188:191], a[212:215], v[152:155]\n"
    "v_mfma_f32_16x16x32_bf16 v[156:159], v[184:187], a[216:219], v[156:159]\n"
    "ds_read_b64_tr_b16 v[166:167], v232 offset:35840\n"
    "v_mfma_f32_16x16x32_bf16 v[156:159], v[188:191], a[220:223], v[156:159]\n"
    "s_waitcnt lgkmcnt(8)\n"
    "v_mfma_f32_16x16x32_bf16 v[96:99], v[192:195], a[224:227], v[96:99]\n"
    "ds_read_b64_tr_b16 v[168:169], v232 offset:36864\n"
    "v_mfma_f32_16x16x32_bf16 v[96:99], v[196:199], a[228:231], v[96:99]\n"
    "v_mfma_f32_16x16x32_bf16 v[100:103], v[192:195], a[232:235], v[100:103]\n"
    "ds_read_b64_tr_b16 v[170:171], v232 offset:37888\n"
    "v_mfma_f32_16x16x32_bf16 v[100:103], v[196:199], a[236:239], v[100:103]\n"
    "v_mfma_f32_16x16x32_bf16 v[104:107], v[192:195], a[240:243], v[104:107]\n"
    "ds_read_b64_tr_b16 v[172:173], v232 offset:38912\n"
    "v_mfma_f32_16x16x32_bf16 v[104:107], v[196:199], a[244:247], v[104:107]\n"
    "v_mfma_f32_16x16x32_bf16 v[108:111], v[192:195], a[248:251], v[108:111]\n"
    "ds_read_b64_tr_b16 v[174:175], v232 offset:39936\n"
    "v_mfma_f32_16x16x32_bf16 v[108:111], v[196:199], a[252:255], v[108:111]\n"
    "s_waitcnt lgkmcnt(8)\n"
    "v_mfma_f32_16x16x32_bf16 v[112:115], v[200:203], a[224:227], v[112:115]\n"
    "ds_read_b64_tr_b16 v[176:177], v229 offset:40960\n"
    "v_mfma_f32_16x16x32_bf16 v[112:115], v[204:207], a[228:231], v[112:115]\n"
    "v_mfma_f32_16x16x32_bf16 v[116:119], v[200:203], a[232:235], v[116:119]\n"
    "ds_read_b64_tr_b16 v[178:179], v229 offset:41984\n"
    "v_mfma_f32_16x16x32_bf16 v[116:119], v[204:207], a[236:239], v[116:119]\n"
    "v_mfma_f32_16x16x32_bf16 v[120:123], v[200:203], a[240:243], v[120:123]\n"
    "ds_read_b64_tr_b16 v[180:181], v229 offset:43008\n"
    "v_mfma_f32_16x16x32_bf16 v[120:123], v[204:207], a[244:247], v[120:123]\n"
    "v_mfma_f32_16x16x32_bf16 v[124:127], v[200:203], a[248:251], v[124:127]\n"
    "ds_read_b64_tr_b16 v[182:183], v229 offset:44032\n"
    "v_mfma_f32_16x16x32_bf16 v[124:127], v[204:207], a[252:255], v[124:127]\n"
    "s_waitcnt lgkmcnt(8)\n"
    "v_mfma_f32_16x16x32_bf16 v[128:131], v[160:163], a[224:227], v[128:131]\n"
    "ds_read_b64_tr_b16 v[184:185], v229 offset:45056\n"
    "v_mfma_f32_16x16x32_bf16 v[128:131], v[164:167], a[228:231], v[128:131]\n"
    "v_mfma_f32_16x16x32_bf16 v[132:135], v[160:163], a[232:235], v[132:135]\n"
    "ds_read_b64_tr_b16 v[186:187], v229 offset:46080\n"
    "v_mfma_f32_16x16x32_bf16 v[132:135], v[164:167], a[236:239], v[132:135]\n"
    "v_mfma_f32_16x16x32_bf16 v[136:139], v[160:163], a[240:243], v[136:139]\n"
    "ds_read_b64_tr_b16 v[188:189], v229 offset:47104\n"
    "v_mfma_f32_16x16x32_bf16 v[136:139], v[164:167], a[244:247], v[136:139]\n"
    "v_mfma_f32_16x16x32_bf16 v[140:143], v[160:163], a[248:251], v[140:143]\n"
    "ds_read_b64_tr_b16 v[190:191], v229 offset:48128\n"
    "v_mfma_f32_16x16x32_bf16 v[140:143], v[164:167], a[252:255], v[140:143]\n"
    "s_waitcnt lgkmcnt(8)\n"
    "v_mfma_f32_16x16x32_bf16 v[144:147], v[168:171], a[224:227], v[144:147]\n"
    "ds_read_b64_tr_b16 v[192:193], v232 offset:40960\n"
    "v_mfma_f32_16x16x32_bf16 v[144:147], v[172:175], a[228:231], v[144:147]\n"
    "v_mfma_f32_16x16x32_bf16 v[148:151], v[168:171], a[232:235], v[148:151]\n"
    "ds_read_b64_tr_b16 v[194:195], v232 offset:41984\n"
    "v_mfma_f32_16x16x32_bf16 v[148:151], v[172:175], a[236:239], v[148:151]\n"
    "v_mfma_f32_16x16x32_bf16 v[152:155], v[168:171], a[240:243], v[152:155]\n"
    "ds_read_b64_tr_b16 v[196:197], v232 offset:43008\n"
    "v_mfma_f32_16x16x32_bf16 v[152:155], v[172:175], a[244:247], v[152:155]\n"
    "v_mfma_f32_16x16x32_bf16 v[156:159], v[168:171], a[248:251], v[156:159]\n"
    "ds_read_b64_tr_b16 v[198:199], v232 offset:44032\n"
    "v_mfma_f32_16x16x32_bf16 v[156:159], v[172:175], a[252:255], v[156:159]\n"
    "s_waitcnt lgkmcnt(8)\n"
    "v_mfma_f32_16x16x32_bf16 v[96:99], v[176:179], v[0:3], v[96:99]\n"
    "ds_read_b64_tr_b16 v[200:201], v232 offset:45056\n"
    "v_mfma_f32_16x16x32_bf16 v[96:99], v[180:183], v[4:7], v[96:99]\n"
    "v_mfma_f32_16x16x32_bf16 v[100:103], v[176:179], v[8:11], v[100:103]\n"
    "ds_read_b64_tr_b16 v[202:203], v232 offset:46080\n"
    "v_mfma_f32_16x16x32_bf16 v[100:103], v[180:183], v[12:15], v[100:103]\n"
    "v_mfma_f32_16x16x32_bf16 v[104:107], v[176:179], v[16:19], v[104:107]\n"
    "ds_read_b64_tr_b16 v[204:205], v232 offset:47104\n"
    "v_mfma_f32_16x16x32_bf16 v[104:107], v[180:183], v[20:23], v[104:107]\n"
    "v_mfma_f32_16x16x32_bf16 v[108:111], v[176:179], v[24:27], v[108:111]\n"
    "s_waitcnt vmcnt(8)\n"
    "ds_read_b64_tr_b16 v[206:207], v232 offset:48128\n"
    "v_mfma_f32_16x16x32_bf16 v[108:111], v[180:183], v[28:31], v[108:111]\n"
    "s_waitcnt lgkmcnt(8)\n"
    "s_barrier\n"
    "v_mfma_f32_16x16x32_bf16 v[112:115], v[184:187], v[0:3], v[112:115]\n"
    "ds_read_b64_tr_b16 v[160:161], v223 offset:0\n"
    "v_mfma_f32_16x16x32_bf16 v[112:115], v[188:191], v[4:7], v[112:115]\n"
    "v_mfma_f32_16x16x32_bf16 v[116:119], v[184:187], v[8:11], v[116:119]\n"
    "ds_read_b64_tr_b16 v[162:163], v223 offset:512\n"
    "v_mfma_f32_16x16x32_bf16 v[116:119], v[188:191], v[12:15], v[116:119]\n"
    "v_max_f32 v96, 0, v96\n"
    "v_max_f32 v97, 0, v97\n"
    "v_mfma_f32_16x16x32_bf16 v[120:123], v[184:187], v[16:19], v[120:123]\n"
    "ds_read_b64_tr_b16 v[164:165], v223 offset:1024\n"
    "v_max_f32 v98, 0, v98\n"
    "v_mfma_f32_16x16x32_bf16 v[120:123], v[188:191], v[20:23], v[120:123]\n"
    "v_max_f32 v99, 0, v99\n"
    "v_mul_f32 v96, v96, v112\n"
    "v_mfma_f32_16x16x32_bf16 v[124:127], v[184:187], v[24:27], v[124:127]\n"
    "ds_read_b64_tr_b16 v[166:167], v223 offset:1536\n"
    "v_mul_f32 v97, v97, v113\n"
    "v_mfma_f32_16x16x32_bf16 v[124:127], v[188:191], v[28:31], v[124:127]\n"
    "s_waitcnt lgkmcnt(8)\n"
    "v_mul_f32 v98, v98, v114\n"
    "v_mul_f32 v99, v99, v115\n"
    "v_mfma_f32_16x16x32_bf16 v[128:131], v[192:195], v[0:3], v[128:131]\n"
    "ds_read_b64_tr_b16 v[168:169], v223 offset:2048\n"
    "v_cvt_pk_bf16_f32 v96, v96, v97\n"
    "v_mfma_f32_16x16x32_bf16 v[128:131], v[196:199], v[4:7], v[128:131]\n"
    "v_cvt_pk_bf16_f32 v97, v98, v99\n"
    "v_max_f32 v100, 0, v100\n"
    "v_mfma_f32_16x16x32_bf16 v[132:135], v[192:195], v[8:11], v[132:135]\n"
    "ds_read_b64_tr_b16 v[170:171], v223 offset:2560\n"
    "v_max_f32 v101, 0, v101\n"
    "v_mfma_f32_16x16x32_bf16 v[132:135], v[196:199], v[12:15], v[132:135]\n"
    "v_max_f32 v102, 0, v102\n"
    "v_max_f32 v103, 0, v103\n"
    "v_mfma_f32_16x16x32_bf16 v[136:139], v[192:195], v[16:19], v[136:139]\n"
    "ds_read_b64_tr_b16 v[172:173], v223 offset:3072\n"
    "v_mul_f32 v100, v100, v116\n"
    "v_mfma_f32_16x16x32_bf16 v[136:139], v[196:199], v[20:23], v[136:139]\n"
    "v_mul_f32 v101, v101, v117\n"
    "v_mul_f32 v102, v102, v118\n"
    "v_mfma_f32_16x16x32_bf16 v[140:143], v[192:195], v[24:27], v[140:143]\n"
    "ds_read_b64_tr_b16 v[174:175], v223 offset:3584\n"
    "v_mul_f32 v103, v103, v119\n"
    "v_mfma_f32_16x16x32_bf16 v[140:143], v[196:199], v[28:31], v[140:143]\n"
    "s_waitcnt lgkmcnt(8)\n"
    "v_cvt_pk_bf16_f32 v100, v100, v101\n"
    "v_cvt_pk_bf16_f32 v101, v102, v103\n"
    "v_mfma_f32_16x16x32_bf16 v[144:147], v[200:203], v[0:3], v[144:147]\n"
    "ds_read_b64_tr_b16 v[176:177], v223 offset:4096\n"
    "v_max_f32 v104, 0, v104\n"
    "v_mfma_f32_16x16x32_bf16 v[144:147], v[204:207], v[4:7], v[144:147]\n"
    "v_max_f32 v105, 0, v105\n"
    "v_max_f32 v106, 0, v106\n"
    "v_mfma_f32_16x16x32_bf16 v[148:151], v[200:203], v[8:11], v[148:151]\n"
    "ds_read_b64_tr_b16 v[178:179], v223 offset:4608\n"
    "v_max_f32 v107, 0, v107\n"
    "v_mfma_f32_16x16x32_bf16 v[148:151], v[204:207], v[12:15], v[148:151]\n"
    "v_mul_f32 v104, v104, v120\n"
    "v_mul_f32 v105, v105, v121\n"
    "v_mfma_f32_16x16x32_bf16 v[152:155], v[200:203], v[16:19], v[152:155]\n"
    "ds_read_b64_tr_b16 v[180:181], v223 offset:5120\n"
    "v_mul_f32 v106, v106, v122\n"
    "v_mfma_f32_16x16x32_bf16 v[152:155], v[204:207], v[20:23], v[152:155]\n"
    "v_mul_f32 v107, v107, v123\n"
    "v_cvt_pk_bf16_f32 v104, v104, v105\n"
    "v_mfma_f32_16x16x32_bf16 v[156:159], v[200:203], v[24:27], v[156:159]\n"
    "ds_read_b64_tr_b16 v[182:183], v223 offset:5632\n"
    "v_cvt_pk_bf16_f32 v105, v106, v107\n"
    "v_mfma_f32_16x16x32_bf16 v[156:159], v[204:207], v[28:31], v[156:159]\n"
    "s_waitcnt lgkmcnt(8)\n"
    "v_max_f32 v108, 0, v108\n"
    "v_max_f32 v109, 0, v109\n"
    "v_mfma_f32_16x16x32_bf16 v[32:35], v[160:163], a[0:3], 0\n"
    "ds_read_b64_tr_b16 v[184:185], v223 offset:6144\n"
    "v_max_f32 v110, 0, v110\n"
    "v_mfma_f32_16x16x32_bf16 v[32:35], v[164:167], a[4:7], v[32:35]\n"
    "v_max_f32 v111, 0, v111\n"
    "v_mul_f32 v108, v108, v124\n"
    "v_mfma_f32_16x16x32_bf16 v[36:39], v[160:163], a[8:11], 0\n"
    "ds_read_b64_tr_b16 v[186:187], v223 offset:6656\n"
    "v_mul_f32 v109, v109, v125\n"
    "v_mfma_f32_16x16x32_bf16 v[36:39], v[164:167], a[12:15], v[36:39]\n"
    "v_mul_f32 v110, v110, v126\n"
    "v_mul_f32 v111, v111, v127\n"
    "v_mfma_f32_16x16x32_bf16 v[40:43], v[160:163], a[16:19], 0\n"
    "ds_read_b64_tr_b16 v[188:189], v223 offset:7168\n"
    "v_cvt_pk_bf16_f32 v108, v108, v109\n"
    "v_mfma_f32_16x16x32_bf16 v[40:43], v[164:167], a[20:23], v[40:43]\n"
    "v_cvt_pk_bf16_f32 v109, v110, v111\n"
    "v_max_f32 v128, 0, v128\n"
    "v_mfma_f32_16x16x32_bf16 v[44:47], v[160:163], a[24:27], 0\n"
    "ds_read_b64_tr_b16 v[190:191], v223 offset:7680\n"
    "v_max_f32 v129, 0, v129\n"
    "v_mfma_f32_16x16x32_bf16 v[44:47], v[164:167], a[28:31], v[44:47]\n"
    "s_waitcnt lgkmcnt(8)\n"
    "v_max_f32 v130, 0, v130\n"
    "v_max_f32 v131, 0, v131\n"
    "v_mfma_f32_16x16x32_bf16 v[48:51], v[168:171], a[0:3], 0\n"
    "ds_read_b64_tr_b16 v[192:193], v223 offset:8192\n"
    "v_mul_f32 v128, v128, v144\n"
    "v_mfma_f32_16x16x32_bf16 v[48:51], v[172:175], a[4:7], v[48:51]\n"
    "s_barrier\n"
    "v_mul_f32 v129, v129, v145\n"
    "v_mul_f32 v130, v130, v146\n"
    "v_mfma_f32_16x16x32_bf16 v[52:55], v[168:171], a[8:11], 0\n"
    "s_add_u32 m0, s60, 69632\n"
    "ds_read_b64_tr_b16 v[194:195], v223 offset:8704\n"
    "v_mul_f32 v131, v131, v147\n"
    "v_mfma_f32_16x16x32_bf16 v[52:55], v[172:175], a[12:15], v[52:55]\n"
    "s_add_u32 s63, s62, 2359424\n"
    "v_cvt_pk_bf16_f32 v98, v128, v129\n"
    "v_cvt_pk_bf16_f32 v99, v130, v131\n"
    "v_mfma_f32_16x16x32_bf16 v[56:59], v[168:171], a[16:19], 0\n"
    "ds_read_b64_tr_b16 v[196:197], v223 offset:9216\n"
    "v_max_f32 v132, 0, v132\n"
    "v_mfma_f32_16x16x32_bf16 v[56:59], v[172:175], a[20:23], v[56:59]\n"
    "buffer_store_dwordx4 v[96:99], v217, s[48:51], s64 offen offset:1024 sc1\n"
    "v_max_f32 v133, 0, v133\n"
    "v_max_f32 v134, 0, v134\n"
    "v_mfma_f32_16x16x32_bf16 v[60:63], v[168:171], a[24:27], 0\n"
    "ds_read_b64_tr_b16 v[198:199], v223 offset:9728\n"
    "v_max_f32 v135, 0, v135\n"
    "v_mfma_f32_16x16x32_bf16 v[60:63], v[172:175], a[28:31], v[60:63]\n"
    "s_waitcnt lgkmcnt(8)\n"
    "v_mul_f32 v132, v132, v148\n"
    "v_mul_f32 v133, v133, v149\n"
    "v_mfma_f32_16x16x32_bf16 v[32:35], v[176:179], a[32:35], v[32:35]\n"
    "ds_read_b64_tr_b16 v[200:201], v223 offset:10240\n"
    "v_mul_f32 v134, v134, v150\n"
    "v_mfma_f32_16x16x32_bf16 v[32:35], v[180:183], a[36:39], v[32:35]\n"
    "v_mul_f32 v135, v135, v151\n"
    "v_cvt_pk_bf16_f32 v102, v132, v133\n"
    "v_mfma_f32_16x16x32_bf16 v[36:39], v[176:179], a[40:43], v[36:39]\n"
    "ds_read_b64_tr_b16 v[202:203], v223 offset:10752\n"
    "v_cvt_pk_bf16_f32 v103, v134, v135\n"
    "v_mfma_f32_16x16x32_bf16 v[36:39], v[180:183], a[44:47], v[36:39]\n"
    "v_max_f32 v136, 0, v136\n"
    "v_max_f32 v137, 0, v137\n"
    "v_mfma_f32_16x16x32_bf16 v[40:43], v[176:179], a[48:51], v[40:43]\n"
    "ds_read_b64_tr_b16 v[204:205], v223 offset:11264\n"
    "v_max_f32 v138, 0, v138\n"
    "v_mfma_f32_16x16x32_bf16 v[40:43], v[180:183], a[52:55], v[40:43]\n"
    "buffer_store_dwordx4 v[100:103], v218, s[48:51], s64 offen offset:1024 sc1\n"
    "v_max_f32 v139, 0, v139\n"
    "v_mul_f32 v136, v136, v152\n"
    "v_mfma_f32_16x16x32_bf16 v[44:47], v[176:179], a[56:59], v[44:47]\n"
    "ds_read_b64_tr_b16 v[206:207], v223 offset:11776\n"
    "v_mul_f32 v137, v137, v153\n"
    "v_mfma_f32_16x16x32_bf16 v[44:47], v[180:183], a[60:63], v[44:47]\n"
    "s_waitcnt lgkmcnt(8)\n"
    "v_mul_f32 v138, v138, v154\n"
    "v_mul_f32 v139, v139, v155\n"
    "v_mfma_f32_16x16x32_bf16 v[48:51], v[184:187], a[32:35], v[48:51]\n"
    "ds_read_b64_tr_b16 v[160:161], v223 offset:12288\n"
    "v_cvt_pk_bf16_f32 v106, v136, v137\n"
    "v_mfma_f32_16x16x32_bf16 v[48:51], v[188:191], a[36:39], v[48:51]\n"
    "v_cvt_pk_bf16_f32 v107, v138, v139\n"
    "v_max_f32 v140, 0, v140\n"
    "v_mfma_f32_16x16x32_bf16 v[52:55], v[184:187], a[40:43], v[52:55]\n"
    "ds_read_b64_tr_b16 v[162:163], v223 offset:12800\n"
    "v_max_f32 v141, 0, v141\n"
    "v_mfma_f32_16x16x32_bf16 v[52:55], v[188:191], a[44:47], v[52:55]\n"
    "v_max_f32 v142, 0, v142\n"
    "v_max_f32 v143, 0, v143\n"
    "v_mfma_f32_16x16x32_bf16 v[56:59], v[184:187], a[48:51], v[56:59]\n"
    "ds_read_b64_tr_b16 v[164:165], v223 offset:13312\n"
    "v_mul_f32 v140, v140, v156\n"
    "v_mfma_f32_16x16x32_bf16 v[56:59], v[188:191], a[52:55], v[56:59]\n"
    "buffer_store_dwordx4 v[104:107], v219, s[48:51], s64 offen offset:1024 sc1\n"
    "v_mul_f32 v141, v141, v157\n"
    "v_mul_f32 v142, v142, v158\n"
    "v_mfma_f32_16x16x32_bf16 v[60:63], v[184:187], a[56:59], v[60:63]\n"
    "ds_read_b64_tr_b16 v[166:167], v223 offset:13824\n"
    "v_mul_f32 v143, v143, v159\n"
    "v_mfma_f32_16x16x32_bf16 v[60:63], v[188:191], a[60:63], v[60:63]\n"
    "s_waitcnt lgkmcnt(8)\n"
    "v_cvt_pk_bf16_f32 v110, v140, v141\n"
    "v_cvt_pk_bf16_f32 v111, v142, v143\n"
    "v_mfma_f32_16x16x32_bf16 v[32:35], v[192:195], a[64:67], v[32:35]\n"
    "ds_read_b64_tr_b16 v[168:169], v223 offset:14336\n"
    "v_mfma_f32_16x16x32_bf16 v[32:35], v[196:199], a[68:71], v[32:35]\n"
    "v_mfma_f32_16x16x32_bf16 v[36:39], v[192:195], a[72:75], v[36:39]\n"
    "ds_read_b64_tr_b16 v[170:171], v223 offset:14848\n"
    "v_mfma_f32_16x16x32_bf16 v[36:39], v[196:199], a[76:79], v[36:39]\n"
    "v_mfma_f32_16x16x32_bf16 v[40:43], v[192:195], a[80:83], v[40:43]\n"
    "ds_read_b64_tr_b16 v[172:173], v223 offset:15360\n"
    "v_mfma_f32_16x16x32_bf16 v[40:43], v[196:199], a[84:87], v[40:43]\n"
    "buffer_store_dwordx4 v[108:111], v220, s[48:51], s64 offen offset:1024 sc1\n"
    "v_mfma_f32_16x16x32_bf16 v[44:47], v[192:195], a[88:91], v[44:47]\n"
    "ds_read_b64_tr_b16 v[174:175], v223 offset:15872\n"
    "v_mfma_f32_16x16x32_bf16 v[44:47], v[196:199], a[92:95], v[44:47]\n"
    "s_waitcnt lgkmcnt(8)\n"
    "v_mfma_f32_16x16x32_bf16 v[48:51], v[200:203], a[64:67], v[48:51]\n"
    "ds_read_b64_tr_b16 v[176:177], v223 offset:16384\n"
    "v_mfma_f32_16x16x32_bf16 v[48:51], v[204:207], a[68:71], v[48:51]\n"
    "v_mfma_f32_16x16x32_bf16 v[52:55], v[200:203], a[72:75], v[52:55]\n"
    "ds_read_b64_tr_b16 v[178:179], v223 offset:16896\n"
    "v_mfma_f32_16x16x32_bf16 v[52:55], v[204:207], a[76:79], v[52:55]\n"
    "v_mfma_f32_16x16x32_bf16 v[56:59], v[200:203], a[80:83], v[56:59]\n"
    "ds_read_b64_tr_b16 v[180:181], v223 offset:17408\n"
    "v_mfma_f32_16x16x32_bf16 v[56:59], v[204:207], a[84:87], v[56:59]\n"
    "buffer_load_dwordx4 v208, s[44:47], s63 offen lds\n"
    "v_mfma_f32_16x16x32_bf16 v[60:63], v[200:203], a[88:91], v[60:63]\n"
    "ds_read_b64_tr_b16 v[182:183], v223 offset:17920\n"
    "v_mfma_f32_16x16x32_bf16 v[60:63], v[204:207], a[92:95], v[60:63]\n"
    "s_waitcnt lgkmcnt(8)\n"
    "v_mfma_f32_16x16x32_bf16 v[32:35], v[160:163], a[96:99], v[32:35]\n"
    "ds_read_b64_tr_b16 v[184:185], v223 offset:18432\n"
    "v_mfma_f32_16x16x32_bf16 v[32:35], v[164:167], a[100:103], v[32:35]\n"
    "v_mfma_f32_16x16x32_bf16 v[36:39], v[160:163], a[104:107], v[36:39]\n"
    "ds_read_b64_tr_b16 v[186:187], v223 offset:18944\n"
    "v_mfma_f32_16x16x32_bf16 v[36:39], v[164:167], a[108:111], v[36:39]\n"
    "v_mfma_f32_16x16x32_bf16 v[40:43], v[160:163], a[112:115], v[40:43]\n"
    "ds_read_b64_tr_b16 v[188:189], v223 offset:19456\n"
    "v_mfma_f32_16x16x32_bf16 v[40:43], v[164:167], a[116:119], v[40:43]\n"
    "v_mfma_f32_16x16x32_bf16 v[44:47], v[160:163], a[120:123], v[44:47]\n"
    "ds_read_b64_tr_b16 v[190:191], v223 offset:19968\n"
    "v_mfma_f32_16x16x32_bf16 v[44:47], v[164:167], a[124:127], v[44:47]\n"
    "s_waitcnt lgkmcnt(8)\n"
    "buffer_load_dwordx4 v209, s[44:47], s63 offen offset:1024 lds\n"
    "v_mfma_f32_16x16x32_bf16 v[48:51], v[168:171], a[96:99], v[48:51]\n"
    "s_add_u32 m0, s60, 77824\n"
    "ds_read_b64_tr_b16 v[192:193], v223 offset:20480\n"
    "v_mfma_f32_16x16x32_bf16 v[48:51], v[172:175], a[100:103], v[48:51]\n"
    "s_add_u32 s63, s62, 2949248\n"
    "v_mfma_f32_16x16x32_bf16 v[52:55], v[168:171], a[104:107], v[52:55]\n"
    "ds_read_b64_tr_b16 v[194:195], v223 offset:20992\n"
    "v_mfma_f32_16x16x32_bf16 v[52:55], v[172:175], a[108:111], v[52:55]\n"
    "v_mfma_f32_16x16x32_bf16 v[56:59], v[168:171], a[112:115], v[56:59]\n"
    "ds_read_b64_tr_b16 v[196:197], v223 offset:21504\n"
    "v_mfma_f32_16x16x32_bf16 v[56:59], v[172:175], a[116:119], v[56:59]\n"
    "v_mfma_f32_16x16x32_bf16 v[60:63], v[168:171], a[120:123], v[60:63]\n"
    "ds_read_b64_tr_b16 v[198:199], v223 offset:22016\n"
    "v_mfma_f32_16x16x32_bf16 v[60:63], v[172:175], a[124:127], v[60:63]\n"
    "s_waitcnt lgkmcnt(8)\n"
    "v_mfma_f32_16x16x32_bf16 v[32:35], v[176:179], a[128:131], v[32:35]\n"
    "ds_read_b64_tr_b16 v[200:201], v223 offset:22528\n"
    "v_mfma_f32_16x16x32_bf16 v[32:35], v[180:183], a[132:135], v[32:35]\n"
    "buffer_load_dwordx4 v208, s[44:47], s63 offen lds\n"
    "v_mfma_f32_16x16x32_bf16 v[36:39], v[176:179], a[136:139], v[36:39]\n"
    "ds_read_b64_tr_b16 v[202:203], v223 offset:23040\n"
    "v_mfma_f32_16x16x32_bf16 v[36:39], v[180:183], a[140:143], v[36:39]\n"
    "v_mfma_f32_16x16x32_bf16 v[40:43], v[176:179], a[144:147], v[40:43]\n"
    "ds_read_b64_tr_b16 v[204:205], v223 offset:23552\n"
    "v_mfma_f32_16x16x32_bf16 v[40:43], v[180:183], a[148:151], v[40:43]\n"
    "v_mfma_f32_16x16x32_bf16 v[44:47], v[176:179], a[152:155], v[44:47]\n"
    "ds_read_b64_tr_b16 v[206:207], v223 offset:24064\n"
    "v_mfma_f32_16x16x32_bf16 v[44:47], v[180:183], a[156:159], v[44:47]\n"
    "s_waitcnt lgkmcnt(8)\n"
    "v_mfma_f32_16x16x32_bf16 v[48:51], v[184:187], a[128:131], v[48:51]\n"
    "ds_read_b64_tr_b16 v[160:161], v223 offset:24576\n"
    "v_mfma_f32_16x16x32_bf16 v[48:51], v[188:191], a[132:135], v[48:51]\n"
    "v_mfma_f32_16x16x32_bf16 v[52:55], v[184:187], a[136:139], v[52:55]\n"
    "ds_read_b64_tr_b16 v[162:163], v223 offset:25088\n"
    "v_mfma_f32_16x16x32_bf16 v[52:55], v[188:191], a[140:143], v[52:55]\n"
    "buffer_load_dwordx4 v209, s[44:47], s63 offen offset:1024 lds\n"
    "v_mfma_f32_16x16x32_bf16 v[56:59], v[184:187], a[144:147], v[56:59]\n"
    "s_add_u32 m0, s60, 86016\n"
    "ds_read_b64_tr_b16 v[164:165], v223 offset:25600\n"
    "v_mfma_f32_16x16x32_bf16 v[56:59], v[188:191], a[148:151], v[56:59]\n"
    "s_add_u32 s63, s62, 3539072\n"
    "v_mfma_f32_16x16x32_bf16 v[60:63], v[184:187], a[152:155], v[60:63]\n"
    "ds_read_b64_tr_b16 v[166:167], v223 offset:26112\n"
    "v_mfma_f32_16x16x32_bf16 v[60:63], v[188:191], a[156:159], v[60:63]\n"
    "s_waitcnt lgkmcnt(8)\n"
    "v_mfma_f32_16x16x32_bf16 v[32:35], v[192:195], a[160:163], v[32:35]\n"
    "ds_read_b64_tr_b16 v[168:169], v223 offset:26624\n"
    "v_mfma_f32_16x16x32_bf16 v[32:35], v[196:199], a[164:167], v[32:35]\n"
    "v_mfma_f32_16x16x32_bf16 v[36:39], v[192:195], a[168:171], v[36:39]\n"
    "ds_read_b64_tr_b16 v[170:171], v223 offset:27136\n"
    "v_mfma_f32_16x16x32_bf16 v[36:39], v[196:199], a[172:175], v[36:39]\n"
    "v_mfma_f32_16x16x32_bf16 v[40:43], v[192:195], a[176:179], v[40:43]\n"
    "ds_read_b64_tr_b16 v[172:173], v223 offset:27648\n"
    "v_mfma_f32_16x16x32_bf16 v[40:43], v[196:199], a[180:183], v[40:43]\n"
    "buffer_load_dwordx4 v208, s[44:47], s63 offen lds\n"
    "v_mfma_f32_16x16x32_bf16 v[44:47], v[192:195], a[184:187], v[44:47]\n"
    "ds_read_b64_tr_b16 v[174:175], v223 offset:28160\n"
    "v_mfma_f32_16x16x32_bf16 v[44:47], v[196:199], a[188:191], v[44:47]\n"
    "s_waitcnt lgkmcnt(8)\n"
    "v_mfma_f32_16x16x32_bf16 v[48:51], v[200:203], a[160:163], v[48:51]\n"
    "ds_read_b64_tr_b16 v[176:177], v223 offset:28672\n"
    "v_mfma_f32_16x16x32_bf16 v[48:51], v[204:207], a[164:167], v[48:51]\n"
    "v_mfma_f32_16x16x32_bf16 v[52:55], v[200:203], a[168:171], v[52:55]\n"
    "ds_read_b64_tr_b16 v[178:179], v223 offset:29184\n"
    "v_mfma_f32_16x16x32_bf16 v[52:55], v[204:207], a[172:175], v[52:55]\n"
    "v_mfma_f32_16x16x32_bf16 v[56:59], v[200:203], a[176:179], v[56:59]\n"
    "ds_read_b64_tr_b16 v[180:181], v223 offset:29696\n"
    "v_mfma_f32_16x16x32_bf16 v[56:59], v[204:207], a[180:183], v[56:59]\n"
    "v_mfma_f32_16x16x32_bf16 v[60:63], v[200:203], a[184:187], v[60:63]\n"
    "ds_read_b64_tr_b16 v[182:183], v223 offset:30208\n"
    "v_mfma_f32_16x16x32_bf16 v[60:63], v[204:207], a[188:191], v[60:63]\n"
    "s_waitcnt lgkmcnt(8)\n"
    "buffer_load_dwordx4 v209, s[44:47], s63 offen offset:1024 lds\n"
    "v_mfma_f32_16x16x32_bf16 v[32:35], v[160:163], a[192:195], v[32:35]\n"
    "s_add_u32 m0, s60, 94208\n"
    "ds_read_b64_tr_b16 v[184:185], v223 offset:30720\n"
    "v_mfma_f32_16x16x32_bf16 v[32:35], v[164:167], a[196:199], v[32:35]\n"
    "s_add_u32 s63, s62, 4128896\n"
    "v_mfma_f32_16x16x32_bf16 v[36:39], v[160:163], a[200:203], v[36:39]\n"
    "ds_read_b64_tr_b16 v[186:187], v223 offset:31232\n"
    "v_mfma_f32_16x16x32_bf16 v[36:39], v[164:167], a[204:207], v[36:39]\n"
    "v_mfma_f32_16x16x32_bf16 v[40:43], v[160:163], a[208:211], v[40:43]\n"
    "ds_read_b64_tr_b16 v[188:189], v223 offset:31744\n"
    "v_mfma_f32_16x16x32_bf16 v[40:43], v[164:167], a[212:215], v[40:43]\n"
    "v_mfma_f32_16x16x32_bf16 v[44:47], v[160:163], a[216:219], v[44:47]\n"
    "ds_read_b64_tr_b16 v[190:191], v223 offset:32256\n"
    "v_mfma_f32_16x16x32_bf16 v[44:47], v[164:167], a[220:223], v[44:47]\n"
    "s_waitcnt lgkmcnt(8)\n"
    "v_mfma_f32_16x16x32_bf16 v[48:51], v[168:171], a[192:195], v[48:51]\n"
    "ds_read_b64_tr_b16 v[192:193], v223 offset:32768\n"
    "v_mfma_f32_16x16x32_bf16 v[48:51], v[172:175], a[196:199], v[48:51]\n"
    "buffer_load_dwordx4 v208, s[44:47], s63 offen lds\n"
    "v_mfma_f32_16x16x32_bf16 v[52:55], v[168:171], a[200:203], v[52:55]\n"
    "ds_read_b64_tr_b16 v[194:195], v223 offset:33280\n"
    "v_mfma_f32_16x16x32_bf16 v[52:55], v[172:175], a[204:207], v[52:55]\n"
    "v_mfma_f32_16x16x32_bf16 v[56:59], v[168:171], a[208:211], v[56:59]\n"
    "ds_read_b64_tr_b16 v[196:197], v223 offset:33792\n"
    "v_mfma_f32_16x16x32_bf16 v[56:59], v[172:175], a[212:215], v[56:59]\n"
    "v_mfma_f32_16x16x32_bf16 v[60:63], v[168:171], a[216:219], v[60:63]\n"
    "ds_read_b64_tr_b16 v[198:199], v223 offset:34304\n"
    "v_mfma_f32_16x16x32_bf16 v[60:63], v[172:175], a[220:223], v[60:63]\n"
    "s_waitcnt lgkmcnt(8)\n"
    "v_mfma_f32_16x16x32_bf16 v[32:35], v[176:179], a[224:227], v[32:35]\n"
    "ds_read_b64_tr_b16 v[200:201], v223 offset:34816\n"
    "v_mfma_f32_16x16x32_bf16 v[32:35], v[180:183], a[228:231], v[32:35]\n"
    "v_mfma_f32_16x16x32_bf16 v[36:39], v[176:179], a[232:235], v[36:39]\n"
    "ds_read_b64_tr_b16 v[202:203], v223 offset:35328\n"
    "v_mfma_f32_16x16x32_bf16 v[36:39], v[180:183], a[236:239], v[36:39]\n"
    "buffer_load_dwordx4 v209, s[44:47], s63 offen offset:1024 lds\n"
    "v_mfma_f32_16x16x32_bf16 v[40:43], v[176:179], a[240:243], v[40:43]\n"
    "s_add_u32 m0, s60, 102400\n"
    "ds_read_b64_tr_b16 v[204:205], v223 offset:35840\n"
    "v_mfma_f32_16x16x32_bf16 v[40:43], v[180:183], a[244:247], v[40:43]\n"
    "s_add_u32 s63, s62, 4718720\n"
    "v_mfma_f32_16x16x32_bf16 v[44:47], v[176:179], a[248:251], v[44:47]\n"
    "s_waitcnt vmcnt(12)\n"
    "ds_read_b64_tr_b16 v[206:207], v223 offset:36352\n"
    "v_mfma_f32_16x16x32_bf16 v[44:47], v[180:183], a[252:255], v[44:47]\n"
    "s_waitcnt lgkmcnt(8)\n"
    "s_barrier\n"
    "v_mfma_f32_16x16x32_bf16 v[48:51], v[184:187], a[224:227], v[48:51]\n"
    "ds_read_b64_tr_b16 v[160:161], v227 offset:36864\n"
    "v_mfma_f32_16x16x32_bf16 v[48:51], v[188:191], a[228:231], v[48:51]\n"
    "v_mfma_f32_16x16x32_bf16 v[52:55], v[184:187], a[232:235], v[52:55]\n"
    "ds_read_b64_tr_b16 v[162:163], v227 offset:37888\n"
    "v_mfma_f32_16x16x32_bf16 v[52:55], v[188:191], a[236:239], v[52:55]\n"
    "v_mfma_f32_16x16x32_bf16 v[56:59], v[184:187], a[240:243], v[56:59]\n"
    "ds_read_b64_tr_b16 v[164:165], v227 offset:38912\n"
    "v_mfma_f32_16x16x32_bf16 v[56:59], v[188:191], a[244:247], v[56:59]\n"
    "buffer_load_dwordx4 v208, s[44:47], s63 offen lds\n"
    "v_mfma_f32_16x16x32_bf16 v[60:63], v[184:187], a[248:251], v[60:63]\n"
    "ds_read_b64_tr_b16 v[166:167], v227 offset:39936\n"
    "v_mfma_f32_16x16x32_bf16 v[60:63], v[188:191], a[252:255], v[60:63]\n"
    "s_waitcnt lgkmcnt(8)\n"
    "v_mfma_f32_16x16x32_bf16 v[32:35], v[192:195], v[0:3], v[32:35]\n"
    "ds_read_b64_tr_b16 v[168:169], v227 offset:40960\n"
    "v_mfma_f32_16x16x32_bf16 v[32:35], v[196:199], v[4:7], v[32:35]\n"
    "v_mfma_f32_16x16x32_bf16 v[36:39], v[192:195], v[8:11], v[36:39]\n"
    "ds_read_b64_tr_b16 v[170:171], v227 offset:41984\n"
    "v_mfma_f32_16x16x32_bf16 v[36:39], v[196:199], v[12:15], v[36:39]\n"
    "v_mfma_f32_16x16x32_bf16 v[40:43], v[192:195], v[16:19], v[40:43]\n"
    "ds_read_b64_tr_b16 v[172:173], v227 offset:43008\n"
    "v_mfma_f32_16x16x32_bf16 v[40:43], v[196:199], v[20:23], v[40:43]\n"
    "v_mfma_f32_16x16x32_bf16 v[44:47], v[192:195], v[24:27], v[44:47]\n"
    "ds_read_b64_tr_b16 v[174:175], v227 offset:44032\n"
    "v_mfma_f32_16x16x32_bf16 v[44:47], v[196:199], v[28:31], v[44:47]\n"
    "s_waitcnt lgkmcnt(8)\n"
    "buffer_load_dwordx4 v209, s[44:47], s63 offen offset:1024 lds\n"
    "v_mfma_f32_16x16x32_bf16 v[48:51], v[200:203], v[0:3], v[48:51]\n"
    "s_add_u32 m0, s60, 110592\n"
    "ds_read_b64_tr_b16 v[176:177], v230 offset:36864\n"
    "v_mfma_f32_16x16x32_bf16 v[48:51], v[204:207], v[4:7], v[48:51]\n"
    "s_add_u32 s63, s62, 192\n"
    "v_mfma_f32_16x16x32_bf16 v[52:55], v[200:203], v[8:11], v[52:55]\n"
    "ds_read_b64_tr_b16 v[178:179], v230 offset:37888\n"
    "v_mfma_f32_16x16x32_bf16 v[52:55], v[204:207], v[12:15], v[52:55]\n"
    "v_max_f32 v32, 0, v32\n"
    "v_max_f32 v33, 0, v33\n"
    "v_mfma_f32_16x16x32_bf16 v[56:59], v[200:203], v[16:19], v[56:59]\n"
    "ds_read_b64_tr_b16 v[180:181], v230 offset:38912\n"
    "v_max_f32 v34, 0, v34\n"
    "v_mfma_f32_16x16x32_bf16 v[56:59], v[204:207], v[20:23], v[56:59]\n"
    "v_max_f32 v35, 0, v35\n"
    "v_mul_f32 v32, v32, v48\n"
    "v_mfma_f32_16x16x32_bf16 v[60:63], v[200:203], v[24:27], v[60:63]\n"
    "ds_read_b64_tr_b16 v[182:183], v230 offset:39936\n"
    "v_mul_f32 v33, v33, v49\n"
    "v_mfma_f32_16x16x32_bf16 v[60:63], v[204:207], v[28:31], v[60:63]\n"
    "s_waitcnt lgkmcnt(8)\n"
    "v_mul_f32 v34, v34, v50\n"
    "v_mul_f32 v35, v35, v51\n"
    "v_mfma_f32_16x16x32_bf16 v[96:99], v[160:163], a[0:3], 0\n"
    "ds_read_b64_tr_b16 v[184:185], v230 offset:40960\n"
    "v_cvt_pk_bf16_f32 v32, v32, v33\n"
    "v_mfma_f32_16x16x32_bf16 v[96:99], v[164:167], a[4:7], v[96:99]\n"
    "v_cvt_pk_bf16_f32 v33, v34, v35\n"
    "v_max_f32 v36, 0, v36\n"
    "v_mfma_f32_16x16x32_bf16 v[100:103], v[160:163], a[8:11], 0\n"
    "ds_read_b64_tr_b16 v[186:187], v230 offset:41984\n"
    "v_max_f32 v37, 0, v37\n"
    "v_mfma_f32_16x16x32_bf16 v[100:103], v[164:167], a[12:15], v[100:103]\n"
    "buffer_store_dwordx2 v[32:33], v217, s[48:51], s65 offen sc1\n"
    "v_max_f32 v38, 0, v38\n"
    "v_max_f32 v39, 0, v39\n"
    "v_mfma_f32_16x16x32_bf16 v[104:107], v[160:163], a[16:19], 0\n"
    "ds_read_b64_tr_b16 v[188:189], v230 offset:43008\n"
    "v_mul_f32 v36, v36, v52\n"
    "v_mfma_f32_16x16x32_bf16 v[104:107], v[164:167], a[20:23], v[104:107]\n"
    "v_mul_f32 v37, v37, v53\n"
    "v_mul_f32 v38, v38, v54\n"
    "v_mfma_f32_16x16x32_bf16 v[108:111], v[160:163], a[24:27], 0\n"
    "ds_read_b64_tr_b16 v[190:191], v230 offset:44032\n"
    "v_mul_f32 v39, v39, v55\n"
    "v_mfma_f32_16x16x32_bf16 v[108:111], v[164:167], a[28:31], v[108:111]\n"
    "s_waitcnt lgkmcnt(8)\n"
    "v_cvt_pk_bf16_f32 v36, v36, v37\n"
    "v_cvt_pk_bf16_f32 v37, v38, v39\n"
    "v_mfma_f32_16x16x32_bf16 v[112:115], v[168:171], a[0:3], 0\n"
    "ds_read_b64_tr_b16 v[192:193], v227 offset:45056\n"
    "v_max_f32 v40, 0, v40\n"
    "v_mfma_f32_16x16x32_bf16 v[112:115], v[172:175], a[4:7], v[112:115]\n"
    "v_max_f32 v41, 0, v41\n"
    "v_max_f32 v42, 0, v42\n"
    "v_mfma_f32_16x16x32_bf16 v[116:119], v[168:171], a[8:11], 0\n"
    "ds_read_b64_tr_b16 v[194:195], v227 offset:46080\n"
    "v_max_f32 v43, 0, v43\n"
    "v_mfma_f32_16x16x32_bf16 v[116:119], v[172:175], a[12:15], v[116:119]\n"
    "v_mul_f32 v40, v40, v56\n"
    "v_mul_f32 v41, v41, v57\n"
    "v_mfma_f32_16x16x32_bf16 v[120:123], v[168:171], a[16:19], 0\n"
    "ds_read_b64_tr_b16 v[196:197], v227 offset:47104\n"
    "v_mul_f32 v42, v42, v58\n"
    "v_mfma_f32_16x16x32_bf16 v[120:123], v[172:175], a[20:23], v[120:123]\n"
    "v_mul_f32 v43, v43, v59\n"
    "v_cvt_pk_bf16_f32 v40, v40, v41\n"
    "v_mfma_f32_16x16x32_bf16 v[124:127], v[168:171], a[24:27], 0\n"
    "ds_read_b64_tr_b16 v[198:199], v227 offset:48128\n"
    "v_cvt_pk_bf16_f32 v41, v42, v43\n"
    "v_mfma_f32_16x16x32_bf16 v[124:127], v[172:175], a[28:31], v[124:127]\n"
    "s_waitcnt lgkmcnt(8)\n"
    "buffer_store_dwordx2 v[36:37], v218, s[48:51], s65 offen sc1\n"
    "v_max_f32 v44, 0, v44\n"
    "v_max_f32 v45, 0, v45\n"
    "v_mfma_f32_16x16x32_bf16 v[128:131], v[176:179], a[0:3], 0\n"
    "ds_read_b64_tr_b16 v[200:201], v228 offset:0\n"
    "v_max_f32 v46, 0, v46\n"
    "v_mfma_f32_16x16x32_bf16 v[128:131], v[180:183], a[4:7], v[128:131]\n"
    "v_max_f32 v47, 0, v47\n"
    "v_mul_f32 v44, v44, v60\n"
    "v_mfma_f32_16x16x32_bf16 v[132:135], v[176:179], a[8:11], 0\n"
    "ds_read_b64_tr_b16 v[202:203], v228 offset:1024\n"
    "v_mul_f32 v45, v45, v61\n"
    "v_mfma_f32_16x16x32_bf16 v[132:135], v[180:183], a[12:15], v[132:135]\n"
    "v_mul_f32 v46, v46, v62\n"
    "v_mul_f32 v47, v47, v63\n"
    "v_mfma_f32_16x16x32_bf16 v[136:139], v[176:179], a[16:19], 0\n"
    "ds_read_b64_tr_b16 v[204:205], v228 offset:2048\n"
    "v_cvt_pk_bf16_f32 v44, v44, v45\n"
    "v_mfma_f32_16x16x32_bf16 v[136:139], v[180:183], a[20:23], v[136:139]\n"
    "v_cvt_pk_bf16_f32 v45, v46, v47\n"
    "v_mfma_f32_16x16x32_bf16 v[140:143], v[176:179], a[24:27], 0\n"
    "ds_read_b64_tr_b16 v[206:207], v228 offset:3072\n"
    "v_mfma_f32_16x16x32_bf16 v[140:143], v[180:183], a[28:31], v[140:143]\n"
    "s_waitcnt lgkmcnt(8)\n"
    "v_mfma_f32_16x16x32_bf16 v[144:147], v[184:187], a[0:3], 0\n"
    "ds_read_b64_tr_b16 v[160:161], v230 offset:45056\n"
    "v_mfma_f32_16x16x32_bf16 v[144:147], v[188:191], a[4:7], v[144:147]\n"
    "v_mfma_f32_16x16x32_bf16 v[148:151], v[184:187], a[8:11], 0\n"
    "ds_read_b64_tr_b16 v[162:163], v230 offset:46080\n"
    "v_mfma_f32_16x16x32_bf16 v[148:151], v[188:191], a[12:15], v[148:151]\n"
    "buffer_store_dwordx2 v[40:41], v219, s[48:51], s65 offen sc1\n"
    "v_mfma_f32_16x16x32_bf16 v[152:155], v[184:187], a[16:19], 0\n"
    "ds_read_b64_tr_b16 v[164:165], v230 offset:47104\n"
    "v_mfma_f32_16x16x32_bf16 v[152:155], v[188:191], a[20:23], v[152:155]\n"
    "v_mfma_f32_16x16x32_bf16 v[156:159], v[184:187], a[24:27], 0\n"
    "ds_read_b64_tr_b16 v[166:167], v230 offset:48128\n"
    "v_mfma_f32_16x16x32_bf16 v[156:159], v[188:191], a[28:31], v[156:159]\n"
    "s_waitcnt lgkmcnt(8)\n"
    "v_mfma_f32_16x16x32_bf16 v[96:99], v[192:195], a[32:35], v[96:99]\n"
    "ds_read_b64_tr_b16 v[168:169], v231 offset:0\n"
    "v_mfma_f32_16x16x32_bf16 v[96:99], v[196:199], a[36:39], v[96:99]\n"
    "v_mfma_f32_16x16x32_bf16 v[100:103], v[192:195], a[40:43], v[100:103]\n"
    "ds_read_b64_tr_b16 v[170:171], v231 offset:1024\n"
    "v_mfma_f32_16x16x32_bf16 v[100:103], v[196:199], a[44:47], v[100:103]\n"
    "v_mfma_f32_16x16x32_bf16 v[104:107], v[192:195], a[48:51], v[104:107]\n"
    "ds_read_b64_tr_b16 v[172:173], v231 offset:2048\n"
    "v_mfma_f32_16x16x32_bf16 v[104:107], v[196:199], a[52:55], v[104:107]\n"
    "v_mfma_f32_16x16x32_bf16 v[108:111], v[192:195], a[56:59], v[108:111]\n"
    "ds_read_b64_tr_b16 v[174:175], v231 offset:3072\n"
    "v_mfma_f32_16x16x32_bf16 v[108:111], v[196:199], a[60:63], v[108:111]\n"
    "s_waitcnt lgkmcnt(8)\n"
    "buffer_store_dwordx2 v[44:45], v220, s[48:51], s65 offen sc1\n"
    "v_mfma_f32_16x16x32_bf16 v[112:115], v[200:203], a[32:35], v[112:115]\n"
    "ds_read_b64_tr_b16 v[176:177], v228 offset:4096\n"
    "v_mfma_f32_16x16x32_bf16 v[112:115], v[204:207], a[36:39], v[112:115]\n"
    "s_barrier\n"
    "v_mfma_f32_16x16x32_bf16 v[116:119], v[200:203], a[40:43], v[116:119]\n"
    "ds_read_b64_tr_b16 v[178:179], v228 offset:5120\n"
    "v_mfma_f32_16x16x32_bf16 v[116:119], v[204:207], a[44:47], v[116:119]\n"
    "v_mfma_f32_16x16x32_bf16 v[120:123], v[200:203], a[48:51], v[120:123]\n"
    "ds_read_b64_tr_b16 v[180:181], v228 offset:6144\n"
    "v_mfma_f32_16x16x32_bf16 v[120:123], v[204:207], a[52:55], v[120:123]\n"
    "v_mfma_f32_16x16x32_bf16 v[124:127], v[200:203], a[56:59], v[124:127]\n"
    "ds_read_b64_tr_b16 v[182:183], v228 offset:7168\n"
    "v_mfma_f32_16x16x32_bf16 v[124:127], v[204:207], a[60:63], v[124:127]\n"
    "s_waitcnt lgkmcnt(8)\n"
    "v_mfma_f32_16x16x32_bf16 v[128:131], v[160:163], a[32:35], v[128:131]\n"
    "ds_read_b64_tr_b16 v[184:185], v228 offset:8192\n"
    "v_mfma_f32_16x16x32_bf16 v[128:131], v[164:167], a[36:39], v[128:131]\n"
    "v_mfma_f32_16x16x32_bf16 v[132:135], v[160:163], a[40:43], v[132:135]\n"
    "ds_read_b64_tr_b16 v[186:187], v228 offset:9216\n"
    "v_mfma_f32_16x16x32_bf16 v[132:135], v[164:167], a[44:47], v[132:135]\n"
    "buffer_load_dwordx4 v208, s[44:47], s63 offen lds\n"
    "v_mfma_f32_16x16x32_bf16 v[136:139], v[160:163], a[48:51], v[136:139]\n"
    "ds_read_b64_tr_b16 v[188:189], v228 offset:10240\n"
    "v_mfma_f32_16x16x32_bf16 v[136:139], v[164:167], a[52:55], v[136:139]\n"
    "v_mfma_f32_16x16x32_bf16 v[140:143], v[160:163], a[56:59], v[140:143]\n"
    "ds_read_b64_tr_b16 v[190:191], v228 offset:11264\n"
    "v_mfma_f32_16x16x32_bf16 v[140:143], v[164:167], a[60:63], v[140:143]\n"
    "s_waitcnt lgkmcnt(8)\n"
    "v_mfma_f32_16x16x32_bf16 v[144:147], v[168:171], a[32:35], v[144:147]\n"
    "ds_read_b64_tr_b16 v[192:193], v231 offset:4096\n"
    "v_mfma_f32_16x16x32_bf16 v[144:147], v[172:175], a[36:39], v[144:147]\n"
    "v_mfma_f32_16x16x32_bf16 v[148:151], v[168:171], a[40:43], v[148:151]\n"
    "ds_read_b64_tr_b16 v[194:195], v231 offset:5120\n"
    "v_mfma_f32_16x16x32_bf16 v[148:151], v[172:175], a[44:47], v[148:151]\n"
    "v_mfma_f32_16x16x32_bf16 v[152:155], v[168:171], a[48:51], v[152:155]\n"
    "ds_read_b64_tr_b16 v[196:197], v231 offset:6144\n"
    "v_mfma_f32_16x16x32_bf16 v[152:155], v[172:175], a[52:55], v[152:155]\n"
    "buffer_load_dwordx4 v209, s[44:47], s63 offen offset:1024 lds\n"
    "v_mfma_f32_16x16x32_bf16 v[156:159], v[168:171], a[56:59], v[156:159]\n"
    "s_add_u32 m0, s60, 118784\n"
    "ds_read_b64_tr_b16 v[198:199], v231 offset:7168\n"
    "v_mfma_f32_16x16x32_bf16 v[156:159], v[172:175], a[60:63], v[156:159]\n"
    "s_waitcnt lgkmcnt(8)\n"
    "s_add_u32 s63, s62, 590016\n"
    "v_mfma_f32_16x16x32_bf16 v[96:99], v[176:179], a[64:67], v[96:99]\n"
    "ds_read_b64_tr_b16 v[200:201], v231 offset:8192\n"
    "v_mfma_f32_16x16x32_bf16 v[96:99], v[180:183], a[68:71], v[96:99]\n"
    "v_mfma_f32_16x16x32_bf16 v[100:103], v[176:179], a[72:75], v[100:103]\n"
    "ds_read_b64_tr_b16 v[202:203], v231 offset:9216\n"
    "v_mfma_f32_16x16x32_bf16 v[100:103], v[180:183], a[76:79], v[100:103]\n"
    "v_mfma_f32_16x16x32_bf16 v[104:107], v[176:179], a[80:83], v[104:107]\n"
    "ds_read_b64_tr_b16 v[204:205], v231 offset:10240\n"
    "v_mfma_f32_16x16x32_bf16 v[104:107], v[180:183], a[84:87], v[104:107]\n"
    "v_mfma_f32_16x16x32_bf16 v[108:111], v[176:179], a[88:91], v[108:111]\n"
    "ds_read_b64_tr_b16 v[206:207], v231 offset:11264\n"
    "v_mfma_f32_16x16x32_bf16 v[108:111], v[180:183], a[92:95], v[108:111]\n"
    "s_waitcnt lgkmcnt(8)\n"
    "buffer_load_dwordx4 v208, s[44:47], s63 offen lds\n"
    "v_mfma_f32_16x16x32_bf16 v[112:115], v[184:187], a[64:67], v[112:115]\n"
    "ds_read_b64_tr_b16 v[160:161], v228 offset:12288\n"
    "v_mfma_f32_16x16x32_bf16 v[112:115], v[188:191], a[68:71], v[112:115]\n"
    "v_mfma_f32_16x16x32_bf16 v[116:119], v[184:187], a[72:75], v[116:119]\n"
    "ds_read_b64_tr_b16 v[162:163], v228 offset:13312\n"
    "v_mfma_f32_16x16x32_bf16 v[116:119], v[188:191], a[76:79], v[116:119]\n"
    "v_mfma_f32_16x16x32_bf16 v[120:123], v[184:187], a[80:83], v[120:123]\n"
    "ds_read_b64_tr_b16 v[164:165], v228 offset:14336\n"
    "v_mfma_f32_16x16x32_bf16 v[120:123], v[188:191], a[84:87], v[120:123]\n"
    "v_mfma_f32_16x16x32_bf16 v[124:127], v[184:187], a[88:91], v[124:127]\n"
    "ds_read_b64_tr_b16 v[166:167], v228 offset:15360\n"
    "v_mfma_f32_16x16x32_bf16 v[124:127], v[188:191], a[92:95], v[124:127]\n"
    "s_waitcnt lgkmcnt(8)\n"
    "v_mfma_f32_16x16x32_bf16 v[128:131], v[192:195], a[64:67], v[128:131]\n"
    "ds_read_b64_tr_b16 v[168:169], v228 offset:16384\n"
    "v_mfma_f32_16x16x32_bf16 v[128:131], v[196:199], a[68:71], v[128:131]\n"
    "buffer_load_dwordx4 v209, s[44:47], s63 offen offset:1024 lds\n"
    "v_mfma_f32_16x16x32_bf16 v[132:135], v[192:195], a[72:75], v[132:135]\n"
    "s_add_u32 m0, s60, 126976\n"
    "ds_read_b64_tr_b16 v[170:171], v228 offset:17408\n"
    "v_mfma_f32_16x16x32_bf16 v[132:135], v[196:199], a[76:79], v[132:135]\n"
    "s_add_u32 s63, s62, 1179840\n"
    "v_mfma_f32_16x16x32_bf16 v[136:139], v[192:195], a[80:83], v[136:139]\n"
    "ds_read_b64_tr_b16 v[172:173], v228 offset:18432\n"
    "v_mfma_f32_16x16x32_bf16 v[136:139], v[196:199], a[84:87], v[136:139]\n"
    "v_mfma_f32_16x16x32_bf16 v[140:143], v[192:195], a[88:91], v[140:143]\n"
    "ds_read_b64_tr_b16 v[174:175], v228 offset:19456\n"
    "v_mfma_f32_16x16x32_bf16 v[140:143], v[196:199], a[92:95], v[140:143]\n"
    "s_waitcnt lgkmcnt(8)\n"
    "v_mfma_f32_16x16x32_bf16 v[144:147], v[200:203], a[64:67], v[144:147]\n"
    "ds_read_b64_tr_b16 v[176:177], v231 offset:12288\n"
    "v_mfma_f32_16x16x32_bf16 v[144:147], v[204:207], a[68:71], v[144:147]\n"
    "v_mfma_f32_16x16x32_bf16 v[148:151], v[200:203], a[72:75], v[148:151]\n"
    "ds_read_b64_tr_b16 v[178:179], v231 offset:13312\n"
    "v_mfma_f32_16x16x32_bf16 v[148:151], v[204:207], a[76:79], v[148:151]\n"
    "buffer_load_dwordx4 v208, s[44:47], s63 offen lds\n"
    "v_mfma_f32_16x16x32_bf16 v[152:155], v[200:203], a[80:83], v[152:155]\n"
    "ds_read_b64_tr_b16 v[180:181], v231 offset:14336\n"
    "v_mfma_f32_16x16x32_bf16 v[152:155], v[204:207], a[84:87], v[152:155]\n"
    "v_mfma_f32_16x16x32_bf16 v[156:159], v[200:203], a[88:91], v[156:159]\n"
    "ds_read_b64_tr_b16 v[182:183], v231 offset:15360\n"
    "v_mfma_f32_16x16x32_bf16 v[156:159], v[204:207], a[92:95], v[156:159]\n"
    "s_waitcnt lgkmcnt(8)\n"
    "v_mfma_f32_16x16x32_bf16 v[96:99], v[160:163], a[96:99], v[96:99]\n"
    "ds_read_b64_tr_b16 v[184:185], v231 offset:16384\n"
    "v_mfma_f32_16x16x32_bf16 v[96:99], v[164:167], a[100:103], v[96:99]\n"
    "v_mfma_f32_16x16x32_bf16 v[100:103], v[160:163], a[104:107], v[100:103]\n"
    "ds_read_b64_tr_b16 v[186:187], v231 offset:17408\n"
    "v_mfma_f32_16x16x32_bf16 v[100:103], v[164:167], a[108:111], v[100:103]\n"
    "v_mfma_f32_16x16x32_bf16 v[104:107], v[160:163], a[112:115], v[104:107]\n"
    "ds_read_b64_tr_b16 v[188:189], v231 offset:18432\n"
    "v_mfma_f32_16x16x32_bf16 v[104:107], v[164:167], a[116:119], v[104:107]\n"
    "buffer_load_dwordx4 v209, s[44:47], s63 offen offset:1024 lds\n"
    "v_mfma_f32_16x16x32_bf16 v[108:111], v[160:163], a[120:123], v[108:111]\n"
    "s_waitcnt vmcnt(10)\n"
    "ds_read_b64_tr_b16 v[190:191], v231 offset:19456\n"
    "v_mfma_f32_16x16x32_bf16 v[108:111], v[164:167], a[124:127], v[108:111]\n"
    "s_waitcnt lgkmcnt(8)\n"
    "s_barrier\n"
    "v_mfma_f32_16x16x32_bf16 v[112:115], v[168:171], a[96:99], v[112:115]\n"
    "s_add_u32 m0, s60, 135168\n"
    "ds_read_b64_tr_b16 v[192:193], v228 offset:20480\n"
    "v_mfma_f32_16x16x32_bf16 v[112:115], v[172:175], a[100:103], v[112:115]\n"
    "s_add_u32 s63, s62, 1769664\n"
    "v_mfma_f32_16x16x32_bf16 v[116:119], v[168:171], a[104:107], v[116:119]\n"
    "ds_read_b64_tr_b16 v[194:195], v228 offset:21504\n"
    "v_mfma_f32_16x16x32_bf16 v[116:119], v[172:175], a[108:111], v[116:119]\n"
    "v_mfma_f32_16x16x32_bf16 v[120:123], v[168:171], a[112:115], v[120:123]\n"
    "ds_read_b64_tr_b16 v[196:197], v228 offset:22528\n"
    "v_mfma_f32_16x16x32_bf16 v[120:123], v[172:175], a[116:119], v[120:123]\n"
    "v_mfma_f32_16x16x32_bf16 v[124:127], v[168:171], a[120:123], v[124:127]\n"
    "ds_read_b64_tr_b16 v[198:199], v228 offset:23552\n"
    "v_mfma_f32_16x16x32_bf16 v[124:127], v[172:175], a[124:127], v[124:127]\n"
    "s_waitcnt lgkmcnt(8)\n"
    "buffer_load_dwordx4 v208, s[44:47], s63 offen lds\n"
    "v_mfma_f32_16x16x32_bf16 v[128:131], v[176:179], a[96:99], v[128:131]\n"
    "ds_read_b64_tr_b16 v[200:201], v228 offset:24576\n"
    "v_mfma_f32_16x16x32_bf16 v[128:131], v[180:183], a[100:103], v[128:131]\n"
    "v_mfma_f32_16x16x32_bf16 v[132:135], v[176:179], a[104:107], v[132:135]\n"
    "ds_read_b64_tr_b16 v[202:203], v228 offset:25600\n"
    "v_mfma_f32_16x16x32_bf16 v[132:135], v[180:183], a[108:111], v[132:135]\n"
    "v_mfma_f32_16x16x32_bf16 v[136:139], v[176:179], a[112:115], v[136:139]\n"
    "ds_read_b64_tr_b16 v[204:205], v228 offset:26624\n"
    "v_mfma_f32_16x16x32_bf16 v[136:139], v[180:183], a[116:119], v[136:139]\n"
    "v_mfma_f32_16x16x32_bf16 v[140:143], v[176:179], a[120:123], v[140:143]\n"
    "ds_read_b64_tr_b16 v[206:207], v228 offset:27648\n"
    "v_mfma_f32_16x16x32_bf16 v[140:143], v[180:183], a[124:127], v[140:143]\n"
    "s_waitcnt lgkmcnt(8)\n"
    "v_mfma_f32_16x16x32_bf16 v[144:147], v[184:187], a[96:99], v[144:147]\n"
    "ds_read_b64_tr_b16 v[160:161], v231 offset:20480\n"
    "v_mfma_f32_16x16x32_bf16 v[144:147], v[188:191], a[100:103], v[144:147]\n"
    "buffer_load_dwordx4 v209, s[44:47], s63 offen offset:1024 lds\n"
    "v_mfma_f32_16x16x32_bf16 v[148:151], v[184:187], a[104:107], v[148:151]\n"
    "s_add_u32 m0, s60, 0\n"
    "ds_read_b64_tr_b16 v[162:163], v231 offset:21504\n"
    "v_mfma_f32_16x16x32_bf16 v[148:151], v[188:191], a[108:111], v[148:151]\n"
    "s_add_u32 s63, s62, 2359488\n"
    "v_mfma_f32_16x16x32_bf16 v[152:155], v[184:187], a[112:115], v[152:155]\n"
    "ds_read_b64_tr_b16 v[164:165], v231 offset:22528\n"
    "v_mfma_f32_16x16x32_bf16 v[152:155], v[188:191], a[116:119], v[152:155]\n"
    "v_mfma_f32_16x16x32_bf16 v[156:159], v[184:187], a[120:123], v[156:159]\n"
    "ds_read_b64_tr_b16 v[166:167], v231 offset:23552\n"
    "v_mfma_f32_16x16x32_bf16 v[156:159], v[188:191], a[124:127], v[156:159]\n"
    "s_waitcnt lgkmcnt(8)\n"
    "v_mfma_f32_16x16x32_bf16 v[96:99], v[192:195], a[128:131], v[96:99]\n"
    "ds_read_b64_tr_b16 v[168:169], v231 offset:24576\n"
    "v_mfma_f32_16x16x32_bf16 v[96:99], v[196:199], a[132:135], v[96:99]\n"
    "v_mfma_f32_16x16x32_bf16 v[100:103], v[192:195], a[136:139], v[100:103]\n"
    "ds_read_b64_tr_b16 v[170:171], v231 offset:25600\n"
    "v_mfma_f32_16x16x32_bf16 v[100:103], v[196:199], a[140:143], v[100:103]\n"
    "v_mfma_f32_16x16x32_bf16 v[104:107], v[192:195], a[144:147], v[104:107]\n"
    "ds_read_b64_tr_b16 v[172:173], v231 offset:26624\n"
    "v_mfma_f32_16x16x32_bf16 v[104:107], v[196:199], a[148:151], v[104:107]\n"
    "v_mfma_f32_16x16x32_bf16 v[108:111], v[192:195], a[152:155], v[108:111]\n"
    "ds_read_b64_tr_b16 v[174:175], v231 offset:27648\n"
    "v_mfma_f32_16x16x32_bf16 v[108:111], v[196:199], a[156:159], v[108:111]\n"
    "s_waitcnt lgkmcnt(8)\n"
    "v_mfma_f32_16x16x32_bf16 v[112:115], v[200:203], a[128:131], v[112:115]\n"
    "ds_read_b64_tr_b16 v[176:177], v228 offset:28672\n"
    "v_mfma_f32_16x16x32_bf16 v[112:115], v[204:207], a[132:135], v[112:115]\n"
    "v_mfma_f32_16x16x32_bf16 v[116:119], v[200:203], a[136:139], v[116:119]\n"
    "ds_read_b64_tr_b16 v[178:179], v228 offset:29696\n"
    "v_mfma_f32_16x16x32_bf16 v[116:119], v[204:207], a[140:143], v[116:119]\n"
    "s_waitcnt vmcnt(8)\n"
    "v_mov_b32 v237, %21\n"
    "s_mov_b64 exec, 1\n"
    "global_store_dword v235, v237, %11 offset:2048 sc1\n"
    "s_mov_b64 exec, -1\n"
    "v_mfma_f32_16x16x32_bf16 v[120:123], v[200:203], a[144:147], v[120:123]\n"
    "ds_read_b64_tr_b16 v[180:181], v228 offset:30720\n"
    "v_mfma_f32_16x16x32_bf16 v[120:123], v[204:207], a[148:151], v[120:123]\n"
    "buffer_load_dwordx4 v208, s[44:47], s63 offen lds\n"
    "v_mfma_f32_16x16x32_bf16 v[124:127], v[200:203], a[152:155], v[124:127]\n"
    "ds_read_b64_tr_b16 v[182:183], v228 offset:31744\n"
    "v_mfma_f32_16x16x32_bf16 v[124:127], v[204:207], a[156:159], v[124:127]\n"
    "s_waitcnt lgkmcnt(8)\n"
    "v_mfma_f32_16x16x32_bf16 v[128:131], v[160:163], a[128:131], v[128:131]\n"
    "ds_read_b64_tr_b16 v[184:185], v228 offset:32768\n"
    "v_mfma_f32_16x16x32_bf16 v[128:131], v[164:167], a[132:135], v[128:131]\n"
    "v_mfma_f32_16x16x32_bf16 v[132:135], v[160:163], a[136:139], v[132:135]\n"
    "ds_read_b64_tr_b16 v[186:187], v228 offset:33792\n"
    "v_mfma_f32_16x16x32_bf16 v[132:135], v[164:167], a[140:143], v[132:135]\n"
    "v_mfma_f32_16x16x32_bf16 v[136:139], v[160:163], a[144:147], v[136:139]\n"
    "ds_read_b64_tr_b16 v[188:189], v228 offset:34816\n"
    "v_mfma_f32_16x16x32_bf16 v[136:139], v[164:167], a[148:151], v[136:139]\n"
    "v_mfma_f32_16x16x32_bf16 v[140:143], v[160:163], a[152:155], v[140:143]\n"
    "ds_read_b64_tr_b16 v[190:191], v228 offset:35840\n"
    "v_mfma_f32_16x16x32_bf16 v[140:143], v[164:167], a[156:159], v[140:143]\n"
    "s_waitcnt lgkmcnt(8)\n"
    "buffer_load_dwordx4 v209, s[44:47], s63 offen offset:1024 lds\n"
    "v_mfma_f32_16x16x32_bf16 v[144:147], v[168:171], a[128:131], v[144:147]\n"
    "s_add_u32 m0, s60, 8192\n"
    "ds_read_b64_tr_b16 v[192:193], v231 offset:28672\n"
    "v_mfma_f32_16x16x32_bf16 v[144:147], v[172:175], a[132:135], v[144:147]\n"
    "s_add_u32 s63, s62, 2949312\n"
    "v_mfma_f32_16x16x32_bf16 v[148:151], v[168:171], a[136:139], v[148:151]\n"
    "ds_read_b64_tr_b16 v[194:195], v231 offset:29696\n"
    "v_mfma_f32_16x16x32_bf16 v[148:151], v[172:175], a[140:143], v[148:151]\n"
    "v_mfma_f32_16x16x32_bf16 v[152:155], v[168:171], a[144:147], v[152:155]\n"
    "ds_read_b64_tr_b16 v[196:197], v231 offset:30720\n"
    "v_mfma_f32_16x16x32_bf16 v[152:155], v[172:175], a[148:151], v[152:155]\n"
    "v_mfma_f32_16x16x32_bf16 v[156:159], v[168:171], a[152:155], v[156:159]\n"
    "ds_read_b64_tr_b16 v[198:199], v231 offset:31744\n"
    "v_mfma_f32_16x16x32_bf16 v[156:159], v[172:175], a[156:159], v[156:159]\n"
    "s_waitcnt lgkmcnt(8)\n"
    "v_mfma_f32_16x16x32_bf16 v[96:99], v[176:179], a[160:163], v[96:99]\n"
    "ds_read_b64_tr_b16 v[200:201], v231 offset:32768\n"
    "v_mfma_f32_16x16x32_bf16 v[96:99], v[180:183], a[164:167], v[96:99]\n"
    "buffer_load_dwordx4 v208, s[44:47], s63 offen lds\n"
    "v_mfma_f32_16x16x32_bf16 v[100:103], v[176:179], a[168:171], v[100:103]\n"
    "ds_read_b64_tr_b16 v[202:203], v231 offset:33792\n"
    "v_mfma_f32_16x16x32_bf16 v[100:103], v[180:183], a[172:175], v[100:103]\n"
    "v_mfma_f32_16x16x32_bf16 v[104:107], v[176:179], a[176:179], v[104:107]\n"
    "ds_read_b64_tr_b16 v[204:205], v231 offset:34816\n"
    "v_mfma_f32_16x16x32_bf16 v[104:107], v[180:183], a[180:183], v[104:107]\n"
    "v_mfma_f32_16x16x32_bf16 v[108:111], v[176:179], a[184:187], v[108:111]\n"
    "ds_read_b64_tr_b16 v[206:207], v231 offset:35840\n"
    "v_mfma_f32_16x16x32_bf16 v[108:111], v[180:183], a[188:191], v[108:111]\n"
    "s_waitcnt lgkmcnt(8)\n"
    "v_mfma_f32_16x16x32_bf16 v[112:115], v[184:187], a[160:163], v[112:115]\n"
    "ds_read_b64_tr_b16 v[160:161], v228 offset:36864\n"
    "v_mfma_f32_16x16x32_bf16 v[112:115], v[188:191], a[164:167], v[112:115]\n"
    "v_mfma_f32_16x16x32_bf16 v[116:119], v[184:187], a[168:171], v[116:119]\n"
    "ds_read_b64_tr_b16 v[162:163], v228 offset:37888\n"
    "v_mfma_f32_16x16x32_bf16 v[116:119], v[188:191], a[172:175], v[116:119]\n"
    "buffer_load_dwordx4 v209, s[44:47], s63 offen offset:1024 lds\n"
    "v_mfma_f32_16x16x32_bf16 v[120:123], v[184:187], a[176:179], v[120:123]\n"
    "s_add_u32 m0, s60, 16384\n"
    "ds_read_b64_tr_b16 v[164:165], v228 offset:38912\n"
    "v_mfma_f32_16x16x32_bf16 v[120:123], v[188:191], a[180:183], v[120:123]\n"
    "s_add_u32 s63, s62, 3539136\n"
    "v_mfma_f32_16x16x32_bf16 v[124:127], v[184:187], a[184:187], v[124:127]\n"
    "ds_read_b64_tr_b16 v[166:167], v228 offset:39936\n"
    "v_mfma_f32_16x16x32_bf16 v[124:127], v[188:191], a[188:191], v[124:127]\n"
    "s_waitcnt lgkmcnt(8)\n"
    "v_mfma_f32_16x16x32_bf16 v[128:131], v[192:195], a[160:163], v[128:131]\n"
    "ds_read_b64_tr_b16 v[168:169], v228 offset:40960\n"
    "v_mfma_f32_16x16x32_bf16 v[128:131], v[196:199], a[164:167], v[128:131]\n"
    "v_mfma_f32_16x16x32_bf16 v[132:135], v[192:195], a[168:171], v[132:135]\n"
    "ds_read_b64_tr_b16 v[170:171], v228 offset:41984\n"
    "v_mfma_f32_16x16x32_bf16 v[132:135], v[196:199], a[172:175], v[132:135]\n"
    "v_mfma_f32_16x16x32_bf16 v[136:139], v[192:195], a[176:179], v[136:139]\n"
    "ds_read_b64_tr_b16 v[172:173], v228 offset:43008\n"
    "v_mfma_f32_16x16x32_bf16 v[136:139], v[196:199], a[180:183], v[136:139]\n"
    "buffer_load_dwordx4 v208, s[44:47], s63 offen lds\n"
    "v_mfma_f32_16x16x32_bf16 v[140:143], v[192:195], a[184:187], v[140:143]\n"
    "ds_read_b64_tr_b16 v[174:175], v228 offset:44032\n"
    "v_mfma_f32_16x16x32_bf16 v[140:143], v[196:199], a[188:191], v[140:143]\n"
    "s_waitcnt lgkmcnt(8)\n"
    "v_mfma_f32_16x16x32_bf16 v[144:147], v[200:203], a[160:163], v[144:147]\n"
    "ds_read_b64_tr_b16 v[176:177], v231 offset:36864\n"
    "v_mfma_f32_16x16x32_bf16 v[144:147], v[204:207], a[164:167], v[144:147]\n"
    "v_mfma_f32_16x16x32_bf16 v[148:151], v[200:203], a[168:171], v[148:151]\n"
    "ds_read_b64_tr_b16 v[178:179], v231 offset:37888\n"
    "v_mfma_f32_16x16x32_bf16 v[148:151], v[204:207], a[172:175], v[148:151]\n"
    "v_mfma_f32_16x16x32_bf16 v[152:155], v[200:203], a[176:179], v[152:155]\n"
    "ds_read_b64_tr_b16 v[180:181], v231 offset:38912\n"
    "v_mfma_f32_16x16x32_bf16 v[152:155], v[204:207], a[180:183], v[152:155]\n"
    "v_mfma_f32_16x16x32_bf16 v[156:159], v[200:203], a[184:187], v[156:159]\n"
    "ds_read_b64_tr_b16 v[182:183], v231 offset:39936\n"
    "v_mfma_f32_16x16x32_bf16 v[156:159], v[204:207], a[188:191], v[156:159]\n"
    "s_waitcnt lgkmcnt(8)\n"
    "buffer_load_dwordx4 v209, s[44:47], s63 offen offset:1024 lds\n"
    "v_mfma_f32_16x16x32_bf16 v[96:99], v[160:163], a[192:195], v[96:99]\n"
    "s_add_u32 m0, s60, 24576\n"
    "ds_read_b64_tr_b16 v[184:185], v231 offset:40960\n"
    "v_mfma_f32_16x16x32_bf16 v[96:99], v[164:167], a[196:199], v[96:99]\n"
    "s_add_u32 s63, s62, 4128960\n"
    "v_mfma_f32_16x16x32_bf16 v[100:103], v[160:163], a[200:203], v[100:103]\n"
    "ds_read_b64_tr_b16 v[186:187], v231 offset:41984\n"
    "v_mfma_f32_16x16x32_bf16 v[100:103], v[164:167], a[204:207], v[100:103]\n"
    "v_mfma_f32_16x16x32_bf16 v[104:107], v[160:163], a[208:211], v[104:107]\n"
    "ds_read_b64_tr_b16 v[188:189], v231 offset:43008\n"
    "v_mfma_f32_16x16x32_bf16 v[104:107], v[164:167], a[212:215], v[104:107]\n"
    "v_mfma_f32_16x16x32_bf16 v[108:111], v[160:163], a[216:219], v[108:111]\n"
    "ds_read_b64_tr_b16 v[190:191], v231 offset:44032\n"
    "v_mfma_f32_16x16x32_bf16 v[108:111], v[164:167], a[220:223], v[108:111]\n"
    "s_waitcnt lgkmcnt(8)\n"
    "v_mfma_f32_16x16x32_bf16 v[112:115], v[168:171], a[192:195], v[112:115]\n"
    "ds_read_b64_tr_b16 v[192:193], v228 offset:45056\n"
    "v_mfma_f32_16x16x32_bf16 v[112:115], v[172:175], a[196:199], v[112:115]\n"
    "buffer_load_dwordx4 v208, s[44:47], s63 offen lds\n"
    "v_mfma_f32_16x16x32_bf16 v[116:119], v[168:171], a[200:203], v[116:119]\n"
    "ds_read_b64_tr_b16 v[194:195], v228 offset:46080\n"
    "v_mfma_f32_16x16x32_bf16 v[116:119], v[172:175], a[204:207], v[116:119]\n"
    "v_mfma_f32_16x16x32_bf16 v[120:123], v[168:171], a[208:211], v[120:123]\n"
    "ds_read_b64_tr_b16 v[196:197], v228 offset:47104\n"
    "v_mfma_f32_16x16x32_bf16 v[120:123], v[172:175], a[212:215], v[120:123]\n"
    "v_mfma_f32_16x16x32_bf16 v[124:127], v[168:171], a[216:219], v[124:127]\n"
    "ds_read_b64_tr_b16 v[198:199], v228 offset:48128\n"
    "v_mfma_f32_16x16x32_bf16 v[124:127], v[172:175], a[220:223], v[124:127]\n"
    "s_waitcnt lgkmcnt(8)\n"
    "v_mfma_f32_16x16x32_bf16 v[128:131], v[176:179], a[192:195], v[128:131]\n"
    "ds_read_b64_tr_b16 v[200:201], v229 offset:0\n"
    "v_mfma_f32_16x16x32_bf16 v[128:131], v[180:183], a[196:199], v[128:131]\n"
    "v_mfma_f32_16x16x32_bf16 v[132:135], v[176:179], a[200:203], v[132:135]\n"
    "ds_read_b64_tr_b16 v[202:203], v229 offset:1024\n"
    "v_mfma_f32_16x16x32_bf16 v[132:135], v[180:183], a[204:207], v[132:135]\n"
    "buffer_load_dwordx4 v209, s[44:47], s63 offen offset:1024 lds\n"
    "v_mfma_f32_16x16x32_bf16 v[136:139], v[176:179], a[208:211], v[136:139]\n"
    "s_add_u32 m0, s60, 32768\n"
    "ds_read_b64_tr_b16 v[204:205], v229 offset:2048\n"
    "v_mfma_f32_16x16x32_bf16 v[136:139], v[180:183], a[212:215], v[136:139]\n"
    "s_add_u32 s63, s62, 4718784\n"
    "v_mfma_f32_16x16x32_bf16 v[140:143], v[176:179], a[216:219], v[140:143]\n"
    "ds_read_b64_tr_b16 v[206:207], v229 offset:3072\n"
    "v_mfma_f32_16x16x32_bf16 v[140:143], v[180:183], a[220:223], v[140:143]\n"
    "s_waitcnt lgkmcnt(8)\n"
    "v_mfma_f32_16x16x32_bf16 v[144:147], v[184:187], a[192:195], v[144:147]\n"
    "ds_read_b64_tr_b16 v[160:161], v231 offset:45056\n"
    "v_mfma_f32_16x16x32_bf16 v[144:147], v[188:191], a[196:199], v[144:147]\n"
    "v_mfma_f32_16x16x32_bf16 v[148:151], v[184:187], a[200:203], v[148:151]\n"
    "ds_read_b64_tr_b16 v[162:163], v231 offset:46080\n"
    "v_mfma_f32_16x16x32_bf16 v[148:151], v[188:191], a[204:207], v[148:151]\n"
    "v_mfma_f32_16x16x32_bf16 v[152:155], v[184:187], a[208:211], v[152:155]\n"
    "ds_read_b64_tr_b16 v[164:165], v231 offset:47104\n"
    "v_mfma_f32_16x16x32_bf16 v[152:155], v[188:191], a[212:215], v[152:155]\n"
    "buffer_load_dwordx4 v208, s[44:47], s63 offen lds\n"
    "v_mfma_f32_16x16x32_bf16 v[156:159], v[184:187], a[216:219], v[156:159]\n"
    "ds_read_b64_tr_b16 v[166:167], v231 offset:48128\n"
    "v_mfma_f32_16x16x32_bf16 v[156:159], v[188:191], a[220:223], v[156:159]\n"
    "s_waitcnt lgkmcnt(8)\n"
    "v_mfma_f32_16x16x32_bf16 v[96:99], v[192:195], a[224:227], v[96:99]\n"
    "ds_read_b64_tr_b16 v[168:169], v232 offset:0\n"
    "v_mfma_f32_16x16x32_bf16 v[96:99], v[196:199], a[228:231], v[96:99]\n"
    "v_mfma_f32_16x16x32_bf16 v[100:103], v[192:195], a[232:235], v[100:103]\n"
    "ds_read_b64_tr_b16 v[170:171], v232 offset:1024\n"
    "v_mfma_f32_16x16x32_bf16 v[100:103], v[196:199], a[236:239], v[100:103]\n"
    "v_mfma_f32_16x16x32_bf16 v[104:107], v[192:195], a[240:243], v[104:107]\n"
    "ds_read_b64_tr_b16 v[172:173], v232 offset:2048\n"
    "v_mfma_f32_16x16x32_bf16 v[104:107], v[196:199], a[244:247], v[104:107]\n"
    "v_mfma_f32_16x16x32_bf16 v[108:111], v[192:195], a[248:251], v[108:111]\n"
    "ds_read_b64_tr_b16 v[174:175], v232 offset:3072\n"
    "v_mfma_f32_16x16x32_bf16 v[108:111], v[196:199], a[252:255], v[108:111]\n"
    "s_waitcnt lgkmcnt(8)\n"
    "buffer_load_dwordx4 v209, s[44:47], s63 offen offset:1024 lds\n"
    "v_mfma_f32_16x16x32_bf16 v[112:115], v[200:203], a[224:227], v[112:115]\n"
    "ds_read_b64_tr_b16 v[176:177], v229 offset:4096\n"
    "v_mfma_f32_16x16x32_bf16 v[112:115], v[204:207], a[228:231], v[112:115]\n"
    "v_mfma_f32_16x16x32_bf16 v[116:119], v[200:203], a[232:235], v[116:119]\n"
    "ds_read_b64_tr_b16 v[178:179], v229 offset:5120\n"
    "v_mfma_f32_16x16x32_bf16 v[116:119], v[204:207], a[236:239], v[116:119]\n"
    "v_mfma_f32_16x16x32_bf16 v[120:123], v[200:203], a[240:243], v[120:123]\n"
    "ds_read_b64_tr_b16 v[180:181], v229 offset:6144\n"
    "v_mfma_f32_16x16x32_bf16 v[120:123], v[204:207], a[244:247], v[120:123]\n"
    "v_mfma_f32_16x16x32_bf16 v[124:127], v[200:203], a[248:251], v[124:127]\n"
    "s_mov_b64 s[40:41], %14\n"
    "ds_read_b64_tr_b16 v[182:183], v229 offset:7168\n"
    "v_mfma_f32_16x16x32_bf16 v[124:127], v[204:207], a[252:255], v[124:127]\n"
    "s_waitcnt lgkmcnt(8)\n"
    "s_and_b32 s41, s41, 0xffff\n"
    "v_mfma_f32_16x16x32_bf16 v[128:131], v[160:163], a[224:227], v[128:131]\n"
    "s_mov_b32 s42, 663552\n"
    "ds_read_b64_tr_b16 v[184:185], v229 offset:8192\n"
    "v_mfma_f32_16x16x32_bf16 v[128:131], v[164:167], a[228:231], v[128:131]\n"
    "s_mov_b32 s43, 0x20000\n"
    "v_mfma_f32_16x16x32_bf16 v[132:135], v[160:163], a[232:235], v[132:135]\n"
    "s_lshr_b32 s66, %8, 1\n"
    "ds_read_b64_tr_b16 v[186:187], v229 offset:9216\n"
    "v_mfma_f32_16x16x32_bf16 v[132:135], v[164:167], a[236:239], v[132:135]\n"
    "s_mul_i32 s70, s66, 9216\n"
    "v_mfma_f32_16x16x32_bf16 v[136:139], v[160:163], a[240:243], v[136:139]\n"
    "s_add_u32 s70, s70, %9\n"
    "ds_read_b64_tr_b16 v[188:189], v229 offset:10240\n"
    "v_mfma_f32_16x16x32_bf16 v[136:139], v[164:167], a[244:247], v[136:139]\n"
    "s_add_u32 s66, s66, 0\n"
    "v_mfma_f32_16x16x32_bf16 v[140:143], v[160:163], a[248:251], v[140:143]\n"
    "s_mul_i32 s69, s66, 36864\n"
    "ds_read_b64_tr_b16 v[190:191], v229 offset:11264\n"
    "v_mfma_f32_16x16x32_bf16 v[140:143], v[164:167], a[252:255], v[140:143]\n"
    "s_waitcnt lgkmcnt(8)\n"
    "s_and_b32 s66, %8, 1\n"
    "v_mfma_f32_16x16x32_bf16 v[144:147], v[168:171], a[224:227], v[144:147]\n"
    "s_mul_i32 s71, s66, 18432\n"
    "ds_read_b64_tr_b16 v[192:193], v232 offset:4096\n"
    "v_mfma_f32_16x16x32_bf16 v[144:147], v[172:175], a[228:231], v[144:147]\n"
    "s_add_u32 s69, s69, s71\n"
    "v_mfma_f32_16x16x32_bf16 v[148:151], v[168:171], a[232:235], v[148:151]\n"
    "s_lshl_b32 s71, s66, 12\n"
    "ds_read_b64_tr_b16 v[194:195], v232 offset:5120\n"
    "v_mfma_f32_16x16x32_bf16 v[148:151], v[172:175], a[236:239], v[148:151]\n"
    "s_add_u32 s70, s70, s71\n"
    "v_mfma_f32_16x16x32_bf16 v[152:155], v[168:171], a[240:243], v[152:155]\n"
    "s_sub_u32 s71, 8192, s71\n"
    "ds_read_b64_tr_b16 v[196:197], v232 offset:6144\n"
    "v_mfma_f32_16x16x32_bf16 v[152:155], v[172:175], a[244:247], v[152:155]\n"
    "s_add_u32 s71, s71, s70\n"
    "v_mfma_f32_16x16x32_bf16 v[156:159], v[168:171], a[248:251], v[156:159]\n"
    "s_cmp_eq_u32 s66, 0\n"
    "s_cselect_b32 s72, -1, 0\n"
    "s_cselect_b32 s73, 0, -1\n"
    "ds_read_b64_tr_b16 v[198:199], v232 offset:7168\n"
    "v_mfma_f32_16x16x32_bf16 v[156:159], v[172:175], a[252:255], v[156:159]\n"
    "s_waitcnt lgkmcnt(8)\n"
    "s_lshr_b32 s78, %8, 1\n"
    "v_mfma_f32_16x16x32_bf16 v[96:99], v[176:179], v[0:3], v[96:99]\n"
    "s_and_b32 s79, %8, 1\n"
    "ds_read_b64_tr_b16 v[200:201], v232 offset:8192\n"
    "v_mfma_f32_16x16x32_bf16 v[96:99], v[180:183], v[4:7], v[96:99]\n"
    "s_mul_i32 s79, s79, 18432\n"
    "v_bfe_u32 v233, v234, 2, 2\n"
    "v_mul_u32_u24 v233, 1152, v233\n"
    "v_mfma_f32_16x16x32_bf16 v[100:103], v[176:179], v[8:11], v[100:103]\n"
    "ds_read_b64_tr_b16 v[202:203], v232 offset:9216\n"
    "v_lshrrev_b32 v240, 4, v234\n"
    "v_mfma_f32_16x16x32_bf16 v[100:103], v[180:183], v[12:15], v[100:103]\n"
    "v_lshl_add_u32 v233, v240, 6, v233\n"
    "v_add_u32 v233, %15, v233\n"
    "v_mfma_f32_16x16x32_bf16 v[104:107], v[176:179], v[16:19], v[104:107]\n"
    "ds_read_b64_tr_b16 v[204:205], v232 offset:10240\n"
    "v_and_b32 v240, 3, v234\n"
    "v_mfma_f32_16x16x32_bf16 v[104:107], v[180:183], v[20:23], v[104:107]\n"
    "v_xor_b32 v241, 2, v240\n"
    "v_lshl_add_u32 v240, v240, 4, v233\n"
    "v_mfma_f32_16x16x32_bf16 v[108:111], v[176:179], v[24:27], v[108:111]\n"
    "s_waitcnt vmcnt(11)\n"
    "ds_read_b64_tr_b16 v[206:207], v232 offset:11264\n"
    "v_lshl_add_u32 v241, v241, 4, v233\n"
    "v_mfma_f32_16x16x32_bf16 v[108:111], v[180:183], v[28:31], v[108:111]\n"
    "s_waitcnt lgkmcnt(8)\n"
    "s_barrier\n"
    "v_add_u32 v208, 0, v240\n"
    "v_add_u32 v209, 3584, v241\n"
    "v_mfma_f32_16x16x32_bf16 v[112:115], v[184:187], v[0:3], v[112:115]\n"
    "ds_read_b64_tr_b16 v[160:161], v229 offset:12288\n"
    "v_add_u32 v210, 7168, v240\n"
    "v_mfma_f32_16x16x32_bf16 v[112:115], v[188:191], v[4:7], v[112:115]\n"
    "v_add_u32 v211, 10752, v241\n"
    "v_bfe_u32 v233, v234, 1, 4\n"
    "v_mfma_f32_16x16x32_bf16 v[116:119], v[184:187], v[8:11], v[116:119]\n"
    "ds_read_b64_tr_b16 v[162:163], v229 offset:13312\n"
    "v_mul_u32_u24 v233, 1152, v233\n"
    "v_mfma_f32_16x16x32_bf16 v[116:119], v[188:191], v[12:15], v[116:119]\n"
    "v_max_f32 v96, 0, v96\n"
    "v_max_f32 v97, 0, v97\n"
    "v_mfma_f32_16x16x32_bf16 v[120:123], v[184:187], v[16:19], v[120:123]\n"
    "ds_read_b64_tr_b16 v[164:165], v229 offset:14336\n"
    "v_max_f32 v98, 0, v98\n"
    "v_mfma_f32_16x16x32_bf16 v[120:123], v[188:191], v[20:23], v[120:123]\n"
    "v_max_f32 v99, 0, v99\n"
    "v_mul_f32 v96, v96, v112\n"
    "v_mfma_f32_16x16x32_bf16 v[124:127], v[184:187], v[24:27], v[124:127]\n"
    "ds_read_b64_tr_b16 v[166:167], v229 offset:15360\n"
    "v_mul_f32 v97, v97, v113\n"
    "v_mfma_f32_16x16x32_bf16 v[124:127], v[188:191], v[28:31], v[124:127]\n"
    "s_waitcnt lgkmcnt(8)\n"
    "v_mul_f32 v98, v98, v114\n"
    "v_mul_f32 v99, v99, v115\n"
    "v_mfma_f32_16x16x32_bf16 v[128:131], v[192:195], v[0:3], v[128:131]\n"
    "ds_read_b64_tr_b16 v[168:169], v229 offset:16384\n"
    "v_cvt_pk_bf16_f32 v96, v96, v97\n"
    "v_mfma_f32_16x16x32_bf16 v[128:131], v[196:199], v[4:7], v[128:131]\n"
    "v_cvt_pk_bf16_f32 v97, v98, v99\n"
    "v_max_f32 v100, 0, v100\n"
    "v_mfma_f32_16x16x32_bf16 v[132:135], v[192:195], v[8:11], v[132:135]\n"
    "ds_read_b64_tr_b16 v[170:171], v229 offset:17408\n"
    "v_max_f32 v101, 0, v101\n"
    "v_mfma_f32_16x16x32_bf16 v[132:135], v[196:199], v[12:15], v[132:135]\n"
    "v_max_f32 v102, 0, v102\n"
    "v_max_f32 v103, 0, v103\n"
    "v_mfma_f32_16x16x32_bf16 v[136:139], v[192:195], v[16:19], v[136:139]\n"
    "ds_read_b64_tr_b16 v[172:173], v229 offset:18432\n"
    "v_mul_f32 v100, v100, v116\n"
    "v_mfma_f32_16x16x32_bf16 v[136:139], v[196:199], v[20:23], v[136:139]\n"
    "v_mul_f32 v101, v101, v117\n"
    "v_mul_f32 v102, v102, v118\n"
    "v_mfma_f32_16x16x32_bf16 v[140:143], v[192:195], v[24:27], v[140:143]\n"
    "ds_read_b64_tr_b16 v[174:175], v229 offset:19456\n"
    "v_mul_f32 v103, v103, v119\n"
    "v_mfma_f32_16x16x32_bf16 v[140:143], v[196:199], v[28:31], v[140:143]\n"
    "s_waitcnt lgkmcnt(8)\n"
    "v_cvt_pk_bf16_f32 v100, v100, v101\n"
    "v_cvt_pk_bf16_f32 v101, v102, v103\n"
    "v_mfma_f32_16x16x32_bf16 v[144:147], v[200:203], v[0:3], v[144:147]\n"
    "ds_read_b64_tr_b16 v[176:177], v232 offset:12288\n"
    "v_max_f32 v104, 0, v104\n"
    "v_mfma_f32_16x16x32_bf16 v[144:147], v[204:207], v[4:7], v[144:147]\n"
    "v_max_f32 v105, 0, v105\n"
    "v_max_f32 v106, 0, v106\n"
    "v_mfma_f32_16x16x32_bf16 v[148:151], v[200:203], v[8:11], v[148:151]\n"
    "ds_read_b64_tr_b16 v[178:179], v232 offset:13312\n"
    "v_max_f32 v107, 0, v107\n"
    "v_mfma_f32_16x16x32_bf16 v[148:151], v[204:207], v[12:15], v[148:151]\n"
    "v_mul_f32 v104, v104, v120\n"
    "v_mul_f32 v105, v105, v121\n"
    "v_mfma_f32_16x16x32_bf16 v[152:155], v[200:203], v[16:19], v[152:155]\n"
    "ds_read_b64_tr_b16 v[180:181], v232 offset:14336\n"
    "v_mul_f32 v106, v106, v122\n"
    "v_mfma_f32_16x16x32_bf16 v[152:155], v[204:207], v[20:23], v[152:155]\n"
    "v_mul_f32 v107, v107, v123\n"
    "v_cvt_pk_bf16_f32 v104, v104, v105\n"
    "v_mfma_f32_16x16x32_bf16 v[156:159], v[200:203], v[24:27], v[156:159]\n"
    "ds_read_b64_tr_b16 v[182:183], v232 offset:15360\n"
    "v_cvt_pk_bf16_f32 v105, v106, v107\n"
    "v_mfma_f32_16x16x32_bf16 v[156:159], v[204:207], v[28:31], v[156:159]\n"
    "s_waitcnt lgkmcnt(8)\n"
    "v_max_f32 v108, 0, v108\n"
    "v_max_f32 v109, 0, v109\n"
    "v_mfma_f32_16x16x32_bf16 v[32:35], v[160:163], a[0:3], 0\n"
    "s_mov_b64 s[52:53], %13\n"
    "ds_read_b64_tr_b16 v[184:185], v232 offset:16384\n"
    "v_max_f32 v110, 0, v110\n"
    "v_mfma_f32_16x16x32_bf16 v[32:35], v[164:167], a[4:7], v[32:35]\n"
    "s_and_b32 s53, s53, 0xffff\n"
    "v_max_f32 v111, 0, v111\n"
    "v_mul_f32 v108, v108, v124\n"
    "v_mfma_f32_16x16x32_bf16 v[36:39], v[160:163], a[8:11], 0\n"
    "s_mov_b32 s54, 460800\n"
    "ds_read_b64_tr_b16 v[186:187], v232 offset:17408\n"
    "v_mul_f32 v109, v109, v125\n"
    "v_mfma_f32_16x16x32_bf16 v[36:39], v[164:167], a[12:15], v[36:39]\n"
    "s_mov_b32 s55, 0x20000\n"
    "v_mul_f32 v110, v110, v126\n"
    "v_mul_f32 v111, v111, v127\n"
    "v_mfma_f32_16x16x32_bf16 v[40:43], v[160:163], a[16:19], 0\n"
    "s_mov_b64 s[56:57], %17\n"
    "ds_read_b64_tr_b16 v[188:189], v232 offset:18432\n"
    "v_cvt_pk_bf16_f32 v108, v108, v109\n"
    "v_mfma_f32_16x16x32_bf16 v[40:43], v[164:167], a[20:23], v[40:43]\n"
    "s_and_b32 s57, s57, 0xffff\n"
    "v_cvt_pk_bf16_f32 v109, v110, v111\n"
    "v_max_f32 v128, 0, v128\n"
    "v_mfma_f32_16x16x32_bf16 v[44:47], v[160:163], a[24:27], 0\n"
    "s_mov_b32 s58, 73728\n"
    "ds_read_b64_tr_b16 v[190:191], v232 offset:19456\n"
    "v_max_f32 v129, 0, v129\n"
    "v_mfma_f32_16x16x32_bf16 v[44:47], v[164:167], a[28:31], v[44:47]\n"
    "s_waitcnt lgkmcnt(8)\n"
    "s_mov_b32 s59, 0x20000\n"
    "v_max_f32 v130, 0, v130\n"
    "v_max_f32 v131, 0, v131\n"
    "v_mfma_f32_16x16x32_bf16 v[48:51], v[168:171], a[0:3], 0\n"
    "s_mov_b32 s76, %18\n"
    "ds_read_b64_tr_b16 v[192:193], v229 offset:20480\n"
    "v_mul_f32 v128, v128, v144\n"
    "v_mfma_f32_16x16x32_bf16 v[48:51], v[172:175], a[4:7], v[48:51]\n"
    "s_barrier\n"
    "v_mul_f32 v129, v129, v145\n"
    "v_mul_f32 v130, v130, v146\n"
    "v_mfma_f32_16x16x32_bf16 v[52:55], v[168:171], a[8:11], 0\n"
    "s_mov_b32 s77, %19\n"
    "ds_read_b64_tr_b16 v[194:195], v229 offset:21504\n"
    "v_mul_f32 v131, v131, v147\n"
    "v_mfma_f32_16x16x32_bf16 v[52:55], v[172:175], a[12:15], v[52:55]\n"
    "v_cvt_pk_bf16_f32 v98, v128, v129\n"
    "v_cvt_pk_bf16_f32 v99, v130, v131\n"
    "v_mfma_f32_16x16x32_bf16 v[56:59], v[168:171], a[16:19], 0\n"
    "ds_read_b64_tr_b16 v[196:197], v229 offset:22528\n"
    "v_max_f32 v132, 0, v132\n"
    "v_mfma_f32_16x16x32_bf16 v[56:59], v[172:175], a[20:23], v[56:59]\n"
    "buffer_store_dwordx4 v[96:99], v217, s[48:51], s64 offen offset:2048 sc1\n"
    "v_max_f32 v133, 0, v133\n"
    "v_max_f32 v134, 0, v134\n"
    "v_mfma_f32_16x16x32_bf16 v[60:63], v[168:171], a[24:27], 0\n"
    "ds_read_b64_tr_b16 v[198:199], v229 offset:23552\n"
    "v_max_f32 v135, 0, v135\n"
    "v_mfma_f32_16x16x32_bf16 v[60:63], v[172:175], a[28:31], v[60:63]\n"
    "s_waitcnt lgkmcnt(8)\n"
    "v_mul_f32 v132, v132, v148\n"
    "v_mul_f32 v133, v133, v149\n"
    "v_mfma_f32_16x16x32_bf16 v[64:67], v[176:179], a[0:3], 0\n"
    "ds_read_b64_tr_b16 v[200:201], v229 offset:24576\n"
    "v_mul_f32 v134, v134, v150\n"
    "v_mfma_f32_16x16x32_bf16 v[64:67], v[180:183], a[4:7], v[64:67]\n"
    "v_mul_f32 v135, v135, v151\n"
    "v_cvt_pk_bf16_f32 v102, v132, v133\n"
    "v_mfma_f32_16x16x32_bf16 v[68:71], v[176:179], a[8:11], 0\n"
    "ds_read_b64_tr_b16 v[202:203], v229 offset:25600\n"
    "v_cvt_pk_bf16_f32 v103, v134, v135\n"
    "v_mfma_f32_16x16x32_bf16 v[68:71], v[180:183], a[12:15], v[68:71]\n"
    "v_max_f32 v136, 0, v136\n"
    "v_max_f32 v137, 0, v137\n"
    "v_mfma_f32_16x16x32_bf16 v[72:75], v[176:179], a[16:19], 0\n"
    "ds_read_b64_tr_b16 v[204:205], v229 offset:26624\n"
    "v_max_f32 v138, 0, v138\n"
    "v_mfma_f32_16x16x32_bf16 v[72:75], v[180:183], a[20:23], v[72:75]\n"
    "buffer_store_dwordx4 v[100:103], v218, s[48:51], s64 offen offset:2048 sc1\n"
    "v_max_f32 v139, 0, v139\n"
    "v_mul_f32 v136, v136, v152\n"
    "v_mfma_f32_16x16x32_bf16 v[76:79], v[176:179], a[24:27], 0\n"
    "ds_read_b64_tr_b16 v[206:207], v229 offset:27648\n"
    "v_mul_f32 v137, v137, v153\n"
    "v_mfma_f32_16x16x32_bf16 v[76:79], v[180:183], a[28:31], v[76:79]\n"
    "s_waitcnt lgkmcnt(8)\n"
    "v_mul_f32 v138, v138, v154\n"
    "v_mul_f32 v139, v139, v155\n"
    "v_mfma_f32_16x16x32_bf16 v[80:83], v[184:187], a[0:3], 0\n"
    "ds_read_b64_tr_b16 v[160:161], v232 offset:20480\n"
    "v_cvt_pk_bf16_f32 v106, v136, v137\n"
    "v_mfma_f32_16x16x32_bf16 v[80:83], v[188:191], a[4:7], v[80:83]\n"
    "v_cvt_pk_bf16_f32 v107, v138, v139\n"
    "v_max_f32 v140, 0, v140\n"
    "v_mfma_f32_16x16x32_bf16 v[84:87], v[184:187], a[8:11], 0\n"
    "ds_read_b64_tr_b16 v[162:163], v232 offset:21504\n"
    "v_max_f32 v141, 0, v141\n"
    "v_mfma_f32_16x16x32_bf16 v[84:87], v[188:191], a[12:15], v[84:87]\n"
    "v_max_f32 v142, 0, v142\n"
    "v_max_f32 v143, 0, v143\n"
    "v_mfma_f32_16x16x32_bf16 v[88:91], v[184:187], a[16:19], 0\n"
    "ds_read_b64_tr_b16 v[164:165], v232 offset:22528\n"
    "v_mul_f32 v140, v140, v156\n"
    "v_mfma_f32_16x16x32_bf16 v[88:91], v[188:191], a[20:23], v[88:91]\n"
    "buffer_store_dwordx4 v[104:107], v219, s[48:51], s64 offen offset:2048 sc1\n"
    "v_mul_f32 v141, v141, v157\n"
    "v_mul_f32 v142, v142, v158\n"
    "v_mfma_f32_16x16x32_bf16 v[92:95], v[184:187], a[24:27], 0\n"
    "ds_read_b64_tr_b16 v[166:167], v232 offset:23552\n"
    "v_mul_f32 v143, v143, v159\n"
    "v_mfma_f32_16x16x32_bf16 v[92:95], v[188:191], a[28:31], v[92:95]\n"
    "s_waitcnt lgkmcnt(8)\n"
    "v_cvt_pk_bf16_f32 v110, v140, v141\n"
    "v_cvt_pk_bf16_f32 v111, v142, v143\n"
    "v_mfma_f32_16x16x32_bf16 v[32:35], v[192:195], a[32:35], v[32:35]\n"
    "ds_read_b64_tr_b16 v[168:169], v232 offset:24576\n"
    "v_and_b32 v240, 1, v234\n"
    "v_mfma_f32_16x16x32_bf16 v[32:35], v[196:199], a[36:39], v[32:35]\n"
    "v_lshl_add_u32 v233, v240, 4, v233\n"
    "v_add_u32 v212, %16, v233\n"
    "v_mfma_f32_16x16x32_bf16 v[36:39], v[192:195], a[40:43], v[36:39]\n"
    "s_cmp_eq_u32 s78, 0\n"
    "s_cselect_b32 s69, 4, 13\n"
    "s_cselect_b32 s66, 0, 0\n"
    "ds_read_b64_tr_b16 v[170:171], v232 offset:25600\n"
    "v_mfma_f32_16x16x32_bf16 v[36:39], v[196:199], a[44:47], v[36:39]\n"
    "s_add_u32 s69, s69, s66\n"
    "v_mfma_f32_16x16x32_bf16 v[40:43], v[192:195], a[48:51], v[40:43]\n"
    "s_mul_i32 s69, s69, 36864\n"
    "ds_read_b64_tr_b16 v[172:173], v232 offset:26624\n"
    "v_mfma_f32_16x16x32_bf16 v[40:43], v[196:199], a[52:55], v[40:43]\n"
    "s_add_u32 s69, s69, s79\n"
    "buffer_store_dwordx4 v[108:111], v220, s[48:51], s64 offen offset:2048 sc1\n"
    "v_mfma_f32_16x16x32_bf16 v[44:47], v[192:195], a[56:59], v[44:47]\n"
    "s_add_u32 m0, s70, 55296\n"
    "ds_read_b64_tr_b16 v[174:175], v232 offset:27648\n"
    "v_mfma_f32_16x16x32_bf16 v[44:47], v[196:199], a[60:63], v[44:47]\n"
    "s_waitcnt lgkmcnt(8)\n"
    "v_mfma_f32_16x16x32_bf16 v[48:51], v[200:203], a[32:35], v[48:51]\n"
    "ds_read_b64_tr_b16 v[176:177], v229 offset:28672\n"
    "v_mfma_f32_16x16x32_bf16 v[48:51], v[204:207], a[36:39], v[48:51]\n"
    "v_mfma_f32_16x16x32_bf16 v[52:55], v[200:203], a[40:43], v[52:55]\n"
    "ds_read_b64_tr_b16 v[178:179], v229 offset:29696\n"
    "v_mfma_f32_16x16x32_bf16 v[52:55], v[204:207], a[44:47], v[52:55]\n"
    "v_mfma_f32_16x16x32_bf16 v[56:59], v[200:203], a[48:51], v[56:59]\n"
    "ds_read_b64_tr_b16 v[180:181], v229 offset:30720\n"
    "v_mfma_f32_16x16x32_bf16 v[56:59], v[204:207], a[52:55], v[56:59]\n"
    "buffer_load_dwordx4 v208, s[40:43], s69 offen offset:0 lds\n"
    "v_mfma_f32_16x16x32_bf16 v[60:63], v[200:203], a[56:59], v[60:63]\n"
    "ds_read_b64_tr_b16 v[182:183], v229 offset:31744\n"
    "v_mfma_f32_16x16x32_bf16 v[60:63], v[204:207], a[60:63], v[60:63]\n"
    "s_waitcnt lgkmcnt(8)\n"
    "v_mfma_f32_16x16x32_bf16 v[64:67], v[160:163], a[32:35], v[64:67]\n"
    "ds_read_b64_tr_b16 v[184:185], v229 offset:32768\n"
    "v_mfma_f32_16x16x32_bf16 v[64:67], v[164:167], a[36:39], v[64:67]\n"
    "buffer_load_dwordx4 v209, s[40:43], s69 offen offset:1024 lds\n"
    "v_mfma_f32_16x16x32_bf16 v[68:71], v[160:163], a[40:43], v[68:71]\n"
    "ds_read_b64_tr_b16 v[186:187], v229 offset:33792\n"
    "v_mfma_f32_16x16x32_bf16 v[68:71], v[164:167], a[44:47], v[68:71]\n"
    "v_mfma_f32_16x16x32_bf16 v[72:75], v[160:163], a[48:51], v[72:75]\n"
    "ds_read_b64_tr_b16 v[188:189], v229 offset:34816\n"
    "v_mfma_f32_16x16x32_bf16 v[72:75], v[164:167], a[52:55], v[72:75]\n"
    "buffer_load_dwordx4 v210, s[40:43], s69 offen offset:2048 lds\n"
    "v_mfma_f32_16x16x32_bf16 v[76:79], v[160:163], a[56:59], v[76:79]\n"
    "ds_read_b64_tr_b16 v[190:191], v229 offset:35840\n"
    "v_mfma_f32_16x16x32_bf16 v[76:79], v[164:167], a[60:63], v[76:79]\n"
    "s_waitcnt lgkmcnt(8)\n"
    "v_mfma_f32_16x16x32_bf16 v[80:83], v[168:171], a[32:35], v[80:83]\n"
    "ds_read_b64_tr_b16 v[192:193], v232 offset:28672\n"
    "v_mfma_f32_16x16x32_bf16 v[80:83], v[172:175], a[36:39], v[80:83]\n"
    "buffer_load_dwordx4 v211, s[40:43], s69 offen offset:3072 lds\n"
    "v_mfma_f32_16x16x32_bf16 v[84:87], v[168:171], a[40:43], v[84:87]\n"
    "s_add_u32 m0, s71, 55296\n"
    "ds_read_b64_tr_b16 v[194:195], v232 offset:29696\n"
    "v_mfma_f32_16x16x32_bf16 v[84:87], v[172:175], a[44:47], v[84:87]\n"
    "v_mfma_f32_16x16x32_bf16 v[88:91], v[168:171], a[48:51], v[88:91]\n"
    "ds_read_b64_tr_b16 v[196:197], v232 offset:30720\n"
    "v_mfma_f32_16x16x32_bf16 v[88:91], v[172:175], a[52:55], v[88:91]\n"
    "s_mov_b64 exec, s[72:73]\n"
    "buffer_load_dwordx4 v212, s[40:43], s69 offen lds\n"
    "s_mov_b64 exec, -1\n"
    "v_mfma_f32_16x16x32_bf16 v[92:95], v[168:171], a[56:59], v[92:95]\n"
    "s_cmp_eq_u32 s78, 0\n"
    "s_cselect_b32 s69, s80, s80\n"
    "s_cselect_b32 s66, 2, 3\n"
    "ds_read_b64_tr_b16 v[198:199], v232 offset:31744\n"
    "v_mfma_f32_16x16x32_bf16 v[92:95], v[172:175], a[60:63], v[92:95]\n"
    "s_waitcnt lgkmcnt(8)\n"
    "s_add_u32 s69, s69, s66\n"
    "v_mfma_f32_16x16x32_bf16 v[32:35], v[176:179], a[64:67], v[32:35]\n"
    "s_mul_i32 s69, s69, 36864\n"
    "ds_read_b64_tr_b16 v[200:201], v232 offset:32768\n"
    "v_mfma_f32_16x16x32_bf16 v[32:35], v[180:183], a[68:71], v[32:35]\n"
    "s_add_u32 s69, s69, s79\n"
    "v_mfma_f32_16x16x32_bf16 v[36:39], v[176:179], a[72:75], v[36:39]\n"
    "s_add_u32 m0, s70, 73728\n"
    "ds_read_b64_tr_b16 v[202:203], v232 offset:33792\n"
    "v_mfma_f32_16x16x32_bf16 v[36:39], v[180:183], a[76:79], v[36:39]\n"
    "v_mfma_f32_16x16x32_bf16 v[40:43], v[176:179], a[80:83], v[40:43]\n"
    "ds_read_b64_tr_b16 v[204:205], v232 offset:34816\n"
    "v_mfma_f32_16x16x32_bf16 v[40:43], v[180:183], a[84:87], v[40:43]\n"
    "global_load_dword v238, v239, %11 offset:2048 sc1\n"
    "v_mfma_f32_16x16x32_bf16 v[44:47], v[176:179], a[88:91], v[44:47]\n"
    "ds_read_b64_tr_b16 v[206:207], v232 offset:35840\n"
    "v_mfma_f32_16x16x32_bf16 v[44:47], v[180:183], a[92:95], v[44:47]\n"
    "s_waitcnt lgkmcnt(8)\n"
    "buffer_load_dwordx4 v208, s[40:43], s69 offen offset:0 lds\n"
    "v_mfma_f32_16x16x32_bf16 v[48:51], v[184:187], a[64:67], v[48:51]\n"
    "ds_read_b64_tr_b16 v[160:161], v229 offset:36864\n"
    "v_mfma_f32_16x16x32_bf16 v[48:51], v[188:191], a[68:71], v[48:51]\n"
    "v_mfma_f32_16x16x32_bf16 v[52:55], v[184:187], a[72:75], v[52:55]\n"
    "ds_read_b64_tr_b16 v[162:163], v229 offset:37888\n"
    "v_mfma_f32_16x16x32_bf16 v[52:55], v[188:191], a[76:79], v[52:55]\n"
    "buffer_load_dwordx4 v209, s[40:43], s69 offen offset:1024 lds\n"
    "v_mfma_f32_16x16x32_bf16 v[56:59], v[184:187], a[80:83], v[56:59]\n"
    "ds_read_b64_tr_b16 v[164:165], v229 offset:38912\n"
    "v_mfma_f32_16x16x32_bf16 v[56:59], v[188:191], a[84:87], v[56:59]\n"
    "v_mfma_f32_16x16x32_bf16 v[60:63], v[184:187], a[88:91], v[60:63]\n"
    "ds_read_b64_tr_b16 v[166:167], v229 offset:39936\n"
    "v_mfma_f32_16x16x32_bf16 v[60:63], v[188:191], a[92:95], v[60:63]\n"
    "s_waitcnt lgkmcnt(8)\n"
    "buffer_load_dwordx4 v210, s[40:43], s69 offen offset:2048 lds\n"
    "v_mfma_f32_16x16x32_bf16 v[64:67], v[192:195], a[64:67], v[64:67]\n"
    "ds_read_b64_tr_b16 v[168:169], v229 offset:40960\n"
    "v_mfma_f32_16x16x32_bf16 v[64:67], v[196:199], a[68:71], v[64:67]\n"
    "v_mfma_f32_16x16x32_bf16 v[68:71], v[192:195], a[72:75], v[68:71]\n"
    "ds_read_b64_tr_b16 v[170:171], v229 offset:41984\n"
    "v_mfma_f32_16x16x32_bf16 v[68:71], v[196:199], a[76:79], v[68:71]\n"
    "buffer_load_dwordx4 v211, s[40:43], s69 offen offset:3072 lds\n"
    "v_mfma_f32_16x16x32_bf16 v[72:75], v[192:195], a[80:83], v[72:75]\n"
    "s_add_u32 m0, s71, 73728\n"
    "ds_read_b64_tr_b16 v[172:173], v229 offset:43008\n"
    "v_mfma_f32_16x16x32_bf16 v[72:75], v[196:199], a[84:87], v[72:75]\n"
    "v_mfma_f32_16x16x32_bf16 v[76:79], v[192:195], a[88:91], v[76:79]\n"
    "ds_read_b64_tr_b16 v[174:175], v229 offset:44032\n"
    "v_mfma_f32_16x16x32_bf16 v[76:79], v[196:199], a[92:95], v[76:79]\n"
    "s_waitcnt lgkmcnt(8)\n"
    "s_mov_b64 exec, s[72:73]\n"
    "buffer_load_dwordx4 v212, s[40:43], s69 offen lds\n"
    "s_mov_b64 exec, -1\n"
    "v_mfma_f32_16x16x32_bf16 v[80:83], v[200:203], a[64:67], v[80:83]\n"
    "s_cmp_eq_u32 s78, 0\n"
    "s_cselect_b32 s69, 9, 14\n"
    "s_cselect_b32 s66, 0, 0\n"
    "ds_read_b64_tr_b16 v[176:177], v232 offset:36864\n"
    "v_mfma_f32_16x16x32_bf16 v[80:83], v[204:207], a[68:71], v[80:83]\n"
    "s_add_u32 s69, s69, s66\n"
    "v_mfma_f32_16x16x32_bf16 v[84:87], v[200:203], a[72:75], v[84:87]\n"
    "s_mul_i32 s69, s69, 36864\n"
    "ds_read_b64_tr_b16 v[178:179], v232 offset:37888\n"
    "v_mfma_f32_16x16x32_bf16 v[84:87], v[204:207], a[76:79], v[84:87]\n"
    "s_add_u32 s69, s69, s79\n"
    "v_mfma_f32_16x16x32_bf16 v[88:91], v[200:203], a[80:83], v[88:91]\n"
    "s_add_u32 m0, s70, 92160\n"
    "ds_read_b64_tr_b16 v[180:181], v232 offset:38912\n"
    "v_mfma_f32_16x16x32_bf16 v[88:91], v[204:207], a[84:87], v[88:91]\n"
    "buffer_load_dwordx4 v208, s[40:43], s69 offen offset:0 lds\n"
    "v_mfma_f32_16x16x32_bf16 v[92:95], v[200:203], a[88:91], v[92:95]\n"
    "ds_read_b64_tr_b16 v[182:183], v232 offset:39936\n"
    "v_mfma_f32_16x16x32_bf16 v[92:95], v[204:207], a[92:95], v[92:95]\n"
    "s_waitcnt lgkmcnt(8)\n"
    "v_mfma_f32_16x16x32_bf16 v[32:35], v[160:163], a[96:99], v[32:35]\n"
    "ds_read_b64_tr_b16 v[184:185], v232 offset:40960\n"
    "v_mfma_f32_16x16x32_bf16 v[32:35], v[164:167], a[100:103], v[32:35]\n"
    "v_mfma_f32_16x16x32_bf16 v[36:39], v[160:163], a[104:107], v[36:39]\n"
    "ds_read_b64_tr_b16 v[186:187], v232 offset:41984\n"
    "s_waitcnt vmcnt(6)\n"
    "v_cmp_eq_u32 vcc, %21, v238\n"
    "s_cmp_eq_u64 vcc, -1\n"
    "s_cbranch_scc1 PDB1\n"
    "s_mov_b32 s84, 0x40000\n"
    "PLB1:\n"
    "global_load_dword v238, v239, %11 offset:2048 sc1\n"
    "s_waitcnt vmcnt(0)\n"
    "v_cmp_eq_u32 vcc, %21, v238\n"
    "s_cmp_eq_u64 vcc, -1\n"
    "s_cbranch_scc1 PDB1\n"
    "s_sleep 1\n"
    "s_sub_u32 s84, s84, 1\n"
    "s_cmp_lg_u32 s84, 0\n"
    "s_cbranch_scc1 PLB1\n"
    "PDB1:\n"
    "v_mfma_f32_16x16x32_bf16 v[36:39], v[164:167], a[108:111], v[36:39]\n"
    "s_lshl_b32 s74, 4, 10\n"
    "buffer_load_dwordx4 a[64:67], v217, s[52:55], s74 offen offset:0 sc1\n"
    "v_mfma_f32_16x16x32_bf16 v[40:43], v[160:163], a[112:115], v[40:43]\n"
    "ds_read_b64_tr_b16 v[188:189], v232 offset:43008\n"
    "v_mfma_f32_16x16x32_bf16 v[40:43], v[164:167], a[116:119], v[40:43]\n"
    "v_mfma_f32_16x16x32_bf16 v[44:47], v[160:163], a[120:123], v[44:47]\n"
    "s_waitcnt vmcnt(17)\n"
    "ds_read_b64_tr_b16 v[190:191], v232 offset:44032\n"
    "v_mfma_f32_16x16x32_bf16 v[44:47], v[164:167], a[124:127], v[44:47]\n"
    "s_waitcnt lgkmcnt(8)\n"
    "s_barrier\n"
    "buffer_load_dwordx4 a[72:75], v218, s[52:55], s74 offen offset:0 sc1\n"
    "v_mfma_f32_16x16x32_bf16 v[48:51], v[168:171], a[96:99], v[48:51]\n"
    "ds_read_b64_tr_b16 v[192:193], v227 offset:0\n"
    "v_mfma_f32_16x16x32_bf16 v[48:51], v[172:175], a[100:103], v[48:51]\n"
    "v_mfma_f32_16x16x32_bf16 v[52:55], v[168:171], a[104:107], v[52:55]\n"
    "ds_read_b64_tr_b16 v[194:195], v227 offset:1024\n"
    "v_mfma_f32_16x16x32_bf16 v[52:55], v[172:175], a[108:111], v[52:55]\n"
    "buffer_load_dwordx4 a[80:83], v219, s[52:55], s74 offen offset:0 sc1\n"
    "v_mfma_f32_16x16x32_bf16 v[56:59], v[168:171], a[112:115], v[56:59]\n"
    "ds_read_b64_tr_b16 v[196:197], v227 offset:2048\n"
    "v_mfma_f32_16x16x32_bf16 v[56:59], v[172:175], a[116:119], v[56:59]\n"
    "v_mfma_f32_16x16x32_bf16 v[60:63], v[168:171], a[120:123], v[60:63]\n"
    "ds_read_b64_tr_b16 v[198:199], v227 offset:3072\n"
    "v_mfma_f32_16x16x32_bf16 v[60:63], v[172:175], a[124:127], v[60:63]\n"
    "s_waitcnt lgkmcnt(8)\n"
    "buffer_load_dwordx4 a[88:91], v220, s[52:55], s74 offen offset:0 sc1\n"
    "v_mfma_f32_16x16x32_bf16 v[64:67], v[176:179], a[96:99], v[64:67]\n"
    "s_lshl_b32 s75, 13, 10\n"
    "ds_read_b64_tr_b16 v[200:201], v227 offset:4096\n"
    "v_mfma_f32_16x16x32_bf16 v[64:67], v[180:183], a[100:103], v[64:67]\n"
    "v_mfma_f32_16x16x32_bf16 v[68:71], v[176:179], a[104:107], v[68:71]\n"
    "ds_read_b64_tr_b16 v[202:203], v227 offset:5120\n"
    "v_mfma_f32_16x16x32_bf16 v[68:71], v[180:183], a[108:111], v[68:71]\n"
    "buffer_load_dwordx4 a[68:71], v217, s[52:55], s75 offen offset:0 sc1\n"
    "v_mfma_f32_16x16x32_bf16 v[72:75], v[176:179], a[112:115], v[72:75]\n"
    "ds_read_b64_tr_b16 v[204:205], v227 offset:6144\n"
    "v_mfma_f32_16x16x32_bf16 v[72:75], v[180:183], a[116:119], v[72:75]\n"
    "v_mfma_f32_16x16x32_bf16 v[76:79], v[176:179], a[120:123], v[76:79]\n"
    "ds_read_b64_tr_b16 v[206:207], v227 offset:7168\n"
    "v_mfma_f32_16x16x32_bf16 v[76:79], v[180:183], a[124:127], v[76:79]\n"
    "s_waitcnt lgkmcnt(8)\n"
    "buffer_load_dwordx4 a[76:79], v218, s[52:55], s75 offen offset:0 sc1\n"
    "v_mfma_f32_16x16x32_bf16 v[80:83], v[184:187], a[96:99], v[80:83]\n"
    "ds_read_b64_tr_b16 v[160:161], v230 offset:0\n"
    "v_mfma_f32_16x16x32_bf16 v[80:83], v[188:191], a[100:103], v[80:83]\n"
    "v_mfma_f32_16x16x32_bf16 v[84:87], v[184:187], a[104:107], v[84:87]\n"
    "ds_read_b64_tr_b16 v[162:163], v230 offset:1024\n"
    "v_mfma_f32_16x16x32_bf16 v[84:87], v[188:191], a[108:111], v[84:87]\n"
    "buffer_load_dwordx4 a[84:87], v219, s[52:55], s75 offen offset:0 sc1\n"
    "v_mfma_f32_16x16x32_bf16 v[88:91], v[184:187], a[112:115], v[88:91]\n"
    "ds_read_b64_tr_b16 v[164:165], v230 offset:2048\n"
    "v_mfma_f32_16x16x32_bf16 v[88:91], v[188:191], a[116:119], v[88:91]\n"
    "v_mfma_f32_16x16x32_bf16 v[92:95], v[184:187], a[120:123], v[92:95]\n"
    "ds_read_b64_tr_b16 v[166:167], v230 offset:3072\n"
    "v_mfma_f32_16x16x32_bf16 v[92:95], v[188:191], a[124:127], v[92:95]\n"
    "s_waitcnt lgkmcnt(8)\n"
    "buffer_load_dwordx4 a[92:95], v220, s[52:55], s75 offen offset:0 sc1\n"
    "v_mfma_f32_16x16x32_bf16 v[32:35], v[192:195], a[128:131], v[32:35]\n"
    "s_lshl_b32 s74, 0, 10\n"
    "ds_read_b64_tr_b16 v[168:169], v230 offset:4096\n"
    "v_mfma_f32_16x16x32_bf16 v[32:35], v[196:199], a[132:135], v[32:35]\n"
    "v_mfma_f32_16x16x32_bf16 v[36:39], v[192:195], a[136:139], v[36:39]\n"
    "ds_read_b64_tr_b16 v[170:171], v230 offset:5120\n"
    "v_mfma_f32_16x16x32_bf16 v[36:39], v[196:199], a[140:143], v[36:39]\n"
    "buffer_load_dwordx4 a[96:99], v217, s[52:55], s74 offen offset:0 sc1\n"
    "v_mfma_f32_16x16x32_bf16 v[40:43], v[192:195], a[144:147], v[40:43]\n"
    "ds_read_b64_tr_b16 v[172:173], v230 offset:6144\n"
    "v_mfma_f32_16x16x32_bf16 v[40:43], v[196:199], a[148:151], v[40:43]\n"
    "v_mfma_f32_16x16x32_bf16 v[44:47], v[192:195], a[152:155], v[44:47]\n"
    "ds_read_b64_tr_b16 v[174:175], v230 offset:7168\n"
    "v_mfma_f32_16x16x32_bf16 v[44:47], v[196:199], a[156:159], v[44:47]\n"
    "s_waitcnt lgkmcnt(8)\n"
    "buffer_load_dwordx4 a[104:107], v218, s[52:55], s74 offen offset:0 sc1\n"
    "v_mfma_f32_16x16x32_bf16 v[48:51], v[200:203], a[128:131], v[48:51]\n"
    "ds_read_b64_tr_b16 v[176:177], v227 offset:8192\n"
    "v_mfma_f32_16x16x32_bf16 v[48:51], v[204:207], a[132:135], v[48:51]\n"
    "s_barrier\n"
    "v_mfma_f32_16x16x32_bf16 v[52:55], v[200:203], a[136:139], v[52:55]\n"
    "ds_read_b64_tr_b16 v[178:179], v227 offset:9216\n"
    "v_mfma_f32_16x16x32_bf16 v[52:55], v[204:207], a[140:143], v[52:55]\n"
    "buffer_load_dwordx4 a[112:115], v219, s[52:55], s74 offen offset:0 sc1\n"
    "v_mfma_f32_16x16x32_bf16 v[56:59], v[200:203], a[144:147], v[56:59]\n"
    "ds_read_b64_tr_b16 v[180:181], v227 offset:10240\n"
    "v_mfma_f32_16x16x32_bf16 v[56:59], v[204:207], a[148:151], v[56:59]\n"
    "v_mfma_f32_16x16x32_bf16 v[60:63], v[200:203], a[152:155], v[60:63]\n"
    "ds_read_b64_tr_b16 v[182:183], v227 offset:11264\n"
    "v_mfma_f32_16x16x32_bf16 v[60:63], v[204:207], a[156:159], v[60:63]\n"
    "s_waitcnt lgkmcnt(8)\n"
    "buffer_load_dwordx4 a[120:123], v220, s[52:55], s74 offen offset:0 sc1\n"
    "v_mfma_f32_16x16x32_bf16 v[64:67], v[160:163], a[128:131], v[64:67]\n"
    "s_lshl_b32 s75, 5, 10\n"
    "ds_read_b64_tr_b16 v[184:185], v227 offset:12288\n"
    "v_mfma_f32_16x16x32_bf16 v[64:67], v[164:167], a[132:135], v[64:67]\n"
    "v_mfma_f32_16x16x32_bf16 v[68:71], v[160:163], a[136:139], v[68:71]\n"
    "ds_read_b64_tr_b16 v[186:187], v227 offset:13312\n"
    "v_mfma_f32_16x16x32_bf16 v[68:71], v[164:167], a[140:143], v[68:71]\n"
    "buffer_load_dwordx4 a[100:103], v217, s[52:55], s75 offen offset:0 sc1\n"
    "v_mfma_f32_16x16x32_bf16 v[72:75], v[160:163], a[144:147], v[72:75]\n"
    "ds_read_b64_tr_b16 v[188:189], v227 offset:14336\n"
    "v_mfma_f32_16x16x32_bf16 v[72:75], v[164:167], a[148:151], v[72:75]\n"
    "v_mfma_f32_16x16x32_bf16 v[76:79], v[160:163], a[152:155], v[76:79]\n"
    "ds_read_b64_tr_b16 v[190:191], v227 offset:15360\n"
    "v_mfma_f32_16x16x32_bf16 v[76:79], v[164:167], a[156:159], v[76:79]\n"
    "s_waitcnt lgkmcnt(8)\n"
    "buffer_load_dwordx4 a[108:111], v218, s[52:55], s75 offen offset:0 sc1\n"
    "v_mfma_f32_16x16x32_bf16 v[80:83], v[168:171], a[128:131], v[80:83]\n"
    "ds_read_b64_tr_b16 v[192:193], v230 offset:8192\n"
    "v_mfma_f32_16x16x32_bf16 v[80:83], v[172:175], a[132:135], v[80:83]\n"
    "v_mfma_f32_16x16x32_bf16 v[84:87], v[168:171], a[136:139], v[84:87]\n"
    "ds_read_b64_tr_b16 v[194:195], v230 offset:9216\n"
    "v_mfma_f32_16x16x32_bf16 v[84:87], v[172:175], a[140:143], v[84:87]\n"
    "buffer_load_dwordx4 a[116:119], v219, s[52:55], s75 offen offset:0 sc1\n"
    "v_mfma_f32_16x16x32_bf16 v[88:91], v[168:171], a[144:147], v[88:91]\n"
    "ds_read_b64_tr_b16 v[196:197], v230 offset:10240\n"
    "v_mfma_f32_16x16x32_bf16 v[88:91], v[172:175], a[148:151], v[88:91]\n"
    "v_mfma_f32_16x16x32_bf16 v[92:95], v[168:171], a[152:155], v[92:95]\n"
    "ds_read_b64_tr_b16 v[198:199], v230 offset:11264\n"
    "v_mfma_f32_16x16x32_bf16 v[92:95], v[172:175], a[156:159], v[92:95]\n"
    "s_waitcnt lgkmcnt(8)\n"
    "buffer_load_dwordx4 a[124:127], v220, s[52:55], s75 offen offset:0 sc1\n"
    "v_mfma_f32_16x16x32_bf16 v[32:35], v[176:179], a[160:163], v[32:35]\n"
    "ds_read_b64_tr_b16 v[200:201], v230 offset:12288\n"
    "v_mfma_f32_16x16x32_bf16 v[32:35], v[180:183], a[164:167], v[32:35]\n"
    "v_add_u32 v224, 55296, v221\n"
    "v_mfma_f32_16x16x32_bf16 v[36:39], v[176:179], a[168:171], v[36:39]\n"
    "ds_read_b64_tr_b16 v[202:203], v230 offset:13312\n"
    "v_mfma_f32_16x16x32_bf16 v[36:39], v[180:183], a[172:175], v[36:39]\n"
    "s_waitcnt vmcnt(28)\n"
    "v_mov_b32 v237, %21\n"
    "s_mov_b64 exec, 1\n"
    "global_store_dword v235, v237, %11 offset:3072 sc1\n"
    "s_mov_b64 exec, -1\n"
    "v_add_u32 v225, 55296, v222\n"
    "v_mfma_f32_16x16x32_bf16 v[40:43], v[176:179], a[176:179], v[40:43]\n"
    "s_lshl_b32 s74, 9, 10\n"
    "ds_read_b64_tr_b16 v[204:205], v230 offset:14336\n"
    "v_mfma_f32_16x16x32_bf16 v[40:43], v[180:183], a[180:183], v[40:43]\n"
    "buffer_load_dwordx4 a[128:131], v217, s[52:55], s74 offen offset:0 sc1\n"
    "v_add_u32 v226, 55296, v223\n"
    "v_mfma_f32_16x16x32_bf16 v[44:47], v[176:179], a[184:187], v[44:47]\n"
    "ds_read_b64_tr_b16 v[206:207], v230 offset:15360\n"
    "v_mfma_f32_16x16x32_bf16 v[44:47], v[180:183], a[188:191], v[44:47]\n"
    "s_waitcnt lgkmcnt(8)\n"
    "v_mfma_f32_16x16x32_bf16 v[48:51], v[184:187], a[160:163], v[48:51]\n"
    "ds_read_b64_tr_b16 v[160:161], v227 offset:16384\n"
    "v_mfma_f32_16x16x32_bf16 v[48:51], v[188:191], a[164:167], v[48:51]\n"
    "buffer_load_dwordx4 a[136:139], v218, s[52:55], s74 offen offset:0 sc1\n"
    "v_mfma_f32_16x16x32_bf16 v[52:55], v[184:187], a[168:171], v[52:55]\n"
    "ds_read_b64_tr_b16 v[162:163], v227 offset:17408\n"
    "v_mfma_f32_16x16x32_bf16 v[52:55], v[188:191], a[172:175], v[52:55]\n"
    "v_mfma_f32_16x16x32_bf16 v[56:59], v[184:187], a[176:179], v[56:59]\n"
    "ds_read_b64_tr_b16 v[164:165], v227 offset:18432\n"
    "v_mfma_f32_16x16x32_bf16 v[56:59], v[188:191], a[180:183], v[56:59]\n"
    "buffer_load_dwordx4 a[144:147], v219, s[52:55], s74 offen offset:0 sc1\n"
    "v_mfma_f32_16x16x32_bf16 v[60:63], v[184:187], a[184:187], v[60:63]\n"
    "ds_read_b64_tr_b16 v[166:167], v227 offset:19456\n"
    "v_mfma_f32_16x16x32_bf16 v[60:63], v[188:191], a[188:191], v[60:63]\n"
    "s_waitcnt lgkmcnt(8)\n"
    "v_mfma_f32_16x16x32_bf16 v[64:67], v[192:195], a[160:163], v[64:67]\n"
    "ds_read_b64_tr_b16 v[168:169], v227 offset:20480\n"
    "v_mfma_f32_16x16x32_bf16 v[64:67], v[196:199], a[164:167], v[64:67]\n"
    "buffer_load_dwordx4 a[152:155], v220, s[52:55], s74 offen offset:0 sc1\n"
    "v_mfma_f32_16x16x32_bf16 v[68:71], v[192:195], a[168:171], v[68:71]\n"
    "s_lshl_b32 s75, 14, 10\n"
    "ds_read_b64_tr_b16 v[170:171], v227 offset:21504\n"
    "v_mfma_f32_16x16x32_bf16 v[68:71], v[196:199], a[172:175], v[68:71]\n"
    "v_mfma_f32_16x16x32_bf16 v[72:75], v[192:195], a[176:179], v[72:75]\n"
    "ds_read_b64_tr_b16 v[172:173], v227 offset:22528\n"
    "v_mfma_f32_16x16x32_bf16 v[72:75], v[196:199], a[180:183], v[72:75]\n"
    "buffer_load_dwordx4 a[132:135], v217, s[52:55], s75 offen offset:0 sc1\n"
    "v_mfma_f32_16x16x32_bf16 v[76:79], v[192:195], a[184:187], v[76:79]\n"
    "ds_read_b64_tr_b16 v[174:175], v227 offset:23552\n"
    "v_mfma_f32_16x16x32_bf16 v[76:79], v[196:199], a[188:191], v[76:79]\n"
    "s_waitcnt lgkmcnt(8)\n"
    "v_mfma_f32_16x16x32_bf16 v[80:83], v[200:203], a[160:163], v[80:83]\n"
    "ds_read_b64_tr_b16 v[176:177], v230 offset:16384\n"
    "v_mfma_f32_16x16x32_bf16 v[80:83], v[204:207], a[164:167], v[80:83]\n"
    "buffer_load_dwordx4 a[140:143], v218, s[52:55], s75 offen offset:0 sc1\n"
    "v_mfma_f32_16x16x32_bf16 v[84:87], v[200:203], a[168:171], v[84:87]\n"
    "ds_read_b64_tr_b16 v[178:179], v230 offset:17408\n"
    "v_mfma_f32_16x16x32_bf16 v[84:87], v[204:207], a[172:175], v[84:87]\n"
    "v_mfma_f32_16x16x32_bf16 v[88:91], v[200:203], a[176:179], v[88:91]\n"
    "ds_read_b64_tr_b16 v[180:181], v230 offset:18432\n"
    "v_mfma_f32_16x16x32_bf16 v[88:91], v[204:207], a[180:183], v[88:91]\n"
    "buffer_load_dwordx4 a[148:151], v219, s[52:55], s75 offen offset:0 sc1\n"
    "v_mfma_f32_16x16x32_bf16 v[92:95], v[200:203], a[184:187], v[92:95]\n"
    "ds_read_b64_tr_b16 v[182:183], v230 offset:19456\n"
    "v_mfma_f32_16x16x32_bf16 v[92:95], v[204:207], a[188:191], v[92:95]\n"
    "s_waitcnt lgkmcnt(8)\n"
    "v_mfma_f32_16x16x32_bf16 v[32:35], v[160:163], a[192:195], v[32:35]\n"
    "ds_read_b64_tr_b16 v[184:185], v230 offset:20480\n"
    "v_mfma_f32_16x16x32_bf16 v[32:35], v[164:167], a[196:199], v[32:35]\n"
    "buffer_load_dwordx4 a[156:159], v220, s[52:55], s75 offen offset:0 sc1\n"
    "v_mfma_f32_16x16x32_bf16 v[36:39], v[160:163], a[200:203], v[36:39]\n"
    "s_lshl_b32 s74, 0, 10\n"
    "ds_read_b64_tr_b16 v[186:187], v230 offset:21504\n"
    "v_mfma_f32_16x16x32_bf16 v[36:39], v[164:167], a[204:207], v[36:39]\n"
    "v_mfma_f32_16x16x32_bf16 v[40:43], v[160:163], a[208:211], v[40:43]\n"
    "ds_read_b64_tr_b16 v[188:189], v230 offset:22528\n"
    "v_mfma_f32_16x16x32_bf16 v[40:43], v[164:167], a[212:215], v[40:43]\n"
    "buffer_load_dwordx4 a[160:163], v217, s[52:55], s74 offen offset:1024 sc1\n"
    "v_mfma_f32_16x16x32_bf16 v[44:47], v[160:163], a[216:219], v[44:47]\n"
    "ds_read_b64_tr_b16 v[190:191], v230 offset:23552\n"
    "v_mfma_f32_16x16x32_bf16 v[44:47], v[164:167], a[220:223], v[44:47]\n"
    "s_waitcnt lgkmcnt(8)\n"
    "v_mfma_f32_16x16x32_bf16 v[48:51], v[168:171], a[192:195], v[48:51]\n"
    "ds_read_b64_tr_b16 v[192:193], v227 offset:24576\n"
    "v_mfma_f32_16x16x32_bf16 v[48:51], v[172:175], a[196:199], v[48:51]\n"
    "buffer_load_dwordx4 a[168:171], v218, s[52:55], s74 offen offset:1024 sc1\n"
    "v_mfma_f32_16x16x32_bf16 v[52:55], v[168:171], a[200:203], v[52:55]\n"
    "ds_read_b64_tr_b16 v[194:195], v227 offset:25600\n"
    "v_mfma_f32_16x16x32_bf16 v[52:55], v[172:175], a[204:207], v[52:55]\n"
    "v_mfma_f32_16x16x32_bf16 v[56:59], v[168:171], a[208:211], v[56:59]\n"
    "ds_read_b64_tr_b16 v[196:197], v227 offset:26624\n"
    "v_mfma_f32_16x16x32_bf16 v[56:59], v[172:175], a[212:215], v[56:59]\n"
    "buffer_load_dwordx4 a[176:179], v219, s[52:55], s74 offen offset:1024 sc1\n"
    "v_mfma_f32_16x16x32_bf16 v[60:63], v[168:171], a[216:219], v[60:63]\n"
    "ds_read_b64_tr_b16 v[198:199], v227 offset:27648\n"
    "v_mfma_f32_16x16x32_bf16 v[60:63], v[172:175], a[220:223], v[60:63]\n"
    "s_waitcnt lgkmcnt(8)\n"
    "v_mfma_f32_16x16x32_bf16 v[64:67], v[176:179], a[192:195], v[64:67]\n"
    "ds_read_b64_tr_b16 v[200:201], v227 offset:28672\n"
    "v_mfma_f32_16x16x32_bf16 v[64:67], v[180:183], a[196:199], v[64:67]\n"
    "buffer_load_dwordx4 a[184:187], v220, s[52:55], s74 offen offset:1024 sc1\n"
    "v_mfma_f32_16x16x32_bf16 v[68:71], v[176:179], a[200:203], v[68:71]\n"
    "s_lshl_b32 s75, 5, 10\n"
    "ds_read_b64_tr_b16 v[202:203], v227 offset:29696\n"
    "v_mfma_f32_16x16x32_bf16 v[68:71], v[180:183], a[204:207], v[68:71]\n"
    "v_mfma_f32_16x16x32_bf16 v[72:75], v[176:179], a[208:211], v[72:75]\n"
    "ds_read_b64_tr_b16 v[204:205], v227 offset:30720\n"
    "v_mfma_f32_16x16x32_bf16 v[72:75], v[180:183], a[212:215], v[72:75]\n"
    "buffer_load_dwordx4 a[164:167], v217, s[52:55], s75 offen offset:1024 sc1\n"
    "v_mfma_f32_16x16x32_bf16 v[76:79], v[176:179], a[216:219], v[76:79]\n"
    "ds_read_b64_tr_b16 v[206:207], v227 offset:31744\n"
    "v_mfma_f32_16x16x32_bf16 v[76:79], v[180:183], a[220:223], v[76:79]\n"
    "s_waitcnt lgkmcnt(8)\n"
    "v_mfma_f32_16x16x32_bf16 v[80:83], v[184:187], a[192:195], v[80:83]\n"
    "ds_read_b64_tr_b16 v[160:161], v230 offset:24576\n"
    "v_mfma_f32_16x16x32_bf16 v[80:83], v[188:191], a[196:199], v[80:83]\n"
    "buffer_load_dwordx4 a[172:175], v218, s[52:55], s75 offen offset:1024 sc1\n"
    "v_mfma_f32_16x16x32_bf16 v[84:87], v[184:187], a[200:203], v[84:87]\n"
    "ds_read_b64_tr_b16 v[162:163], v230 offset:25600\n"
    "v_mfma_f32_16x16x32_bf16 v[84:87], v[188:191], a[204:207], v[84:87]\n"
    "v_mfma_f32_16x16x32_bf16 v[88:91], v[184:187], a[208:211], v[88:91]\n"
    "ds_read_b64_tr_b16 v[164:165], v230 offset:26624\n"
    "v_mfma_f32_16x16x32_bf16 v[88:91], v[188:191], a[212:215], v[88:91]\n"
    "buffer_load_dwordx4 a[180:183], v219, s[52:55], s75 offen offset:1024 sc1\n"
    "v_mfma_f32_16x16x32_bf16 v[92:95], v[184:187], a[216:219], v[92:95]\n"
    "ds_read_b64_tr_b16 v[166:167], v230 offset:27648\n"
    "v_mfma_f32_16x16x32_bf16 v[92:95], v[188:191], a[220:223], v[92:95]\n"
    "s_waitcnt lgkmcnt(8)\n"
    "v_mfma_f32_16x16x32_bf16 v[32:35], v[192:195], a[224:227], v[32:35]\n"
    "ds_read_b64_tr_b16 v[168:169], v230 offset:28672\n"
    "v_mfma_f32_16x16x32_bf16 v[32:35], v[196:199], a[228:231], v[32:35]\n"
    "buffer_load_dwordx4 a[188:191], v220, s[52:55], s75 offen offset:1024 sc1\n"
    "v_mfma_f32_16x16x32_bf16 v[36:39], v[192:195], a[232:235], v[36:39]\n"
    "s_lshl_b32 s74, 9, 10\n"
    "ds_read_b64_tr_b16 v[170:171], v230 offset:29696\n"
    "v_mfma_f32_16x16x32_bf16 v[36:39], v[196:199], a[236:239], v[36:39]\n"
    "v_mfma_f32_16x16x32_bf16 v[40:43], v[192:195], a[240:243], v[40:43]\n"
    "ds_read_b64_tr_b16 v[172:173], v230 offset:30720\n"
    "v_mfma_f32_16x16x32_bf16 v[40:43], v[196:199], a[244:247], v[40:43]\n"
    "buffer_load_dwordx4 a[192:195], v217, s[52:55], s74 offen offset:1024 sc1\n"
    "v_mfma_f32_16x16x32_bf16 v[44:47], v[192:195], a[248:251], v[44:47]\n"
    "ds_read_b64_tr_b16 v[174:175], v230 offset:31744\n"
    "v_mfma_f32_16x16x32_bf16 v[44:47], v[196:199], a[252:255], v[44:47]\n"
    "s_waitcnt lgkmcnt(8)\n"
    "v_mfma_f32_16x16x32_bf16 v[48:51], v[200:203], a[224:227], v[48:51]\n"
    "ds_read_b64_tr_b16 v[176:177], v227 offset:32768\n"
    "v_mfma_f32_16x16x32_bf16 v[48:51], v[204:207], a[228:231], v[48:51]\n"
    "buffer_load_dwordx4 a[200:203], v218, s[52:55], s74 offen offset:1024 sc1\n"
    "v_mfma_f32_16x16x32_bf16 v[52:55], v[200:203], a[232:235], v[52:55]\n"
    "ds_read_b64_tr_b16 v[178:179], v227 offset:33792\n"
    "v_mfma_f32_16x16x32_bf16 v[52:55], v[204:207], a[236:239], v[52:55]\n"
    "v_mfma_f32_16x16x32_bf16 v[56:59], v[200:203], a[240:243], v[56:59]\n"
    "ds_read_b64_tr_b16 v[180:181], v227 offset:34816\n"
    "v_mfma_f32_16x16x32_bf16 v[56:59], v[204:207], a[244:247], v[56:59]\n"
    "buffer_load_dwordx4 a[208:211], v219, s[52:55], s74 offen offset:1024 sc1\n"
    "v_mfma_f32_16x16x32_bf16 v[60:63], v[200:203], a[248:251], v[60:63]\n"
    "ds_read_b64_tr_b16 v[182:183], v227 offset:35840\n"
    "v_mfma_f32_16x16x32_bf16 v[60:63], v[204:207], a[252:255], v[60:63]\n"
    "s_waitcnt lgkmcnt(8)\n"
    "v_mfma_f32_16x16x32_bf16 v[64:67], v[160:163], a[224:227], v[64:67]\n"
    "ds_read_b64_tr_b16 v[184:185], v227 offset:36864\n"
    "v_mfma_f32_16x16x32_bf16 v[64:67], v[164:167], a[228:231], v[64:67]\n"
    "buffer_load_dwordx4 a[216:219], v220, s[52:55], s74 offen offset:1024 sc1\n"
    "v_mfma_f32_16x16x32_bf16 v[68:71], v[160:163], a[232:235], v[68:71]\n"
    "s_lshl_b32 s75, 14, 10\n"
    "ds_read_b64_tr_b16 v[186:187], v227 offset:37888\n"
    "v_mfma_f32_16x16x32_bf16 v[68:71], v[164:167], a[236:239], v[68:71]\n"
    "v_mfma_f32_16x16x32_bf16 v[72:75], v[160:163], a[240:243], v[72:75]\n"
    "ds_read_b64_tr_b16 v[188:189], v227 offset:38912\n"
    "v_mfma_f32_16x16x32_bf16 v[72:75], v[164:167], a[244:247], v[72:75]\n"
    "buffer_load_dwordx4 a[196:199], v217, s[52:55], s75 offen offset:1024 sc1\n"
    "v_mfma_f32_16x16x32_bf16 v[76:79], v[160:163], a[248:251], v[76:79]\n"
    "ds_read_b64_tr_b16 v[190:191], v227 offset:39936\n"
    "v_mfma_f32_16x16x32_bf16 v[76:79], v[164:167], a[252:255], v[76:79]\n"
    "s_waitcnt lgkmcnt(8)\n"
    "v_mfma_f32_16x16x32_bf16 v[80:83], v[168:171], a[224:227], v[80:83]\n"
    "ds_read_b64_tr_b16 v[192:193], v230 offset:32768\n"
    "v_mfma_f32_16x16x32_bf16 v[80:83], v[172:175], a[228:231], v[80:83]\n"
    "buffer_load_dwordx4 a[204:207], v218, s[52:55], s75 offen offset:1024 sc1\n"
    "v_mfma_f32_16x16x32_bf16 v[84:87], v[168:171], a[232:235], v[84:87]\n"
    "ds_read_b64_tr_b16 v[194:195], v230 offset:33792\n"
    "v_mfma_f32_16x16x32_bf16 v[84:87], v[172:175], a[236:239], v[84:87]\n"
    "v_mfma_f32_16x16x32_bf16 v[88:91], v[168:171], a[240:243], v[88:91]\n"
    "ds_read_b64_tr_b16 v[196:197], v230 offset:34816\n"
    "v_mfma_f32_16x16x32_bf16 v[88:91], v[172:175], a[244:247], v[88:91]\n"
    "buffer_load_dwordx4 a[212:215], v219, s[52:55], s75 offen offset:1024 sc1\n"
    "v_mfma_f32_16x16x32_bf16 v[92:95], v[168:171], a[248:251], v[92:95]\n"
    "ds_read_b64_tr_b16 v[198:199], v230 offset:35840\n"
    "v_mfma_f32_16x16x32_bf16 v[92:95], v[172:175], a[252:255], v[92:95]\n"
    "s_waitcnt lgkmcnt(8)\n"
    "v_mfma_f32_16x16x32_bf16 v[32:35], v[176:179], v[0:3], v[32:35]\n"
    "ds_read_b64_tr_b16 v[200:201], v230 offset:36864\n"
    "v_mfma_f32_16x16x32_bf16 v[32:35], v[180:183], v[4:7], v[32:35]\n"
    "buffer_load_dwordx4 a[220:223], v220, s[52:55], s75 offen offset:1024 sc1\n"
    "v_mfma_f32_16x16x32_bf16 v[36:39], v[176:179], v[8:11], v[36:39]\n"
    "ds_read_b64_tr_b16 v[202:203], v230 offset:37888\n"
    "v_mfma_f32_16x16x32_bf16 v[36:39], v[180:183], v[12:15], v[36:39]\n"
    "v_mfma_f32_16x16x32_bf16 v[40:43], v[176:179], v[16:19], v[40:43]\n"
    "ds_read_b64_tr_b16 v[204:205], v230 offset:38912\n"
    "v_mfma_f32_16x16x32_bf16 v[40:43], v[180:183], v[20:23], v[40:43]\n"
    "global_load_dword v238, v239, %11 offset:3072 sc1\n"
    "v_mfma_f32_16x16x32_bf16 v[44:47], v[176:179], v[24:27], v[44:47]\n"
    "s_waitcnt vmcnt(49)\n"
    "ds_read_b64_tr_b16 v[206:207], v230 offset:39936\n"
    "v_mfma_f32_16x16x32_bf16 v[44:47], v[180:183], v[28:31], v[44:47]\n"
    "s_waitcnt lgkmcnt(8)\n"
    "s_barrier\n"
    "buffer_load_dwordx4 v209, s[40:43], s69 offen offset:1024 lds\n"
    "v_mfma_f32_16x16x32_bf16 v[48:51], v[184:187], v[0:3], v[48:51]\n"
    "ds_read_b64_tr_b16 a[0:1], v224 offset:0\n"
    "v_mfma_f32_16x16x32_bf16 v[48:51], v[188:191], v[4:7], v[48:51]\n"
    "v_mfma_f32_16x16x32_bf16 v[52:55], v[184:187], v[8:11], v[52:55]\n"
    "ds_read_b64_tr_b16 a[2:3], v224 offset:4096\n"
    "v_mfma_f32_16x16x32_bf16 v[52:55], v[188:191], v[12:15], v[52:55]\n"
    "v_max_f32 v32, 0, v32\n"
    "v_max_f32 v33, 0, v33\n"
    "v_max_f32 v34, 0, v34\n"
    "v_max_f32 v35, 0, v35\n"
    "v_mfma_f32_16x16x32_bf16 v[56:59], v[184:187], v[16:19], v[56:59]\n"
    "ds_read_b64_tr_b16 a[4:5], v224 offset:9216\n"
    "v_mul_f32 v32, v32, v48\n"
    "v_mul_f32 v33, v33, v49\n"
    "v_mul_f32 v34, v34, v50\n"
    "v_mfma_f32_16x16x32_bf16 v[56:59], v[188:191], v[20:23], v[56:59]\n"
    "v_mul_f32 v35, v35, v51\n"
    "v_cvt_pk_bf16_f32 v32, v32, v33\n"
    "v_cvt_pk_bf16_f32 v33, v34, v35\n"
    "v_max_f32 v36, 0, v36\n"
    "v_mfma_f32_16x16x32_bf16 v[60:63], v[184:187], v[24:27], v[60:63]\n"
    "ds_read_b64_tr_b16 a[6:7], v224 offset:13312\n"
    "v_max_f32 v37, 0, v37\n"
    "v_max_f32 v38, 0, v38\n"
    "v_max_f32 v39, 0, v39\n"
    "v_mfma_f32_16x16x32_bf16 v[60:63], v[188:191], v[28:31], v[60:63]\n"
    "s_waitcnt lgkmcnt(8)\n"
    "v_mul_f32 v36, v36, v52\n"
    "v_mul_f32 v37, v37, v53\n"
    "v_mul_f32 v38, v38, v54\n"
    "v_mul_f32 v39, v39, v55\n"
    "v_mfma_f32_16x16x32_bf16 v[64:67], v[192:195], v[0:3], v[64:67]\n"
    "ds_read_b64_tr_b16 a[8:9], v225 offset:0\n"
    "v_cvt_pk_bf16_f32 v36, v36, v37\n"
    "v_cvt_pk_bf16_f32 v37, v38, v39\n"
    "v_max_f32 v40, 0, v40\n"
    "v_mfma_f32_16x16x32_bf16 v[64:67], v[196:199], v[4:7], v[64:67]\n"
    "v_max_f32 v41, 0, v41\n"
    "v_max_f32 v42, 0, v42\n"
    "v_max_f32 v43, 0, v43\n"
    "v_mul_f32 v40, v40, v56\n"
    "v_mfma_f32_16x16x32_bf16 v[68:71], v[192:195], v[8:11], v[68:71]\n"
    "ds_read_b64_tr_b16 a[10:11], v225 offset:4096\n"
    "v_mul_f32 v41, v41, v57\n"
    "v_mul_f32 v42, v42, v58\n"
    "v_mul_f32 v43, v43, v59\n"
    "v_mfma_f32_16x16x32_bf16 v[68:71], v[196:199], v[12:15], v[68:71]\n"
    "v_cvt_pk_bf16_f32 v40, v40, v41\n"
    "v_cvt_pk_bf16_f32 v41, v42, v43\n"
    "v_max_f32 v44, 0, v44\n"
    "v_max_f32 v45, 0, v45\n"
    "v_mfma_f32_16x16x32_bf16 v[72:75], v[192:195], v[16:19], v[72:75]\n"
    "ds_read_b64_tr_b16 a[12:13], v225 offset:9216\n"
    "v_max_f32 v46, 0, v46\n"
    "v_max_f32 v47, 0, v47\n"
    "v_mul_f32 v44, v44, v60\n"
    "v_mfma_f32_16x16x32_bf16 v[72:75], v[196:199], v[20:23], v[72:75]\n"
    "v_mul_f32 v45, v45, v61\n"
    "v_mul_f32 v46, v46, v62\n"
    "v_mul_f32 v47, v47, v63\n"
    "v_cvt_pk_bf16_f32 v44, v44, v45\n"
    "v_mfma_f32_16x16x32_bf16 v[76:79], v[192:195], v[24:27], v[76:79]\n"
    "ds_read_b64_tr_b16 a[14:15], v225 offset:13312\n"
    "v_cvt_pk_bf16_f32 v45, v46, v47\n"
    "v_mfma_f32_16x16x32_bf16 v[76:79], v[196:199], v[28:31], v[76:79]\n"
    "s_waitcnt lgkmcnt(8)\n"
    "buffer_load_dwordx4 v210, s[40:43], s69 offen offset:2048 lds\n"
    "v_mfma_f32_16x16x32_bf16 v[80:83], v[200:203], v[0:3], v[80:83]\n"
    "ds_read_b64_tr_b16 a[16:17], v224 offset:256\n"
    "v_mfma_f32_16x16x32_bf16 v[80:83], v[204:207], v[4:7], v[80:83]\n"
    "v_mfma_f32_16x16x32_bf16 v[84:87], v[200:203], v[8:11], v[84:87]\n"
    "ds_read_b64_tr_b16 a[18:19], v224 offset:4352\n"
    "v_mfma_f32_16x16x32_bf16 v[84:87], v[204:207], v[12:15], v[84:87]\n"
    "v_max_f32 v64, 0, v64\n"
    "v_max_f32 v65, 0, v65\n"
    "v_max_f32 v66, 0, v66\n"
    "v_max_f32 v67, 0, v67\n"
    "v_mfma_f32_16x16x32_bf16 v[88:91], v[200:203], v[16:19], v[88:91]\n"
    "ds_read_b64_tr_b16 a[20:21], v224 offset:9472\n"
    "v_mul_f32 v64, v64, v80\n"
    "v_mul_f32 v65, v65, v81\n"
    "v_mul_f32 v66, v66, v82\n"
    "v_mfma_f32_16x16x32_bf16 v[88:91], v[204:207], v[20:23], v[88:91]\n"
    "v_mul_f32 v67, v67, v83\n"
    "v_cvt_pk_bf16_f32 v34, v64, v65\n"
    "v_cvt_pk_bf16_f32 v35, v66, v67\n"
    "v_max_f32 v68, 0, v68\n"
    "v_mfma_f32_16x16x32_bf16 v[92:95], v[200:203], v[24:27], v[92:95]\n"
    "ds_read_b64_tr_b16 a[22:23], v224 offset:13568\n"
    "v_max_f32 v69, 0, v69\n"
    "v_max_f32 v70, 0, v70\n"
    "v_max_f32 v71, 0, v71\n"
    "s_waitcnt vmcnt(2)\n"
    "v_cmp_eq_u32 vcc, %21, v238\n"
    "s_cmp_eq_u64 vcc, -1\n"
    "s_cbranch_scc1 PDB2\n"
    "s_mov_b32 s84, 0x40000\n"
    "PLB2:\n"
    "global_load_dword v238, v239, %11 offset:3072 sc1\n"
    "s_waitcnt vmcnt(0)\n"
    "v_cmp_eq_u32 vcc, %21, v238\n"
    "s_cmp_eq_u64 vcc, -1\n"
    "s_cbranch_scc1 PDB2\n"
    "s_sleep 1\n"
    "s_sub_u32 s84, s84, 1\n"
    "s_cmp_lg_u32 s84, 0\n"
    "s_cbranch_scc1 PLB2\n"
    "PDB2:\n"
    "v_mfma_f32_16x16x32_bf16 v[92:95], v[204:207], v[28:31], v[92:95]\n"
    "s_waitcnt lgkmcnt(8)\n"
    "s_waitcnt vmcnt(36)\n"
    "s_lshl_b32 s74, s81, 10\n"
    "buffer_store_dwordx4 v[32:35], v217, s[48:51], s64 offen offset:3072 sc1\n"
    "v_mul_f32 v68, v68, v84\n"
    "v_mul_f32 v69, v69, v85\n"
    "v_mul_f32 v70, v70, v86\n"
    "v_mul_f32 v71, v71, v87\n"
    "v_mfma_f32_16x16x32_bf16 v[112:115], a[0:3], a[64:67], 0\n"
    "ds_read_b64_tr_b16 a[24:25], v225 offset:256\n"
    "v_cvt_pk_bf16_f32 v38, v68, v69\n"
    "v_cvt_pk_bf16_f32 v39, v70, v71\n"
    "v_max_f32 v72, 0, v72\n"
    "v_mfma_f32_16x16x32_bf16 v[112:115], a[4:7], a[68:71], v[112:115]\n"
    "buffer_store_dwordx4 v[36:39], v218, s[48:51], s64 offen offset:3072 sc1\n"
    "v_max_f32 v73, 0, v73\n"
    "v_max_f32 v74, 0, v74\n"
    "v_max_f32 v75, 0, v75\n"
    "v_mul_f32 v72, v72, v88\n"
    "v_mfma_f32_16x16x32_bf16 v[116:119], a[0:3], a[72:75], 0\n"
    "ds_read_b64_tr_b16 a[26:27], v225 offset:4352\n"
    "v_mul_f32 v73, v73, v89\n"
    "v_mul_f32 v74, v74, v90\n"
    "v_mul_f32 v75, v75, v91\n"
    "v_mfma_f32_16x16x32_bf16 v[116:119], a[4:7], a[76:79], v[116:119]\n"
    "v_cvt_pk_bf16_f32 v42, v72, v73\n"
    "v_cvt_pk_bf16_f32 v43, v74, v75\n"
    "v_max_f32 v76, 0, v76\n"
    "v_max_f32 v77, 0, v77\n"
    "v_mfma_f32_16x16x32_bf16 v[120:123], a[0:3], a[80:83], 0\n"
    "ds_read_b64_tr_b16 a[28:29], v225 offset:9472\n"
    "v_max_f32 v78, 0, v78\n"
    "v_max_f32 v79, 0, v79\n"
    "v_mul_f32 v76, v76, v92\n"
    "v_mfma_f32_16x16x32_bf16 v[120:123], a[4:7], a[84:87], v[120:123]\n"
    "buffer_store_dwordx4 v[40:43], v219, s[48:51], s64 offen offset:3072 sc1\n"
    "v_mul_f32 v77, v77, v93\n"
    "v_mul_f32 v78, v78, v94\n"
    "v_mul_f32 v79, v79, v95\n"
    "v_cvt_pk_bf16_f32 v46, v76, v77\n"
    "v_mfma_f32_16x16x32_bf16 v[124:127], a[0:3], a[88:91], 0\n"
    "ds_read_b64_tr_b16 a[30:31], v225 offset:13568\n"
    "v_cvt_pk_bf16_f32 v47, v78, v79\n"
    "v_mfma_f32_16x16x32_bf16 v[124:127], a[4:7], a[92:95], v[124:127]\n"
    "s_waitcnt lgkmcnt(8)\n"
    "buffer_store_dwordx4 v[44:47], v220, s[48:51], s64 offen offset:3072 sc1\n"
    "v_mfma_f32_16x16x32_bf16 v[128:131], a[8:11], a[64:67], 0\n"
    "ds_read_b64_tr_b16 a[32:33], v224 offset:512\n"
    "v_mfma_f32_16x16x32_bf16 v[128:131], a[12:15], a[68:71], v[128:131]\n"
    "s_barrier\n"
    "buffer_load_dwordx4 a[224:227], v217, s[52:55], s74 offen offset:2048 sc1\n"
    "v_mfma_f32_16x16x32_bf16 v[132:135], a[8:11], a[72:75], 0\n"
    "ds_read_b64_tr_b16 a[34:35], v224 offset:4608\n"
    "v_mfma_f32_16x16x32_bf16 v[132:135], a[12:15], a[76:79], v[132:135]\n"
    "v_mfma_f32_16x16x32_bf16 v[136:139], a[8:11], a[80:83], 0\n"
    "ds_read_b64_tr_b16 a[36:37], v224 offset:9728\n"
    "v_mfma_f32_16x16x32_bf16 v[136:139], a[12:15], a[84:87], v[136:139]\n"
    "buffer_load_dwordx4 a[232:235], v218, s[52:55], s74 offen offset:2048 sc1\n"
    "v_mfma_f32_16x16x32_bf16 v[140:143], a[8:11], a[88:91], 0\n"
    "ds_read_b64_tr_b16 a[38:39], v224 offset:13824\n"
    "v_mfma_f32_16x16x32_bf16 v[140:143], a[12:15], a[92:95], v[140:143]\n"
    "s_waitcnt lgkmcnt(8)\n"
    "v_mfma_f32_16x16x32_bf16 v[144:147], a[16:19], a[64:67], 0\n"
    "ds_read_b64_tr_b16 a[40:41], v225 offset:512\n"
    "v_mfma_f32_16x16x32_bf16 v[144:147], a[20:23], a[68:71], v[144:147]\n"
    "buffer_load_dwordx4 a[240:243], v219, s[52:55], s74 offen offset:2048 sc1\n"
    "v_mfma_f32_16x16x32_bf16 v[148:151], a[16:19], a[72:75], 0\n"
    "ds_read_b64_tr_b16 a[42:43], v225 offset:4608\n"
    "v_mfma_f32_16x16x32_bf16 v[148:151], a[20:23], a[76:79], v[148:151]\n"
    "v_mfma_f32_16x16x32_bf16 v[152:155], a[16:19], a[80:83], 0\n"
    "ds_read_b64_tr_b16 a[44:45], v225 offset:9728\n"
    "v_mfma_f32_16x16x32_bf16 v[152:155], a[20:23], a[84:87], v[152:155]\n"
    "buffer_load_dwordx4 a[248:251], v220, s[52:55], s74 offen offset:2048 sc1\n"
    "v_mfma_f32_16x16x32_bf16 v[156:159], a[16:19], a[88:91], 0\n"
    "s_lshl_b32 s75, s82, 10\n"
    "ds_read_b64_tr_b16 a[46:47], v225 offset:13824\n"
    "v_mfma_f32_16x16x32_bf16 v[156:159], a[20:23], a[92:95], v[156:159]\n"
    "s_waitcnt lgkmcnt(8)\n"
    "v_mfma_f32_16x16x32_bf16 v[160:163], a[24:27], a[64:67], 0\n"
    "ds_read_b64_tr_b16 a[0:1], v224 offset:768\n"
    "v_mfma_f32_16x16x32_bf16 v[160:163], a[28:31], a[68:71], v[160:163]\n"
    "buffer_load_dwordx4 a[228:231], v217, s[52:55], s75 offen offset:2048 sc1\n"
    "v_mfma_f32_16x16x32_bf16 v[164:167], a[24:27], a[72:75], 0\n"
    "ds_read_b64_tr_b16 a[2:3], v224 offset:4864\n"
    "v_mfma_f32_16x16x32_bf16 v[164:167], a[28:31], a[76:79], v[164:167]\n"
    "v_mfma_f32_16x16x32_bf16 v[168:171], a[24:27], a[80:83], 0\n"
    "ds_read_b64_tr_b16 a[4:5], v224 offset:9984\n"
    "v_mfma_f32_16x16x32_bf16 v[168:171], a[28:31], a[84:87], v[168:171]\n"
    "buffer_load_dwordx4 a[236:239], v218, s[52:55], s75 offen offset:2048 sc1\n"
    "v_mfma_f32_16x16x32_bf16 v[172:175], a[24:27], a[88:91], 0\n"
    "ds_read_b64_tr_b16 a[6:7], v224 offset:14080\n"
    "v_mfma_f32_16x16x32_bf16 v[172:175], a[28:31], a[92:95], v[172:175]\n"
    "s_waitcnt lgkmcnt(8)\n"
    "v_add_u32 v224, 73728, v221\n"
    "v_mfma_f32_16x16x32_bf16 v[176:179], a[32:35], a[64:67], 0\n"
    "ds_read_b64_tr_b16 a[8:9], v225 offset:768\n"
    "v_mfma_f32_16x16x32_bf16 v[176:179], a[36:39], a[68:71], v[176:179]\n"
    "buffer_load_dwordx4 a[244:247], v219, s[52:55], s75 offen offset:2048 sc1\n"
    "v_mfma_f32_16x16x32_bf16 v[180:183], a[32:35], a[72:75], 0\n"
    "ds_read_b64_tr_b16 a[10:11], v225 offset:4864\n"
    "v_mfma_f32_16x16x32_bf16 v[180:183], a[36:39], a[76:79], v[180:183]\n"
    "v_mfma_f32_16x16x32_bf16 v[184:187], a[32:35], a[80:83], 0\n"
    "ds_read_b64_tr_b16 a[12:13], v225 offset:9984\n"
    "v_mfma_f32_16x16x32_bf16 v[184:187], a[36:39], a[84:87], v[184:187]\n"
    "buffer_load_dwordx4 a[252:255], v220, s[52:55], s75 offen offset:2048 sc1\n"
    "v_mfma_f32_16x16x32_bf16 v[188:191], a[32:35], a[88:91], 0\n"
    "ds_read_b64_tr_b16 a[14:15], v225 offset:14080\n"
    "v_mfma_f32_16x16x32_bf16 v[188:191], a[36:39], a[92:95], v[188:191]\n"
    "s_waitcnt lgkmcnt(8)\n"
    "v_add_u32 v225, 73728, v222\n"
    "v_mfma_f32_16x16x32_bf16 v[192:195], a[40:43], a[64:67], 0\n"
    "ds_read_b64_tr_b16 a[16:17], v226 offset:8192\n"
    "v_mfma_f32_16x16x32_bf16 v[192:195], a[44:47], a[68:71], v[192:195]\n"
    "buffer_load_dwordx4 v211, s[40:43], s69 offen offset:3072 lds\n"
    "v_mfma_f32_16x16x32_bf16 v[196:199], a[40:43], a[72:75], 0\n"
    "s_add_u32 m0, s71, 92160\n"
    "ds_read_b64_tr_b16 a[18:19], v226 offset:8704\n"
    "v_mfma_f32_16x16x32_bf16 v[196:199], a[44:47], a[76:79], v[196:199]\n"
    "v_mfma_f32_16x16x32_bf16 v[200:203], a[40:43], a[80:83], 0\n"
    "ds_read_b64_tr_b16 a[20:21], v226 offset:17408\n"
    "v_mfma_f32_16x16x32_bf16 v[200:203], a[44:47], a[84:87], v[200:203]\n"
    "s_mov_b64 exec, s[72:73]\n"
    "buffer_load_dwordx4 v212, s[40:43], s69 offen lds\n"
    "s_mov_b64 exec, -1\n"
    "v_mfma_f32_16x16x32_bf16 v[204:207], a[40:43], a[88:91], 0\n"
    "s_waitcnt vmcnt(59)\n"
    "ds_read_b64_tr_b16 a[22:23], v226 offset:17920\n"
    "v_mfma_f32_16x16x32_bf16 v[204:207], a[44:47], a[92:95], v[204:207]\n"
    "s_waitcnt lgkmcnt(8)\n"
    "s_barrier\n"
    "v_add_u32 v226, 73728, v223\n"
    "v_mfma_f32_16x16x32_bf16 v[0:3], a[0:3], a[64:67], 0\n"
    "s_cmp_eq_u32 s78, 0\n"
    "s_cselect_b32 s69, 0, 5\n"
    "s_cselect_b32 s66, 1, 1\n"
    "ds_read_b64_tr_b16 a[24:25], v224 offset:0\n"
    "v_mfma_f32_16x16x32_bf16 v[0:3], a[4:7], a[68:71], v[0:3]\n"
    "s_add_u32 s69, s69, s66\n"
    "v_mfma_f32_16x16x32_bf16 v[4:7], a[0:3], a[72:75], 0\n"
    "s_mul_i32 s69, s69, 36864\n"
    "ds_read_b64_tr_b16 a[26:27], v224 offset:4096\n"
    "v_mfma_f32_16x16x32_bf16 v[4:7], a[4:7], a[76:79], v[4:7]\n"
    "s_add_u32 s69, s69, s79\n"
    "v_mfma_f32_16x16x32_bf16 v[8:11], a[0:3], a[80:83], 0\n"
    "s_add_u32 m0, s70, 110592\n"
    "ds_read_b64_tr_b16 a[28:29], v224 offset:9216\n"
    "v_mfma_f32_16x16x32_bf16 v[8:11], a[4:7], a[84:87], v[8:11]\n"
    "buffer_load_dwordx4 v208, s[40:43], s69 offen offset:0 lds\n"
    "v_mfma_f32_16x16x32_bf16 v[12:15], a[0:3], a[88:91], 0\n"
    "ds_read_b64_tr_b16 a[30:31], v224 offset:13312\n"
    "v_mfma_f32_16x16x32_bf16 v[12:15], a[4:7], a[92:95], v[12:15]\n"
    "s_waitcnt lgkmcnt(8)\n"
    "v_mfma_f32_16x16x32_bf16 v[16:19], a[8:11], a[64:67], 0\n"
    "ds_read_b64_tr_b16 a[32:33], v225 offset:0\n"
    "v_mfma_f32_16x16x32_bf16 v[16:19], a[12:15], a[68:71], v[16:19]\n"
    "buffer_load_dwordx4 v209, s[40:43], s69 offen offset:1024 lds\n"
    "v_mfma_f32_16x16x32_bf16 v[20:23], a[8:11], a[72:75], 0\n"
    "ds_read_b64_tr_b16 a[34:35], v225 offset:4096\n"
    "v_mfma_f32_16x16x32_bf16 v[20:23], a[12:15], a[76:79], v[20:23]\n"
    "v_mfma_f32_16x16x32_bf16 v[24:27], a[8:11], a[80:83], 0\n"
    "ds_read_b64_tr_b16 a[36:37], v225 offset:9216\n"
    "v_mfma_f32_16x16x32_bf16 v[24:27], a[12:15], a[84:87], v[24:27]\n"
    "buffer_load_dwordx4 v210, s[40:43], s69 offen offset:2048 lds\n"
    "v_mfma_f32_16x16x32_bf16 v[28:31], a[8:11], a[88:91], 0\n"
    "ds_read_b64_tr_b16 a[38:39], v225 offset:13312\n"
    "v_mfma_f32_16x16x32_bf16 v[28:31], a[12:15], a[92:95], v[28:31]\n"
    "s_waitcnt lgkmcnt(8)\n"
    "v_mfma_f32_16x16x32_bf16 v[48:51], a[16:19], a[64:67], 0\n"
    "ds_read_b64_tr_b16 a[40:41], v224 offset:256\n"
    "v_mfma_f32_16x16x32_bf16 v[48:51], a[20:23], a[68:71], v[48:51]\n"
    "v_mfma_f32_16x16x32_bf16 v[52:55], a[16:19], a[72:75], 0\n"
    "ds_read_b64_tr_b16 a[42:43], v224 offset:4352\n"
    "v_mfma_f32_16x16x32_bf16 v[52:55], a[20:23], a[76:79], v[52:55]\n"
    "s_waitcnt vmcnt(13)\n"
    "v_mov_b32 v237, %21\n"
    "s_mov_b64 exec, 1\n"
    "global_store_dword v235, v237, %11 offset:3584 sc1\n"
    "s_mov_b64 exec, -1\n"
    "v_mfma_f32_16x16x32_bf16 v[56:59], a[16:19], a[80:83], 0\n"
    "ds_read_b64_tr_b16 a[44:45], v224 offset:9472\n"
    "v_mfma_f32_16x16x32_bf16 v[56:59], a[20:23], a[84:87], v[56:59]\n"
    "buffer_load_dwordx4 v211, s[40:43], s69 offen offset:3072 lds\n"
    "v_mfma_f32_16x16x32_bf16 v[60:63], a[16:19], a[88:91], 0\n"
    "s_add_u32 m0, s71, 110592\n"
    "ds_read_b64_tr_b16 a[46:47], v224 offset:13568\n"
    "v_mfma_f32_16x16x32_bf16 v[60:63], a[20:23], a[92:95], v[60:63]\n"
    "s_waitcnt lgkmcnt(8)\n"
    "v_mfma_f32_16x16x32_bf16 v[112:115], a[24:27], v[96:99], v[112:115]\n"
    "ds_read_b64_tr_b16 a[0:1], v225 offset:256\n"
    "v_mfma_f32_16x16x32_bf16 v[112:115], a[28:31], v[32:35], v[112:115]\n"
    "s_mov_b64 exec, s[72:73]\n"
    "buffer_load_dwordx4 v212, s[40:43], s69 offen lds\n"
    "s_mov_b64 exec, -1\n"
    "v_mfma_f32_16x16x32_bf16 v[116:119], a[24:27], v[100:103], v[116:119]\n"
    "s_cmp_eq_u32 s78, 0\n"
    "s_cselect_b32 s69, 9, 14\n"
    "s_cselect_b32 s66, 1, 1\n"
    "ds_read_b64_tr_b16 a[2:3], v225 offset:4352\n"
    "v_mfma_f32_16x16x32_bf16 v[116:119], a[28:31], v[36:39], v[116:119]\n"
    "s_add_u32 s69, s69, s66\n"
    "v_mfma_f32_16x16x32_bf16 v[120:123], a[24:27], v[104:107], v[120:123]\n"
    "s_mul_i32 s69, s69, 36864\n"
    "ds_read_b64_tr_b16 a[4:5], v225 offset:9472\n"
    "v_mfma_f32_16x16x32_bf16 v[120:123], a[28:31], v[40:43], v[120:123]\n"
    "s_add_u32 s69, s69, s79\n"
    "v_mfma_f32_16x16x32_bf16 v[124:127], a[24:27], v[108:111], v[124:127]\n"
    "s_add_u32 m0, s70, 129024\n"
    "ds_read_b64_tr_b16 a[6:7], v225 offset:13568\n"
    "v_mfma_f32_16x16x32_bf16 v[124:127], a[28:31], v[44:47], v[124:127]\n"
    "s_waitcnt lgkmcnt(8)\n"
    "buffer_load_dwordx4 v208, s[40:43], s69 offen offset:0 lds\n"
    "v_mfma_f32_16x16x32_bf16 v[128:131], a[32:35], v[96:99], v[128:131]\n"
    "ds_read_b64_tr_b16 a[8:9], v224 offset:512\n"
    "v_mfma_f32_16x16x32_bf16 v[128:131], a[36:39], v[32:35], v[128:131]\n"
    "v_mfma_f32_16x16x32_bf16 v[132:135], a[32:35], v[100:103], v[132:135]\n"
    "ds_read_b64_tr_b16 a[10:11], v224 offset:4608\n"
    "v_mfma_f32_16x16x32_bf16 v[132:135], a[36:39], v[36:39], v[132:135]\n"
    "buffer_load_dwordx4 v209, s[40:43], s69 offen offset:1024 lds\n"
    "v_mfma_f32_16x16x32_bf16 v[136:139], a[32:35], v[104:107], v[136:139]\n"
    "ds_read_b64_tr_b16 a[12:13], v224 offset:9728\n"
    "v_mfma_f32_16x16x32_bf16 v[136:139], a[36:39], v[40:43], v[136:139]\n"
    "v_mfma_f32_16x16x32_bf16 v[140:143], a[32:35], v[108:111], v[140:143]\n"
    "ds_read_b64_tr_b16 a[14:15], v224 offset:13824\n"
    "v_mfma_f32_16x16x32_bf16 v[140:143], a[36:39], v[44:47], v[140:143]\n"
    "s_waitcnt lgkmcnt(8)\n"
    "buffer_load_dwordx4 v210, s[40:43], s69 offen offset:2048 lds\n"
    "v_mfma_f32_16x16x32_bf16 v[144:147], a[40:43], v[96:99], v[144:147]\n"
    "ds_read_b64_tr_b16 a[16:17], v225 offset:512\n"
    "v_mfma_f32_16x16x32_bf16 v[144:147], a[44:47], v[32:35], v[144:147]\n"
    "v_mfma_f32_16x16x32_bf16 v[148:151], a[40:43], v[100:103], v[148:151]\n"
    "ds_read_b64_tr_b16 a[18:19], v225 offset:4608\n"
    "v_mfma_f32_16x16x32_bf16 v[148:151], a[44:47], v[36:39], v[148:151]\n"
    "buffer_load_dwordx4 v211, s[40:43], s69 offen offset:3072 lds\n"
    "v_mfma_f32_16x16x32_bf16 v[152:155], a[40:43], v[104:107], v[152:155]\n"
    "s_add_u32 m0, s71, 129024\n"
    "ds_read_b64_tr_b16 a[20:21], v225 offset:9728\n"
    "v_mfma_f32_16x16x32_bf16 v[152:155], a[44:47], v[40:43], v[152:155]\n"
    "v_mfma_f32_16x16x32_bf16 v[156:159], a[40:43], v[108:111], v[156:159]\n"
    "ds_read_b64_tr_b16 a[22:23], v225 offset:13824\n"
    "v_mfma_f32_16x16x32_bf16 v[156:159], a[44:47], v[44:47], v[156:159]\n"
    "s_waitcnt lgkmcnt(8)\n"
    "s_mov_b64 exec, s[72:73]\n"
    "buffer_load_dwordx4 v212, s[40:43], s69 offen lds\n"
    "s_mov_b64 exec, -1\n"
    "s_cmp_eq_u32 s78, 0\n"
    "s_cselect_b32 s69, 0, 5\n"
    "s_cselect_b32 s66, 0, 0\n"
    "s_add_u32 s69, s69, s66\n"
    "s_mul_i32 s69, s69, 36864\n"
    "s_add_u32 s69, s69, s79\n"
    "s_add_u32 m0, s70, 36864\n"
    "s_nop 0\n"
    "buffer_load_dwordx4 v208, s[40:43], s69 offen offset:0 lds\n"
    "buffer_load_dwordx4 v209, s[40:43], s69 offen offset:1024 lds\n"
    "buffer_load_dwordx4 v210, s[40:43], s69 offen offset:2048 lds\n"
    "buffer_load_dwordx4 v211, s[40:43], s69 offen offset:3072 lds\n"
    "s_add_u32 m0, s71, 36864\n"
    "s_mov_b64 exec, s[72:73]\n"
    "buffer_load_dwordx4 v212, s[40:43], s69 offen lds\n"
    "s_mov_b64 exec, -1\n"
    "v_mfma_f32_16x16x32_bf16 v[160:163], a[0:3], v[96:99], v[160:163]\n"
    "s_cmp_eq_u32 s78, 0\n"
    "s_cselect_b32 s69, s81, s82\n"
    "s_cselect_b32 s66, 2, 2\n"
    "ds_read_b64_tr_b16 a[24:25], v224 offset:768\n"
    "v_mfma_f32_16x16x32_bf16 v[160:163], a[4:7], v[32:35], v[160:163]\n"
    "s_add_u32 s69, s69, s66\n"
    "v_mfma_f32_16x16x32_bf16 v[164:167], a[0:3], v[100:103], v[164:167]\n"
    "s_mul_i32 s69, s69, 36864\n"
    "ds_read_b64_tr_b16 a[26:27], v224 offset:4864\n"
    "v_mfma_f32_16x16x32_bf16 v[164:167], a[4:7], v[36:39], v[164:167]\n"
    "s_add_u32 s69, s69, s79\n"
    "v_mfma_f32_16x16x32_bf16 v[168:171], a[0:3], v[104:107], v[168:171]\n"
    "s_add_u32 m0, s70, 0\n"
    "ds_read_b64_tr_b16 a[28:29], v224 offset:9984\n"
    "v_mfma_f32_16x16x32_bf16 v[168:171], a[4:7], v[40:43], v[168:171]\n"
    "buffer_load_dwordx4 v208, s[40:43], s69 offen offset:0 lds\n"
    "v_mfma_f32_16x16x32_bf16 v[172:175], a[0:3], v[108:111], v[172:175]\n"
    "ds_read_b64_tr_b16 a[30:31], v224 offset:14080\n"
    "v_mfma_f32_16x16x32_bf16 v[172:175], a[4:7], v[44:47], v[172:175]\n"
    "s_waitcnt lgkmcnt(8)\n"
    "v_add_u32 v224, 36864, v221\n"
    "v_mfma_f32_16x16x32_bf16 v[176:179], a[8:11], v[96:99], v[176:179]\n"
    "ds_read_b64_tr_b16 a[32:33], v225 offset:768\n"
    "v_mfma_f32_16x16x32_bf16 v[176:179], a[12:15], v[32:35], v[176:179]\n"
    "buffer_load_dwordx4 v209, s[40:43], s69 offen offset:1024 lds\n"
    "v_mfma_f32_16x16x32_bf16 v[180:183], a[8:11], v[100:103], v[180:183]\n"
    "ds_read_b64_tr_b16 a[34:35], v225 offset:4864\n"
    "v_mfma_f32_16x16x32_bf16 v[180:183], a[12:15], v[36:39], v[180:183]\n"
    "v_mfma_f32_16x16x32_bf16 v[184:187], a[8:11], v[104:107], v[184:187]\n"
    "ds_read_b64_tr_b16 a[36:37], v225 offset:9984\n"
    "v_mfma_f32_16x16x32_bf16 v[184:187], a[12:15], v[40:43], v[184:187]\n"
    "buffer_load_dwordx4 v210, s[40:43], s69 offen offset:2048 lds\n"
    "v_mfma_f32_16x16x32_bf16 v[188:191], a[8:11], v[108:111], v[188:191]\n"
    "ds_read_b64_tr_b16 a[38:39], v225 offset:14080\n"
    "v_mfma_f32_16x16x32_bf16 v[188:191], a[12:15], v[44:47], v[188:191]\n"
    "s_waitcnt lgkmcnt(8)\n"
    "v_add_u32 v225, 36864, v222\n"
    "v_mfma_f32_16x16x32_bf16 v[192:195], a[16:19], v[96:99], v[192:195]\n"
    "ds_read_b64_tr_b16 a[40:41], v226 offset:8192\n"
    "v_mfma_f32_16x16x32_bf16 v[192:195], a[20:23], v[32:35], v[192:195]\n"
    "buffer_load_dwordx4 v211, s[40:43], s69 offen offset:3072 lds\n"
    "v_mfma_f32_16x16x32_bf16 v[196:199], a[16:19], v[100:103], v[196:199]\n"
    "s_add_u32 m0, s71, 0\n"
    "ds_read_b64_tr_b16 a[42:43], v226 offset:8704\n"
    "v_mfma_f32_16x16x32_bf16 v[196:199], a[20:23], v[36:39], v[196:199]\n"
    "v_mfma_f32_16x16x32_bf16 v[200:203], a[16:19], v[104:107], v[200:203]\n"
    "ds_read_b64_tr_b16 a[44:45], v226 offset:17408\n"
    "v_mfma_f32_16x16x32_bf16 v[200:203], a[20:23], v[40:43], v[200:203]\n"
    "s_mov_b64 exec, s[72:73]\n"
    "buffer_load_dwordx4 v212, s[40:43], s69 offen lds\n"
    "s_mov_b64 exec, -1\n"
    "v_mfma_f32_16x16x32_bf16 v[204:207], a[16:19], v[108:111], v[204:207]\n"
    "s_waitcnt vmcnt(5)\n"
    "ds_read_b64_tr_b16 a[46:47], v226 offset:17920\n"
    "v_mfma_f32_16x16x32_bf16 v[204:207], a[20:23], v[44:47], v[204:207]\n"
    "s_waitcnt lgkmcnt(8)\n"
    "s_barrier\n"
    "v_add_u32 v226, 36864, v223\n"
    "v_mfma_f32_16x16x32_bf16 v[0:3], a[24:27], v[96:99], v[0:3]\n"
    "s_cmp_eq_u32 s78, 0\n"
    "s_cselect_b32 s69, s83, s81\n"
    "s_cselect_b32 s66, 2, 3\n"
    "ds_read_b64_tr_b16 a[0:1], v224 offset:0\n"
    "v_mfma_f32_16x16x32_bf16 v[0:3], a[28:31], v[32:35], v[0:3]\n"
    "s_add_u32 s69, s69, s66\n"
    "v_mfma_f32_16x16x32_bf16 v[4:7], a[24:27], v[100:103], v[4:7]\n"
    "s_mul_i32 s69, s69, 36864\n"
    "ds_read_b64_tr_b16 a[2:3], v224 offset:4096\n"
    "v_mfma_f32_16x16x32_bf16 v[4:7], a[28:31], v[36:39], v[4:7]\n"
    "s_add_u32 s69, s69, s79\n"
    "v_mfma_f32_16x16x32_bf16 v[8:11], a[24:27], v[104:107], v[8:11]\n"
    "s_add_u32 m0, s70, 18432\n"
    "ds_read_b64_tr_b16 a[4:5], v224 offset:9216\n"
    "v_mfma_f32_16x16x32_bf16 v[8:11], a[28:31], v[40:43], v[8:11]\n"
    "buffer_load_dwordx4 v208, s[40:43], s69 offen offset:0 lds\n"
    "v_mfma_f32_16x16x32_bf16 v[12:15], a[24:27], v[108:111], v[12:15]\n"
    "ds_read_b64_tr_b16 a[6:7], v224 offset:13312\n"
    "v_mfma_f32_16x16x32_bf16 v[12:15], a[28:31], v[44:47], v[12:15]\n"
    "s_waitcnt lgkmcnt(8)\n"
    "v_mfma_f32_16x16x32_bf16 v[16:19], a[32:35], v[96:99], v[16:19]\n"
    "ds_read_b64_tr_b16 a[8:9], v225 offset:0\n"
    "v_mfma_f32_16x16x32_bf16 v[16:19], a[36:39], v[32:35], v[16:19]\n"
    "buffer_load_dwordx4 v209, s[40:43], s69 offen offset:1024 lds\n"
    "v_mfma_f32_16x16x32_bf16 v[20:23], a[32:35], v[100:103], v[20:23]\n"
    "ds_read_b64_tr_b16 a[10:11], v225 offset:4096\n"
    "v_mfma_f32_16x16x32_bf16 v[20:23], a[36:39], v[36:39], v[20:23]\n"
    "v_mfma_f32_16x16x32_bf16 v[24:27], a[32:35], v[104:107], v[24:27]\n"
    "ds_read_b64_tr_b16 a[12:13], v225 offset:9216\n"
    "v_mfma_f32_16x16x32_bf16 v[24:27], a[36:39], v[40:43], v[24:27]\n"
    "buffer_load_dwordx4 v210, s[40:43], s69 offen offset:2048 lds\n"
    "v_mfma_f32_16x16x32_bf16 v[28:31], a[32:35], v[108:111], v[28:31]\n"
    "ds_read_b64_tr_b16 a[14:15], v225 offset:13312\n"
    "v_mfma_f32_16x16x32_bf16 v[28:31], a[36:39], v[44:47], v[28:31]\n"
    "s_waitcnt lgkmcnt(8)\n"
    "v_mfma_f32_16x16x32_bf16 v[48:51], a[40:43], v[96:99], v[48:51]\n"
    "ds_read_b64_tr_b16 a[16:17], v224 offset:256\n"
    "v_mfma_f32_16x16x32_bf16 v[48:51], a[44:47], v[32:35], v[48:51]\n"
    "buffer_load_dwordx4 v211, s[40:43], s69 offen offset:3072 lds\n"
    "v_mfma_f32_16x16x32_bf16 v[52:55], a[40:43], v[100:103], v[52:55]\n"
    "s_add_u32 m0, s71, 18432\n"
    "ds_read_b64_tr_b16 a[18:19], v224 offset:4352\n"
    "v_mfma_f32_16x16x32_bf16 v[52:55], a[44:47], v[36:39], v[52:55]\n"
    "v_mfma_f32_16x16x32_bf16 v[56:59], a[40:43], v[104:107], v[56:59]\n"
    "ds_read_b64_tr_b16 a[20:21], v224 offset:9472\n"
    "v_mfma_f32_16x16x32_bf16 v[56:59], a[44:47], v[40:43], v[56:59]\n"
    "s_mov_b64 exec, s[72:73]\n"
    "buffer_load_dwordx4 v212, s[40:43], s69 offen lds\n"
    "s_mov_b64 exec, -1\n"
    "v_mfma_f32_16x16x32_bf16 v[60:63], a[40:43], v[108:111], v[60:63]\n"
    "ds_read_b64_tr_b16 a[22:23], v224 offset:13568\n"
    "v_mfma_f32_16x16x32_bf16 v[60:63], a[44:47], v[44:47], v[60:63]\n"
    "s_waitcnt lgkmcnt(8)\n"
    "s_waitcnt vmcnt(63)\n"
    "v_mfma_f32_16x16x32_bf16 v[112:115], a[0:3], a[96:99], v[112:115]\n"
    "ds_read_b64_tr_b16 a[24:25], v225 offset:256\n"
    "v_mfma_f32_16x16x32_bf16 v[112:115], a[4:7], a[100:103], v[112:115]\n"
    "v_mfma_f32_16x16x32_bf16 v[116:119], a[0:3], a[104:107], v[116:119]\n"
    "ds_read_b64_tr_b16 a[26:27], v225 offset:4352\n"
    "v_mfma_f32_16x16x32_bf16 v[116:119], a[4:7], a[108:111], v[116:119]\n"
    "v_mfma_f32_16x16x32_bf16 v[120:123], a[0:3], a[112:115], v[120:123]\n"
    "ds_read_b64_tr_b16 a[28:29], v225 offset:9472\n"
    "v_mfma_f32_16x16x32_bf16 v[120:123], a[4:7], a[116:119], v[120:123]\n"
    "v_mfma_f32_16x16x32_bf16 v[124:127], a[0:3], a[120:123], v[124:127]\n"
    "ds_read_b64_tr_b16 a[30:31], v225 offset:13568\n"
    "v_mfma_f32_16x16x32_bf16 v[124:127], a[4:7], a[124:127], v[124:127]\n"
    "s_waitcnt lgkmcnt(8)\n"
    "v_mfma_f32_16x16x32_bf16 v[128:131], a[8:11], a[96:99], v[128:131]\n"
    "ds_read_b64_tr_b16 a[32:33], v224 offset:512\n"
    "v_mfma_f32_16x16x32_bf16 v[128:131], a[12:15], a[100:103], v[128:131]\n"
    "v_mfma_f32_16x16x32_bf16 v[132:135], a[8:11], a[104:107], v[132:135]\n"
    "ds_read_b64_tr_b16 a[34:35], v224 offset:4608\n"
    "v_mfma_f32_16x16x32_bf16 v[132:135], a[12:15], a[108:111], v[132:135]\n"
    "v_mfma_f32_16x16x32_bf16 v[136:139], a[8:11], a[112:115], v[136:139]\n"
    "ds_read_b64_tr_b16 a[36:37], v224 offset:9728\n"
    "v_mfma_f32_16x16x32_bf16 v[136:139], a[12:15], a[116:119], v[136:139]\n"
    "v_mfma_f32_16x16x32_bf16 v[140:143], a[8:11], a[120:123], v[140:143]\n"
    "ds_read_b64_tr_b16 a[38:39], v224 offset:13824\n"
    "v_mfma_f32_16x16x32_bf16 v[140:143], a[12:15], a[124:127], v[140:143]\n"
    "s_waitcnt lgkmcnt(8)\n"
    "v_mfma_f32_16x16x32_bf16 v[144:147], a[16:19], a[96:99], v[144:147]\n"
    "ds_read_b64_tr_b16 a[40:41], v225 offset:512\n"
    "v_mfma_f32_16x16x32_bf16 v[144:147], a[20:23], a[100:103], v[144:147]\n"
    "v_mfma_f32_16x16x32_bf16 v[148:151], a[16:19], a[104:107], v[148:151]\n"
    "ds_read_b64_tr_b16 a[42:43], v225 offset:4608\n"
    "v_mfma_f32_16x16x32_bf16 v[148:151], a[20:23], a[108:111], v[148:151]\n"
    "v_mfma_f32_16x16x32_bf16 v[152:155], a[16:19], a[112:115], v[152:155]\n"
    "ds_read_b64_tr_b16 a[44:45], v225 offset:9728\n"
    "v_mfma_f32_16x16x32_bf16 v[152:155], a[20:23], a[116:119], v[152:155]\n"
    "v_mfma_f32_16x16x32_bf16 v[156:159], a[16:19], a[120:123], v[156:159]\n"
    "ds_read_b64_tr_b16 a[46:47], v225 offset:13824\n"
    "v_mfma_f32_16x16x32_bf16 v[156:159], a[20:23], a[124:127], v[156:159]\n"
    "s_waitcnt lgkmcnt(8)\n"
    "global_load_dword v238, v239, %11 offset:3584 sc1\n"
    "v_mfma_f32_16x16x32_bf16 v[160:163], a[24:27], a[96:99], v[160:163]\n"
    "ds_read_b64_tr_b16 a[0:1], v224 offset:768\n"
    "v_mfma_f32_16x16x32_bf16 v[160:163], a[28:31], a[100:103], v[160:163]\n"
    "v_mfma_f32_16x16x32_bf16 v[164:167], a[24:27], a[104:107], v[164:167]\n"
    "ds_read_b64_tr_b16 a[2:3], v224 offset:4864\n"
    "v_mfma_f32_16x16x32_bf16 v[164:167], a[28:31], a[108:111], v[164:167]\n"
    "v_mfma_f32_16x16x32_bf16 v[168:171], a[24:27], a[112:115], v[168:171]\n"
    "ds_read_b64_tr_b16 a[4:5], v224 offset:9984\n"
    "v_mfma_f32_16x16x32_bf16 v[168:171], a[28:31], a[116:119], v[168:171]\n"
    "v_mfma_f32_16x16x32_bf16 v[172:175], a[24:27], a[120:123], v[172:175]\n"
    "ds_read_b64_tr_b16 a[6:7], v224 offset:14080\n"
    "v_mfma_f32_16x16x32_bf16 v[172:175], a[28:31], a[124:127], v[172:175]\n"
    "s_waitcnt lgkmcnt(8)\n"
    "v_add_u32 v224, 92160, v221\n"
    "v_mfma_f32_16x16x32_bf16 v[176:179], a[32:35], a[96:99], v[176:179]\n"
    "ds_read_b64_tr_b16 a[8:9], v225 offset:768\n"
    "v_mfma_f32_16x16x32_bf16 v[176:179], a[36:39], a[100:103], v[176:179]\n"
    "v_mfma_f32_16x16x32_bf16 v[180:183], a[32:35], a[104:107], v[180:183]\n"
    "ds_read_b64_tr_b16 a[10:11], v225 offset:4864\n"
    "v_mfma_f32_16x16x32_bf16 v[180:183], a[36:39], a[108:111], v[180:183]\n"
    "v_mfma_f32_16x16x32_bf16 v[184:187], a[32:35], a[112:115], v[184:187]\n"
    "ds_read_b64_tr_b16 a[12:13], v225 offset:9984\n"
    "v_mfma_f32_16x16x32_bf16 v[184:187], a[36:39], a[116:119], v[184:187]\n"
    "v_mfma_f32_16x16x32_bf16 v[188:191], a[32:35], a[120:123], v[188:191]\n"
    "ds_read_b64_tr_b16 a[14:15], v225 offset:14080\n"
    "v_mfma_f32_16x16x32_bf16 v[188:191], a[36:39], a[124:127], v[188:191]\n"
    "s_waitcnt lgkmcnt(8)\n"
    "v_add_u32 v225, 92160, v222\n"
    "v_mfma_f32_16x16x32_bf16 v[192:195], a[40:43], a[96:99], v[192:195]\n"
    "ds_read_b64_tr_b16 a[16:17], v226 offset:8192\n"
    "v_mfma_f32_16x16x32_bf16 v[192:195], a[44:47], a[100:103], v[192:195]\n"
    "v_mfma_f32_16x16x32_bf16 v[196:199], a[40:43], a[104:107], v[196:199]\n"
    "ds_read_b64_tr_b16 a[18:19], v226 offset:8704\n"
    "v_mfma_f32_16x16x32_bf16 v[196:199], a[44:47], a[108:111], v[196:199]\n"
    "v_mfma_f32_16x16x32_bf16 v[200:203], a[40:43], a[112:115], v[200:203]\n"
    "ds_read_b64_tr_b16 a[20:21], v226 offset:17408\n"
    "v_mfma_f32_16x16x32_bf16 v[200:203], a[44:47], a[116:119], v[200:203]\n"
    "v_mfma_f32_16x16x32_bf16 v[204:207], a[40:43], a[120:123], v[204:207]\n"
    "s_waitcnt vmcnt(16)\n"
    "ds_read_b64_tr_b16 a[22:23], v226 offset:17920\n"
    "v_mfma_f32_16x16x32_bf16 v[204:207], a[44:47], a[124:127], v[204:207]\n"
    "s_waitcnt lgkmcnt(8)\n"
    "s_barrier\n"
    "v_add_u32 v226, 92160, v223\n"
    "v_mfma_f32_16x16x32_bf16 v[0:3], a[0:3], a[96:99], v[0:3]\n"
    "ds_read_b64_tr_b16 a[24:25], v226 offset:8192\n"
    "v_mfma_f32_16x16x32_bf16 v[0:3], a[4:7], a[100:103], v[0:3]\n"
    "v_mfma_f32_16x16x32_bf16 v[4:7], a[0:3], a[104:107], v[4:7]\n"
    "ds_read_b64_tr_b16 a[26:27], v226 offset:8704\n"
    "v_mfma_f32_16x16x32_bf16 v[4:7], a[4:7], a[108:111], v[4:7]\n"
    "v_mfma_f32_16x16x32_bf16 v[8:11], a[0:3], a[112:115], v[8:11]\n"
    "ds_read_b64_tr_b16 a[28:29], v226 offset:17408\n"
    "v_mfma_f32_16x16x32_bf16 v[8:11], a[4:7], a[116:119], v[8:11]\n"
    "s_waitcnt vmcnt(0)\n"
    "v_cmp_eq_u32 vcc, %21, v238\n"
    "s_cmp_eq_u64 vcc, -1\n"
    "s_cbranch_scc1 PDB3\n"
    "s_mov_b32 s84, 0x40000\n"
    "PLB3:\n"
    "global_load_dword v238, v239, %11 offset:3584 sc1\n"
    "s_waitcnt vmcnt(0)\n"
    "v_cmp_eq_u32 vcc, %21, v238\n"
    "s_cmp_eq_u64 vcc, -1\n"
    "s_cbranch_scc1 PDB3\n"
    "s_sleep 1\n"
    "s_sub_u32 s84, s84, 1\n"
    "s_cmp_lg_u32 s84, 0\n"
    "s_cbranch_scc1 PLB3\n"
    "PDB3:\n"
    "v_mfma_f32_16x16x32_bf16 v[12:15], a[0:3], a[120:123], v[12:15]\n"
    "s_lshl_b32 s74, s83, 10\n"
    "ds_read_b64_tr_b16 a[30:31], v226 offset:17920\n"
    "v_mfma_f32_16x16x32_bf16 v[12:15], a[4:7], a[124:127], v[12:15]\n"
    "s_waitcnt lgkmcnt(8)\n"
    "buffer_load_dwordx4 a[64:67], v217, s[52:55], s74 offen offset:2048 sc1\n"
    "v_mfma_f32_16x16x32_bf16 v[16:19], a[8:11], a[96:99], v[16:19]\n"
    "ds_read_b64_tr_b16 a[32:33], v224 offset:0\n"
    "v_mfma_f32_16x16x32_bf16 v[16:19], a[12:15], a[100:103], v[16:19]\n"
    "v_mfma_f32_16x16x32_bf16 v[20:23], a[8:11], a[104:107], v[20:23]\n"
    "ds_read_b64_tr_b16 a[34:35], v224 offset:4096\n"
    "v_mfma_f32_16x16x32_bf16 v[20:23], a[12:15], a[108:111], v[20:23]\n"
    "buffer_load_dwordx4 a[72:75], v218, s[52:55], s74 offen offset:2048 sc1\n"
    "v_mfma_f32_16x16x32_bf16 v[24:27], a[8:11], a[112:115], v[24:27]\n"
    "ds_read_b64_tr_b16 a[36:37], v224 offset:9216\n"
    "v_mfma_f32_16x16x32_bf16 v[24:27], a[12:15], a[116:119], v[24:27]\n"
    "v_mfma_f32_16x16x32_bf16 v[28:31], a[8:11], a[120:123], v[28:31]\n"
    "ds_read_b64_tr_b16 a[38:39], v224 offset:13312\n"
    "v_mfma_f32_16x16x32_bf16 v[28:31], a[12:15], a[124:127], v[28:31]\n"
    "s_waitcnt lgkmcnt(8)\n"
    "buffer_load_dwordx4 a[80:83], v219, s[52:55], s74 offen offset:2048 sc1\n"
    "v_mfma_f32_16x16x32_bf16 v[48:51], a[16:19], a[96:99], v[48:51]\n"
    "ds_read_b64_tr_b16 a[40:41], v225 offset:0\n"
    "v_mfma_f32_16x16x32_bf16 v[48:51], a[20:23], a[100:103], v[48:51]\n"
    "v_mfma_f32_16x16x32_bf16 v[52:55], a[16:19], a[104:107], v[52:55]\n"
    "ds_read_b64_tr_b16 a[42:43], v225 offset:4096\n"
    "v_mfma_f32_16x16x32_bf16 v[52:55], a[20:23], a[108:111], v[52:55]\n"
    "buffer_load_dwordx4 a[88:91], v220, s[52:55], s74 offen offset:2048 sc1\n"
    "v_mfma_f32_16x16x32_bf16 v[56:59], a[16:19], a[112:115], v[56:59]\n"
    "s_lshl_b32 s75, s81, 10\n"
    "ds_read_b64_tr_b16 a[44:45], v225 offset:9216\n"
    "v_mfma_f32_16x16x32_bf16 v[56:59], a[20:23], a[116:119], v[56:59]\n"
    "v_mfma_f32_16x16x32_bf16 v[60:63], a[16:19], a[120:123], v[60:63]\n"
    "ds_read_b64_tr_b16 a[46:47], v225 offset:13312\n"
    "v_mfma_f32_16x16x32_bf16 v[60:63], a[20:23], a[124:127], v[60:63]\n"
    "s_waitcnt lgkmcnt(8)\n"
    "s_waitcnt vmcnt(63)\n"
    "buffer_load_dwordx4 a[68:71], v217, s[52:55], s75 offen offset:3072 sc1\n"
    "v_mfma_f32_16x16x32_bf16 v[48:51], a[24:27], a[128:131], v[48:51]\n"
    "ds_read_b64_tr_b16 a[0:1], v226 offset:26624\n"
    "v_mfma_f32_16x16x32_bf16 v[48:51], a[28:31], a[132:135], v[48:51]\n"
    "v_mfma_f32_16x16x32_bf16 v[52:55], a[24:27], a[136:139], v[52:55]\n"
    "ds_read_b64_tr_b16 a[2:3], v226 offset:27136\n"
    "v_mfma_f32_16x16x32_bf16 v[52:55], a[28:31], a[140:143], v[52:55]\n"
    "buffer_load_dwordx4 a[76:79], v218, s[52:55], s75 offen offset:3072 sc1\n"
    "v_mfma_f32_16x16x32_bf16 v[56:59], a[24:27], a[144:147], v[56:59]\n"
    "ds_read_b64_tr_b16 a[4:5], v226 offset:35840\n"
    "v_mfma_f32_16x16x32_bf16 v[56:59], a[28:31], a[148:151], v[56:59]\n"
    "v_mfma_f32_16x16x32_bf16 v[60:63], a[24:27], a[152:155], v[60:63]\n"
    "ds_read_b64_tr_b16 a[6:7], v226 offset:36352\n"
    "v_mfma_f32_16x16x32_bf16 v[60:63], a[28:31], a[156:159], v[60:63]\n"
    "s_waitcnt lgkmcnt(8)\n"
    "buffer_load_dwordx4 a[84:87], v219, s[52:55], s75 offen offset:3072 sc1\n"
    "v_mfma_f32_16x16x32_bf16 v[112:115], a[32:35], a[128:131], v[112:115]\n"
    "ds_read_b64_tr_b16 a[8:9], v224 offset:18432\n"
    "v_mfma_f32_16x16x32_bf16 v[112:115], a[36:39], a[132:135], v[112:115]\n"
    "s_barrier\n"
    "v_mfma_f32_16x16x32_bf16 v[116:119], a[32:35], a[136:139], v[116:119]\n"
    "s_cmp_eq_u32 s78, 0\n"
    "s_cselect_b32 s69, s82, s83\n"
    "s_cselect_b32 s66, 3, 3\n"
    "ds_read_b64_tr_b16 a[10:11], v224 offset:22528\n"
    "v_mfma_f32_16x16x32_bf16 v[116:119], a[36:39], a[140:143], v[116:119]\n"
    "s_add_u32 s69, s69, s66\n"
    "buffer_load_dwordx4 a[92:95], v220, s[52:55], s75 offen offset:3072 sc1\n"
    "v_mfma_f32_16x16x32_bf16 v[120:123], a[32:35], a[144:147], v[120:123]\n"
    "s_lshl_b32 s74, s82, 10\n"
    "ds_read_b64_tr_b16 a[12:13], v224 offset:27648\n"
    "v_mfma_f32_16x16x32_bf16 v[120:123], a[36:39], a[148:151], v[120:123]\n"
    "s_mul_i32 s69, s69, 36864\n"
    "v_mfma_f32_16x16x32_bf16 v[124:127], a[32:35], a[152:155], v[124:127]\n"
    "s_add_u32 s69, s69, s79\n"
    "ds_read_b64_tr_b16 a[14:15], v224 offset:31744\n"
    "v_mfma_f32_16x16x32_bf16 v[124:127], a[36:39], a[156:159], v[124:127]\n"
    "s_waitcnt lgkmcnt(8)\n"
    "s_add_u32 m0, s70, 36864\n"
    "buffer_load_dwordx4 a[96:99], v217, s[52:55], s74 offen offset:3072 sc1\n"
    "v_mfma_f32_16x16x32_bf16 v[128:131], a[40:43], a[128:131], v[128:131]\n"
    "ds_read_b64_tr_b16 a[16:17], v225 offset:18432\n"
    "v_mfma_f32_16x16x32_bf16 v[128:131], a[44:47], a[132:135], v[128:131]\n"
    "v_mfma_f32_16x16x32_bf16 v[132:135], a[40:43], a[136:139], v[132:135]\n"
    "ds_read_b64_tr_b16 a[18:19], v225 offset:22528\n"
    "v_mfma_f32_16x16x32_bf16 v[132:135], a[44:47], a[140:143], v[132:135]\n"
    "buffer_load_dwordx4 a[104:107], v218, s[52:55], s74 offen offset:3072 sc1\n"
    "v_mfma_f32_16x16x32_bf16 v[136:139], a[40:43], a[144:147], v[136:139]\n"
    "ds_read_b64_tr_b16 a[20:21], v225 offset:27648\n"
    "v_mfma_f32_16x16x32_bf16 v[136:139], a[44:47], a[148:151], v[136:139]\n"
    "v_mfma_f32_16x16x32_bf16 v[140:143], a[40:43], a[152:155], v[140:143]\n"
    "ds_read_b64_tr_b16 a[22:23], v225 offset:31744\n"
    "v_mfma_f32_16x16x32_bf16 v[140:143], a[44:47], a[156:159], v[140:143]\n"
    "s_waitcnt lgkmcnt(8)\n"
    "s_waitcnt vmcnt(62)\n"
    "buffer_load_dwordx4 a[112:115], v219, s[52:55], s74 offen offset:3072 sc1\n"
    "v_mfma_f32_16x16x32_bf16 v[48:51], a[0:3], a[160:163], v[48:51]\n"
    "ds_read_b64_tr_b16 a[24:25], v226 offset:45056\n"
    "v_mfma_f32_16x16x32_bf16 v[48:51], a[4:7], a[164:167], v[48:51]\n"
    "v_mfma_f32_16x16x32_bf16 v[52:55], a[0:3], a[168:171], v[52:55]\n"
    "ds_read_b64_tr_b16 a[26:27], v226 offset:45568\n"
    "v_mfma_f32_16x16x32_bf16 v[52:55], a[4:7], a[172:175], v[52:55]\n"
    "buffer_load_dwordx4 a[120:123], v220, s[52:55], s74 offen offset:3072 sc1\n"
    "v_mfma_f32_16x16x32_bf16 v[56:59], a[0:3], a[176:179], v[56:59]\n"
    "s_lshl_b32 s75, s83, 10\n"
    "ds_read_b64_tr_b16 a[28:29], v226 offset:54272\n"
    "v_mfma_f32_16x16x32_bf16 v[56:59], a[4:7], a[180:183], v[56:59]\n"
    "v_mfma_f32_16x16x32_bf16 v[60:63], a[0:3], a[184:187], v[60:63]\n"
    "ds_read_b64_tr_b16 a[30:31], v226 offset:54784\n"
    "v_mfma_f32_16x16x32_bf16 v[60:63], a[4:7], a[188:191], v[60:63]\n"
    "s_waitcnt lgkmcnt(8)\n"
    "buffer_load_dwordx4 a[100:103], v217, s[52:55], s75 offen offset:3072 sc1\n"
    "v_mfma_f32_16x16x32_bf16 v[112:115], a[8:11], a[160:163], v[112:115]\n"
    "ds_read_b64_tr_b16 a[32:33], v224 offset:36864\n"
    "v_mfma_f32_16x16x32_bf16 v[112:115], a[12:15], a[164:167], v[112:115]\n"
    "v_mfma_f32_16x16x32_bf16 v[116:119], a[8:11], a[168:171], v[116:119]\n"
    "ds_read_b64_tr_b16 a[34:35], v224 offset:40960\n"
    "v_mfma_f32_16x16x32_bf16 v[116:119], a[12:15], a[172:175], v[116:119]\n"
    "buffer_load_dwordx4 a[108:111], v218, s[52:55], s75 offen offset:3072 sc1\n"
    "v_mfma_f32_16x16x32_bf16 v[120:123], a[8:11], a[176:179], v[120:123]\n"
    "ds_read_b64_tr_b16 a[36:37], v224 offset:46080\n"
    "v_mfma_f32_16x16x32_bf16 v[120:123], a[12:15], a[180:183], v[120:123]\n"
    "v_mfma_f32_16x16x32_bf16 v[124:127], a[8:11], a[184:187], v[124:127]\n"
    "ds_read_b64_tr_b16 a[38:39], v224 offset:50176\n"
    "v_mfma_f32_16x16x32_bf16 v[124:127], a[12:15], a[188:191], v[124:127]\n"
    "s_waitcnt lgkmcnt(8)\n"
    "buffer_load_dwordx4 a[116:119], v219, s[52:55], s75 offen offset:3072 sc1\n"
    "v_mfma_f32_16x16x32_bf16 v[128:131], a[16:19], a[160:163], v[128:131]\n"
    "ds_read_b64_tr_b16 a[40:41], v225 offset:36864\n"
    "v_mfma_f32_16x16x32_bf16 v[128:131], a[20:23], a[164:167], v[128:131]\n"
    "v_mfma_f32_16x16x32_bf16 v[132:135], a[16:19], a[168:171], v[132:135]\n"
    "ds_read_b64_tr_b16 a[42:43], v225 offset:40960\n"
    "v_mfma_f32_16x16x32_bf16 v[132:135], a[20:23], a[172:175], v[132:135]\n"
    "buffer_load_dwordx4 a[124:127], v220, s[52:55], s75 offen offset:3072 sc1\n"
    "v_mfma_f32_16x16x32_bf16 v[136:139], a[16:19], a[176:179], v[136:139]\n"
    "ds_read_b64_tr_b16 a[44:45], v225 offset:46080\n"
    "v_mfma_f32_16x16x32_bf16 v[136:139], a[20:23], a[180:183], v[136:139]\n"
    "v_mfma_f32_16x16x32_bf16 v[140:143], a[16:19], a[184:187], v[140:143]\n"
    "s_waitcnt vmcnt(17)\n"
    "ds_read_b64_tr_b16 a[46:47], v225 offset:50176\n"
    "v_mfma_f32_16x16x32_bf16 v[140:143], a[20:23], a[188:191], v[140:143]\n"
    "s_waitcnt lgkmcnt(8)\n"
    "s_waitcnt vmcnt(60)\n"
    "s_barrier\n"
    "buffer_load_dwordx4 v208, s[40:43], s69 offen offset:0 lds\n"
    "v_mfma_f32_16x16x32_bf16 v[48:51], a[24:27], a[192:195], v[48:51]\n"
    "ds_read_b64_tr_b16 a[0:1], v223 offset:8192\n"
    "v_mfma_f32_16x16x32_bf16 v[48:51], a[28:31], a[196:199], v[48:51]\n"
    "v_mfma_f32_16x16x32_bf16 v[52:55], a[24:27], a[200:203], v[52:55]\n"
    "ds_read_b64_tr_b16 a[2:3], v223 offset:8704\n"
    "v_mfma_f32_16x16x32_bf16 v[52:55], a[28:31], a[204:207], v[52:55]\n"
    "buffer_load_dwordx4 v209, s[40:43], s69 offen offset:1024 lds\n"
    "v_mfma_f32_16x16x32_bf16 v[56:59], a[24:27], a[208:211], v[56:59]\n"
    "ds_read_b64_tr_b16 a[4:5], v223 offset:17408\n"
    "v_mfma_f32_16x16x32_bf16 v[56:59], a[28:31], a[212:215], v[56:59]\n"
    "v_mfma_f32_16x16x32_bf16 v[60:63], a[24:27], a[216:219], v[60:63]\n"
    "ds_read_b64_tr_b16 a[6:7], v223 offset:17920\n"
    "v_mfma_f32_16x16x32_bf16 v[60:63], a[28:31], a[220:223], v[60:63]\n"
    "s_waitcnt lgkmcnt(8)\n"
    "buffer_load_dwordx4 v210, s[40:43], s69 offen offset:2048 lds\n"
    "v_mfma_f32_16x16x32_bf16 v[112:115], a[32:35], a[192:195], v[112:115]\n"
    "ds_read_b64_tr_b16 a[8:9], v221 offset:0\n"
    "v_mfma_f32_16x16x32_bf16 v[112:115], a[36:39], a[196:199], v[112:115]\n"
    "v_mfma_f32_16x16x32_bf16 v[116:119], a[32:35], a[200:203], v[116:119]\n"
    "ds_read_b64_tr_b16 a[10:11], v221 offset:4096\n"
    "v_mfma_f32_16x16x32_bf16 v[116:119], a[36:39], a[204:207], v[116:119]\n"
    "buffer_load_dwordx4 v211, s[40:43], s69 offen offset:3072 lds\n"
    "v_mfma_f32_16x16x32_bf16 v[120:123], a[32:35], a[208:211], v[120:123]\n"
    "s_add_u32 m0, s71, 36864\n"
    "ds_read_b64_tr_b16 a[12:13], v221 offset:9216\n"
    "v_mfma_f32_16x16x32_bf16 v[120:123], a[36:39], a[212:215], v[120:123]\n"
    "v_mfma_f32_16x16x32_bf16 v[124:127], a[32:35], a[216:219], v[124:127]\n"
    "ds_read_b64_tr_b16 a[14:15], v221 offset:13312\n"
    "v_mfma_f32_16x16x32_bf16 v[124:127], a[36:39], a[220:223], v[124:127]\n"
    "s_waitcnt lgkmcnt(8)\n"
    "s_mov_b64 exec, s[72:73]\n"
    "buffer_load_dwordx4 v212, s[40:43], s69 offen lds\n"
    "s_mov_b64 exec, -1\n"
    "v_mfma_f32_16x16x32_bf16 v[128:131], a[40:43], a[192:195], v[128:131]\n"
    "ds_read_b64_tr_b16 a[16:17], v222 offset:0\n"
    "v_mfma_f32_16x16x32_bf16 v[128:131], a[44:47], a[196:199], v[128:131]\n"
    "v_mfma_f32_16x16x32_bf16 v[132:135], a[40:43], a[200:203], v[132:135]\n"
    "ds_read_b64_tr_b16 a[18:19], v222 offset:4096\n"
    "v_mfma_f32_16x16x32_bf16 v[132:135], a[44:47], a[204:207], v[132:135]\n"
    "v_mfma_f32_16x16x32_bf16 v[136:139], a[40:43], a[208:211], v[136:139]\n"
    "ds_read_b64_tr_b16 a[20:21], v222 offset:9216\n"
    "v_mfma_f32_16x16x32_bf16 v[136:139], a[44:47], a[212:215], v[136:139]\n"
    "v_mfma_f32_16x16x32_bf16 v[140:143], a[40:43], a[216:219], v[140:143]\n"
    "ds_read_b64_tr_b16 a[22:23], v222 offset:13312\n"
    "v_mfma_f32_16x16x32_bf16 v[140:143], a[44:47], a[220:223], v[140:143]\n"
    "s_waitcnt lgkmcnt(8)\n"
    "s_waitcnt vmcnt(50)\n"
    "v_mfma_f32_16x16x32_bf16 v[48:51], a[0:3], a[224:227], v[48:51]\n"
    "ds_read_b64_tr_b16 a[24:25], v223 offset:26624\n"
    "v_mfma_f32_16x16x32_bf16 v[48:51], a[4:7], a[228:231], v[48:51]\n"
    "v_mfma_f32_16x16x32_bf16 v[52:55], a[0:3], a[232:235], v[52:55]\n"
    "ds_read_b64_tr_b16 a[26:27], v223 offset:27136\n"
    "v_mfma_f32_16x16x32_bf16 v[52:55], a[4:7], a[236:239], v[52:55]\n"
    "v_mfma_f32_16x16x32_bf16 v[56:59], a[0:3], a[240:243], v[56:59]\n"
    "ds_read_b64_tr_b16 a[28:29], v223 offset:35840\n"
    "v_mfma_f32_16x16x32_bf16 v[56:59], a[4:7], a[244:247], v[56:59]\n"
    "v_mfma_f32_16x16x32_bf16 v[60:63], a[0:3], a[248:251], v[60:63]\n"
    "ds_read_b64_tr_b16 a[30:31], v223 offset:36352\n"
    "v_mfma_f32_16x16x32_bf16 v[60:63], a[4:7], a[252:255], v[60:63]\n"
    "s_waitcnt lgkmcnt(8)\n"
    "v_mfma_f32_16x16x32_bf16 v[112:115], a[8:11], a[224:227], v[112:115]\n"
    "ds_read_b64_tr_b16 a[32:33], v221 offset:18432\n"
    "v_mfma_f32_16x16x32_bf16 v[112:115], a[12:15], a[228:231], v[112:115]\n"
    "v_mfma_f32_16x16x32_bf16 v[116:119], a[8:11], a[232:235], v[116:119]\n"
    "ds_read_b64_tr_b16 a[34:35], v221 offset:22528\n"
    "v_mfma_f32_16x16x32_bf16 v[116:119], a[12:15], a[236:239], v[116:119]\n"
    "v_mfma_f32_16x16x32_bf16 v[120:123], a[8:11], a[240:243], v[120:123]\n"
    "ds_read_b64_tr_b16 a[36:37], v221 offset:27648\n"
    "v_mfma_f32_16x16x32_bf16 v[120:123], a[12:15], a[244:247], v[120:123]\n"
    "v_mfma_f32_16x16x32_bf16 v[124:127], a[8:11], a[248:251], v[124:127]\n"
    "ds_read_b64_tr_b16 a[38:39], v221 offset:31744\n"
    "v_mfma_f32_16x16x32_bf16 v[124:127], a[12:15], a[252:255], v[124:127]\n"
    "s_waitcnt lgkmcnt(8)\n"
    "v_mfma_f32_16x16x32_bf16 v[128:131], a[16:19], a[224:227], v[128:131]\n"
    "ds_read_b64_tr_b16 a[40:41], v222 offset:18432\n"
    "v_mfma_f32_16x16x32_bf16 v[128:131], a[20:23], a[228:231], v[128:131]\n"
    "v_mfma_f32_16x16x32_bf16 v[132:135], a[16:19], a[232:235], v[132:135]\n"
    "ds_read_b64_tr_b16 a[42:43], v222 offset:22528\n"
    "v_mfma_f32_16x16x32_bf16 v[132:135], a[20:23], a[236:239], v[132:135]\n"
    "v_mfma_f32_16x16x32_bf16 v[136:139], a[16:19], a[240:243], v[136:139]\n"
    "ds_read_b64_tr_b16 a[44:45], v222 offset:27648\n"
    "v_mfma_f32_16x16x32_bf16 v[136:139], a[20:23], a[244:247], v[136:139]\n"
    "v_mfma_f32_16x16x32_bf16 v[140:143], a[16:19], a[248:251], v[140:143]\n"
    "s_waitcnt vmcnt(0)\n"
    "ds_read_b64_tr_b16 a[46:47], v222 offset:31744\n"
    "v_mfma_f32_16x16x32_bf16 v[140:143], a[20:23], a[252:255], v[140:143]\n"
    "s_waitcnt lgkmcnt(8)\n"
    "s_waitcnt vmcnt(13)\n"
    "s_barrier\n"
    "v_mfma_f32_16x16x32_bf16 v[48:51], a[24:27], a[64:67], v[48:51]\n"
    "ds_read_b64_tr_b16 a[0:1], v223 offset:45056\n"
    "v_mfma_f32_16x16x32_bf16 v[48:51], a[28:31], a[68:71], v[48:51]\n"
    "v_mfma_f32_16x16x32_bf16 v[52:55], a[24:27], a[72:75], v[52:55]\n"
    "ds_read_b64_tr_b16 a[2:3], v223 offset:45568\n"
    "v_mfma_f32_16x16x32_bf16 v[52:55], a[28:31], a[76:79], v[52:55]\n"
    "v_mfma_f32_16x16x32_bf16 v[56:59], a[24:27], a[80:83], v[56:59]\n"
    "ds_read_b64_tr_b16 a[4:5], v223 offset:54272\n"
    "v_mfma_f32_16x16x32_bf16 v[56:59], a[28:31], a[84:87], v[56:59]\n"
    "v_mfma_f32_16x16x32_bf16 v[60:63], a[24:27], a[88:91], v[60:63]\n"
    "ds_read_b64_tr_b16 a[6:7], v223 offset:54784\n"
    "v_mfma_f32_16x16x32_bf16 v[60:63], a[28:31], a[92:95], v[60:63]\n"
    "s_waitcnt lgkmcnt(8)\n"
    "v_mfma_f32_16x16x32_bf16 v[112:115], a[32:35], a[64:67], v[112:115]\n"
    "ds_read_b64_tr_b16 a[8:9], v221 offset:36864\n"
    "v_mfma_f32_16x16x32_bf16 v[112:115], a[36:39], a[68:71], v[112:115]\n"
    "v_mfma_f32_16x16x32_bf16 v[116:119], a[32:35], a[72:75], v[116:119]\n"
    "ds_read_b64_tr_b16 a[10:11], v221 offset:40960\n"
    "v_mfma_f32_16x16x32_bf16 v[116:119], a[36:39], a[76:79], v[116:119]\n"
    "v_mfma_f32_16x16x32_bf16 v[120:123], a[32:35], a[80:83], v[120:123]\n"
    "ds_read_b64_tr_b16 a[12:13], v221 offset:46080\n"
    "v_mfma_f32_16x16x32_bf16 v[120:123], a[36:39], a[84:87], v[120:123]\n"
    "v_mfma_f32_16x16x32_bf16 v[124:127], a[32:35], a[88:91], v[124:127]\n"
    "ds_read_b64_tr_b16 a[14:15], v221 offset:50176\n"
    "v_mfma_f32_16x16x32_bf16 v[124:127], a[36:39], a[92:95], v[124:127]\n"
    "s_waitcnt lgkmcnt(8)\n"
    "v_mfma_f32_16x16x32_bf16 v[128:131], a[40:43], a[64:67], v[128:131]\n"
    "ds_read_b64_tr_b16 a[16:17], v222 offset:36864\n"
    "v_mfma_f32_16x16x32_bf16 v[128:131], a[44:47], a[68:71], v[128:131]\n"
    "v_mfma_f32_16x16x32_bf16 v[132:135], a[40:43], a[72:75], v[132:135]\n"
    "ds_read_b64_tr_b16 a[18:19], v222 offset:40960\n"
    "v_mfma_f32_16x16x32_bf16 v[132:135], a[44:47], a[76:79], v[132:135]\n"
    "v_mfma_f32_16x16x32_bf16 v[136:139], a[40:43], a[80:83], v[136:139]\n"
    "ds_read_b64_tr_b16 a[20:21], v222 offset:46080\n"
    "v_mfma_f32_16x16x32_bf16 v[136:139], a[44:47], a[84:87], v[136:139]\n"
    "v_mfma_f32_16x16x32_bf16 v[140:143], a[40:43], a[88:91], v[140:143]\n"
    "ds_read_b64_tr_b16 a[22:23], v222 offset:50176\n"
    "v_mfma_f32_16x16x32_bf16 v[140:143], a[44:47], a[92:95], v[140:143]\n"
    "s_waitcnt lgkmcnt(8)\n"
    "s_waitcnt vmcnt(5)\n"
    "v_mfma_f32_16x16x32_bf16 v[48:51], a[0:3], a[96:99], v[48:51]\n"
    "ds_read_b64_tr_b16 a[24:25], v224 offset:256\n"
    "v_mfma_f32_16x16x32_bf16 v[48:51], a[4:7], a[100:103], v[48:51]\n"
    "v_mfma_f32_16x16x32_bf16 v[52:55], a[0:3], a[104:107], v[52:55]\n"
    "ds_read_b64_tr_b16 a[26:27], v224 offset:4352\n"
    "v_mfma_f32_16x16x32_bf16 v[52:55], a[4:7], a[108:111], v[52:55]\n"
    "v_cvt_pk_bf16_f32 v48, v48, v49\n"
    "v_cvt_pk_bf16_f32 v49, v50, v51\n"
    "v_mfma_f32_16x16x32_bf16 v[56:59], a[0:3], a[112:115], v[56:59]\n"
    "ds_read_b64_tr_b16 a[28:29], v224 offset:9472\n"
    "v_mfma_f32_16x16x32_bf16 v[56:59], a[4:7], a[116:119], v[56:59]\n"
    "buffer_store_dwordx2 v[48:49], v213, s[56:59], s77 offen sc1\n"
    "v_cvt_pk_bf16_f32 v52, v52, v53\n"
    "v_cvt_pk_bf16_f32 v53, v54, v55\n"
    "v_mfma_f32_16x16x32_bf16 v[60:63], a[0:3], a[120:123], v[60:63]\n"
    "ds_read_b64_tr_b16 a[30:31], v224 offset:13568\n"
    "v_mfma_f32_16x16x32_bf16 v[60:63], a[4:7], a[124:127], v[60:63]\n"
    "s_waitcnt lgkmcnt(8)\n"
    "v_cvt_pk_bf16_f32 v56, v56, v57\n"
    "v_cvt_pk_bf16_f32 v57, v58, v59\n"
    "v_mfma_f32_16x16x32_bf16 v[112:115], a[8:11], a[96:99], v[112:115]\n"
    "ds_read_b64_tr_b16 a[32:33], v224 offset:18688\n"
    "v_mfma_f32_16x16x32_bf16 v[112:115], a[12:15], a[100:103], v[112:115]\n"
    "v_cvt_pk_bf16_f32 v60, v60, v61\n"
    "v_cvt_pk_bf16_f32 v61, v62, v63\n"
    "v_mfma_f32_16x16x32_bf16 v[116:119], a[8:11], a[104:107], v[116:119]\n"
    "ds_read_b64_tr_b16 a[34:35], v224 offset:22784\n"
    "v_mfma_f32_16x16x32_bf16 v[116:119], a[12:15], a[108:111], v[116:119]\n"
    "v_mfma_f32_16x16x32_bf16 v[120:123], a[8:11], a[112:115], v[120:123]\n"
    "ds_read_b64_tr_b16 a[36:37], v224 offset:27904\n"
    "v_mfma_f32_16x16x32_bf16 v[120:123], a[12:15], a[116:119], v[120:123]\n"
    "v_mfma_f32_16x16x32_bf16 v[124:127], a[8:11], a[120:123], v[124:127]\n"
    "ds_read_b64_tr_b16 a[38:39], v224 offset:32000\n"
    "v_mfma_f32_16x16x32_bf16 v[124:127], a[12:15], a[124:127], v[124:127]\n"
    "s_waitcnt lgkmcnt(8)\n"
    "v_mfma_f32_16x16x32_bf16 v[128:131], a[16:19], a[96:99], v[128:131]\n"
    "ds_read_b64_tr_b16 a[40:41], v224 offset:37120\n"
    "v_mfma_f32_16x16x32_bf16 v[128:131], a[20:23], a[100:103], v[128:131]\n"
    "buffer_store_dwordx2 v[52:53], v214, s[56:59], s77 offen sc1\n"
    "v_mfma_f32_16x16x32_bf16 v[132:135], a[16:19], a[104:107], v[132:135]\n"
    "ds_read_b64_tr_b16 a[42:43], v224 offset:41216\n"
    "v_mfma_f32_16x16x32_bf16 v[132:135], a[20:23], a[108:111], v[132:135]\n"
    "v_cvt_pk_bf16_f32 v112, v112, v113\n"
    "v_cvt_pk_bf16_f32 v113, v114, v115\n"
    "v_mfma_f32_16x16x32_bf16 v[136:139], a[16:19], a[112:115], v[136:139]\n"
    "ds_read_b64_tr_b16 a[44:45], v224 offset:46336\n"
    "v_cvt_pk_bf16_f32 v114, v128, v129\n"
    "v_mfma_f32_16x16x32_bf16 v[136:139], a[20:23], a[116:119], v[136:139]\n"
    "v_cvt_pk_bf16_f32 v115, v130, v131\n"
    "v_cvt_pk_bf16_f32 v116, v116, v117\n"
    "v_mfma_f32_16x16x32_bf16 v[140:143], a[16:19], a[120:123], v[140:143]\n"
    "ds_read_b64_tr_b16 a[46:47], v224 offset:50432\n"
    "v_cvt_pk_bf16_f32 v117, v118, v119\n"
    "v_mfma_f32_16x16x32_bf16 v[140:143], a[20:23], a[124:127], v[140:143]\n"
    "s_waitcnt lgkmcnt(8)\n"
    "v_cvt_pk_bf16_f32 v118, v132, v133\n"
    "v_cvt_pk_bf16_f32 v119, v134, v135\n"
    "v_mfma_f32_16x16x32_bf16 v[144:147], a[24:27], a[128:131], v[144:147]\n"
    "ds_read_b64_tr_b16 a[0:1], v221 offset:256\n"
    "v_cvt_pk_bf16_f32 v120, v120, v121\n"
    "v_mfma_f32_16x16x32_bf16 v[144:147], a[28:31], a[132:135], v[144:147]\n"
    "v_cvt_pk_bf16_f32 v121, v122, v123\n"
    "v_cvt_pk_bf16_f32 v122, v136, v137\n"
    "v_mfma_f32_16x16x32_bf16 v[148:151], a[24:27], a[136:139], v[148:151]\n"
    "ds_read_b64_tr_b16 a[2:3], v221 offset:4352\n"
    "v_cvt_pk_bf16_f32 v123, v138, v139\n"
    "v_mfma_f32_16x16x32_bf16 v[148:151], a[28:31], a[140:143], v[148:151]\n"
    "v_cvt_pk_bf16_f32 v124, v124, v125\n"
    "v_cvt_pk_bf16_f32 v125, v126, v127\n"
    "v_mfma_f32_16x16x32_bf16 v[152:155], a[24:27], a[144:147], v[152:155]\n"
    "ds_read_b64_tr_b16 a[4:5], v221 offset:9472\n"
    "v_cvt_pk_bf16_f32 v126, v140, v141\n"
    "v_mfma_f32_16x16x32_bf16 v[152:155], a[28:31], a[148:151], v[152:155]\n"
    "buffer_store_dwordx2 v[56:57], v215, s[56:59], s77 offen sc1\n"
    "v_cvt_pk_bf16_f32 v127, v142, v143\n"
    "v_mfma_f32_16x16x32_bf16 v[156:159], a[24:27], a[152:155], v[156:159]\n"
    "ds_read_b64_tr_b16 a[6:7], v221 offset:13568\n"
    "v_mfma_f32_16x16x32_bf16 v[156:159], a[28:31], a[156:159], v[156:159]\n"
    "s_waitcnt lgkmcnt(8)\n"
    "v_mfma_f32_16x16x32_bf16 v[144:147], a[32:35], a[160:163], v[144:147]\n"
    "ds_read_b64_tr_b16 a[8:9], v221 offset:18688\n"
    "v_mfma_f32_16x16x32_bf16 v[144:147], a[36:39], a[164:167], v[144:147]\n"
    "v_mfma_f32_16x16x32_bf16 v[148:151], a[32:35], a[168:171], v[148:151]\n"
    "ds_read_b64_tr_b16 a[10:11], v221 offset:22784\n"
    "v_mfma_f32_16x16x32_bf16 v[148:151], a[36:39], a[172:175], v[148:151]\n"
    "v_mfma_f32_16x16x32_bf16 v[152:155], a[32:35], a[176:179], v[152:155]\n"
    "ds_read_b64_tr_b16 a[12:13], v221 offset:27904\n"
    "v_mfma_f32_16x16x32_bf16 v[152:155], a[36:39], a[180:183], v[152:155]\n"
    "v_mfma_f32_16x16x32_bf16 v[156:159], a[32:35], a[184:187], v[156:159]\n"
    "ds_read_b64_tr_b16 a[14:15], v221 offset:32000\n"
    "v_mfma_f32_16x16x32_bf16 v[156:159], a[36:39], a[188:191], v[156:159]\n"
    "s_waitcnt lgkmcnt(8)\n"
    "v_mfma_f32_16x16x32_bf16 v[144:147], a[40:43], a[192:195], v[144:147]\n"
    "ds_read_b64_tr_b16 a[16:17], v221 offset:37120\n"
    "v_mfma_f32_16x16x32_bf16 v[144:147], a[44:47], a[196:199], v[144:147]\n"
    "buffer_store_dwordx2 v[60:61], v216, s[56:59], s77 offen sc1\n"
    "v_mfma_f32_16x16x32_bf16 v[148:151], a[40:43], a[200:203], v[148:151]\n"
    "ds_read_b64_tr_b16 a[18:19], v221 offset:41216\n"
    "v_mfma_f32_16x16x32_bf16 v[148:151], a[44:47], a[204:207], v[148:151]\n"
    "v_mfma_f32_16x16x32_bf16 v[152:155], a[40:43], a[208:211], v[152:155]\n"
    "ds_read_b64_tr_b16 a[20:21], v221 offset:46336\n"
    "v_mfma_f32_16x16x32_bf16 v[152:155], a[44:47], a[212:215], v[152:155]\n"
    "v_mfma_f32_16x16x32_bf16 v[156:159], a[40:43], a[216:219], v[156:159]\n"
    "ds_read_b64_tr_b16 a[22:23], v221 offset:50432\n"
    "v_mfma_f32_16x16x32_bf16 v[156:159], a[44:47], a[220:223], v[156:159]\n"
    "s_waitcnt lgkmcnt(8)\n"
    "v_mfma_f32_16x16x32_bf16 v[144:147], a[0:3], a[224:227], v[144:147]\n"
    "ds_read_b64_tr_b16 a[24:25], v225 offset:256\n"
    "v_mfma_f32_16x16x32_bf16 v[144:147], a[4:7], a[228:231], v[144:147]\n"
    "v_mfma_f32_16x16x32_bf16 v[148:151], a[0:3], a[232:235], v[148:151]\n"
    "ds_read_b64_tr_b16 a[26:27], v225 offset:4352\n"
    "v_mfma_f32_16x16x32_bf16 v[148:151], a[4:7], a[236:239], v[148:151]\n"
    "v_mfma_f32_16x16x32_bf16 v[152:155], a[0:3], a[240:243], v[152:155]\n"
    "ds_read_b64_tr_b16 a[28:29], v225 offset:9472\n"
    "v_mfma_f32_16x16x32_bf16 v[152:155], a[4:7], a[244:247], v[152:155]\n"
    "buffer_store_dwordx4 v[112:115], v213, s[56:59], s76 offen offset:0 sc1\n"
    "v_mfma_f32_16x16x32_bf16 v[156:159], a[0:3], a[248:251], v[156:159]\n"
    "ds_read_b64_tr_b16 a[30:31], v225 offset:13568\n"
    "v_mfma_f32_16x16x32_bf16 v[156:159], a[4:7], a[252:255], v[156:159]\n"
    "s_waitcnt lgkmcnt(8)\n"
    "v_mfma_f32_16x16x32_bf16 v[144:147], a[8:11], a[64:67], v[144:147]\n"
    "ds_read_b64_tr_b16 a[32:33], v225 offset:18688\n"
    "v_mfma_f32_16x16x32_bf16 v[144:147], a[12:15], a[68:71], v[144:147]\n"
    "v_mfma_f32_16x16x32_bf16 v[148:151], a[8:11], a[72:75], v[148:151]\n"
    "ds_read_b64_tr_b16 a[34:35], v225 offset:22784\n"
    "v_mfma_f32_16x16x32_bf16 v[148:151], a[12:15], a[76:79], v[148:151]\n"
    "v_mfma_f32_16x16x32_bf16 v[152:155], a[8:11], a[80:83], v[152:155]\n"
    "ds_read_b64_tr_b16 a[36:37], v225 offset:27904\n"
    "v_mfma_f32_16x16x32_bf16 v[152:155], a[12:15], a[84:87], v[152:155]\n"
    "buffer_store_dwordx4 v[116:119], v214, s[56:59], s76 offen offset:0 sc1\n"
    "v_mfma_f32_16x16x32_bf16 v[156:159], a[8:11], a[88:91], v[156:159]\n"
    "ds_read_b64_tr_b16 a[38:39], v225 offset:32000\n"
    "v_mfma_f32_16x16x32_bf16 v[156:159], a[12:15], a[92:95], v[156:159]\n"
    "s_waitcnt lgkmcnt(8)\n"
    "v_mfma_f32_16x16x32_bf16 v[144:147], a[16:19], a[96:99], v[144:147]\n"
    "ds_read_b64_tr_b16 a[40:41], v225 offset:37120\n"
    "v_mfma_f32_16x16x32_bf16 v[144:147], a[20:23], a[100:103], v[144:147]\n"
    "v_mfma_f32_16x16x32_bf16 v[148:151], a[16:19], a[104:107], v[148:151]\n"
    "ds_read_b64_tr_b16 a[42:43], v225 offset:41216\n"
    "v_mfma_f32_16x16x32_bf16 v[148:151], a[20:23], a[108:111], v[148:151]\n"
    "v_mfma_f32_16x16x32_bf16 v[152:155], a[16:19], a[112:115], v[152:155]\n"
    "ds_read_b64_tr_b16 a[44:45], v225 offset:46336\n"
    "v_mfma_f32_16x16x32_bf16 v[152:155], a[20:23], a[116:119], v[152:155]\n"
    "buffer_store_dwordx4 v[120:123], v215, s[56:59], s76 offen offset:0 sc1\n"
    "v_mfma_f32_16x16x32_bf16 v[156:159], a[16:19], a[120:123], v[156:159]\n"
    "ds_read_b64_tr_b16 a[46:47], v225 offset:50432\n"
    "v_mfma_f32_16x16x32_bf16 v[156:159], a[20:23], a[124:127], v[156:159]\n"
    "s_waitcnt lgkmcnt(8)\n"
    "v_mfma_f32_16x16x32_bf16 v[160:163], a[24:27], a[128:131], v[160:163]\n"
    "ds_read_b64_tr_b16 a[0:1], v222 offset:256\n"
    "v_mfma_f32_16x16x32_bf16 v[160:163], a[28:31], a[132:135], v[160:163]\n"
    "v_mfma_f32_16x16x32_bf16 v[164:167], a[24:27], a[136:139], v[164:167]\n"
    "ds_read_b64_tr_b16 a[2:3], v222 offset:4352\n"
    "v_mfma_f32_16x16x32_bf16 v[164:167], a[28:31], a[140:143], v[164:167]\n"
    "v_mfma_f32_16x16x32_bf16 v[168:171], a[24:27], a[144:147], v[168:171]\n"
    "ds_read_b64_tr_b16 a[4:5], v222 offset:9472\n"
    "v_mfma_f32_16x16x32_bf16 v[168:171], a[28:31], a[148:151], v[168:171]\n"
    "buffer_store_dwordx4 v[124:127], v216, s[56:59], s76 offen offset:0 sc1\n"
    "v_mfma_f32_16x16x32_bf16 v[172:175], a[24:27], a[152:155], v[172:175]\n"
    "ds_read_b64_tr_b16 a[6:7], v222 offset:13568\n"
    "v_mfma_f32_16x16x32_bf16 v[172:175], a[28:31], a[156:159], v[172:175]\n"
    "s_waitcnt lgkmcnt(8)\n"
    "v_mfma_f32_16x16x32_bf16 v[160:163], a[32:35], a[160:163], v[160:163]\n"
    "ds_read_b64_tr_b16 a[8:9], v222 offset:18688\n"
    "v_mfma_f32_16x16x32_bf16 v[160:163], a[36:39], a[164:167], v[160:163]\n"
    "v_mfma_f32_16x16x32_bf16 v[164:167], a[32:35], a[168:171], v[164:167]\n"
    "ds_read_b64_tr_b16 a[10:11], v222 offset:22784\n"
    "v_mfma_f32_16x16x32_bf16 v[164:167], a[36:39], a[172:175], v[164:167]\n"
    "v_mfma_f32_16x16x32_bf16 v[168:171], a[32:35], a[176:179], v[168:171]\n"
    "ds_read_b64_tr_b16 a[12:13], v222 offset:27904\n"
    "v_mfma_f32_16x16x32_bf16 v[168:171], a[36:39], a[180:183], v[168:171]\n"
    "v_mfma_f32_16x16x32_bf16 v[172:175], a[32:35], a[184:187], v[172:175]\n"
    "ds_read_b64_tr_b16 a[14:15], v222 offset:32000\n"
    "v_mfma_f32_16x16x32_bf16 v[172:175], a[36:39], a[188:191], v[172:175]\n"
    "s_waitcnt lgkmcnt(8)\n"
    "v_mfma_f32_16x16x32_bf16 v[160:163], a[40:43], a[192:195], v[160:163]\n"
    "ds_read_b64_tr_b16 a[16:17], v222 offset:37120\n"
    "v_mfma_f32_16x16x32_bf16 v[160:163], a[44:47], a[196:199], v[160:163]\n"
    "v_mfma_f32_16x16x32_bf16 v[164:167], a[40:43], a[200:203], v[164:167]\n"
    "ds_read_b64_tr_b16 a[18:19], v222 offset:41216\n"
    "v_mfma_f32_16x16x32_bf16 v[164:167], a[44:47], a[204:207], v[164:167]\n"
    "v_mfma_f32_16x16x32_bf16 v[168:171], a[40:43], a[208:211], v[168:171]\n"
    "ds_read_b64_tr_b16 a[20:21], v222 offset:46336\n"
    "v_mfma_f32_16x16x32_bf16 v[168:171], a[44:47], a[212:215], v[168:171]\n"
    "v_mfma_f32_16x16x32_bf16 v[172:175], a[40:43], a[216:219], v[172:175]\n"
    "ds_read_b64_tr_b16 a[22:23], v222 offset:50432\n"
    "v_mfma_f32_16x16x32_bf16 v[172:175], a[44:47], a[220:223], v[172:175]\n"
    "s_waitcnt lgkmcnt(8)\n"
    "v_mfma_f32_16x16x32_bf16 v[160:163], a[0:3], a[224:227], v[160:163]\n"
    "ds_read_b64_tr_b16 a[24:25], v224 offset:512\n"
    "v_mfma_f32_16x16x32_bf16 v[160:163], a[4:7], a[228:231], v[160:163]\n"
    "v_mfma_f32_16x16x32_bf16 v[164:167], a[0:3], a[232:235], v[164:167]\n"
    "ds_read_b64_tr_b16 a[26:27], v224 offset:4608\n"
    "v_mfma_f32_16x16x32_bf16 v[164:167], a[4:7], a[236:239], v[164:167]\n"
    "v_mfma_f32_16x16x32_bf16 v[168:171], a[0:3], a[240:243], v[168:171]\n"
    "ds_read_b64_tr_b16 a[28:29], v224 offset:9728\n"
    "v_mfma_f32_16x16x32_bf16 v[168:171], a[4:7], a[244:247], v[168:171]\n"
    "v_mfma_f32_16x16x32_bf16 v[172:175], a[0:3], a[248:251], v[172:175]\n"
    "ds_read_b64_tr_b16 a[30:31], v224 offset:13824\n"
    "v_mfma_f32_16x16x32_bf16 v[172:175], a[4:7], a[252:255], v[172:175]\n"
    "s_waitcnt lgkmcnt(8)\n"
    "v_mfma_f32_16x16x32_bf16 v[160:163], a[8:11], a[64:67], v[160:163]\n"
    "ds_read_b64_tr_b16 a[32:33], v224 offset:18944\n"
    "v_mfma_f32_16x16x32_bf16 v[160:163], a[12:15], a[68:71], v[160:163]\n"
    "v_mfma_f32_16x16x32_bf16 v[164:167], a[8:11], a[72:75], v[164:167]\n"
    "ds_read_b64_tr_b16 a[34:35], v224 offset:23040\n"
    "v_mfma_f32_16x16x32_bf16 v[164:167], a[12:15], a[76:79], v[164:167]\n"
    "v_mfma_f32_16x16x32_bf16 v[168:171], a[8:11], a[80:83], v[168:171]\n"
    "ds_read_b64_tr_b16 a[36:37], v224 offset:28160\n"
    "v_mfma_f32_16x16x32_bf16 v[168:171], a[12:15], a[84:87], v[168:171]\n"
    "v_mfma_f32_16x16x32_bf16 v[172:175], a[8:11], a[88:91], v[172:175]\n"
    "ds_read_b64_tr_b16 a[38:39], v224 offset:32256\n"
    "v_mfma_f32_16x16x32_bf16 v[172:175], a[12:15], a[92:95], v[172:175]\n"
    "s_waitcnt lgkmcnt(8)\n"
    "v_mfma_f32_16x16x32_bf16 v[160:163], a[16:19], a[96:99], v[160:163]\n"
    "ds_read_b64_tr_b16 a[40:41], v224 offset:37376\n"
    "v_mfma_f32_16x16x32_bf16 v[160:163], a[20:23], a[100:103], v[160:163]\n"
    "v_mfma_f32_16x16x32_bf16 v[164:167], a[16:19], a[104:107], v[164:167]\n"
    "ds_read_b64_tr_b16 a[42:43], v224 offset:41472\n"
    "v_mfma_f32_16x16x32_bf16 v[164:167], a[20:23], a[108:111], v[164:167]\n"
    "v_cvt_pk_bf16_f32 v144, v144, v145\n"
    "v_cvt_pk_bf16_f32 v145, v146, v147\n"
    "v_mfma_f32_16x16x32_bf16 v[168:171], a[16:19], a[112:115], v[168:171]\n"
    "ds_read_b64_tr_b16 a[44:45], v224 offset:46592\n"
    "v_cvt_pk_bf16_f32 v146, v160, v161\n"
    "v_mfma_f32_16x16x32_bf16 v[168:171], a[20:23], a[116:119], v[168:171]\n"
    "v_cvt_pk_bf16_f32 v147, v162, v163\n"
    "v_cvt_pk_bf16_f32 v148, v148, v149\n"
    "v_mfma_f32_16x16x32_bf16 v[172:175], a[16:19], a[120:123], v[172:175]\n"
    "ds_read_b64_tr_b16 a[46:47], v224 offset:50688\n"
    "v_cvt_pk_bf16_f32 v149, v150, v151\n"
    "v_mfma_f32_16x16x32_bf16 v[172:175], a[20:23], a[124:127], v[172:175]\n"
    "s_waitcnt lgkmcnt(8)\n"
    "buffer_store_dwordx4 v[144:147], v213, s[56:59], s76 offen offset:1024 sc1\n"
    "v_cvt_pk_bf16_f32 v150, v164, v165\n"
    "v_cvt_pk_bf16_f32 v151, v166, v167\n"
    "v_mfma_f32_16x16x32_bf16 v[176:179], a[24:27], a[128:131], v[176:179]\n"
    "ds_read_b64_tr_b16 a[0:1], v221 offset:512\n"
    "v_cvt_pk_bf16_f32 v152, v152, v153\n"
    "v_mfma_f32_16x16x32_bf16 v[176:179], a[28:31], a[132:135], v[176:179]\n"
    "v_cvt_pk_bf16_f32 v153, v154, v155\n"
    "v_cvt_pk_bf16_f32 v154, v168, v169\n"
    "v_mfma_f32_16x16x32_bf16 v[180:183], a[24:27], a[136:139], v[180:183]\n"
    "ds_read_b64_tr_b16 a[2:3], v221 offset:4608\n"
    "v_cvt_pk_bf16_f32 v155, v170, v171\n"
    "v_mfma_f32_16x16x32_bf16 v[180:183], a[28:31], a[140:143], v[180:183]\n"
    "v_cvt_pk_bf16_f32 v156, v156, v157\n"
    "v_cvt_pk_bf16_f32 v157, v158, v159\n"
    "v_mfma_f32_16x16x32_bf16 v[184:187], a[24:27], a[144:147], v[184:187]\n"
    "ds_read_b64_tr_b16 a[4:5], v221 offset:9728\n"
    "v_cvt_pk_bf16_f32 v158, v172, v173\n"
    "v_mfma_f32_16x16x32_bf16 v[184:187], a[28:31], a[148:151], v[184:187]\n"
    "v_cvt_pk_bf16_f32 v159, v174, v175\n"
    "v_mfma_f32_16x16x32_bf16 v[188:191], a[24:27], a[152:155], v[188:191]\n"
    "ds_read_b64_tr_b16 a[6:7], v221 offset:13824\n"
    "v_mfma_f32_16x16x32_bf16 v[188:191], a[28:31], a[156:159], v[188:191]\n"
    "s_waitcnt lgkmcnt(8)\n"
    "buffer_store_dwordx4 v[148:151], v214, s[56:59], s76 offen offset:1024 sc1\n"
    "v_mfma_f32_16x16x32_bf16 v[176:179], a[32:35], a[160:163], v[176:179]\n"
    "ds_read_b64_tr_b16 a[8:9], v221 offset:18944\n"
    "v_mfma_f32_16x16x32_bf16 v[176:179], a[36:39], a[164:167], v[176:179]\n"
    "v_mfma_f32_16x16x32_bf16 v[180:183], a[32:35], a[168:171], v[180:183]\n"
    "ds_read_b64_tr_b16 a[10:11], v221 offset:23040\n"
    "v_mfma_f32_16x16x32_bf16 v[180:183], a[36:39], a[172:175], v[180:183]\n"
    "v_mfma_f32_16x16x32_bf16 v[184:187], a[32:35], a[176:179], v[184:187]\n"
    "ds_read_b64_tr_b16 a[12:13], v221 offset:28160\n"
    "v_mfma_f32_16x16x32_bf16 v[184:187], a[36:39], a[180:183], v[184:187]\n"
    "v_mfma_f32_16x16x32_bf16 v[188:191], a[32:35], a[184:187], v[188:191]\n"
    "ds_read_b64_tr_b16 a[14:15], v221 offset:32256\n"
    "v_mfma_f32_16x16x32_bf16 v[188:191], a[36:39], a[188:191], v[188:191]\n"
    "s_waitcnt lgkmcnt(8)\n"
    "buffer_store_dwordx4 v[152:155], v215, s[56:59], s76 offen offset:1024 sc1\n"
    "v_mfma_f32_16x16x32_bf16 v[176:179], a[40:43], a[192:195], v[176:179]\n"
    "ds_read_b64_tr_b16 a[16:17], v221 offset:37376\n"
    "v_mfma_f32_16x16x32_bf16 v[176:179], a[44:47], a[196:199], v[176:179]\n"
    "v_mfma_f32_16x16x32_bf16 v[180:183], a[40:43], a[200:203], v[180:183]\n"
    "ds_read_b64_tr_b16 a[18:19], v221 offset:41472\n"
    "v_mfma_f32_16x16x32_bf16 v[180:183], a[44:47], a[204:207], v[180:183]\n"
    "v_mfma_f32_16x16x32_bf16 v[184:187], a[40:43], a[208:211], v[184:187]\n"
    "ds_read_b64_tr_b16 a[20:21], v221 offset:46592\n"
    "v_mfma_f32_16x16x32_bf16 v[184:187], a[44:47], a[212:215], v[184:187]\n"
    "v_mfma_f32_16x16x32_bf16 v[188:191], a[40:43], a[216:219], v[188:191]\n"
    "ds_read_b64_tr_b16 a[22:23], v221 offset:50688\n"
    "v_mfma_f32_16x16x32_bf16 v[188:191], a[44:47], a[220:223], v[188:191]\n"
    "s_waitcnt lgkmcnt(8)\n"
    "buffer_store_dwordx4 v[156:159], v216, s[56:59], s76 offen offset:1024 sc1\n"
    "v_mfma_f32_16x16x32_bf16 v[176:179], a[0:3], a[224:227], v[176:179]\n"
    "ds_read_b64_tr_b16 a[24:25], v225 offset:512\n"
    "v_mfma_f32_16x16x32_bf16 v[176:179], a[4:7], a[228:231], v[176:179]\n"
    "v_mfma_f32_16x16x32_bf16 v[180:183], a[0:3], a[232:235], v[180:183]\n"
    "ds_read_b64_tr_b16 a[26:27], v225 offset:4608\n"
    "v_mfma_f32_16x16x32_bf16 v[180:183], a[4:7], a[236:239], v[180:183]\n"
    "v_mfma_f32_16x16x32_bf16 v[184:187], a[0:3], a[240:243], v[184:187]\n"
    "ds_read_b64_tr_b16 a[28:29], v225 offset:9728\n"
    "v_mfma_f32_16x16x32_bf16 v[184:187], a[4:7], a[244:247], v[184:187]\n"
    "v_mfma_f32_16x16x32_bf16 v[188:191], a[0:3], a[248:251], v[188:191]\n"
    "ds_read_b64_tr_b16 a[30:31], v225 offset:13824\n"
    "v_mfma_f32_16x16x32_bf16 v[188:191], a[4:7], a[252:255], v[188:191]\n"
    "s_waitcnt lgkmcnt(8)\n"
    "v_mfma_f32_16x16x32_bf16 v[176:179], a[8:11], a[64:67], v[176:179]\n"
    "ds_read_b64_tr_b16 a[32:33], v225 offset:18944\n"
    "v_mfma_f32_16x16x32_bf16 v[176:179], a[12:15], a[68:71], v[176:179]\n"
    "v_mfma_f32_16x16x32_bf16 v[180:183], a[8:11], a[72:75], v[180:183]\n"
    "ds_read_b64_tr_b16 a[34:35], v225 offset:23040\n"
    "v_mfma_f32_16x16x32_bf16 v[180:183], a[12:15], a[76:79], v[180:183]\n"
    "v_mfma_f32_16x16x32_bf16 v[184:187], a[8:11], a[80:83], v[184:187]\n"
    "ds_read_b64_tr_b16 a[36:37], v225 offset:28160\n"
    "v_mfma_f32_16x16x32_bf16 v[184:187], a[12:15], a[84:87], v[184:187]\n"
    "v_mfma_f32_16x16x32_bf16 v[188:191], a[8:11], a[88:91], v[188:191]\n"
    "ds_read_b64_tr_b16 a[38:39], v225 offset:32256\n"
    "v_mfma_f32_16x16x32_bf16 v[188:191], a[12:15], a[92:95], v[188:191]\n"
    "s_waitcnt lgkmcnt(8)\n"
    "v_mfma_f32_16x16x32_bf16 v[176:179], a[16:19], a[96:99], v[176:179]\n"
    "ds_read_b64_tr_b16 a[40:41], v225 offset:37376\n"
    "v_mfma_f32_16x16x32_bf16 v[176:179], a[20:23], a[100:103], v[176:179]\n"
    "v_mfma_f32_16x16x32_bf16 v[180:183], a[16:19], a[104:107], v[180:183]\n"
    "ds_read_b64_tr_b16 a[42:43], v225 offset:41472\n"
    "v_mfma_f32_16x16x32_bf16 v[180:183], a[20:23], a[108:111], v[180:183]\n"
    "v_mfma_f32_16x16x32_bf16 v[184:187], a[16:19], a[112:115], v[184:187]\n"
    "ds_read_b64_tr_b16 a[44:45], v225 offset:46592\n"
    "v_mfma_f32_16x16x32_bf16 v[184:187], a[20:23], a[116:119], v[184:187]\n"
    "v_mfma_f32_16x16x32_bf16 v[188:191], a[16:19], a[120:123], v[188:191]\n"
    "ds_read_b64_tr_b16 a[46:47], v225 offset:50688\n"
    "v_mfma_f32_16x16x32_bf16 v[188:191], a[20:23], a[124:127], v[188:191]\n"
    "s_waitcnt lgkmcnt(8)\n"
    "v_mfma_f32_16x16x32_bf16 v[192:195], a[24:27], a[128:131], v[192:195]\n"
    "ds_read_b64_tr_b16 a[0:1], v222 offset:512\n"
    "v_mfma_f32_16x16x32_bf16 v[192:195], a[28:31], a[132:135], v[192:195]\n"
    "v_mfma_f32_16x16x32_bf16 v[196:199], a[24:27], a[136:139], v[196:199]\n"
    "ds_read_b64_tr_b16 a[2:3], v222 offset:4608\n"
    "v_mfma_f32_16x16x32_bf16 v[196:199], a[28:31], a[140:143], v[196:199]\n"
    "v_mfma_f32_16x16x32_bf16 v[200:203], a[24:27], a[144:147], v[200:203]\n"
    "ds_read_b64_tr_b16 a[4:5], v222 offset:9728\n"
    "v_mfma_f32_16x16x32_bf16 v[200:203], a[28:31], a[148:151], v[200:203]\n"
    "v_mfma_f32_16x16x32_bf16 v[204:207], a[24:27], a[152:155], v[204:207]\n"
    "ds_read_b64_tr_b16 a[6:7], v222 offset:13824\n"
    "v_mfma_f32_16x16x32_bf16 v[204:207], a[28:31], a[156:159], v[204:207]\n"
    "s_waitcnt lgkmcnt(8)\n"
    "v_mfma_f32_16x16x32_bf16 v[192:195], a[32:35], a[160:163], v[192:195]\n"
    "ds_read_b64_tr_b16 a[8:9], v222 offset:18944\n"
    "v_mfma_f32_16x16x32_bf16 v[192:195], a[36:39], a[164:167], v[192:195]\n"
    "v_mfma_f32_16x16x32_bf16 v[196:199], a[32:35], a[168:171], v[196:199]\n"
    "ds_read_b64_tr_b16 a[10:11], v222 offset:23040\n"
    "v_mfma_f32_16x16x32_bf16 v[196:199], a[36:39], a[172:175], v[196:199]\n"
    "v_mfma_f32_16x16x32_bf16 v[200:203], a[32:35], a[176:179], v[200:203]\n"
    "ds_read_b64_tr_b16 a[12:13], v222 offset:28160\n"
    "v_mfma_f32_16x16x32_bf16 v[200:203], a[36:39], a[180:183], v[200:203]\n"
    "v_mfma_f32_16x16x32_bf16 v[204:207], a[32:35], a[184:187], v[204:207]\n"
    "ds_read_b64_tr_b16 a[14:15], v222 offset:32256\n"
    "v_mfma_f32_16x16x32_bf16 v[204:207], a[36:39], a[188:191], v[204:207]\n"
    "s_waitcnt lgkmcnt(8)\n"
    "v_mfma_f32_16x16x32_bf16 v[192:195], a[40:43], a[192:195], v[192:195]\n"
    "ds_read_b64_tr_b16 a[16:17], v222 offset:37376\n"
    "v_mfma_f32_16x16x32_bf16 v[192:195], a[44:47], a[196:199], v[192:195]\n"
    "v_mfma_f32_16x16x32_bf16 v[196:199], a[40:43], a[200:203], v[196:199]\n"
    "ds_read_b64_tr_b16 a[18:19], v222 offset:41472\n"
    "v_mfma_f32_16x16x32_bf16 v[196:199], a[44:47], a[204:207], v[196:199]\n"
    "v_mfma_f32_16x16x32_bf16 v[200:203], a[40:43], a[208:211], v[200:203]\n"
    "ds_read_b64_tr_b16 a[20:21], v222 offset:46592\n"
    "v_mfma_f32_16x16x32_bf16 v[200:203], a[44:47], a[212:215], v[200:203]\n"
    "v_mfma_f32_16x16x32_bf16 v[204:207], a[40:43], a[216:219], v[204:207]\n"
    "ds_read_b64_tr_b16 a[22:23], v222 offset:50688\n"
    "v_mfma_f32_16x16x32_bf16 v[204:207], a[44:47], a[220:223], v[204:207]\n"
    "s_waitcnt lgkmcnt(8)\n"
    "v_mfma_f32_16x16x32_bf16 v[192:195], a[0:3], a[224:227], v[192:195]\n"
    "ds_read_b64_tr_b16 a[24:25], v224 offset:768\n"
    "v_mfma_f32_16x16x32_bf16 v[192:195], a[4:7], a[228:231], v[192:195]\n"
    "v_mfma_f32_16x16x32_bf16 v[196:199], a[0:3], a[232:235], v[196:199]\n"
    "ds_read_b64_tr_b16 a[26:27], v224 offset:4864\n"
    "v_mfma_f32_16x16x32_bf16 v[196:199], a[4:7], a[236:239], v[196:199]\n"
    "v_mfma_f32_16x16x32_bf16 v[200:203], a[0:3], a[240:243], v[200:203]\n"
    "ds_read_b64_tr_b16 a[28:29], v224 offset:9984\n"
    "v_mfma_f32_16x16x32_bf16 v[200:203], a[4:7], a[244:247], v[200:203]\n"
    "v_mfma_f32_16x16x32_bf16 v[204:207], a[0:3], a[248:251], v[204:207]\n"
    "ds_read_b64_tr_b16 a[30:31], v224 offset:14080\n"
    "v_mfma_f32_16x16x32_bf16 v[204:207], a[4:7], a[252:255], v[204:207]\n"
    "s_waitcnt lgkmcnt(8)\n"
    "v_mfma_f32_16x16x32_bf16 v[192:195], a[8:11], a[64:67], v[192:195]\n"
    "ds_read_b64_tr_b16 a[32:33], v224 offset:19200\n"
    "v_mfma_f32_16x16x32_bf16 v[192:195], a[12:15], a[68:71], v[192:195]\n"
    "v_mfma_f32_16x16x32_bf16 v[196:199], a[8:11], a[72:75], v[196:199]\n"
    "ds_read_b64_tr_b16 a[34:35], v224 offset:23296\n"
    "v_mfma_f32_16x16x32_bf16 v[196:199], a[12:15], a[76:79], v[196:199]\n"
    "v_mfma_f32_16x16x32_bf16 v[200:203], a[8:11], a[80:83], v[200:203]\n"
    "ds_read_b64_tr_b16 a[36:37], v224 offset:28416\n"
    "v_mfma_f32_16x16x32_bf16 v[200:203], a[12:15], a[84:87], v[200:203]\n"
    "v_mfma_f32_16x16x32_bf16 v[204:207], a[8:11], a[88:91], v[204:207]\n"
    "ds_read_b64_tr_b16 a[38:39], v224 offset:32512\n"
    "v_mfma_f32_16x16x32_bf16 v[204:207], a[12:15], a[92:95], v[204:207]\n"
    "s_waitcnt lgkmcnt(8)\n"
    "v_mfma_f32_16x16x32_bf16 v[192:195], a[16:19], a[96:99], v[192:195]\n"
    "ds_read_b64_tr_b16 a[40:41], v224 offset:37632\n"
    "v_mfma_f32_16x16x32_bf16 v[192:195], a[20:23], a[100:103], v[192:195]\n"
    "v_mfma_f32_16x16x32_bf16 v[196:199], a[16:19], a[104:107], v[196:199]\n"
    "ds_read_b64_tr_b16 a[42:43], v224 offset:41728\n"
    "v_mfma_f32_16x16x32_bf16 v[196:199], a[20:23], a[108:111], v[196:199]\n"
    "v_cvt_pk_bf16_f32 v176, v176, v177\n"
    "v_cvt_pk_bf16_f32 v177, v178, v179\n"
    "v_mfma_f32_16x16x32_bf16 v[200:203], a[16:19], a[112:115], v[200:203]\n"
    "ds_read_b64_tr_b16 a[44:45], v224 offset:46848\n"
    "v_cvt_pk_bf16_f32 v178, v192, v193\n"
    "v_mfma_f32_16x16x32_bf16 v[200:203], a[20:23], a[116:119], v[200:203]\n"
    "v_cvt_pk_bf16_f32 v179, v194, v195\n"
    "v_cvt_pk_bf16_f32 v180, v180, v181\n"
    "v_mfma_f32_16x16x32_bf16 v[204:207], a[16:19], a[120:123], v[204:207]\n"
    "ds_read_b64_tr_b16 a[46:47], v224 offset:50944\n"
    "v_cvt_pk_bf16_f32 v181, v182, v183\n"
    "v_mfma_f32_16x16x32_bf16 v[204:207], a[20:23], a[124:127], v[204:207]\n"
    "s_waitcnt lgkmcnt(8)\n"
    "buffer_store_dwordx4 v[176:179], v213, s[56:59], s76 offen offset:2048 sc1\n"
    "v_cvt_pk_bf16_f32 v182, v196, v197\n"
    "v_cvt_pk_bf16_f32 v183, v198, v199\n"
    "v_mfma_f32_16x16x32_bf16 v[0:3], a[24:27], a[128:131], v[0:3]\n"
    "ds_read_b64_tr_b16 a[0:1], v221 offset:768\n"
    "v_cvt_pk_bf16_f32 v184, v184, v185\n"
    "v_mfma_f32_16x16x32_bf16 v[0:3], a[28:31], a[132:135], v[0:3]\n"
    "v_cvt_pk_bf16_f32 v185, v186, v187\n"
    "v_cvt_pk_bf16_f32 v186, v200, v201\n"
    "v_mfma_f32_16x16x32_bf16 v[4:7], a[24:27], a[136:139], v[4:7]\n"
    "ds_read_b64_tr_b16 a[2:3], v221 offset:4864\n"
    "v_cvt_pk_bf16_f32 v187, v202, v203\n"
    "v_mfma_f32_16x16x32_bf16 v[4:7], a[28:31], a[140:143], v[4:7]\n"
    "v_cvt_pk_bf16_f32 v188, v188, v189\n"
    "v_cvt_pk_bf16_f32 v189, v190, v191\n"
    "v_mfma_f32_16x16x32_bf16 v[8:11], a[24:27], a[144:147], v[8:11]\n"
    "ds_read_b64_tr_b16 a[4:5], v221 offset:9984\n"
    "v_cvt_pk_bf16_f32 v190, v204, v205\n"
    "v_mfma_f32_16x16x32_bf16 v[8:11], a[28:31], a[148:151], v[8:11]\n"
    "v_cvt_pk_bf16_f32 v191, v206, v207\n"
    "v_mfma_f32_16x16x32_bf16 v[12:15], a[24:27], a[152:155], v[12:15]\n"
    "ds_read_b64_tr_b16 a[6:7], v221 offset:14080\n"
    "v_mfma_f32_16x16x32_bf16 v[12:15], a[28:31], a[156:159], v[12:15]\n"
    "s_waitcnt lgkmcnt(8)\n"
    "buffer_store_dwordx4 v[180:183], v214, s[56:59], s76 offen offset:2048 sc1\n"
    "v_mfma_f32_16x16x32_bf16 v[0:3], a[32:35], a[160:163], v[0:3]\n"
    "ds_read_b64_tr_b16 a[8:9], v221 offset:19200\n"
    "v_mfma_f32_16x16x32_bf16 v[0:3], a[36:39], a[164:167], v[0:3]\n"
    "v_mfma_f32_16x16x32_bf16 v[4:7], a[32:35], a[168:171], v[4:7]\n"
    "ds_read_b64_tr_b16 a[10:11], v221 offset:23296\n"
    "v_mfma_f32_16x16x32_bf16 v[4:7], a[36:39], a[172:175], v[4:7]\n"
    "v_mfma_f32_16x16x32_bf16 v[8:11], a[32:35], a[176:179], v[8:11]\n"
    "ds_read_b64_tr_b16 a[12:13], v221 offset:28416\n"
    "v_mfma_f32_16x16x32_bf16 v[8:11], a[36:39], a[180:183], v[8:11]\n"
    "v_mfma_f32_16x16x32_bf16 v[12:15], a[32:35], a[184:187], v[12:15]\n"
    "ds_read_b64_tr_b16 a[14:15], v221 offset:32512\n"
    "v_mfma_f32_16x16x32_bf16 v[12:15], a[36:39], a[188:191], v[12:15]\n"
    "s_waitcnt lgkmcnt(8)\n"
    "buffer_store_dwordx4 v[184:187], v215, s[56:59], s76 offen offset:2048 sc1\n"
    "v_mfma_f32_16x16x32_bf16 v[0:3], a[40:43], a[192:195], v[0:3]\n"
    "ds_read_b64_tr_b16 a[16:17], v221 offset:37632\n"
    "v_mfma_f32_16x16x32_bf16 v[0:3], a[44:47], a[196:199], v[0:3]\n"
    "v_mfma_f32_16x16x32_bf16 v[4:7], a[40:43], a[200:203], v[4:7]\n"
    "ds_read_b64_tr_b16 a[18:19], v221 offset:41728\n"
    "v_mfma_f32_16x16x32_bf16 v[4:7], a[44:47], a[204:207], v[4:7]\n"
    "v_mfma_f32_16x16x32_bf16 v[8:11], a[40:43], a[208:211], v[8:11]\n"
    "ds_read_b64_tr_b16 a[20:21], v221 offset:46848\n"
    "v_mfma_f32_16x16x32_bf16 v[8:11], a[44:47], a[212:215], v[8:11]\n"
    "v_mfma_f32_16x16x32_bf16 v[12:15], a[40:43], a[216:219], v[12:15]\n"
    "ds_read_b64_tr_b16 a[22:23], v221 offset:50944\n"
    "v_mfma_f32_16x16x32_bf16 v[12:15], a[44:47], a[220:223], v[12:15]\n"
    "s_waitcnt lgkmcnt(8)\n"
    "buffer_store_dwordx4 v[188:191], v216, s[56:59], s76 offen offset:2048 sc1\n"
    "v_mfma_f32_16x16x32_bf16 v[0:3], a[0:3], a[224:227], v[0:3]\n"
    "ds_read_b64_tr_b16 a[24:25], v225 offset:768\n"
    "v_mfma_f32_16x16x32_bf16 v[0:3], a[4:7], a[228:231], v[0:3]\n"
    "v_mfma_f32_16x16x32_bf16 v[4:7], a[0:3], a[232:235], v[4:7]\n"
    "ds_read_b64_tr_b16 a[26:27], v225 offset:4864\n"
    "v_mfma_f32_16x16x32_bf16 v[4:7], a[4:7], a[236:239], v[4:7]\n"
    "v_mfma_f32_16x16x32_bf16 v[8:11], a[0:3], a[240:243], v[8:11]\n"
    "ds_read_b64_tr_b16 a[28:29], v225 offset:9984\n"
    "v_mfma_f32_16x16x32_bf16 v[8:11], a[4:7], a[244:247], v[8:11]\n"
    "v_mfma_f32_16x16x32_bf16 v[12:15], a[0:3], a[248:251], v[12:15]\n"
    "ds_read_b64_tr_b16 a[30:31], v225 offset:14080\n"
    "v_mfma_f32_16x16x32_bf16 v[12:15], a[4:7], a[252:255], v[12:15]\n"
    "s_waitcnt lgkmcnt(8)\n"
    "v_mfma_f32_16x16x32_bf16 v[0:3], a[8:11], a[64:67], v[0:3]\n"
    "ds_read_b64_tr_b16 a[32:33], v225 offset:19200\n"
    "v_mfma_f32_16x16x32_bf16 v[0:3], a[12:15], a[68:71], v[0:3]\n"
    "v_mfma_f32_16x16x32_bf16 v[4:7], a[8:11], a[72:75], v[4:7]\n"
    "ds_read_b64_tr_b16 a[34:35], v225 offset:23296\n"
    "v_mfma_f32_16x16x32_bf16 v[4:7], a[12:15], a[76:79], v[4:7]\n"
    "v_mfma_f32_16x16x32_bf16 v[8:11], a[8:11], a[80:83], v[8:11]\n"
    "ds_read_b64_tr_b16 a[36:37], v225 offset:28416\n"
    "v_mfma_f32_16x16x32_bf16 v[8:11], a[12:15], a[84:87], v[8:11]\n"
    "v_mfma_f32_16x16x32_bf16 v[12:15], a[8:11], a[88:91], v[12:15]\n"
    "ds_read_b64_tr_b16 a[38:39], v225 offset:32512\n"
    "v_mfma_f32_16x16x32_bf16 v[12:15], a[12:15], a[92:95], v[12:15]\n"
    "s_waitcnt lgkmcnt(8)\n"
    "v_mfma_f32_16x16x32_bf16 v[0:3], a[16:19], a[96:99], v[0:3]\n"
    "ds_read_b64_tr_b16 a[40:41], v225 offset:37632\n"
    "v_mfma_f32_16x16x32_bf16 v[0:3], a[20:23], a[100:103], v[0:3]\n"
    "v_mfma_f32_16x16x32_bf16 v[4:7], a[16:19], a[104:107], v[4:7]\n"
    "ds_read_b64_tr_b16 a[42:43], v225 offset:41728\n"
    "v_mfma_f32_16x16x32_bf16 v[4:7], a[20:23], a[108:111], v[4:7]\n"
    "v_mfma_f32_16x16x32_bf16 v[8:11], a[16:19], a[112:115], v[8:11]\n"
    "ds_read_b64_tr_b16 a[44:45], v225 offset:46848\n"
    "v_mfma_f32_16x16x32_bf16 v[8:11], a[20:23], a[116:119], v[8:11]\n"
    "v_mfma_f32_16x16x32_bf16 v[12:15], a[16:19], a[120:123], v[12:15]\n"
    "ds_read_b64_tr_b16 a[46:47], v225 offset:50944\n"
    "v_mfma_f32_16x16x32_bf16 v[12:15], a[20:23], a[124:127], v[12:15]\n"
    "s_waitcnt lgkmcnt(8)\n"
    "v_mfma_f32_16x16x32_bf16 v[16:19], a[24:27], a[128:131], v[16:19]\n"
    "ds_read_b64_tr_b16 a[0:1], v222 offset:768\n"
    "v_mfma_f32_16x16x32_bf16 v[16:19], a[28:31], a[132:135], v[16:19]\n"
    "v_mfma_f32_16x16x32_bf16 v[20:23], a[24:27], a[136:139], v[20:23]\n"
    "ds_read_b64_tr_b16 a[2:3], v222 offset:4864\n"
    "v_mfma_f32_16x16x32_bf16 v[20:23], a[28:31], a[140:143], v[20:23]\n"
    "v_mfma_f32_16x16x32_bf16 v[24:27], a[24:27], a[144:147], v[24:27]\n"
    "ds_read_b64_tr_b16 a[4:5], v222 offset:9984\n"
    "v_mfma_f32_16x16x32_bf16 v[24:27], a[28:31], a[148:151], v[24:27]\n"
    "v_mfma_f32_16x16x32_bf16 v[28:31], a[24:27], a[152:155], v[28:31]\n"
    "ds_read_b64_tr_b16 a[6:7], v222 offset:14080\n"
    "v_mfma_f32_16x16x32_bf16 v[28:31], a[28:31], a[156:159], v[28:31]\n"
    "s_waitcnt lgkmcnt(8)\n"
    "v_mfma_f32_16x16x32_bf16 v[16:19], a[32:35], a[160:163], v[16:19]\n"
    "ds_read_b64_tr_b16 a[8:9], v222 offset:19200\n"
    "v_mfma_f32_16x16x32_bf16 v[16:19], a[36:39], a[164:167], v[16:19]\n"
    "v_mfma_f32_16x16x32_bf16 v[20:23], a[32:35], a[168:171], v[20:23]\n"
    "ds_read_b64_tr_b16 a[10:11], v222 offset:23296\n"
    "v_mfma_f32_16x16x32_bf16 v[20:23], a[36:39], a[172:175], v[20:23]\n"
    "v_mfma_f32_16x16x32_bf16 v[24:27], a[32:35], a[176:179], v[24:27]\n"
    "ds_read_b64_tr_b16 a[12:13], v222 offset:28416\n"
    "v_mfma_f32_16x16x32_bf16 v[24:27], a[36:39], a[180:183], v[24:27]\n"
    "v_mfma_f32_16x16x32_bf16 v[28:31], a[32:35], a[184:187], v[28:31]\n"
    "ds_read_b64_tr_b16 a[14:15], v222 offset:32512\n"
    "v_mfma_f32_16x16x32_bf16 v[28:31], a[36:39], a[188:191], v[28:31]\n"
    "s_waitcnt lgkmcnt(8)\n"
    "v_mfma_f32_16x16x32_bf16 v[16:19], a[40:43], a[192:195], v[16:19]\n"
    "ds_read_b64_tr_b16 a[16:17], v222 offset:37632\n"
    "v_mfma_f32_16x16x32_bf16 v[16:19], a[44:47], a[196:199], v[16:19]\n"
    "v_mfma_f32_16x16x32_bf16 v[20:23], a[40:43], a[200:203], v[20:23]\n"
    "ds_read_b64_tr_b16 a[18:19], v222 offset:41728\n"
    "v_mfma_f32_16x16x32_bf16 v[20:23], a[44:47], a[204:207], v[20:23]\n"
    "v_mfma_f32_16x16x32_bf16 v[24:27], a[40:43], a[208:211], v[24:27]\n"
    "ds_read_b64_tr_b16 a[20:21], v222 offset:46848\n"
    "v_mfma_f32_16x16x32_bf16 v[24:27], a[44:47], a[212:215], v[24:27]\n"
    "v_mfma_f32_16x16x32_bf16 v[28:31], a[40:43], a[216:219], v[28:31]\n"
    "ds_read_b64_tr_b16 a[22:23], v222 offset:50944\n"
    "v_mfma_f32_16x16x32_bf16 v[28:31], a[44:47], a[220:223], v[28:31]\n"
    "s_waitcnt lgkmcnt(8)\n"
    "v_mfma_f32_16x16x32_bf16 v[16:19], a[0:3], a[224:227], v[16:19]\n"
    "v_mfma_f32_16x16x32_bf16 v[16:19], a[4:7], a[228:231], v[16:19]\n"
    "v_mfma_f32_16x16x32_bf16 v[20:23], a[0:3], a[232:235], v[20:23]\n"
    "v_mfma_f32_16x16x32_bf16 v[20:23], a[4:7], a[236:239], v[20:23]\n"
    "v_mfma_f32_16x16x32_bf16 v[24:27], a[0:3], a[240:243], v[24:27]\n"
    "v_mfma_f32_16x16x32_bf16 v[24:27], a[4:7], a[244:247], v[24:27]\n"
    "v_mfma_f32_16x16x32_bf16 v[28:31], a[0:3], a[248:251], v[28:31]\n"
    "v_mfma_f32_16x16x32_bf16 v[28:31], a[4:7], a[252:255], v[28:31]\n"
    "s_waitcnt lgkmcnt(4)\n"
    "v_mfma_f32_16x16x32_bf16 v[16:19], a[8:11], a[64:67], v[16:19]\n"
    "v_mfma_f32_16x16x32_bf16 v[16:19], a[12:15], a[68:71], v[16:19]\n"
    "v_mfma_f32_16x16x32_bf16 v[20:23], a[8:11], a[72:75], v[20:23]\n"
    "v_mfma_f32_16x16x32_bf16 v[20:23], a[12:15], a[76:79], v[20:23]\n"
    "v_mfma_f32_16x16x32_bf16 v[24:27], a[8:11], a[80:83], v[24:27]\n"
    "v_mfma_f32_16x16x32_bf16 v[24:27], a[12:15], a[84:87], v[24:27]\n"
    "v_mfma_f32_16x16x32_bf16 v[28:31], a[8:11], a[88:91], v[28:31]\n"
    "v_mfma_f32_16x16x32_bf16 v[28:31], a[12:15], a[92:95], v[28:31]\n"
    "s_waitcnt lgkmcnt(0)\n"
    "v_mfma_f32_16x16x32_bf16 v[16:19], a[16:19], a[96:99], v[16:19]\n"
    "v_mfma_f32_16x16x32_bf16 v[16:19], a[20:23], a[100:103], v[16:19]\n"
    "v_mfma_f32_16x16x32_bf16 v[20:23], a[16:19], a[104:107], v[20:23]\n"
    "v_mfma_f32_16x16x32_bf16 v[20:23], a[20:23], a[108:111], v[20:23]\n"
    "v_cvt_pk_bf16_f32 v0, v0, v1\n"
    "v_cvt_pk_bf16_f32 v1, v2, v3\n"
    "v_mfma_f32_16x16x32_bf16 v[24:27], a[16:19], a[112:115], v[24:27]\n"
    "v_cvt_pk_bf16_f32 v2, v16, v17\n"
    "v_cvt_pk_bf16_f32 v3, v18, v19\n"
    "v_mfma_f32_16x16x32_bf16 v[24:27], a[20:23], a[116:119], v[24:27]\n"
    "buffer_store_dwordx4 v[0:3], v213, s[56:59], s76 offen offset:3072 sc1\n"
    "v_cvt_pk_bf16_f32 v4, v4, v5\n"
    "v_cvt_pk_bf16_f32 v5, v6, v7\n"
    "v_mfma_f32_16x16x32_bf16 v[28:31], a[16:19], a[120:123], v[28:31]\n"
    "v_cvt_pk_bf16_f32 v6, v20, v21\n"
    "v_cvt_pk_bf16_f32 v7, v22, v23\n"
    "v_mfma_f32_16x16x32_bf16 v[28:31], a[20:23], a[124:127], v[28:31]\n"
    "v_cvt_pk_bf16_f32 v8, v8, v9\n"
    "v_cvt_pk_bf16_f32 v9, v10, v11\n"
    "v_cvt_pk_bf16_f32 v10, v24, v25\n"
    "v_cvt_pk_bf16_f32 v11, v26, v27\n"
    "v_cvt_pk_bf16_f32 v12, v12, v13\n"
    "v_cvt_pk_bf16_f32 v13, v14, v15\n"
    "s_nop 1\n"
    "v_cvt_pk_bf16_f32 v14, v28, v29\n"
    "v_cvt_pk_bf16_f32 v15, v30, v31\n"
    "buffer_store_dwordx4 v[4:7], v214, s[56:59], s76 offen offset:3072 sc1\n"
    "buffer_store_dwordx4 v[8:11], v215, s[56:59], s76 offen offset:3072 sc1\n"
    "buffer_store_dwordx4 v[12:15], v216, s[56:59], s76 offen offset:3072 sc1\n"
    "s_waitcnt vmcnt(0)\n"
    "v_mov_b32 v237, %21\n"
    "s_mov_b64 exec, 1\n"
    "global_store_dword v235, v237, %11 offset:2560 sc1\n"
    "s_mov_b64 exec, -1\n"
    "s_waitcnt lgkmcnt(0)\n"
    "LB_end:\n"
    :: "s"((const char*)h2t + tb0 * 18432u), "s"(2359296u - tb0 * 18432u), "s"((const char*)((wv & 1u) ? wu : wg)), "s"((144u * (mi) + 16u * ((mi) & 1u)) * 2u), "s"((144u * (mi) + (((mi) & 1u) ? 0u : 128u)) * 2u), "s"((char*)hm + (size_t)tb0 * 147456u), "s"(((144u * (mi) + 16u * ((mi) & 1u)) >> 5) * 1024u), "s"(((144u * (mi) + (((mi) & 1u) ? 0u : 128u)) >> 5) * 1024u + 8u * (((144u * (mi) + (((mi) & 1u) ? 0u : 128u)) >> 4) & 1u)), "s"(wv), "s"(ldsb), "s"(0), "s"(fl), "s"(mi), "s"((const char*)hm + (size_t)tb0 * 147456u + xx * 18432u), "s"((const char*)wd + (size_t)xx * 663552u), "s"((144u * (ii) + 16u * ((ii) & 1u)) * 2u), "s"((144u * (ii) + (((ii) & 1u) ? 0u : 128u)) * 2u), "s"((char*)part2 + (size_t)((rt * 8u + xx) * 16u + wv * 4u) * 18432u), "s"(((144u * (ii) + 16u * ((ii) & 1u)) >> 5) * 1024u), "s"(((144u * (ii) + (((ii) & 1u) ? 0u : 128u)) >> 5) * 1024u + 8u * (((144u * (ii) + (((ii) & 1u) ? 0u : 128u)) >> 4) & 1u)), "s"((unsigned)(0x0E0905000E090500ull >> (8u * ii))), "s"(tag)
    : "v0","v1","v2","v3","v4","v5","v6","v7","v8","v9","v10","v11","v12","v13","v14","v15","v16","v17","v18","v19","v20","v21","v22","v23","v24","v25","v26","v27","v28","v29","v30","v31","v32","v33","v34","v35","v36","v37","v38","v39","v40","v41","v42","v43","v44","v45","v46","v47","v48","v49","v50","v51","v52","v53","v54","v55","v56","v57","v58","v59","v60","v61","v62","v63","v64","v65","v66","v67","v68","v69","v70","v71","v72","v73","v74","v75","v76","v77","v78","v79","v80","v81","v82","v83","v84","v85","v86","v87","v88","v89","v90","v91","v92","v93","v94","v95","v96","v97","v98","v99","v100","v101","v102","v103","v104","v105","v106","v107","v108","v109","v110","v111","v112","v113","v114","v115","v116","v117","v118","v119","v120","v121","v122","v123","v124","v125","v126","v127","v128","v129","v130","v131","v132","v133","v134","v135","v136","v137","v138","v139","v140","v141","v142","v143","v144","v145","v146","v147","v148","v149","v150","v151","v152","v153","v154","v155","v156","v157","v158","v159","v160","v161","v162","v163","v164","v165","v166","v167","v168","v169","v170","v171","v172","v173","v174","v175","v176","v177","v178","v179","v180","v181","v182","v183","v184","v185","v186","v187","v188","v189","v190","v191","v192","v193","v194","v195","v196","v197","v198","v199","v200","v201","v202","v203","v204","v205","v206","v207","v208","v209","v210","v211","v212","v213","v214","v215","v216","v217","v218","v219","v220","v221","v222","v223","v224","v225","v226","v227","v228","v229","v230","v231","v232","v233","v234","v235","v236","v237","v238","v239","v240","v241","v242","v243","v244","v245","v246","v247","v248","v249","v250","v251","a0","a1","a2","a3","a4","a5","a6","a7","a8","a9","a10","a11","a12","a13","a14","a15","a16","a17","a18","a19","a20","a21","a22","a23","a24","a25","a26","a27","a28","a29","a30","a31","a32","a33","a34","a35","a36","a37","a38","a39","a40","a41","a42","a43","a44","a45","a46","a47","a48","a49","a50","a51","a52","a53","a54","a55","a56","a57","a58","a59","a60","a61","a62","a63","a64","a65","a66","a67","a68","a69","a70","a71","a72","a73","a74","a75","a76","a77","a78","a79","a80","a81","a82","a83","a84","a85","a86","a87","a88","a89","a90","a91","a92","a93","a94","a95","a96","a97","a98","a99","a100","a101","a102","a103","a104","a105","a106","a107","a108","a109","a110","a111","a112","a113","a114","a115","a116","a117","a118","a119","a120","a121","a122","a123","a124","a125","a126","a127","a128","a129","a130","a131","a132","a133","a134","a135","a136","a137","a138","a139","a140","a141","a142","a143","a144","a145","a146","a147","a148","a149","a150","a151","a152","a153","a154","a155","a156","a157","a158","a159","a160","a161","a162","a163","a164","a165","a166","a167","a168","a169","a170","a171","a172","a173","a174","a175","a176","a177","a178","a179","a180","a181","a182","a183","a184","a185","a186","a187","a188","a189","a190","a191","a192","a193","a194","a195","a196","a197","a198","a199","a200","a201","a202","a203","a204","a205","a206","a207","a208","a209","a210","a211","a212","a213","a214","a215","a216","a217","a218","a219","a220","a221","a222","a223","a224","a225","a226","a227","a228","a229","a230","a231","a232","a233","a234","a235","a236","a237","a238","a239","a240","a241","a242","a243","a244","a245","a246","a247","a248","a249","a250","a251","a252","a253","a254","a255","s40","s41","s42","s43","s44","s45","s46","s47","s48","s49","s50","s51","s52","s53","s54","s55","s56","s57","s58","s59","s60","s61","s62","s63","s64","s65","s66","s67","s68","s69","s70","s71","s72","s73","s74","s75","s76","s77","s78","s79","s80","s81","s82","s83","s84","s85","s86","s87","vcc","scc","memory");

  __syncthreads();
  sweep_mod<308>(gran + 5 * D * 8, modl, tag, wv);
  wait_flags(fl + 5 * 128 + (mi >> 3) * 32u, 32, tag, wv);
  row_phase<2>([](int) {}, nullptr, x, part2, nullptr, nullptr, modl, x1s, red, outp, row0, wv);
  if (bid == 0u && tid(wv) == 0u) __hip_atomic_store(epoch, tag, __ATOMIC_RELAXED, __HIP_MEMORY_SCOPE_AGENT);

}

extern "C" void kernel_launch(void* const* d_in, const int* in_sizes, int n_in, void* d_out, int out_size, void* d_ws, size_t ws_size, hipStream_t stream) {
  char* ws = (char*)d_ws;
  k_mega<<<256, 256, 0, stream>>>((const u16*)d_in[0], (const u16*)d_in[1], (const u16*)d_in[2], (const u16*)d_in[3], (const u16*)d_in[4],
                                  (const u16*)d_in[6], (const u16*)d_in[7], (const u16*)d_in[8], (const u16*)d_in[9], (const u16*)d_in[10], ws, (u16*)d_out);
}
```
